```python
import torch

def _softmax(x, axis=-1):
    return torch.softmax(x, dim=axis)
def _gelu(x):
    return torch.nn.functional.gelu(x, approximate="tanh")
def _silu(x):
    return torch.nn.functional.silu(x)
def _sigmoid(x):
    return torch.sigmoid(x)
def _layernorm(x, axis=-1, eps=1e-5):
    m = x.mean(dim=axis, keepdim=True)
    v = x.var(dim=axis, keepdim=True, unbiased=False)
    return (x - m) / torch.sqrt(v + eps)
def _scatter_add(tgt, idx, src, axis=0):
    return tgt.clone().index_add_(axis, idx, src)
def _segment_sum(src, seg, n):
    out = torch.zeros((n,) + src.shape[1:], dtype=src.dtype)
    return out.index_add_(0, seg, src)
def _pad(x, pw):
    flat = tuple(v for pair in reversed(pw) for v in pair)
    return torch.nn.functional.pad(x, flat)

kernel_name = 'concat_heads__mha_block_cached__mha_block_cached__near_ridge'

def setup_inputs():

    _w = (lambda a: (lambda t: t.to(torch.bfloat16) if t.is_floating_point() else t)(torch.as_tensor(a)))
    def _A_setup():
        B, Sq, D = 8, 256, 448
        H, Sk, Dh = 16, 512, 256
        return {
            "x": _w((lambda *s: torch.randn(*s, dtype=torch.bfloat16) * 0.8)(B, Sq, D)),
            "k_cache": _w((lambda *s: torch.randn(*s, dtype=torch.bfloat16) * 0.8)(B, H, Sk, Dh)),
            "v_cache": _w((lambda *s: torch.randn(*s, dtype=torch.bfloat16) * 0.05)(B, H, Sk, Dh)),
            "wq": _w((lambda *s: torch.randn(*s, dtype=torch.bfloat16) * 0.8)(D, H * Dh) * (D ** -0.5)),
            "wo": _w((lambda *s: torch.randn(*s, dtype=torch.bfloat16) * 0.05)(H * Dh, D) * ((H * Dh) ** -0.5)),
        }
    _A_in = _A_setup()

    def _B_setup():
        B, Sq, D = 8, 256, 448
        H, Sk, Dh = 16, 512, 256
        return {
            "x": _w((lambda *s: torch.randn(*s, dtype=torch.bfloat16) * 0.8)(B, Sq, D)),
            "k_cache": _w((lambda *s: torch.randn(*s, dtype=torch.bfloat16) * 0.8)(B, H, Sk, Dh)),
            "v_cache": _w((lambda *s: torch.randn(*s, dtype=torch.bfloat16) * 0.05)(B, H, Sk, Dh)),
            "wq": _w((lambda *s: torch.randn(*s, dtype=torch.bfloat16) * 0.8)(D, H * Dh) * (D ** -0.5)),
            "wo": _w((lambda *s: torch.randn(*s, dtype=torch.bfloat16) * 0.05)(H * Dh, D) * ((H * Dh) ** -0.5)),
        }
    _B_in = _B_setup()

    return {
        "A_x": _A_in["x"],
        "A_k_cache": _A_in["k_cache"],
        "A_v_cache": _A_in["v_cache"],
        "A_wq": _A_in["wq"],
        "A_wo": _A_in["wo"],
        "B_x": _B_in["x"],
        "B_k_cache": _B_in["k_cache"],
        "B_v_cache": _B_in["v_cache"],
        "B_wq": _B_in["wq"],
        "B_wo": _B_in["wo"],
    }

def reference(A_x, A_k_cache, A_v_cache, A_wq, A_wo, B_x, B_k_cache, B_v_cache, B_wq, B_wo):
    def _A_ref(x, k_cache, v_cache, wq, wo):
        B, Sq, D = x.shape
        H, Dh = 16, 256
        q = (lambda x, *d: x.permute(*d))((x @ wq).reshape(B, Sq, H, Dh), 0, 2, 1, 3)
        scores = torch.einsum("bhqd,bhkd->bhqk", q, k_cache) * (Dh ** -0.5)
        p = _softmax(scores, axis=-1)
        o = torch.einsum("bhqk,bhkd->bhqd", p, v_cache)
        return (lambda x, *d: x.permute(*d))(o, 0, 2, 1, 3).reshape(B, Sq, H * Dh) @ wo

    def _B_ref(x, k_cache, v_cache, wq, wo):
        B, Sq, D = x.shape
        H, Dh = 16, 256
        q = (lambda x, *d: x.permute(*d))((x @ wq).reshape(B, Sq, H, Dh), 0, 2, 1, 3)
        scores = torch.einsum("bhqd,bhkd->bhqk", q, k_cache) * (Dh ** -0.5)
        p = _softmax(scores, axis=-1)
        o = torch.einsum("bhqk,bhkd->bhqd", p, v_cache)
        return (lambda x, *d: x.permute(*d))(o, 0, 2, 1, 3).reshape(B, Sq, H * Dh) @ wo

    a = _A_ref(A_x, A_k_cache, A_v_cache, A_wq, A_wo)
    b = _B_ref(B_x, B_k_cache, B_v_cache, B_wq, B_wo)
    return torch.concatenate([a, b], axis=-1)
```

```cpp
#include <hip/hip_runtime.h>
#include <hip/hip_bf16.h>
#include <stdint.h>

#define STAMPS 0
#define WS_OFF 0
constexpr size_t PART_BYTES = 114688;
constexpr size_t FLAG_OFF = 256 * PART_BYTES;
constexpr size_t DBG_OFF = FLAG_OFF + 32768;

__global__ __launch_bounds__(256, 1) void mha_fused_kernel(const char* xA, const char* kA, const char* vA, const char* wqA, const char* woA,
                                                           const char* xB, const char* kB, const char* vB, const char* wqB, const char* woB,
                                                           char* ws, char* outp) {
  __shared__ __attribute__((aligned(1024))) char lds[163840];
  const unsigned ldsBase = (unsigned)(size_t)(__attribute__((address_space(3))) char*)lds;
  const unsigned bid = blockIdx.x, xcd = bid & 7, i = bid >> 3;
  const unsigned branch = xcd >> 2, hp = (xcd & 3) * 2 + ((i >> 1) & 1), qh = i & 1, b = i >> 2;
  const unsigned wave = __builtin_amdgcn_readfirstlane(threadIdx.x >> 6);
  const unsigned head = hp * 2 + (wave >> 1);
  const char* x = (branch ? xB : xA) + (size_t)(b * 256 + qh * 128) * 896;
  const char* wq = (branch ? wqB : wqA) + (size_t)head * 512;
  const char* k = (branch ? kB : kA) + (size_t)(b * 16 + head) * 262144;
  const char* v = (branch ? vB : vA) + (size_t)(b * 16 + head) * 262144;
  const char* wo = (branch ? woB : woA) + (size_t)head * (256 * 896);
  const unsigned grp = (branch * 8 + b) * 2 + qh;
  char* part = ws + (size_t)grp * 8 * PART_BYTES;
  char* flags = ws + FLAG_OFF + grp * 1024;
  const unsigned row0 = 64 * ((hp >> 1) & 1) + 16 * ((hp & 1) + 2 * (hp >> 2));
  char* out = outp + (size_t)(b * 256 + qh * 128 + row0) * 1792 + branch * 896;
  char* dbg = ws + DBG_OFF + (STAMPS ? (size_t)(bid * 4 + wave) * 4096 : 0);
  asm volatile(R"ASM(
s_mov_b64 s[36:37], %0
s_and_b32 s37, s37, 0xffff
s_mov_b32 s38, 0x1c000
s_mov_b32 s39, 0x20000
s_mov_b64 s[40:41], %1
s_and_b32 s41, s41, 0xffff
s_mov_b32 s42, 0x37e200
s_mov_b32 s43, 0x20000
s_load_dwordx2 s[96:97], %10, 0x200
v_mbcnt_lo_u32_b32 v0, -1, 0
v_mbcnt_hi_u32_b32 v0, -1, v0
s_and_b32 s74, %6, 1
s_lshr_b32 s75, %6, 1
s_lshl_b32 s75, s75, 14
s_add_u32 s75, s75, %7
s_lshl_b32 s60, s74, 12
s_add_u32 s60, s60, s75
s_lshl_b32 s35, s74, 13
s_add_u32 s35, s35, s75
s_lshl_b32 s34, %6, 11
s_add_u32 s34, s34, %7
s_lshl_b32 s70, s74, 3
s_mul_i32 s71, %6, 0x7000
s_mul_i32 s69, s74, 0x1c000
v_bfe_u32 v1, v0, 2, 3
v_add_u32 v1, s70, v1
v_and_b32 v2, 3, v0
v_lshrrev_b32 v3, 5, v0
v_lshl_add_u32 v3, v3, 2, v2
v_lshlrev_b32 v4, 4, v3
v_lshl_add_u32 v233, v1, 13, v4
v_lshrrev_b32 v5, 2, v0
v_mul_u32_u24 v5, 0x380, v5
v_bfe_u32 v4, v0, 5, 1
v_lshlrev_b32 v4, 1, v4
v_xor_b32 v4, v4, v2
v_lshl_add_u32 v4, v4, 4, v5
v_add_u32 v234, s71, v4
s_mov_b32 s61, 0x0
s_add_u32 m0, s60, 0x0
s_nop 0
buffer_load_dwordx4 v233, s[40:43], s61 offen offset:0  lds
s_add_u32 m0, s60, 0x380
s_nop 0
buffer_load_dwordx4 v233, s[40:43], s61 offen offset:128  lds
s_add_u32 m0, s60, 0x700
s_nop 0
buffer_load_dwordx4 v233, s[40:43], s61 offen offset:256  lds
s_add_u32 m0, s60, 0xa80
s_nop 0
buffer_load_dwordx4 v233, s[40:43], s61 offen offset:384  lds
s_mov_b32 s61, 0x20000
s_add_u32 m0, s60, 0x2000
s_nop 0
buffer_load_dwordx4 v233, s[40:43], s61 offen offset:0  lds
s_add_u32 m0, s60, 0x2380
s_nop 0
buffer_load_dwordx4 v233, s[40:43], s61 offen offset:128  lds
s_add_u32 m0, s60, 0x2700
s_nop 0
buffer_load_dwordx4 v233, s[40:43], s61 offen offset:256  lds
s_add_u32 m0, s60, 0x2a80
s_nop 0
buffer_load_dwordx4 v233, s[40:43], s61 offen offset:384  lds
s_mov_b32 s61, 0x0
s_add_u32 m0, s34, 0x20000
s_nop 0
buffer_load_dwordx4 v234, s[36:39], s61 offen offset:0  lds
s_mov_b32 s61, 0x3800
s_add_u32 m0, s34, 0x20400
s_nop 0
buffer_load_dwordx4 v234, s[36:39], s61 offen offset:0  lds
s_mov_b64 s[44:45], %2
s_and_b32 s45, s45, 0xffff
s_mov_b32 s46, 0x40000
s_mov_b32 s47, 0x20000
s_mov_b64 s[48:49], %3
s_and_b32 s49, s49, 0xffff
s_mov_b32 s50, 0x40000
s_mov_b32 s51, 0x20000
s_mov_b64 s[52:53], %4
s_and_b32 s53, s53, 0xffff
s_mov_b32 s54, 0x38000
s_mov_b32 s55, 0x20000
s_mov_b64 s[56:57], %5
s_and_b32 s57, s57, 0xffff
s_mov_b32 s58, 0xe0000
s_mov_b32 s59, 0x20000
s_sub_u32 s44, s44, 0xc00
s_subb_u32 s45, s45, 0
s_sub_u32 s48, s48, 0xc00
s_subb_u32 s49, s49, 0
s_mov_b32 s84, 0xc00
s_mov_b32 s85, 0x880
s_mov_b32 s86, 0x500
s_mov_b32 s87, 0x180
s_mov_b32 s88, 0x2c00
s_mov_b32 s89, 0x2880
s_mov_b32 s90, 0x2500
s_mov_b32 s91, 0x2180
v_bfe_u32 v4, v0, 4, 1
v_lshlrev_b32 v4, 1, v4
v_xor_b32 v2, v4, v2
v_lshl_add_u32 v2, v2, 4, v5
v_add_u32 v235, s69, v2
v_xor_b32 v4, v4, v3
v_lshlrev_b32 v4, 4, v4
v_lshl_add_u32 v232, v1, 9, v4
v_bfe_u32 v4, v1, 3, 1
v_lshlrev_b32 v4, 1, v4
v_xor_b32 v4, v4, v3
v_lshlrev_b32 v4, 4, v4
v_lshl_add_u32 v231, v1, 9, v4
v_lshrrev_b32 v1, 4, v0
v_and_b32 v2, 15, v0
v_lshrrev_b32 v4, 3, v2
v_lshlrev_b32 v3, 1, v4
v_xor_b32 v3, v3, v1
v_lshlrev_b32 v3, 4, v3
v_lshl_add_u32 v5, v2, 6, v3
s_lshl_b32 s70, s74, 12
s_add_u32 s70, s70, %7
s_add_u32 s70, s70, 0x20000
v_add_u32 v5, s70, v5
s_lshr_b32 s70, %6, 1
s_lshl_b32 s70, s70, 11
v_add_u32 v228, s70, v5
v_subrev_u32 v255, s70, v5
v_lshl_add_u32 v3, v4, 12, v3
v_and_b32 v4, 7, v2
v_lshl_add_u32 v3, v4, 6, v3
v_add_u32 v224, s75, v3
v_lshlrev_b32 v3, 4, v2
v_lshl_add_u32 v3, v1, 12, v3
v_add_u32 v227, s75, v3
v_and_b32 v3, 3, v0
v_lshlrev_b32 v3, 3, v3
v_bfe_u32 v4, v0, 2, 2
v_lshl_add_u32 v3, v4, 6, v3
v_and_b32 v4, 1, v1
v_lshl_add_u32 v3, v4, 5, v3
v_lshl_add_u32 v5, v1, 8, v3
v_add_u32 v229, s75, v5
v_xor_b32 v5, 32, v5
v_add_u32 v230, s75, v5
v_lshl_add_u32 v3, v4, 8, v3
v_lshrrev_b32 v4, 1, v1
v_lshl_add_u32 v3, v4, 12, v3
v_add_u32 v225, s75, v3
v_xor_b32 v3, 32, v3
v_add_u32 v226, s75, v3
s_mul_i32 s72, %11, 0x1c000
s_and_b32 s70, %12, 1
s_lshl_b32 s70, s70, 1
s_add_u32 s70, s70, s74
s_lshl_b32 s70, s70, 13
v_lshlrev_b32 v3, 7, v0
v_add_u32 v236, s70, v3
s_lshl_b32 s70, %12, 1
s_add_u32 s70, s70, s74
s_mul_i32 s71, s70, 0x1c00
v_min_u32 v4, 55, v0
v_lshlrev_b32 v4, 7, v4
v_add_u32 v220, s71, v4
s_lshl_b32 s70, %12, 1
s_add_u32 s70, s70, s74
s_mul_i32 s70, s70, 0x1c000
v_lshrrev_b32 v3, 2, v0
v_lshlrev_b32 v3, 13, v3
v_and_b32 v4, 3, v0
v_lshl_add_u32 v3, v4, 7, v3
buffer_load_dword v221, v3, s[40:43], s70 offen nt
buffer_load_dword v221, v3, s[40:43], s70 offen
s_mov_b32 s94, 0x3f803f80
s_mov_b32 s62, 0x3db8aa3b
s_mov_b32 s68, 0x42b17218
s_mov_b32 s63, 0
s_getpc_b64 s[66:67]
LPC:
s_add_u32 s70, s66, LUPD-LPC
s_addc_u32 s71, s67, 0
s_add_u32 s66, s66, LRES-LPC
s_addc_u32 s67, s67, 0
s_add_u32 s92, s70, LST0-LUPD
s_addc_u32 s93, s71, -1
s_lshl_b32 s75, %12, 2
s_and_b32 s75, s75, 12
s_add_u32 s75, s75, %6
s_min_u32 s75, s75, 10
s_lshl_b32 s75, s75, 13
s_lshl_b32 s75, %12, 1
s_and_b32 s64, %11, 1
s_add_u32 s75, s75, s64
s_lshl_b32 s75, s75, 2
s_add_u32 s75, s75, %6
s_mul_i32 s75, s75, 768
v_min_u32 v3, 5, v0
v_lshlrev_b32 v3, 7, v3
v_add_u32 v3, s75, v3
s_mov_b32 s64, LEND-LST0-4
v_min_u32 v3, s64, v3
global_load_dword v221, v3, s[92:93]
s_mov_b64 exec, 0
s_cmp_eq_u32 %6, 0
s_cbranch_scc1 LH0
s_cmp_eq_u32 %6, 1
s_cbranch_scc1 LH1
s_cmp_eq_u32 %6, 2
s_cbranch_scc1 LH2
s_branch LH3
.p2align 12
LHB0:
s_mov_b64 exec, -1
s_waitcnt vmcnt(3)
s_waitcnt lgkmcnt(0)
s_barrier
ds_read_b128 v[128:131], v228 offset:0
ds_read_b128 v[132:135], v228 offset:1024
ds_read_b128 v[136:139], v255 offset:2048
ds_read_b128 v[140:143], v255 offset:3072
ds_read_b64_tr_b16 v[192:193], v227 offset:0
ds_read_b64_tr_b16 v[194:195], v227 offset:256
ds_read_b64_tr_b16 v[196:197], v227 offset:8
ds_read_b64_tr_b16 v[198:199], v227 offset:264
ds_read_b64_tr_b16 v[200:201], v227 offset:512
ds_read_b64_tr_b16 v[202:203], v227 offset:768
LST0:
s_waitcnt lgkmcnt(4)
v_mfma_f32_16x16x32_bf16 v[0:3], v[192:195], v[128:131], 0
ds_read_b64_tr_b16 v[204:205], v227 offset:520
ds_read_b64_tr_b16 v[206:207], v227 offset:776
v_mfma_f32_16x16x32_bf16 v[32:35], v[192:195], v[132:135], 0
s_mov_b32 s61, 0x40000
s_add_u32 m0, s60, 0x8000
ds_read_b64_tr_b16 v[208:209], v227 offset:1024
ds_read_b64_tr_b16 v[210:211], v227 offset:1280
v_mfma_f32_16x16x32_bf16 v[64:67], v[192:195], v[136:139], 0
ds_read_b64_tr_b16 v[212:213], v227 offset:1032
ds_read_b64_tr_b16 v[214:215], v227 offset:1288
v_mfma_f32_16x16x32_bf16 v[96:99], v[192:195], v[140:143], 0
buffer_load_dwordx4 v233, s[40:43], s61 offen offset:0  lds
s_add_u32 m0, s60, 0x8380
s_waitcnt lgkmcnt(8)
v_mfma_f32_16x16x32_bf16 v[4:7], v[196:199], v[128:131], 0
buffer_load_dwordx4 v233, s[40:43], s61 offen offset:128  lds
s_add_u32 m0, s60, 0x8700
v_mfma_f32_16x16x32_bf16 v[36:39], v[196:199], v[132:135], 0
buffer_load_dwordx4 v233, s[40:43], s61 offen offset:256  lds
s_add_u32 m0, s60, 0x8a80
v_mfma_f32_16x16x32_bf16 v[68:71], v[196:199], v[136:139], 0
buffer_load_dwordx4 v233, s[40:43], s61 offen offset:384  lds
s_mov_b32 s61, 0x60000
s_add_u32 m0, s60, 0xa000
v_mfma_f32_16x16x32_bf16 v[100:103], v[196:199], v[140:143], 0
buffer_load_dwordx4 v233, s[40:43], s61 offen offset:0  lds
s_add_u32 m0, s60, 0xa380
s_waitcnt lgkmcnt(6)
v_mfma_f32_16x16x32_bf16 v[8:11], v[200:203], v[128:131], 0
ds_read_b64_tr_b16 v[216:217], v227 offset:1536
ds_read_b64_tr_b16 v[218:219], v227 offset:1792
v_mfma_f32_16x16x32_bf16 v[40:43], v[200:203], v[132:135], 0
buffer_load_dwordx4 v233, s[40:43], s61 offen offset:128  lds
s_add_u32 m0, s60, 0xa700
v_mfma_f32_16x16x32_bf16 v[72:75], v[200:203], v[136:139], 0
buffer_load_dwordx4 v233, s[40:43], s61 offen offset:256  lds
s_add_u32 m0, s60, 0xaa80
v_mfma_f32_16x16x32_bf16 v[104:107], v[200:203], v[140:143], 0
buffer_load_dwordx4 v233, s[40:43], s61 offen offset:384  lds
s_mov_b32 s61, 0x40
s_add_u32 m0, s34, 0x22000
s_waitcnt lgkmcnt(4)
v_mfma_f32_16x16x32_bf16 v[12:15], v[204:207], v[128:131], 0
buffer_load_dwordx4 v234, s[36:39], s61 offen offset:0  lds
s_mov_b32 s61, 0x3840
s_add_u32 m0, s34, 0x22400
v_mfma_f32_16x16x32_bf16 v[44:47], v[204:207], v[132:135], 0
buffer_load_dwordx4 v234, s[36:39], s61 offen offset:0  lds
s_mov_b32 s61, 0x80000
s_add_u32 m0, s60, 0x10000
v_mfma_f32_16x16x32_bf16 v[76:79], v[204:207], v[136:139], 0
ds_read_b64_tr_b16 v[192:193], v227 offset:1544
ds_read_b64_tr_b16 v[194:195], v227 offset:1800
v_mfma_f32_16x16x32_bf16 v[108:111], v[204:207], v[140:143], 0
buffer_load_dwordx4 v233, s[40:43], s61 offen offset:0  lds
s_add_u32 m0, s60, 0x10380
v_mfma_f32_16x16x32_bf16 v[16:19], v[208:211], v[128:131], 0
buffer_load_dwordx4 v233, s[40:43], s61 offen offset:128  lds
s_add_u32 m0, s60, 0x10700
v_mfma_f32_16x16x32_bf16 v[48:51], v[208:211], v[132:135], 0
buffer_load_dwordx4 v233, s[40:43], s61 offen offset:256  lds
s_add_u32 m0, s60, 0x10a80
v_mfma_f32_16x16x32_bf16 v[80:83], v[208:211], v[136:139], 0
buffer_load_dwordx4 v233, s[40:43], s61 offen offset:384  lds
s_mov_b32 s61, 0xa0000
s_add_u32 m0, s60, 0x12000
v_mfma_f32_16x16x32_bf16 v[112:115], v[208:211], v[140:143], 0
buffer_load_dwordx4 v233, s[40:43], s61 offen offset:0  lds
s_add_u32 m0, s60, 0x12380
s_waitcnt lgkmcnt(2)
v_mfma_f32_16x16x32_bf16 v[20:23], v[212:215], v[128:131], 0
ds_read_b64_tr_b16 v[196:197], v227 offset:2048
ds_read_b64_tr_b16 v[198:199], v227 offset:2304
v_mfma_f32_16x16x32_bf16 v[52:55], v[212:215], v[132:135], 0
buffer_load_dwordx4 v233, s[40:43], s61 offen offset:128  lds
s_add_u32 m0, s60, 0x12700
v_mfma_f32_16x16x32_bf16 v[84:87], v[212:215], v[136:139], 0
buffer_load_dwordx4 v233, s[40:43], s61 offen offset:256  lds
s_add_u32 m0, s60, 0x12a80
v_mfma_f32_16x16x32_bf16 v[116:119], v[212:215], v[140:143], 0
buffer_load_dwordx4 v233, s[40:43], s61 offen offset:384  lds
s_mov_b32 s61, 0x80
s_add_u32 m0, s34, 0x24000
v_mfma_f32_16x16x32_bf16 v[24:27], v[216:219], v[128:131], 0
buffer_load_dwordx4 v234, s[36:39], s61 offen offset:0  lds
s_mov_b32 s61, 0x3880
s_add_u32 m0, s34, 0x24400
v_mfma_f32_16x16x32_bf16 v[56:59], v[216:219], v[132:135], 0
ds_read_b64_tr_b16 v[200:201], v227 offset:2056
ds_read_b64_tr_b16 v[202:203], v227 offset:2312
v_mfma_f32_16x16x32_bf16 v[88:91], v[216:219], v[136:139], 0
buffer_load_dwordx4 v234, s[36:39], s61 offen offset:0  lds
v_mfma_f32_16x16x32_bf16 v[120:123], v[216:219], v[140:143], 0
ds_read_b64_tr_b16 v[204:205], v227 offset:2560
ds_read_b64_tr_b16 v[206:207], v227 offset:2816
s_waitcnt lgkmcnt(6)
v_mfma_f32_16x16x32_bf16 v[28:31], v[192:195], v[128:131], 0
ds_read_b64_tr_b16 v[208:209], v227 offset:2568
ds_read_b64_tr_b16 v[210:211], v227 offset:2824
v_mfma_f32_16x16x32_bf16 v[60:63], v[192:195], v[132:135], 0
ds_read_b64_tr_b16 v[212:213], v227 offset:3072
ds_read_b64_tr_b16 v[214:215], v227 offset:3328
v_mfma_f32_16x16x32_bf16 v[92:95], v[192:195], v[136:139], 0
ds_read_b64_tr_b16 v[216:217], v227 offset:3080
ds_read_b64_tr_b16 v[218:219], v227 offset:3336
v_mfma_f32_16x16x32_bf16 v[124:127], v[192:195], v[140:143], 0
s_waitcnt lgkmcnt(10)
v_mfma_f32_16x16x32_bf16 a[128:131], v[196:199], v[128:131], 0
ds_read_b64_tr_b16 v[192:193], v227 offset:3584
v_mfma_f32_16x16x32_bf16 a[132:135], v[196:199], v[132:135], 0
ds_read_b64_tr_b16 v[194:195], v227 offset:3840
v_mfma_f32_16x16x32_bf16 a[136:139], v[196:199], v[136:139], 0
v_mfma_f32_16x16x32_bf16 a[140:143], v[196:199], v[140:143], 0
s_waitcnt lgkmcnt(10)
v_mfma_f32_16x16x32_bf16 a[144:147], v[200:203], v[128:131], 0
ds_read_b64_tr_b16 v[196:197], v227 offset:3592
v_mfma_f32_16x16x32_bf16 a[148:151], v[200:203], v[132:135], 0
ds_read_b64_tr_b16 v[198:199], v227 offset:3848
v_mfma_f32_16x16x32_bf16 a[152:155], v[200:203], v[136:139], 0
v_mfma_f32_16x16x32_bf16 a[156:159], v[200:203], v[140:143], 0
s_waitcnt lgkmcnt(6)
v_mfma_f32_16x16x32_bf16 a[160:163], v[204:207], v[128:131], 0
s_mov_b64 exec, 0
s_cmp_eq_u32 %6, 0
s_cbranch_scc1 LH4
s_cmp_eq_u32 %6, 1
s_cbranch_scc1 LH5
s_cmp_eq_u32 %6, 2
s_cbranch_scc1 LH6
s_branch LH7
LHB1:
s_mov_b64 exec, -1
v_mfma_f32_16x16x32_bf16 a[164:167], v[204:207], v[132:135], 0
s_waitcnt vmcnt(10)
s_waitcnt lgkmcnt(0)
v_mfma_f32_16x16x32_bf16 a[168:171], v[204:207], v[136:139], 0
s_barrier
ds_read_b128 v[144:147], v228 offset:8192
v_mfma_f32_16x16x32_bf16 a[172:175], v[204:207], v[140:143], 0
s_mov_b32 s61, 0xc0000
s_add_u32 m0, s60, 0x18000
ds_read_b128 v[148:151], v228 offset:9216
ds_read_b128 v[152:155], v255 offset:10240
v_mfma_f32_16x16x32_bf16 a[176:179], v[208:211], v[128:131], 0
ds_read_b128 v[156:159], v255 offset:11264
ds_read_b64_tr_b16 v[200:201], v227 offset:32768
v_mfma_f32_16x16x32_bf16 a[180:183], v[208:211], v[132:135], 0
buffer_load_dwordx4 v233, s[40:43], s61 offen offset:0  lds
v_mfma_f32_16x16x32_bf16 a[184:187], v[208:211], v[136:139], 0
s_add_u32 m0, s60, 0x18380
ds_read_b64_tr_b16 v[202:203], v227 offset:33024
ds_read_b64_tr_b16 v[204:205], v227 offset:32776
v_mfma_f32_16x16x32_bf16 a[188:191], v[208:211], v[140:143], 0
ds_read_b64_tr_b16 v[206:207], v227 offset:33032
v_mfma_f32_16x16x32_bf16 a[192:195], v[212:215], v[128:131], 0
buffer_load_dwordx4 v233, s[40:43], s61 offen offset:128  lds
v_mfma_f32_16x16x32_bf16 a[196:199], v[212:215], v[132:135], 0
s_add_u32 m0, s60, 0x18700
ds_read_b64_tr_b16 v[208:209], v227 offset:33280
ds_read_b64_tr_b16 v[210:211], v227 offset:33536
v_mfma_f32_16x16x32_bf16 a[200:203], v[212:215], v[136:139], 0
v_mfma_f32_16x16x32_bf16 a[204:207], v[212:215], v[140:143], 0
buffer_load_dwordx4 v233, s[40:43], s61 offen offset:256  lds
v_mfma_f32_16x16x32_bf16 a[208:211], v[216:219], v[128:131], 0
s_add_u32 m0, s60, 0x18a80
ds_read_b64_tr_b16 v[212:213], v227 offset:33288
v_mfma_f32_16x16x32_bf16 a[212:215], v[216:219], v[132:135], 0
ds_read_b64_tr_b16 v[214:215], v227 offset:33544
v_mfma_f32_16x16x32_bf16 a[216:219], v[216:219], v[136:139], 0
buffer_load_dwordx4 v233, s[40:43], s61 offen offset:384  lds
v_mfma_f32_16x16x32_bf16 a[220:223], v[216:219], v[140:143], 0
s_mov_b32 s61, 0xe0000
s_add_u32 m0, s60, 0x1a000
v_mfma_f32_16x16x32_bf16 a[224:227], v[192:195], v[128:131], 0
ds_read_b64_tr_b16 v[216:217], v227 offset:33792
v_mfma_f32_16x16x32_bf16 a[228:231], v[192:195], v[132:135], 0
buffer_load_dwordx4 v233, s[40:43], s61 offen offset:0  lds
v_mfma_f32_16x16x32_bf16 a[232:235], v[192:195], v[136:139], 0
s_add_u32 m0, s60, 0x1a380
ds_read_b64_tr_b16 v[218:219], v227 offset:34048
v_mfma_f32_16x16x32_bf16 a[236:239], v[192:195], v[140:143], 0
v_mfma_f32_16x16x32_bf16 a[240:243], v[196:199], v[128:131], 0
buffer_load_dwordx4 v233, s[40:43], s61 offen offset:128  lds
v_mfma_f32_16x16x32_bf16 a[244:247], v[196:199], v[132:135], 0
s_add_u32 m0, s60, 0x1a700
ds_read_b64_tr_b16 v[192:193], v227 offset:33800
ds_read_b64_tr_b16 v[194:195], v227 offset:34056
v_mfma_f32_16x16x32_bf16 a[248:251], v[196:199], v[136:139], 0
v_mfma_f32_16x16x32_bf16 a[252:255], v[196:199], v[140:143], 0
LST1:
buffer_load_dwordx4 v233, s[40:43], s61 offen offset:256  lds
s_waitcnt lgkmcnt(4)
v_mfma_f32_16x16x32_bf16 v[0:3], v[200:203], v[144:147], v[0:3]
s_add_u32 m0, s60, 0x1aa80
ds_read_b64_tr_b16 v[196:197], v227 offset:34304
v_mfma_f32_16x16x32_bf16 v[32:35], v[200:203], v[148:151], v[32:35]
ds_read_b64_tr_b16 v[198:199], v227 offset:34560
v_mfma_f32_16x16x32_bf16 v[64:67], v[200:203], v[152:155], v[64:67]
buffer_load_dwordx4 v233, s[40:43], s61 offen offset:384  lds
v_mfma_f32_16x16x32_bf16 v[96:99], v[200:203], v[156:159], v[96:99]
s_mov_b32 s61, 0xc0
s_add_u32 m0, s34, 0x26000
v_mfma_f32_16x16x32_bf16 v[4:7], v[204:207], v[144:147], v[4:7]
ds_read_b64_tr_b16 v[200:201], v227 offset:34312
v_mfma_f32_16x16x32_bf16 v[36:39], v[204:207], v[148:151], v[36:39]
buffer_load_dwordx4 v234, s[36:39], s61 offen offset:0  lds
v_mfma_f32_16x16x32_bf16 v[68:71], v[204:207], v[152:155], v[68:71]
s_mov_b32 s61, 0x38c0
s_add_u32 m0, s34, 0x26400
ds_read_b64_tr_b16 v[202:203], v227 offset:34568
v_mfma_f32_16x16x32_bf16 v[100:103], v[204:207], v[156:159], v[100:103]
v_mfma_f32_16x16x32_bf16 v[8:11], v[208:211], v[144:147], v[8:11]
buffer_load_dwordx4 v234, s[36:39], s61 offen offset:0  lds
v_mfma_f32_16x16x32_bf16 v[40:43], v[208:211], v[148:151], v[40:43]
s_mov_b32 s61, 0x100000
s_add_u32 m0, s60, 0x0
ds_read_b64_tr_b16 v[204:205], v227 offset:34816
ds_read_b64_tr_b16 v[206:207], v227 offset:35072
v_mfma_f32_16x16x32_bf16 v[72:75], v[208:211], v[152:155], v[72:75]
v_mfma_f32_16x16x32_bf16 v[104:107], v[208:211], v[156:159], v[104:107]
buffer_load_dwordx4 v233, s[40:43], s61 offen offset:0  lds
v_mfma_f32_16x16x32_bf16 v[12:15], v[212:215], v[144:147], v[12:15]
s_add_u32 m0, s60, 0x380
ds_read_b64_tr_b16 v[208:209], v227 offset:34824
v_mfma_f32_16x16x32_bf16 v[44:47], v[212:215], v[148:151], v[44:47]
ds_read_b64_tr_b16 v[210:211], v227 offset:35080
v_mfma_f32_16x16x32_bf16 v[76:79], v[212:215], v[152:155], v[76:79]
buffer_load_dwordx4 v233, s[40:43], s61 offen offset:128  lds
v_mfma_f32_16x16x32_bf16 v[108:111], v[212:215], v[156:159], v[108:111]
s_add_u32 m0, s60, 0x700
s_waitcnt lgkmcnt(5)
v_mfma_f32_16x16x32_bf16 v[16:19], v[216:219], v[144:147], v[16:19]
ds_read_b64_tr_b16 v[212:213], v227 offset:35328
v_mfma_f32_16x16x32_bf16 v[48:51], v[216:219], v[148:151], v[48:51]
buffer_load_dwordx4 v233, s[40:43], s61 offen offset:256  lds
v_mfma_f32_16x16x32_bf16 v[80:83], v[216:219], v[152:155], v[80:83]
s_add_u32 m0, s60, 0xa80
ds_read_b64_tr_b16 v[214:215], v227 offset:35584
v_mfma_f32_16x16x32_bf16 v[112:115], v[216:219], v[156:159], v[112:115]
v_mfma_f32_16x16x32_bf16 v[20:23], v[192:195], v[144:147], v[20:23]
buffer_load_dwordx4 v233, s[40:43], s61 offen offset:384  lds
v_mfma_f32_16x16x32_bf16 v[52:55], v[192:195], v[148:151], v[52:55]
s_mov_b32 s61, 0x120000
s_add_u32 m0, s60, 0x2000
ds_read_b64_tr_b16 v[216:217], v227 offset:35336
ds_read_b64_tr_b16 v[218:219], v227 offset:35592
v_mfma_f32_16x16x32_bf16 v[84:87], v[192:195], v[152:155], v[84:87]
v_mfma_f32_16x16x32_bf16 v[116:119], v[192:195], v[156:159], v[116:119]
buffer_load_dwordx4 v233, s[40:43], s61 offen offset:0  lds
v_mfma_f32_16x16x32_bf16 v[24:27], v[196:199], v[144:147], v[24:27]
s_add_u32 m0, s60, 0x2380
ds_read_b64_tr_b16 v[192:193], v227 offset:35840
v_mfma_f32_16x16x32_bf16 v[56:59], v[196:199], v[148:151], v[56:59]
ds_read_b64_tr_b16 v[194:195], v227 offset:36096
v_mfma_f32_16x16x32_bf16 v[88:91], v[196:199], v[152:155], v[88:91]
buffer_load_dwordx4 v233, s[40:43], s61 offen offset:128  lds
v_mfma_f32_16x16x32_bf16 v[120:123], v[196:199], v[156:159], v[120:123]
s_add_u32 m0, s60, 0x2700
s_waitcnt lgkmcnt(5)
v_mfma_f32_16x16x32_bf16 v[28:31], v[200:203], v[144:147], v[28:31]
ds_read_b64_tr_b16 v[196:197], v227 offset:35848
v_mfma_f32_16x16x32_bf16 v[60:63], v[200:203], v[148:151], v[60:63]
buffer_load_dwordx4 v233, s[40:43], s61 offen offset:256  lds
v_mfma_f32_16x16x32_bf16 v[92:95], v[200:203], v[152:155], v[92:95]
s_add_u32 m0, s60, 0x2a80
ds_read_b64_tr_b16 v[198:199], v227 offset:36104
v_mfma_f32_16x16x32_bf16 v[124:127], v[200:203], v[156:159], v[124:127]
v_mfma_f32_16x16x32_bf16 a[128:131], v[204:207], v[144:147], a[128:131]
buffer_load_dwordx4 v233, s[40:43], s61 offen offset:384  lds
v_mfma_f32_16x16x32_bf16 a[132:135], v[204:207], v[148:151], a[132:135]
s_mov_b32 s61, 0x100
s_add_u32 m0, s34, 0x20000
ds_read_b64_tr_b16 v[200:201], v227 offset:36352
ds_read_b64_tr_b16 v[202:203], v227 offset:36608
v_mfma_f32_16x16x32_bf16 a[136:139], v[204:207], v[152:155], a[136:139]
v_mfma_f32_16x16x32_bf16 a[140:143], v[204:207], v[156:159], a[140:143]
buffer_load_dwordx4 v234, s[36:39], s61 offen offset:0  lds
v_mfma_f32_16x16x32_bf16 a[144:147], v[208:211], v[144:147], a[144:147]
ds_read_b64_tr_b16 v[204:205], v227 offset:36360
s_mov_b32 s61, 0x3900
s_add_u32 m0, s34, 0x20400
v_mfma_f32_16x16x32_bf16 a[148:151], v[208:211], v[148:151], a[148:151]
ds_read_b64_tr_b16 v[206:207], v227 offset:36616
v_mfma_f32_16x16x32_bf16 a[152:155], v[208:211], v[152:155], a[152:155]
buffer_load_dwordx4 v234, s[36:39], s61 offen offset:0  lds
v_mfma_f32_16x16x32_bf16 a[156:159], v[208:211], v[156:159], a[156:159]
s_waitcnt lgkmcnt(5)
v_mfma_f32_16x16x32_bf16 a[160:163], v[212:215], v[144:147], a[160:163]
v_mfma_f32_16x16x32_bf16 a[164:167], v[212:215], v[148:151], a[164:167]
s_waitcnt vmcnt(20)
s_waitcnt lgkmcnt(0)
v_mfma_f32_16x16x32_bf16 a[168:171], v[212:215], v[152:155], a[168:171]
s_barrier
ds_read_b128 v[128:131], v228 offset:16384
v_mfma_f32_16x16x32_bf16 a[172:175], v[212:215], v[156:159], a[172:175]
s_mov_b32 s61, 0x140000
s_add_u32 m0, s60, 0x8000
ds_read_b128 v[132:135], v228 offset:17408
ds_read_b128 v[136:139], v255 offset:18432
v_mfma_f32_16x16x32_bf16 a[176:179], v[216:219], v[144:147], a[176:179]
ds_read_b128 v[140:143], v255 offset:19456
v_add_u32 v227, 0x10000, v227
v_mfma_f32_16x16x32_bf16 a[180:183], v[216:219], v[148:151], a[180:183]
buffer_load_dwordx4 v233, s[40:43], s61 offen offset:0  lds
v_mfma_f32_16x16x32_bf16 a[184:187], v[216:219], v[152:155], a[184:187]
ds_read_b64_tr_b16 v[208:209], v227 offset:0
ds_read_b64_tr_b16 v[210:211], v227 offset:256
v_mfma_f32_16x16x32_bf16 a[188:191], v[216:219], v[156:159], a[188:191]
ds_read_b64_tr_b16 v[212:213], v227 offset:8
ds_read_b64_tr_b16 v[214:215], v227 offset:264
v_mfma_f32_16x16x32_bf16 a[192:195], v[192:195], v[144:147], a[192:195]
ds_read_b64_tr_b16 v[216:217], v227 offset:512
v_mfma_f32_16x16x32_bf16 a[196:199], v[192:195], v[148:151], a[196:199]
s_add_u32 m0, s60, 0x8380
ds_read_b64_tr_b16 v[218:219], v227 offset:768
v_mfma_f32_16x16x32_bf16 a[200:203], v[192:195], v[152:155], a[200:203]
v_mfma_f32_16x16x32_bf16 a[204:207], v[192:195], v[156:159], a[204:207]
buffer_load_dwordx4 v233, s[40:43], s61 offen offset:128  lds
v_mfma_f32_16x16x32_bf16 a[208:211], v[196:199], v[144:147], a[208:211]
ds_read_b64_tr_b16 v[192:193], v227 offset:520
v_mfma_f32_16x16x32_bf16 a[212:215], v[196:199], v[148:151], a[212:215]
ds_read_b64_tr_b16 v[194:195], v227 offset:776
v_mfma_f32_16x16x32_bf16 a[216:219], v[196:199], v[152:155], a[216:219]
v_mfma_f32_16x16x32_bf16 a[220:223], v[196:199], v[156:159], a[220:223]
s_add_u32 m0, s60, 0x8700
v_mfma_f32_16x16x32_bf16 a[224:227], v[200:203], v[144:147], a[224:227]
ds_read_b64_tr_b16 v[196:197], v227 offset:1024
v_mfma_f32_16x16x32_bf16 a[228:231], v[200:203], v[148:151], a[228:231]
buffer_load_dwordx4 v233, s[40:43], s61 offen offset:256  lds
v_mfma_f32_16x16x32_bf16 a[232:235], v[200:203], v[152:155], a[232:235]
ds_read_b64_tr_b16 v[198:199], v227 offset:1280
v_mfma_f32_16x16x32_bf16 a[236:239], v[200:203], v[156:159], a[236:239]
v_mfma_f32_16x16x32_bf16 a[240:243], v[204:207], v[144:147], a[240:243]
ds_read_b64_tr_b16 v[200:201], v227 offset:1032
v_mfma_f32_16x16x32_bf16 a[244:247], v[204:207], v[148:151], a[244:247]
s_add_u32 m0, s60, 0x8a80
ds_read_b64_tr_b16 v[202:203], v227 offset:1288
v_mfma_f32_16x16x32_bf16 a[248:251], v[204:207], v[152:155], a[248:251]
v_mfma_f32_16x16x32_bf16 a[252:255], v[204:207], v[156:159], a[252:255]
LST2:
buffer_load_dwordx4 v233, s[40:43], s61 offen offset:384  lds
s_waitcnt lgkmcnt(4)
v_mfma_f32_16x16x32_bf16 v[0:3], v[208:211], v[128:131], v[0:3]
ds_read_b64_tr_b16 v[204:205], v227 offset:1536
v_mfma_f32_16x16x32_bf16 v[32:35], v[208:211], v[132:135], v[32:35]
ds_read_b64_tr_b16 v[206:207], v227 offset:1792
v_mfma_f32_16x16x32_bf16 v[64:67], v[208:211], v[136:139], v[64:67]
v_mfma_f32_16x16x32_bf16 v[96:99], v[208:211], v[140:143], v[96:99]
s_mov_b32 s61, 0x160000
s_add_u32 m0, s60, 0xa000
v_mfma_f32_16x16x32_bf16 v[4:7], v[212:215], v[128:131], v[4:7]
ds_read_b64_tr_b16 v[208:209], v227 offset:1544
v_mfma_f32_16x16x32_bf16 v[36:39], v[212:215], v[132:135], v[36:39]
buffer_load_dwordx4 v233, s[40:43], s61 offen offset:0  lds
v_mfma_f32_16x16x32_bf16 v[68:71], v[212:215], v[136:139], v[68:71]
ds_read_b64_tr_b16 v[210:211], v227 offset:1800
v_mfma_f32_16x16x32_bf16 v[100:103], v[212:215], v[140:143], v[100:103]
v_mfma_f32_16x16x32_bf16 v[8:11], v[216:219], v[128:131], v[8:11]
ds_read_b64_tr_b16 v[212:213], v227 offset:2048
v_mfma_f32_16x16x32_bf16 v[40:43], v[216:219], v[132:135], v[40:43]
s_add_u32 m0, s60, 0xa380
ds_read_b64_tr_b16 v[214:215], v227 offset:2304
v_mfma_f32_16x16x32_bf16 v[72:75], v[216:219], v[136:139], v[72:75]
v_mfma_f32_16x16x32_bf16 v[104:107], v[216:219], v[140:143], v[104:107]
buffer_load_dwordx4 v233, s[40:43], s61 offen offset:128  lds
v_mfma_f32_16x16x32_bf16 v[12:15], v[192:195], v[128:131], v[12:15]
ds_read_b64_tr_b16 v[216:217], v227 offset:2056
v_mfma_f32_16x16x32_bf16 v[44:47], v[192:195], v[132:135], v[44:47]
ds_read_b64_tr_b16 v[218:219], v227 offset:2312
v_mfma_f32_16x16x32_bf16 v[76:79], v[192:195], v[136:139], v[76:79]
v_mfma_f32_16x16x32_bf16 v[108:111], v[192:195], v[140:143], v[108:111]
s_add_u32 m0, s60, 0xa700
s_waitcnt lgkmcnt(5)
v_mfma_f32_16x16x32_bf16 v[16:19], v[196:199], v[128:131], v[16:19]
ds_read_b64_tr_b16 v[192:193], v227 offset:2560
v_mfma_f32_16x16x32_bf16 v[48:51], v[196:199], v[132:135], v[48:51]
buffer_load_dwordx4 v233, s[40:43], s61 offen offset:256  lds
v_mfma_f32_16x16x32_bf16 v[80:83], v[196:199], v[136:139], v[80:83]
ds_read_b64_tr_b16 v[194:195], v227 offset:2816
v_mfma_f32_16x16x32_bf16 v[112:115], v[196:199], v[140:143], v[112:115]
v_mfma_f32_16x16x32_bf16 v[20:23], v[200:203], v[128:131], v[20:23]
ds_read_b64_tr_b16 v[196:197], v227 offset:2568
v_mfma_f32_16x16x32_bf16 v[52:55], v[200:203], v[132:135], v[52:55]
s_add_u32 m0, s60, 0xaa80
ds_read_b64_tr_b16 v[198:199], v227 offset:2824
v_mfma_f32_16x16x32_bf16 v[84:87], v[200:203], v[136:139], v[84:87]
v_mfma_f32_16x16x32_bf16 v[116:119], v[200:203], v[140:143], v[116:119]
buffer_load_dwordx4 v233, s[40:43], s61 offen offset:384  lds
v_mfma_f32_16x16x32_bf16 v[24:27], v[204:207], v[128:131], v[24:27]
ds_read_b64_tr_b16 v[200:201], v227 offset:3072
v_mfma_f32_16x16x32_bf16 v[56:59], v[204:207], v[132:135], v[56:59]
ds_read_b64_tr_b16 v[202:203], v227 offset:3328
v_mfma_f32_16x16x32_bf16 v[88:91], v[204:207], v[136:139], v[88:91]
v_mfma_f32_16x16x32_bf16 v[120:123], v[204:207], v[140:143], v[120:123]
s_mov_b32 s61, 0x140
s_add_u32 m0, s34, 0x22000
s_waitcnt lgkmcnt(5)
v_mfma_f32_16x16x32_bf16 v[28:31], v[208:211], v[128:131], v[28:31]
ds_read_b64_tr_b16 v[204:205], v227 offset:3080
v_mfma_f32_16x16x32_bf16 v[60:63], v[208:211], v[132:135], v[60:63]
buffer_load_dwordx4 v234, s[36:39], s61 offen offset:0  lds
v_mfma_f32_16x16x32_bf16 v[92:95], v[208:211], v[136:139], v[92:95]
ds_read_b64_tr_b16 v[206:207], v227 offset:3336
v_mfma_f32_16x16x32_bf16 v[124:127], v[208:211], v[140:143], v[124:127]
v_mfma_f32_16x16x32_bf16 a[128:131], v[212:215], v[128:131], a[128:131]
ds_read_b64_tr_b16 v[208:209], v227 offset:3584
v_mfma_f32_16x16x32_bf16 a[132:135], v[212:215], v[132:135], a[132:135]
s_mov_b32 s61, 0x3940
s_add_u32 m0, s34, 0x22400
ds_read_b64_tr_b16 v[210:211], v227 offset:3840
v_mfma_f32_16x16x32_bf16 a[136:139], v[212:215], v[136:139], a[136:139]
v_mfma_f32_16x16x32_bf16 a[140:143], v[212:215], v[140:143], a[140:143]
buffer_load_dwordx4 v234, s[36:39], s61 offen offset:0  lds
v_mfma_f32_16x16x32_bf16 a[144:147], v[216:219], v[128:131], a[144:147]
ds_read_b64_tr_b16 v[212:213], v227 offset:3592
v_mfma_f32_16x16x32_bf16 a[148:151], v[216:219], v[132:135], a[148:151]
ds_read_b64_tr_b16 v[214:215], v227 offset:3848
v_mfma_f32_16x16x32_bf16 a[152:155], v[216:219], v[136:139], a[152:155]
v_mfma_f32_16x16x32_bf16 a[156:159], v[216:219], v[140:143], a[156:159]
s_waitcnt lgkmcnt(5)
v_mfma_f32_16x16x32_bf16 a[160:163], v[192:195], v[128:131], a[160:163]
v_mfma_f32_16x16x32_bf16 a[164:167], v[192:195], v[132:135], a[164:167]
s_waitcnt vmcnt(20)
s_waitcnt lgkmcnt(0)
v_mfma_f32_16x16x32_bf16 a[168:171], v[192:195], v[136:139], a[168:171]
s_barrier
ds_read_b128 v[144:147], v228 offset:24576
v_mfma_f32_16x16x32_bf16 a[172:175], v[192:195], v[140:143], a[172:175]
s_mov_b32 s61, 0x180000
s_add_u32 m0, s60, 0x10000
ds_read_b128 v[148:151], v228 offset:25600
ds_read_b128 v[152:155], v255 offset:26624
v_mfma_f32_16x16x32_bf16 a[176:179], v[196:199], v[128:131], a[176:179]
ds_read_b128 v[156:159], v255 offset:27648
ds_read_b64_tr_b16 v[216:217], v227 offset:32768
v_mfma_f32_16x16x32_bf16 a[180:183], v[196:199], v[132:135], a[180:183]
buffer_load_dwordx4 v233, s[40:43], s61 offen offset:0  lds
v_mfma_f32_16x16x32_bf16 a[184:187], v[196:199], v[136:139], a[184:187]
ds_read_b64_tr_b16 v[218:219], v227 offset:33024
ds_read_b64_tr_b16 v[192:193], v227 offset:32776
v_mfma_f32_16x16x32_bf16 a[188:191], v[196:199], v[140:143], a[188:191]
ds_read_b64_tr_b16 v[194:195], v227 offset:33032
v_mfma_f32_16x16x32_bf16 a[192:195], v[200:203], v[128:131], a[192:195]
ds_read_b64_tr_b16 v[196:197], v227 offset:33280
v_mfma_f32_16x16x32_bf16 a[196:199], v[200:203], v[132:135], a[196:199]
s_add_u32 m0, s60, 0x10380
ds_read_b64_tr_b16 v[198:199], v227 offset:33536
v_mfma_f32_16x16x32_bf16 a[200:203], v[200:203], v[136:139], a[200:203]
v_mfma_f32_16x16x32_bf16 a[204:207], v[200:203], v[140:143], a[204:207]
buffer_load_dwordx4 v233, s[40:43], s61 offen offset:128  lds
v_mfma_f32_16x16x32_bf16 a[208:211], v[204:207], v[128:131], a[208:211]
ds_read_b64_tr_b16 v[200:201], v227 offset:33288
v_mfma_f32_16x16x32_bf16 a[212:215], v[204:207], v[132:135], a[212:215]
ds_read_b64_tr_b16 v[202:203], v227 offset:33544
v_mfma_f32_16x16x32_bf16 a[216:219], v[204:207], v[136:139], a[216:219]
v_mfma_f32_16x16x32_bf16 a[220:223], v[204:207], v[140:143], a[220:223]
s_add_u32 m0, s60, 0x10700
v_mfma_f32_16x16x32_bf16 a[224:227], v[208:211], v[128:131], a[224:227]
ds_read_b64_tr_b16 v[204:205], v227 offset:33792
v_mfma_f32_16x16x32_bf16 a[228:231], v[208:211], v[132:135], a[228:231]
buffer_load_dwordx4 v233, s[40:43], s61 offen offset:256  lds
v_mfma_f32_16x16x32_bf16 a[232:235], v[208:211], v[136:139], a[232:235]
ds_read_b64_tr_b16 v[206:207], v227 offset:34048
v_mfma_f32_16x16x32_bf16 a[236:239], v[208:211], v[140:143], a[236:239]
v_mfma_f32_16x16x32_bf16 a[240:243], v[212:215], v[128:131], a[240:243]
ds_read_b64_tr_b16 v[208:209], v227 offset:33800
v_mfma_f32_16x16x32_bf16 a[244:247], v[212:215], v[132:135], a[244:247]
s_add_u32 m0, s60, 0x10a80
ds_read_b64_tr_b16 v[210:211], v227 offset:34056
v_mfma_f32_16x16x32_bf16 a[248:251], v[212:215], v[136:139], a[248:251]
v_mfma_f32_16x16x32_bf16 a[252:255], v[212:215], v[140:143], a[252:255]
LST3:
buffer_load_dwordx4 v233, s[40:43], s61 offen offset:384  lds
s_waitcnt lgkmcnt(4)
v_mfma_f32_16x16x32_bf16 v[0:3], v[216:219], v[144:147], v[0:3]
ds_read_b64_tr_b16 v[212:213], v227 offset:34304
v_mfma_f32_16x16x32_bf16 v[32:35], v[216:219], v[148:151], v[32:35]
ds_read_b64_tr_b16 v[214:215], v227 offset:34560
v_mfma_f32_16x16x32_bf16 v[64:67], v[216:219], v[152:155], v[64:67]
v_mfma_f32_16x16x32_bf16 v[96:99], v[216:219], v[156:159], v[96:99]
s_mov_b32 s61, 0x1a0000
s_add_u32 m0, s60, 0x12000
v_mfma_f32_16x16x32_bf16 v[4:7], v[192:195], v[144:147], v[4:7]
ds_read_b64_tr_b16 v[216:217], v227 offset:34312
v_mfma_f32_16x16x32_bf16 v[36:39], v[192:195], v[148:151], v[36:39]
buffer_load_dwordx4 v233, s[40:43], s61 offen offset:0  lds
v_mfma_f32_16x16x32_bf16 v[68:71], v[192:195], v[152:155], v[68:71]
ds_read_b64_tr_b16 v[218:219], v227 offset:34568
v_mfma_f32_16x16x32_bf16 v[100:103], v[192:195], v[156:159], v[100:103]
v_mfma_f32_16x16x32_bf16 v[8:11], v[196:199], v[144:147], v[8:11]
ds_read_b64_tr_b16 v[192:193], v227 offset:34816
v_mfma_f32_16x16x32_bf16 v[40:43], v[196:199], v[148:151], v[40:43]
s_add_u32 m0, s60, 0x12380
ds_read_b64_tr_b16 v[194:195], v227 offset:35072
v_mfma_f32_16x16x32_bf16 v[72:75], v[196:199], v[152:155], v[72:75]
v_mfma_f32_16x16x32_bf16 v[104:107], v[196:199], v[156:159], v[104:107]
buffer_load_dwordx4 v233, s[40:43], s61 offen offset:128  lds
v_mfma_f32_16x16x32_bf16 v[12:15], v[200:203], v[144:147], v[12:15]
ds_read_b64_tr_b16 v[196:197], v227 offset:34824
v_mfma_f32_16x16x32_bf16 v[44:47], v[200:203], v[148:151], v[44:47]
ds_read_b64_tr_b16 v[198:199], v227 offset:35080
v_mfma_f32_16x16x32_bf16 v[76:79], v[200:203], v[152:155], v[76:79]
v_mfma_f32_16x16x32_bf16 v[108:111], v[200:203], v[156:159], v[108:111]
s_add_u32 m0, s60, 0x12700
s_waitcnt lgkmcnt(5)
v_mfma_f32_16x16x32_bf16 v[16:19], v[204:207], v[144:147], v[16:19]
ds_read_b64_tr_b16 v[200:201], v227 offset:35328
v_mfma_f32_16x16x32_bf16 v[48:51], v[204:207], v[148:151], v[48:51]
buffer_load_dwordx4 v233, s[40:43], s61 offen offset:256  lds
v_mfma_f32_16x16x32_bf16 v[80:83], v[204:207], v[152:155], v[80:83]
ds_read_b64_tr_b16 v[202:203], v227 offset:35584
v_mfma_f32_16x16x32_bf16 v[112:115], v[204:207], v[156:159], v[112:115]
v_mfma_f32_16x16x32_bf16 v[20:23], v[208:211], v[144:147], v[20:23]
ds_read_b64_tr_b16 v[204:205], v227 offset:35336
v_mfma_f32_16x16x32_bf16 v[52:55], v[208:211], v[148:151], v[52:55]
s_add_u32 m0, s60, 0x12a80
ds_read_b64_tr_b16 v[206:207], v227 offset:35592
v_mfma_f32_16x16x32_bf16 v[84:87], v[208:211], v[152:155], v[84:87]
v_mfma_f32_16x16x32_bf16 v[116:119], v[208:211], v[156:159], v[116:119]
buffer_load_dwordx4 v233, s[40:43], s61 offen offset:384  lds
v_mfma_f32_16x16x32_bf16 v[24:27], v[212:215], v[144:147], v[24:27]
ds_read_b64_tr_b16 v[208:209], v227 offset:35840
LH0:
s_cbranch_execz LHB0
v_mfma_f32_16x16x32_bf16 v[56:59], v[212:215], v[148:151], v[56:59]
ds_read_b64_tr_b16 v[210:211], v227 offset:36096
v_mfma_f32_16x16x32_bf16 v[88:91], v[212:215], v[152:155], v[88:91]
v_mfma_f32_16x16x32_bf16 v[120:123], v[212:215], v[156:159], v[120:123]
s_mov_b32 s61, 0x180
s_add_u32 m0, s34, 0x24000
s_waitcnt lgkmcnt(5)
v_mfma_f32_16x16x32_bf16 v[28:31], v[216:219], v[144:147], v[28:31]
ds_read_b64_tr_b16 v[212:213], v227 offset:35848
v_mfma_f32_16x16x32_bf16 v[60:63], v[216:219], v[148:151], v[60:63]
buffer_load_dwordx4 v234, s[36:39], s61 offen offset:0  lds
v_mfma_f32_16x16x32_bf16 v[92:95], v[216:219], v[152:155], v[92:95]
ds_read_b64_tr_b16 v[214:215], v227 offset:36104
v_mfma_f32_16x16x32_bf16 v[124:127], v[216:219], v[156:159], v[124:127]
v_mfma_f32_16x16x32_bf16 a[128:131], v[192:195], v[144:147], a[128:131]
ds_read_b64_tr_b16 v[216:217], v227 offset:36352
v_mfma_f32_16x16x32_bf16 a[132:135], v[192:195], v[148:151], a[132:135]
s_mov_b32 s61, 0x3980
s_add_u32 m0, s34, 0x24400
ds_read_b64_tr_b16 v[218:219], v227 offset:36608
v_mfma_f32_16x16x32_bf16 a[136:139], v[192:195], v[152:155], a[136:139]
v_mfma_f32_16x16x32_bf16 a[140:143], v[192:195], v[156:159], a[140:143]
buffer_load_dwordx4 v234, s[36:39], s61 offen offset:0  lds
v_mfma_f32_16x16x32_bf16 a[144:147], v[196:199], v[144:147], a[144:147]
ds_read_b64_tr_b16 v[192:193], v227 offset:36360
v_mfma_f32_16x16x32_bf16 a[148:151], v[196:199], v[148:151], a[148:151]
ds_read_b64_tr_b16 v[194:195], v227 offset:36616
v_mfma_f32_16x16x32_bf16 a[152:155], v[196:199], v[152:155], a[152:155]
v_mfma_f32_16x16x32_bf16 a[156:159], v[196:199], v[156:159], a[156:159]
s_waitcnt lgkmcnt(5)
v_mfma_f32_16x16x32_bf16 a[160:163], v[200:203], v[144:147], a[160:163]
v_mfma_f32_16x16x32_bf16 a[164:167], v[200:203], v[148:151], a[164:167]
s_waitcnt vmcnt(20)
s_waitcnt lgkmcnt(0)
v_mfma_f32_16x16x32_bf16 a[168:171], v[200:203], v[152:155], a[168:171]
s_barrier
ds_read_b128 v[128:131], v228 offset:0
v_mfma_f32_16x16x32_bf16 a[172:175], v[200:203], v[156:159], a[172:175]
s_mov_b32 s61, 0x1c0000
s_add_u32 m0, s60, 0x18000
ds_read_b128 v[132:135], v228 offset:1024
ds_read_b128 v[136:139], v255 offset:2048
v_mfma_f32_16x16x32_bf16 a[176:179], v[204:207], v[144:147], a[176:179]
ds_read_b128 v[140:143], v255 offset:3072
v_add_u32 v227, 0xffff0000, v227
v_mfma_f32_16x16x32_bf16 a[180:183], v[204:207], v[148:151], a[180:183]
buffer_load_dwordx4 v233, s[40:43], s61 offen offset:0  lds
v_mfma_f32_16x16x32_bf16 a[184:187], v[204:207], v[152:155], a[184:187]
ds_read_b64_tr_b16 v[196:197], v227 offset:0
ds_read_b64_tr_b16 v[198:199], v227 offset:256
v_mfma_f32_16x16x32_bf16 a[188:191], v[204:207], v[156:159], a[188:191]
ds_read_b64_tr_b16 v[200:201], v227 offset:8
ds_read_b64_tr_b16 v[202:203], v227 offset:264
v_mfma_f32_16x16x32_bf16 a[192:195], v[208:211], v[144:147], a[192:195]
ds_read_b64_tr_b16 v[204:205], v227 offset:512
v_mfma_f32_16x16x32_bf16 a[196:199], v[208:211], v[148:151], a[196:199]
s_add_u32 m0, s60, 0x18380
ds_read_b64_tr_b16 v[206:207], v227 offset:768
v_mfma_f32_16x16x32_bf16 a[200:203], v[208:211], v[152:155], a[200:203]
v_mfma_f32_16x16x32_bf16 a[204:207], v[208:211], v[156:159], a[204:207]
buffer_load_dwordx4 v233, s[40:43], s61 offen offset:128  lds
v_mfma_f32_16x16x32_bf16 a[208:211], v[212:215], v[144:147], a[208:211]
ds_read_b64_tr_b16 v[208:209], v227 offset:520
v_mfma_f32_16x16x32_bf16 a[212:215], v[212:215], v[148:151], a[212:215]
ds_read_b64_tr_b16 v[210:211], v227 offset:776
v_mfma_f32_16x16x32_bf16 a[216:219], v[212:215], v[152:155], a[216:219]
v_mfma_f32_16x16x32_bf16 a[220:223], v[212:215], v[156:159], a[220:223]
s_add_u32 m0, s60, 0x18700
v_mfma_f32_16x16x32_bf16 a[224:227], v[216:219], v[144:147], a[224:227]
ds_read_b64_tr_b16 v[212:213], v227 offset:1024
v_mfma_f32_16x16x32_bf16 a[228:231], v[216:219], v[148:151], a[228:231]
buffer_load_dwordx4 v233, s[40:43], s61 offen offset:256  lds
v_mfma_f32_16x16x32_bf16 a[232:235], v[216:219], v[152:155], a[232:235]
ds_read_b64_tr_b16 v[214:215], v227 offset:1280
v_mfma_f32_16x16x32_bf16 a[236:239], v[216:219], v[156:159], a[236:239]
v_mfma_f32_16x16x32_bf16 a[240:243], v[192:195], v[144:147], a[240:243]
ds_read_b64_tr_b16 v[216:217], v227 offset:1032
v_mfma_f32_16x16x32_bf16 a[244:247], v[192:195], v[148:151], a[244:247]
s_add_u32 m0, s60, 0x18a80
ds_read_b64_tr_b16 v[218:219], v227 offset:1288
v_mfma_f32_16x16x32_bf16 a[248:251], v[192:195], v[152:155], a[248:251]
v_mfma_f32_16x16x32_bf16 a[252:255], v[192:195], v[156:159], a[252:255]
LST4:
buffer_load_dwordx4 v233, s[40:43], s61 offen offset:384  lds
s_waitcnt lgkmcnt(4)
v_mfma_f32_16x16x32_bf16 v[0:3], v[196:199], v[128:131], v[0:3]
ds_read_b64_tr_b16 v[192:193], v227 offset:1536
v_mfma_f32_16x16x32_bf16 v[32:35], v[196:199], v[132:135], v[32:35]
ds_read_b64_tr_b16 v[194:195], v227 offset:1792
v_mfma_f32_16x16x32_bf16 v[64:67], v[196:199], v[136:139], v[64:67]
v_mfma_f32_16x16x32_bf16 v[96:99], v[196:199], v[140:143], v[96:99]
s_mov_b32 s61, 0x1e0000
s_add_u32 m0, s60, 0x1a000
v_mfma_f32_16x16x32_bf16 v[4:7], v[200:203], v[128:131], v[4:7]
ds_read_b64_tr_b16 v[196:197], v227 offset:1544
v_mfma_f32_16x16x32_bf16 v[36:39], v[200:203], v[132:135], v[36:39]
buffer_load_dwordx4 v233, s[40:43], s61 offen offset:0  lds
v_mfma_f32_16x16x32_bf16 v[68:71], v[200:203], v[136:139], v[68:71]
ds_read_b64_tr_b16 v[198:199], v227 offset:1800
v_mfma_f32_16x16x32_bf16 v[100:103], v[200:203], v[140:143], v[100:103]
v_mfma_f32_16x16x32_bf16 v[8:11], v[204:207], v[128:131], v[8:11]
ds_read_b64_tr_b16 v[200:201], v227 offset:2048
v_mfma_f32_16x16x32_bf16 v[40:43], v[204:207], v[132:135], v[40:43]
s_add_u32 m0, s60, 0x1a380
ds_read_b64_tr_b16 v[202:203], v227 offset:2304
v_mfma_f32_16x16x32_bf16 v[72:75], v[204:207], v[136:139], v[72:75]
v_mfma_f32_16x16x32_bf16 v[104:107], v[204:207], v[140:143], v[104:107]
buffer_load_dwordx4 v233, s[40:43], s61 offen offset:128  lds
v_mfma_f32_16x16x32_bf16 v[12:15], v[208:211], v[128:131], v[12:15]
ds_read_b64_tr_b16 v[204:205], v227 offset:2056
v_mfma_f32_16x16x32_bf16 v[44:47], v[208:211], v[132:135], v[44:47]
ds_read_b64_tr_b16 v[206:207], v227 offset:2312
v_mfma_f32_16x16x32_bf16 v[76:79], v[208:211], v[136:139], v[76:79]
v_mfma_f32_16x16x32_bf16 v[108:111], v[208:211], v[140:143], v[108:111]
s_add_u32 m0, s60, 0x1a700
s_waitcnt lgkmcnt(5)
v_mfma_f32_16x16x32_bf16 v[16:19], v[212:215], v[128:131], v[16:19]
ds_read_b64_tr_b16 v[208:209], v227 offset:2560
v_mfma_f32_16x16x32_bf16 v[48:51], v[212:215], v[132:135], v[48:51]
buffer_load_dwordx4 v233, s[40:43], s61 offen offset:256  lds
v_mfma_f32_16x16x32_bf16 v[80:83], v[212:215], v[136:139], v[80:83]
ds_read_b64_tr_b16 v[210:211], v227 offset:2816
v_mfma_f32_16x16x32_bf16 v[112:115], v[212:215], v[140:143], v[112:115]
v_mfma_f32_16x16x32_bf16 v[20:23], v[216:219], v[128:131], v[20:23]
ds_read_b64_tr_b16 v[212:213], v227 offset:2568
v_mfma_f32_16x16x32_bf16 v[52:55], v[216:219], v[132:135], v[52:55]
s_add_u32 m0, s60, 0x1aa80
ds_read_b64_tr_b16 v[214:215], v227 offset:2824
v_mfma_f32_16x16x32_bf16 v[84:87], v[216:219], v[136:139], v[84:87]
v_mfma_f32_16x16x32_bf16 v[116:119], v[216:219], v[140:143], v[116:119]
buffer_load_dwordx4 v233, s[40:43], s61 offen offset:384  lds
v_mfma_f32_16x16x32_bf16 v[24:27], v[192:195], v[128:131], v[24:27]
ds_read_b64_tr_b16 v[216:217], v227 offset:3072
v_mfma_f32_16x16x32_bf16 v[56:59], v[192:195], v[132:135], v[56:59]
ds_read_b64_tr_b16 v[218:219], v227 offset:3328
v_mfma_f32_16x16x32_bf16 v[88:91], v[192:195], v[136:139], v[88:91]
v_mfma_f32_16x16x32_bf16 v[120:123], v[192:195], v[140:143], v[120:123]
s_mov_b32 s61, 0x1c0
s_add_u32 m0, s34, 0x26000
s_waitcnt lgkmcnt(5)
v_mfma_f32_16x16x32_bf16 v[28:31], v[196:199], v[128:131], v[28:31]
ds_read_b64_tr_b16 v[192:193], v227 offset:3080
v_mfma_f32_16x16x32_bf16 v[60:63], v[196:199], v[132:135], v[60:63]
buffer_load_dwordx4 v234, s[36:39], s61 offen offset:0  lds
v_mfma_f32_16x16x32_bf16 v[92:95], v[196:199], v[136:139], v[92:95]
ds_read_b64_tr_b16 v[194:195], v227 offset:3336
v_mfma_f32_16x16x32_bf16 v[124:127], v[196:199], v[140:143], v[124:127]
v_mfma_f32_16x16x32_bf16 a[128:131], v[200:203], v[128:131], a[128:131]
ds_read_b64_tr_b16 v[196:197], v227 offset:3584
v_mfma_f32_16x16x32_bf16 a[132:135], v[200:203], v[132:135], a[132:135]
s_mov_b32 s61, 0x39c0
s_add_u32 m0, s34, 0x26400
ds_read_b64_tr_b16 v[198:199], v227 offset:3840
v_mfma_f32_16x16x32_bf16 a[136:139], v[200:203], v[136:139], a[136:139]
v_mfma_f32_16x16x32_bf16 a[140:143], v[200:203], v[140:143], a[140:143]
buffer_load_dwordx4 v234, s[36:39], s61 offen offset:0  lds
v_mfma_f32_16x16x32_bf16 a[144:147], v[204:207], v[128:131], a[144:147]
ds_read_b64_tr_b16 v[200:201], v227 offset:3592
v_mfma_f32_16x16x32_bf16 a[148:151], v[204:207], v[132:135], a[148:151]
ds_read_b64_tr_b16 v[202:203], v227 offset:3848
v_mfma_f32_16x16x32_bf16 a[152:155], v[204:207], v[136:139], a[152:155]
v_mfma_f32_16x16x32_bf16 a[156:159], v[204:207], v[140:143], a[156:159]
s_waitcnt lgkmcnt(5)
v_mfma_f32_16x16x32_bf16 a[160:163], v[208:211], v[128:131], a[160:163]
v_mfma_f32_16x16x32_bf16 a[164:167], v[208:211], v[132:135], a[164:167]
s_waitcnt vmcnt(20)
s_waitcnt lgkmcnt(0)
v_mfma_f32_16x16x32_bf16 a[168:171], v[208:211], v[136:139], a[168:171]
s_barrier
ds_read_b128 v[144:147], v228 offset:8192
v_mfma_f32_16x16x32_bf16 a[172:175], v[208:211], v[140:143], a[172:175]
s_mov_b32 s61, 0x200000
s_add_u32 m0, s60, 0x0
ds_read_b128 v[148:151], v228 offset:9216
ds_read_b128 v[152:155], v255 offset:10240
v_mfma_f32_16x16x32_bf16 a[176:179], v[212:215], v[128:131], a[176:179]
ds_read_b128 v[156:159], v255 offset:11264
ds_read_b64_tr_b16 v[204:205], v227 offset:32768
v_mfma_f32_16x16x32_bf16 a[180:183], v[212:215], v[132:135], a[180:183]
buffer_load_dwordx4 v233, s[40:43], s61 offen offset:0  lds
v_mfma_f32_16x16x32_bf16 a[184:187], v[212:215], v[136:139], a[184:187]
ds_read_b64_tr_b16 v[206:207], v227 offset:33024
ds_read_b64_tr_b16 v[208:209], v227 offset:32776
v_mfma_f32_16x16x32_bf16 a[188:191], v[212:215], v[140:143], a[188:191]
ds_read_b64_tr_b16 v[210:211], v227 offset:33032
v_mfma_f32_16x16x32_bf16 a[192:195], v[216:219], v[128:131], a[192:195]
ds_read_b64_tr_b16 v[212:213], v227 offset:33280
v_mfma_f32_16x16x32_bf16 a[196:199], v[216:219], v[132:135], a[196:199]
s_add_u32 m0, s60, 0x380
ds_read_b64_tr_b16 v[214:215], v227 offset:33536
v_mfma_f32_16x16x32_bf16 a[200:203], v[216:219], v[136:139], a[200:203]
v_mfma_f32_16x16x32_bf16 a[204:207], v[216:219], v[140:143], a[204:207]
buffer_load_dwordx4 v233, s[40:43], s61 offen offset:128  lds
v_mfma_f32_16x16x32_bf16 a[208:211], v[192:195], v[128:131], a[208:211]
ds_read_b64_tr_b16 v[216:217], v227 offset:33288
v_mfma_f32_16x16x32_bf16 a[212:215], v[192:195], v[132:135], a[212:215]
ds_read_b64_tr_b16 v[218:219], v227 offset:33544
v_mfma_f32_16x16x32_bf16 a[216:219], v[192:195], v[136:139], a[216:219]
v_mfma_f32_16x16x32_bf16 a[220:223], v[192:195], v[140:143], a[220:223]
s_add_u32 m0, s60, 0x700
v_mfma_f32_16x16x32_bf16 a[224:227], v[196:199], v[128:131], a[224:227]
ds_read_b64_tr_b16 v[192:193], v227 offset:33792
v_mfma_f32_16x16x32_bf16 a[228:231], v[196:199], v[132:135], a[228:231]
buffer_load_dwordx4 v233, s[40:43], s61 offen offset:256  lds
v_mfma_f32_16x16x32_bf16 a[232:235], v[196:199], v[136:139], a[232:235]
ds_read_b64_tr_b16 v[194:195], v227 offset:34048
v_mfma_f32_16x16x32_bf16 a[236:239], v[196:199], v[140:143], a[236:239]
v_mfma_f32_16x16x32_bf16 a[240:243], v[200:203], v[128:131], a[240:243]
ds_read_b64_tr_b16 v[196:197], v227 offset:33800
v_mfma_f32_16x16x32_bf16 a[244:247], v[200:203], v[132:135], a[244:247]
s_add_u32 m0, s60, 0xa80
ds_read_b64_tr_b16 v[198:199], v227 offset:34056
v_mfma_f32_16x16x32_bf16 a[248:251], v[200:203], v[136:139], a[248:251]
v_mfma_f32_16x16x32_bf16 a[252:255], v[200:203], v[140:143], a[252:255]
LST5:
buffer_load_dwordx4 v233, s[40:43], s61 offen offset:384  lds
s_waitcnt lgkmcnt(4)
v_mfma_f32_16x16x32_bf16 v[0:3], v[204:207], v[144:147], v[0:3]
ds_read_b64_tr_b16 v[200:201], v227 offset:34304
v_mfma_f32_16x16x32_bf16 v[32:35], v[204:207], v[148:151], v[32:35]
ds_read_b64_tr_b16 v[202:203], v227 offset:34560
v_mfma_f32_16x16x32_bf16 v[64:67], v[204:207], v[152:155], v[64:67]
v_mfma_f32_16x16x32_bf16 v[96:99], v[204:207], v[156:159], v[96:99]
s_mov_b32 s61, 0x220000
s_add_u32 m0, s60, 0x2000
v_mfma_f32_16x16x32_bf16 v[4:7], v[208:211], v[144:147], v[4:7]
ds_read_b64_tr_b16 v[204:205], v227 offset:34312
v_mfma_f32_16x16x32_bf16 v[36:39], v[208:211], v[148:151], v[36:39]
buffer_load_dwordx4 v233, s[40:43], s61 offen offset:0  lds
v_mfma_f32_16x16x32_bf16 v[68:71], v[208:211], v[152:155], v[68:71]
ds_read_b64_tr_b16 v[206:207], v227 offset:34568
v_mfma_f32_16x16x32_bf16 v[100:103], v[208:211], v[156:159], v[100:103]
v_mfma_f32_16x16x32_bf16 v[8:11], v[212:215], v[144:147], v[8:11]
ds_read_b64_tr_b16 v[208:209], v227 offset:34816
v_mfma_f32_16x16x32_bf16 v[40:43], v[212:215], v[148:151], v[40:43]
s_add_u32 m0, s60, 0x2380
ds_read_b64_tr_b16 v[210:211], v227 offset:35072
v_mfma_f32_16x16x32_bf16 v[72:75], v[212:215], v[152:155], v[72:75]
v_mfma_f32_16x16x32_bf16 v[104:107], v[212:215], v[156:159], v[104:107]
buffer_load_dwordx4 v233, s[40:43], s61 offen offset:128  lds
v_mfma_f32_16x16x32_bf16 v[12:15], v[216:219], v[144:147], v[12:15]
ds_read_b64_tr_b16 v[212:213], v227 offset:34824
v_mfma_f32_16x16x32_bf16 v[44:47], v[216:219], v[148:151], v[44:47]
ds_read_b64_tr_b16 v[214:215], v227 offset:35080
v_mfma_f32_16x16x32_bf16 v[76:79], v[216:219], v[152:155], v[76:79]
v_mfma_f32_16x16x32_bf16 v[108:111], v[216:219], v[156:159], v[108:111]
s_add_u32 m0, s60, 0x2700
s_waitcnt lgkmcnt(5)
v_mfma_f32_16x16x32_bf16 v[16:19], v[192:195], v[144:147], v[16:19]
ds_read_b64_tr_b16 v[216:217], v227 offset:35328
v_mfma_f32_16x16x32_bf16 v[48:51], v[192:195], v[148:151], v[48:51]
buffer_load_dwordx4 v233, s[40:43], s61 offen offset:256  lds
v_mfma_f32_16x16x32_bf16 v[80:83], v[192:195], v[152:155], v[80:83]
ds_read_b64_tr_b16 v[218:219], v227 offset:35584
v_mfma_f32_16x16x32_bf16 v[112:115], v[192:195], v[156:159], v[112:115]
v_mfma_f32_16x16x32_bf16 v[20:23], v[196:199], v[144:147], v[20:23]
ds_read_b64_tr_b16 v[192:193], v227 offset:35336
v_mfma_f32_16x16x32_bf16 v[52:55], v[196:199], v[148:151], v[52:55]
s_add_u32 m0, s60, 0x2a80
ds_read_b64_tr_b16 v[194:195], v227 offset:35592
v_mfma_f32_16x16x32_bf16 v[84:87], v[196:199], v[152:155], v[84:87]
v_mfma_f32_16x16x32_bf16 v[116:119], v[196:199], v[156:159], v[116:119]
buffer_load_dwordx4 v233, s[40:43], s61 offen offset:384  lds
v_mfma_f32_16x16x32_bf16 v[24:27], v[200:203], v[144:147], v[24:27]
ds_read_b64_tr_b16 v[196:197], v227 offset:35840
v_mfma_f32_16x16x32_bf16 v[56:59], v[200:203], v[148:151], v[56:59]
ds_read_b64_tr_b16 v[198:199], v227 offset:36096
v_mfma_f32_16x16x32_bf16 v[88:91], v[200:203], v[152:155], v[88:91]
v_mfma_f32_16x16x32_bf16 v[120:123], v[200:203], v[156:159], v[120:123]
s_mov_b32 s61, 0x200
s_add_u32 m0, s34, 0x20000
s_waitcnt lgkmcnt(5)
v_mfma_f32_16x16x32_bf16 v[28:31], v[204:207], v[144:147], v[28:31]
ds_read_b64_tr_b16 v[200:201], v227 offset:35848
v_mfma_f32_16x16x32_bf16 v[60:63], v[204:207], v[148:151], v[60:63]
buffer_load_dwordx4 v234, s[36:39], s61 offen offset:0  lds
v_mfma_f32_16x16x32_bf16 v[92:95], v[204:207], v[152:155], v[92:95]
ds_read_b64_tr_b16 v[202:203], v227 offset:36104
v_mfma_f32_16x16x32_bf16 v[124:127], v[204:207], v[156:159], v[124:127]
v_mfma_f32_16x16x32_bf16 a[128:131], v[208:211], v[144:147], a[128:131]
ds_read_b64_tr_b16 v[204:205], v227 offset:36352
v_mfma_f32_16x16x32_bf16 a[132:135], v[208:211], v[148:151], a[132:135]
s_mov_b32 s61, 0x3a00
s_add_u32 m0, s34, 0x20400
ds_read_b64_tr_b16 v[206:207], v227 offset:36608
v_mfma_f32_16x16x32_bf16 a[136:139], v[208:211], v[152:155], a[136:139]
v_mfma_f32_16x16x32_bf16 a[140:143], v[208:211], v[156:159], a[140:143]
buffer_load_dwordx4 v234, s[36:39], s61 offen offset:0  lds
v_mfma_f32_16x16x32_bf16 a[144:147], v[212:215], v[144:147], a[144:147]
ds_read_b64_tr_b16 v[208:209], v227 offset:36360
v_mfma_f32_16x16x32_bf16 a[148:151], v[212:215], v[148:151], a[148:151]
ds_read_b64_tr_b16 v[210:211], v227 offset:36616
v_mfma_f32_16x16x32_bf16 a[152:155], v[212:215], v[152:155], a[152:155]
v_mfma_f32_16x16x32_bf16 a[156:159], v[212:215], v[156:159], a[156:159]
s_waitcnt lgkmcnt(5)
v_mfma_f32_16x16x32_bf16 a[160:163], v[216:219], v[144:147], a[160:163]
v_mfma_f32_16x16x32_bf16 a[164:167], v[216:219], v[148:151], a[164:167]
s_waitcnt vmcnt(20)
s_waitcnt lgkmcnt(0)
v_mfma_f32_16x16x32_bf16 a[168:171], v[216:219], v[152:155], a[168:171]
s_barrier
ds_read_b128 v[128:131], v228 offset:16384
v_mfma_f32_16x16x32_bf16 a[172:175], v[216:219], v[156:159], a[172:175]
s_mov_b32 s61, 0x240000
s_add_u32 m0, s60, 0x8000
ds_read_b128 v[132:135], v228 offset:17408
ds_read_b128 v[136:139], v255 offset:18432
v_mfma_f32_16x16x32_bf16 a[176:179], v[192:195], v[144:147], a[176:179]
ds_read_b128 v[140:143], v255 offset:19456
v_add_u32 v227, 0x10000, v227
v_mfma_f32_16x16x32_bf16 a[180:183], v[192:195], v[148:151], a[180:183]
buffer_load_dwordx4 v233, s[40:43], s61 offen offset:0  lds
v_mfma_f32_16x16x32_bf16 a[184:187], v[192:195], v[152:155], a[184:187]
ds_read_b64_tr_b16 v[212:213], v227 offset:0
ds_read_b64_tr_b16 v[214:215], v227 offset:256
v_mfma_f32_16x16x32_bf16 a[188:191], v[192:195], v[156:159], a[188:191]
ds_read_b64_tr_b16 v[216:217], v227 offset:8
ds_read_b64_tr_b16 v[218:219], v227 offset:264
v_mfma_f32_16x16x32_bf16 a[192:195], v[196:199], v[144:147], a[192:195]
ds_read_b64_tr_b16 v[192:193], v227 offset:512
v_mfma_f32_16x16x32_bf16 a[196:199], v[196:199], v[148:151], a[196:199]
s_add_u32 m0, s60, 0x8380
ds_read_b64_tr_b16 v[194:195], v227 offset:768
v_mfma_f32_16x16x32_bf16 a[200:203], v[196:199], v[152:155], a[200:203]
v_mfma_f32_16x16x32_bf16 a[204:207], v[196:199], v[156:159], a[204:207]
buffer_load_dwordx4 v233, s[40:43], s61 offen offset:128  lds
v_mfma_f32_16x16x32_bf16 a[208:211], v[200:203], v[144:147], a[208:211]
ds_read_b64_tr_b16 v[196:197], v227 offset:520
v_mfma_f32_16x16x32_bf16 a[212:215], v[200:203], v[148:151], a[212:215]
ds_read_b64_tr_b16 v[198:199], v227 offset:776
v_mfma_f32_16x16x32_bf16 a[216:219], v[200:203], v[152:155], a[216:219]
v_mfma_f32_16x16x32_bf16 a[220:223], v[200:203], v[156:159], a[220:223]
s_add_u32 m0, s60, 0x8700
v_mfma_f32_16x16x32_bf16 a[224:227], v[204:207], v[144:147], a[224:227]
ds_read_b64_tr_b16 v[200:201], v227 offset:1024
v_mfma_f32_16x16x32_bf16 a[228:231], v[204:207], v[148:151], a[228:231]
buffer_load_dwordx4 v233, s[40:43], s61 offen offset:256  lds
v_mfma_f32_16x16x32_bf16 a[232:235], v[204:207], v[152:155], a[232:235]
ds_read_b64_tr_b16 v[202:203], v227 offset:1280
v_mfma_f32_16x16x32_bf16 a[236:239], v[204:207], v[156:159], a[236:239]
v_mfma_f32_16x16x32_bf16 a[240:243], v[208:211], v[144:147], a[240:243]
ds_read_b64_tr_b16 v[204:205], v227 offset:1032
v_mfma_f32_16x16x32_bf16 a[244:247], v[208:211], v[148:151], a[244:247]
s_add_u32 m0, s60, 0x8a80
ds_read_b64_tr_b16 v[206:207], v227 offset:1288
v_mfma_f32_16x16x32_bf16 a[248:251], v[208:211], v[152:155], a[248:251]
v_mfma_f32_16x16x32_bf16 a[252:255], v[208:211], v[156:159], a[252:255]
LST6:
buffer_load_dwordx4 v233, s[40:43], s61 offen offset:384  lds
s_waitcnt lgkmcnt(4)
v_mfma_f32_16x16x32_bf16 v[0:3], v[212:215], v[128:131], v[0:3]
ds_read_b64_tr_b16 v[208:209], v227 offset:1536
v_mfma_f32_16x16x32_bf16 v[32:35], v[212:215], v[132:135], v[32:35]
ds_read_b64_tr_b16 v[210:211], v227 offset:1792
v_mfma_f32_16x16x32_bf16 v[64:67], v[212:215], v[136:139], v[64:67]
v_mfma_f32_16x16x32_bf16 v[96:99], v[212:215], v[140:143], v[96:99]
s_mov_b32 s61, 0x260000
s_add_u32 m0, s60, 0xa000
v_mfma_f32_16x16x32_bf16 v[4:7], v[216:219], v[128:131], v[4:7]
ds_read_b64_tr_b16 v[212:213], v227 offset:1544
v_mfma_f32_16x16x32_bf16 v[36:39], v[216:219], v[132:135], v[36:39]
buffer_load_dwordx4 v233, s[40:43], s61 offen offset:0  lds
v_mfma_f32_16x16x32_bf16 v[68:71], v[216:219], v[136:139], v[68:71]
ds_read_b64_tr_b16 v[214:215], v227 offset:1800
v_mfma_f32_16x16x32_bf16 v[100:103], v[216:219], v[140:143], v[100:103]
v_mfma_f32_16x16x32_bf16 v[8:11], v[192:195], v[128:131], v[8:11]
ds_read_b64_tr_b16 v[216:217], v227 offset:2048
v_mfma_f32_16x16x32_bf16 v[40:43], v[192:195], v[132:135], v[40:43]
s_add_u32 m0, s60, 0xa380
ds_read_b64_tr_b16 v[218:219], v227 offset:2304
v_mfma_f32_16x16x32_bf16 v[72:75], v[192:195], v[136:139], v[72:75]
v_mfma_f32_16x16x32_bf16 v[104:107], v[192:195], v[140:143], v[104:107]
buffer_load_dwordx4 v233, s[40:43], s61 offen offset:128  lds
v_mfma_f32_16x16x32_bf16 v[12:15], v[196:199], v[128:131], v[12:15]
ds_read_b64_tr_b16 v[192:193], v227 offset:2056
v_mfma_f32_16x16x32_bf16 v[44:47], v[196:199], v[132:135], v[44:47]
ds_read_b64_tr_b16 v[194:195], v227 offset:2312
v_mfma_f32_16x16x32_bf16 v[76:79], v[196:199], v[136:139], v[76:79]
v_mfma_f32_16x16x32_bf16 v[108:111], v[196:199], v[140:143], v[108:111]
s_add_u32 m0, s60, 0xa700
s_waitcnt lgkmcnt(5)
v_mfma_f32_16x16x32_bf16 v[16:19], v[200:203], v[128:131], v[16:19]
ds_read_b64_tr_b16 v[196:197], v227 offset:2560
v_mfma_f32_16x16x32_bf16 v[48:51], v[200:203], v[132:135], v[48:51]
buffer_load_dwordx4 v233, s[40:43], s61 offen offset:256  lds
v_mfma_f32_16x16x32_bf16 v[80:83], v[200:203], v[136:139], v[80:83]
ds_read_b64_tr_b16 v[198:199], v227 offset:2816
v_mfma_f32_16x16x32_bf16 v[112:115], v[200:203], v[140:143], v[112:115]
v_mfma_f32_16x16x32_bf16 v[20:23], v[204:207], v[128:131], v[20:23]
ds_read_b64_tr_b16 v[200:201], v227 offset:2568
v_mfma_f32_16x16x32_bf16 v[52:55], v[204:207], v[132:135], v[52:55]
s_add_u32 m0, s60, 0xaa80
ds_read_b64_tr_b16 v[202:203], v227 offset:2824
v_mfma_f32_16x16x32_bf16 v[84:87], v[204:207], v[136:139], v[84:87]
v_mfma_f32_16x16x32_bf16 v[116:119], v[204:207], v[140:143], v[116:119]
buffer_load_dwordx4 v233, s[40:43], s61 offen offset:384  lds
v_mfma_f32_16x16x32_bf16 v[24:27], v[208:211], v[128:131], v[24:27]
ds_read_b64_tr_b16 v[204:205], v227 offset:3072
v_mfma_f32_16x16x32_bf16 v[56:59], v[208:211], v[132:135], v[56:59]
ds_read_b64_tr_b16 v[206:207], v227 offset:3328
v_mfma_f32_16x16x32_bf16 v[88:91], v[208:211], v[136:139], v[88:91]
v_mfma_f32_16x16x32_bf16 v[120:123], v[208:211], v[140:143], v[120:123]
s_mov_b32 s61, 0x240
s_add_u32 m0, s34, 0x22000
s_waitcnt lgkmcnt(5)
v_mfma_f32_16x16x32_bf16 v[28:31], v[212:215], v[128:131], v[28:31]
ds_read_b64_tr_b16 v[208:209], v227 offset:3080
v_mfma_f32_16x16x32_bf16 v[60:63], v[212:215], v[132:135], v[60:63]
buffer_load_dwordx4 v234, s[36:39], s61 offen offset:0  lds
v_mfma_f32_16x16x32_bf16 v[92:95], v[212:215], v[136:139], v[92:95]
ds_read_b64_tr_b16 v[210:211], v227 offset:3336
v_mfma_f32_16x16x32_bf16 v[124:127], v[212:215], v[140:143], v[124:127]
v_mfma_f32_16x16x32_bf16 a[128:131], v[216:219], v[128:131], a[128:131]
ds_read_b64_tr_b16 v[212:213], v227 offset:3584
v_mfma_f32_16x16x32_bf16 a[132:135], v[216:219], v[132:135], a[132:135]
s_mov_b32 s61, 0x3a40
s_add_u32 m0, s34, 0x22400
ds_read_b64_tr_b16 v[214:215], v227 offset:3840
v_mfma_f32_16x16x32_bf16 a[136:139], v[216:219], v[136:139], a[136:139]
v_mfma_f32_16x16x32_bf16 a[140:143], v[216:219], v[140:143], a[140:143]
buffer_load_dwordx4 v234, s[36:39], s61 offen offset:0  lds
v_mfma_f32_16x16x32_bf16 a[144:147], v[192:195], v[128:131], a[144:147]
ds_read_b64_tr_b16 v[216:217], v227 offset:3592
v_mfma_f32_16x16x32_bf16 a[148:151], v[192:195], v[132:135], a[148:151]
ds_read_b64_tr_b16 v[218:219], v227 offset:3848
v_mfma_f32_16x16x32_bf16 a[152:155], v[192:195], v[136:139], a[152:155]
v_mfma_f32_16x16x32_bf16 a[156:159], v[192:195], v[140:143], a[156:159]
s_waitcnt lgkmcnt(5)
v_mfma_f32_16x16x32_bf16 a[160:163], v[196:199], v[128:131], a[160:163]
v_mfma_f32_16x16x32_bf16 a[164:167], v[196:199], v[132:135], a[164:167]
s_waitcnt vmcnt(20)
s_waitcnt lgkmcnt(0)
v_mfma_f32_16x16x32_bf16 a[168:171], v[196:199], v[136:139], a[168:171]
s_barrier
ds_read_b128 v[144:147], v228 offset:24576
v_mfma_f32_16x16x32_bf16 a[172:175], v[196:199], v[140:143], a[172:175]
s_mov_b32 s61, 0x280000
s_add_u32 m0, s60, 0x10000
ds_read_b128 v[148:151], v228 offset:25600
ds_read_b128 v[152:155], v255 offset:26624
v_mfma_f32_16x16x32_bf16 a[176:179], v[200:203], v[128:131], a[176:179]
ds_read_b128 v[156:159], v255 offset:27648
ds_read_b64_tr_b16 v[192:193], v227 offset:32768
v_mfma_f32_16x16x32_bf16 a[180:183], v[200:203], v[132:135], a[180:183]
buffer_load_dwordx4 v233, s[40:43], s61 offen offset:0  lds
v_mfma_f32_16x16x32_bf16 a[184:187], v[200:203], v[136:139], a[184:187]
ds_read_b64_tr_b16 v[194:195], v227 offset:33024
ds_read_b64_tr_b16 v[196:197], v227 offset:32776
v_mfma_f32_16x16x32_bf16 a[188:191], v[200:203], v[140:143], a[188:191]
ds_read_b64_tr_b16 v[198:199], v227 offset:33032
v_mfma_f32_16x16x32_bf16 a[192:195], v[204:207], v[128:131], a[192:195]
ds_read_b64_tr_b16 v[200:201], v227 offset:33280
v_mfma_f32_16x16x32_bf16 a[196:199], v[204:207], v[132:135], a[196:199]
s_add_u32 m0, s60, 0x10380
ds_read_b64_tr_b16 v[202:203], v227 offset:33536
v_mfma_f32_16x16x32_bf16 a[200:203], v[204:207], v[136:139], a[200:203]
v_mfma_f32_16x16x32_bf16 a[204:207], v[204:207], v[140:143], a[204:207]
buffer_load_dwordx4 v233, s[40:43], s61 offen offset:128  lds
v_mfma_f32_16x16x32_bf16 a[208:211], v[208:211], v[128:131], a[208:211]
ds_read_b64_tr_b16 v[204:205], v227 offset:33288
v_mfma_f32_16x16x32_bf16 a[212:215], v[208:211], v[132:135], a[212:215]
ds_read_b64_tr_b16 v[206:207], v227 offset:33544
v_mfma_f32_16x16x32_bf16 a[216:219], v[208:211], v[136:139], a[216:219]
v_mfma_f32_16x16x32_bf16 a[220:223], v[208:211], v[140:143], a[220:223]
s_add_u32 m0, s60, 0x10700
v_mfma_f32_16x16x32_bf16 a[224:227], v[212:215], v[128:131], a[224:227]
ds_read_b64_tr_b16 v[208:209], v227 offset:33792
v_mfma_f32_16x16x32_bf16 a[228:231], v[212:215], v[132:135], a[228:231]
buffer_load_dwordx4 v233, s[40:43], s61 offen offset:256  lds
v_mfma_f32_16x16x32_bf16 a[232:235], v[212:215], v[136:139], a[232:235]
ds_read_b64_tr_b16 v[210:211], v227 offset:34048
v_mfma_f32_16x16x32_bf16 a[236:239], v[212:215], v[140:143], a[236:239]
v_mfma_f32_16x16x32_bf16 a[240:243], v[216:219], v[128:131], a[240:243]
ds_read_b64_tr_b16 v[212:213], v227 offset:33800
v_mfma_f32_16x16x32_bf16 a[244:247], v[216:219], v[132:135], a[244:247]
s_add_u32 m0, s60, 0x10a80
ds_read_b64_tr_b16 v[214:215], v227 offset:34056
v_mfma_f32_16x16x32_bf16 a[248:251], v[216:219], v[136:139], a[248:251]
v_mfma_f32_16x16x32_bf16 a[252:255], v[216:219], v[140:143], a[252:255]
LST7:
buffer_load_dwordx4 v233, s[40:43], s61 offen offset:384  lds
s_waitcnt lgkmcnt(4)
v_mfma_f32_16x16x32_bf16 v[0:3], v[192:195], v[144:147], v[0:3]
ds_read_b64_tr_b16 v[216:217], v227 offset:34304
v_mfma_f32_16x16x32_bf16 v[32:35], v[192:195], v[148:151], v[32:35]
ds_read_b64_tr_b16 v[218:219], v227 offset:34560
v_mfma_f32_16x16x32_bf16 v[64:67], v[192:195], v[152:155], v[64:67]
v_mfma_f32_16x16x32_bf16 v[96:99], v[192:195], v[156:159], v[96:99]
s_mov_b32 s61, 0x2a0000
s_add_u32 m0, s60, 0x12000
v_mfma_f32_16x16x32_bf16 v[4:7], v[196:199], v[144:147], v[4:7]
ds_read_b64_tr_b16 v[192:193], v227 offset:34312
v_mfma_f32_16x16x32_bf16 v[36:39], v[196:199], v[148:151], v[36:39]
buffer_load_dwordx4 v233, s[40:43], s61 offen offset:0  lds
v_mfma_f32_16x16x32_bf16 v[68:71], v[196:199], v[152:155], v[68:71]
ds_read_b64_tr_b16 v[194:195], v227 offset:34568
v_mfma_f32_16x16x32_bf16 v[100:103], v[196:199], v[156:159], v[100:103]
v_mfma_f32_16x16x32_bf16 v[8:11], v[200:203], v[144:147], v[8:11]
ds_read_b64_tr_b16 v[196:197], v227 offset:34816
v_mfma_f32_16x16x32_bf16 v[40:43], v[200:203], v[148:151], v[40:43]
s_add_u32 m0, s60, 0x12380
ds_read_b64_tr_b16 v[198:199], v227 offset:35072
v_mfma_f32_16x16x32_bf16 v[72:75], v[200:203], v[152:155], v[72:75]
v_mfma_f32_16x16x32_bf16 v[104:107], v[200:203], v[156:159], v[104:107]
buffer_load_dwordx4 v233, s[40:43], s61 offen offset:128  lds
v_mfma_f32_16x16x32_bf16 v[12:15], v[204:207], v[144:147], v[12:15]
ds_read_b64_tr_b16 v[200:201], v227 offset:34824
v_mfma_f32_16x16x32_bf16 v[44:47], v[204:207], v[148:151], v[44:47]
ds_read_b64_tr_b16 v[202:203], v227 offset:35080
v_mfma_f32_16x16x32_bf16 v[76:79], v[204:207], v[152:155], v[76:79]
v_mfma_f32_16x16x32_bf16 v[108:111], v[204:207], v[156:159], v[108:111]
s_add_u32 m0, s60, 0x12700
s_waitcnt lgkmcnt(5)
v_mfma_f32_16x16x32_bf16 v[16:19], v[208:211], v[144:147], v[16:19]
ds_read_b64_tr_b16 v[204:205], v227 offset:35328
v_mfma_f32_16x16x32_bf16 v[48:51], v[208:211], v[148:151], v[48:51]
buffer_load_dwordx4 v233, s[40:43], s61 offen offset:256  lds
v_mfma_f32_16x16x32_bf16 v[80:83], v[208:211], v[152:155], v[80:83]
ds_read_b64_tr_b16 v[206:207], v227 offset:35584
v_mfma_f32_16x16x32_bf16 v[112:115], v[208:211], v[156:159], v[112:115]
v_mfma_f32_16x16x32_bf16 v[20:23], v[212:215], v[144:147], v[20:23]
ds_read_b64_tr_b16 v[208:209], v227 offset:35336
v_mfma_f32_16x16x32_bf16 v[52:55], v[212:215], v[148:151], v[52:55]
s_add_u32 m0, s60, 0x12a80
ds_read_b64_tr_b16 v[210:211], v227 offset:35592
v_mfma_f32_16x16x32_bf16 v[84:87], v[212:215], v[152:155], v[84:87]
v_mfma_f32_16x16x32_bf16 v[116:119], v[212:215], v[156:159], v[116:119]
buffer_load_dwordx4 v233, s[40:43], s61 offen offset:384  lds
v_mfma_f32_16x16x32_bf16 v[24:27], v[216:219], v[144:147], v[24:27]
ds_read_b64_tr_b16 v[212:213], v227 offset:35840
LH1:
s_cbranch_execz LHB0
v_mfma_f32_16x16x32_bf16 v[56:59], v[216:219], v[148:151], v[56:59]
ds_read_b64_tr_b16 v[214:215], v227 offset:36096
v_mfma_f32_16x16x32_bf16 v[88:91], v[216:219], v[152:155], v[88:91]
v_mfma_f32_16x16x32_bf16 v[120:123], v[216:219], v[156:159], v[120:123]
s_mov_b32 s61, 0x280
s_add_u32 m0, s34, 0x24000
s_waitcnt lgkmcnt(5)
v_mfma_f32_16x16x32_bf16 v[28:31], v[192:195], v[144:147], v[28:31]
ds_read_b64_tr_b16 v[216:217], v227 offset:35848
v_mfma_f32_16x16x32_bf16 v[60:63], v[192:195], v[148:151], v[60:63]
buffer_load_dwordx4 v234, s[36:39], s61 offen offset:0  lds
v_mfma_f32_16x16x32_bf16 v[92:95], v[192:195], v[152:155], v[92:95]
ds_read_b64_tr_b16 v[218:219], v227 offset:36104
v_mfma_f32_16x16x32_bf16 v[124:127], v[192:195], v[156:159], v[124:127]
v_mfma_f32_16x16x32_bf16 a[128:131], v[196:199], v[144:147], a[128:131]
ds_read_b64_tr_b16 v[192:193], v227 offset:36352
v_mfma_f32_16x16x32_bf16 a[132:135], v[196:199], v[148:151], a[132:135]
s_mov_b32 s61, 0x3a80
s_add_u32 m0, s34, 0x24400
ds_read_b64_tr_b16 v[194:195], v227 offset:36608
v_mfma_f32_16x16x32_bf16 a[136:139], v[196:199], v[152:155], a[136:139]
v_mfma_f32_16x16x32_bf16 a[140:143], v[196:199], v[156:159], a[140:143]
buffer_load_dwordx4 v234, s[36:39], s61 offen offset:0  lds
v_mfma_f32_16x16x32_bf16 a[144:147], v[200:203], v[144:147], a[144:147]
ds_read_b64_tr_b16 v[196:197], v227 offset:36360
v_mfma_f32_16x16x32_bf16 a[148:151], v[200:203], v[148:151], a[148:151]
ds_read_b64_tr_b16 v[198:199], v227 offset:36616
v_mfma_f32_16x16x32_bf16 a[152:155], v[200:203], v[152:155], a[152:155]
v_mfma_f32_16x16x32_bf16 a[156:159], v[200:203], v[156:159], a[156:159]
s_waitcnt lgkmcnt(5)
v_mfma_f32_16x16x32_bf16 a[160:163], v[204:207], v[144:147], a[160:163]
v_mfma_f32_16x16x32_bf16 a[164:167], v[204:207], v[148:151], a[164:167]
s_waitcnt vmcnt(20)
s_waitcnt lgkmcnt(0)
v_mfma_f32_16x16x32_bf16 a[168:171], v[204:207], v[152:155], a[168:171]
s_barrier
ds_read_b128 v[128:131], v228 offset:0
v_mfma_f32_16x16x32_bf16 a[172:175], v[204:207], v[156:159], a[172:175]
s_mov_b32 s61, 0x2c0000
s_add_u32 m0, s60, 0x18000
ds_read_b128 v[132:135], v228 offset:1024
ds_read_b128 v[136:139], v255 offset:2048
v_mfma_f32_16x16x32_bf16 a[176:179], v[208:211], v[144:147], a[176:179]
ds_read_b128 v[140:143], v255 offset:3072
v_add_u32 v227, 0xffff0000, v227
v_mfma_f32_16x16x32_bf16 a[180:183], v[208:211], v[148:151], a[180:183]
buffer_load_dwordx4 v233, s[40:43], s61 offen offset:0  lds
v_mfma_f32_16x16x32_bf16 a[184:187], v[208:211], v[152:155], a[184:187]
ds_read_b64_tr_b16 v[200:201], v227 offset:0
ds_read_b64_tr_b16 v[202:203], v227 offset:256
v_mfma_f32_16x16x32_bf16 a[188:191], v[208:211], v[156:159], a[188:191]
ds_read_b64_tr_b16 v[204:205], v227 offset:8
ds_read_b64_tr_b16 v[206:207], v227 offset:264
v_mfma_f32_16x16x32_bf16 a[192:195], v[212:215], v[144:147], a[192:195]
ds_read_b64_tr_b16 v[208:209], v227 offset:512
v_mfma_f32_16x16x32_bf16 a[196:199], v[212:215], v[148:151], a[196:199]
s_add_u32 m0, s60, 0x18380
ds_read_b64_tr_b16 v[210:211], v227 offset:768
v_mfma_f32_16x16x32_bf16 a[200:203], v[212:215], v[152:155], a[200:203]
v_mfma_f32_16x16x32_bf16 a[204:207], v[212:215], v[156:159], a[204:207]
buffer_load_dwordx4 v233, s[40:43], s61 offen offset:128  lds
v_mfma_f32_16x16x32_bf16 a[208:211], v[216:219], v[144:147], a[208:211]
ds_read_b64_tr_b16 v[212:213], v227 offset:520
v_mfma_f32_16x16x32_bf16 a[212:215], v[216:219], v[148:151], a[212:215]
ds_read_b64_tr_b16 v[214:215], v227 offset:776
v_mfma_f32_16x16x32_bf16 a[216:219], v[216:219], v[152:155], a[216:219]
v_mfma_f32_16x16x32_bf16 a[220:223], v[216:219], v[156:159], a[220:223]
s_add_u32 m0, s60, 0x18700
v_mfma_f32_16x16x32_bf16 a[224:227], v[192:195], v[144:147], a[224:227]
ds_read_b64_tr_b16 v[216:217], v227 offset:1024
v_mfma_f32_16x16x32_bf16 a[228:231], v[192:195], v[148:151], a[228:231]
buffer_load_dwordx4 v233, s[40:43], s61 offen offset:256  lds
v_mfma_f32_16x16x32_bf16 a[232:235], v[192:195], v[152:155], a[232:235]
ds_read_b64_tr_b16 v[218:219], v227 offset:1280
v_mfma_f32_16x16x32_bf16 a[236:239], v[192:195], v[156:159], a[236:239]
v_mfma_f32_16x16x32_bf16 a[240:243], v[196:199], v[144:147], a[240:243]
ds_read_b64_tr_b16 v[192:193], v227 offset:1032
v_mfma_f32_16x16x32_bf16 a[244:247], v[196:199], v[148:151], a[244:247]
s_add_u32 m0, s60, 0x18a80
ds_read_b64_tr_b16 v[194:195], v227 offset:1288
v_mfma_f32_16x16x32_bf16 a[248:251], v[196:199], v[152:155], a[248:251]
v_mfma_f32_16x16x32_bf16 a[252:255], v[196:199], v[156:159], a[252:255]
LST8:
buffer_load_dwordx4 v233, s[40:43], s61 offen offset:384  lds
s_waitcnt lgkmcnt(4)
v_mfma_f32_16x16x32_bf16 v[0:3], v[200:203], v[128:131], v[0:3]
ds_read_b64_tr_b16 v[196:197], v227 offset:1536
v_mfma_f32_16x16x32_bf16 v[32:35], v[200:203], v[132:135], v[32:35]
ds_read_b64_tr_b16 v[198:199], v227 offset:1792
v_mfma_f32_16x16x32_bf16 v[64:67], v[200:203], v[136:139], v[64:67]
v_mfma_f32_16x16x32_bf16 v[96:99], v[200:203], v[140:143], v[96:99]
s_mov_b32 s61, 0x2e0000
s_add_u32 m0, s60, 0x1a000
v_mfma_f32_16x16x32_bf16 v[4:7], v[204:207], v[128:131], v[4:7]
ds_read_b64_tr_b16 v[200:201], v227 offset:1544
v_mfma_f32_16x16x32_bf16 v[36:39], v[204:207], v[132:135], v[36:39]
buffer_load_dwordx4 v233, s[40:43], s61 offen offset:0  lds
v_mfma_f32_16x16x32_bf16 v[68:71], v[204:207], v[136:139], v[68:71]
ds_read_b64_tr_b16 v[202:203], v227 offset:1800
v_mfma_f32_16x16x32_bf16 v[100:103], v[204:207], v[140:143], v[100:103]
v_mfma_f32_16x16x32_bf16 v[8:11], v[208:211], v[128:131], v[8:11]
ds_read_b64_tr_b16 v[204:205], v227 offset:2048
v_mfma_f32_16x16x32_bf16 v[40:43], v[208:211], v[132:135], v[40:43]
s_add_u32 m0, s60, 0x1a380
ds_read_b64_tr_b16 v[206:207], v227 offset:2304
v_mfma_f32_16x16x32_bf16 v[72:75], v[208:211], v[136:139], v[72:75]
v_mfma_f32_16x16x32_bf16 v[104:107], v[208:211], v[140:143], v[104:107]
buffer_load_dwordx4 v233, s[40:43], s61 offen offset:128  lds
v_mfma_f32_16x16x32_bf16 v[12:15], v[212:215], v[128:131], v[12:15]
ds_read_b64_tr_b16 v[208:209], v227 offset:2056
v_mfma_f32_16x16x32_bf16 v[44:47], v[212:215], v[132:135], v[44:47]
ds_read_b64_tr_b16 v[210:211], v227 offset:2312
v_mfma_f32_16x16x32_bf16 v[76:79], v[212:215], v[136:139], v[76:79]
v_mfma_f32_16x16x32_bf16 v[108:111], v[212:215], v[140:143], v[108:111]
s_add_u32 m0, s60, 0x1a700
s_waitcnt lgkmcnt(5)
v_mfma_f32_16x16x32_bf16 v[16:19], v[216:219], v[128:131], v[16:19]
ds_read_b64_tr_b16 v[212:213], v227 offset:2560
v_mfma_f32_16x16x32_bf16 v[48:51], v[216:219], v[132:135], v[48:51]
buffer_load_dwordx4 v233, s[40:43], s61 offen offset:256  lds
v_mfma_f32_16x16x32_bf16 v[80:83], v[216:219], v[136:139], v[80:83]
ds_read_b64_tr_b16 v[214:215], v227 offset:2816
v_mfma_f32_16x16x32_bf16 v[112:115], v[216:219], v[140:143], v[112:115]
v_mfma_f32_16x16x32_bf16 v[20:23], v[192:195], v[128:131], v[20:23]
ds_read_b64_tr_b16 v[216:217], v227 offset:2568
v_mfma_f32_16x16x32_bf16 v[52:55], v[192:195], v[132:135], v[52:55]
s_add_u32 m0, s60, 0x1aa80
ds_read_b64_tr_b16 v[218:219], v227 offset:2824
v_mfma_f32_16x16x32_bf16 v[84:87], v[192:195], v[136:139], v[84:87]
v_mfma_f32_16x16x32_bf16 v[116:119], v[192:195], v[140:143], v[116:119]
buffer_load_dwordx4 v233, s[40:43], s61 offen offset:384  lds
v_mfma_f32_16x16x32_bf16 v[24:27], v[196:199], v[128:131], v[24:27]
ds_read_b64_tr_b16 v[192:193], v227 offset:3072
v_mfma_f32_16x16x32_bf16 v[56:59], v[196:199], v[132:135], v[56:59]
ds_read_b64_tr_b16 v[194:195], v227 offset:3328
v_mfma_f32_16x16x32_bf16 v[88:91], v[196:199], v[136:139], v[88:91]
v_mfma_f32_16x16x32_bf16 v[120:123], v[196:199], v[140:143], v[120:123]
s_mov_b32 s61, 0x2c0
s_add_u32 m0, s34, 0x26000
s_waitcnt lgkmcnt(5)
v_mfma_f32_16x16x32_bf16 v[28:31], v[200:203], v[128:131], v[28:31]
ds_read_b64_tr_b16 v[196:197], v227 offset:3080
v_mfma_f32_16x16x32_bf16 v[60:63], v[200:203], v[132:135], v[60:63]
buffer_load_dwordx4 v234, s[36:39], s61 offen offset:0  lds
v_mfma_f32_16x16x32_bf16 v[92:95], v[200:203], v[136:139], v[92:95]
ds_read_b64_tr_b16 v[198:199], v227 offset:3336
v_mfma_f32_16x16x32_bf16 v[124:127], v[200:203], v[140:143], v[124:127]
v_mfma_f32_16x16x32_bf16 a[128:131], v[204:207], v[128:131], a[128:131]
ds_read_b64_tr_b16 v[200:201], v227 offset:3584
v_mfma_f32_16x16x32_bf16 a[132:135], v[204:207], v[132:135], a[132:135]
s_mov_b32 s61, 0x3ac0
s_add_u32 m0, s34, 0x26400
ds_read_b64_tr_b16 v[202:203], v227 offset:3840
v_mfma_f32_16x16x32_bf16 a[136:139], v[204:207], v[136:139], a[136:139]
v_mfma_f32_16x16x32_bf16 a[140:143], v[204:207], v[140:143], a[140:143]
buffer_load_dwordx4 v234, s[36:39], s61 offen offset:0  lds
v_mfma_f32_16x16x32_bf16 a[144:147], v[208:211], v[128:131], a[144:147]
ds_read_b64_tr_b16 v[204:205], v227 offset:3592
v_mfma_f32_16x16x32_bf16 a[148:151], v[208:211], v[132:135], a[148:151]
ds_read_b64_tr_b16 v[206:207], v227 offset:3848
v_mfma_f32_16x16x32_bf16 a[152:155], v[208:211], v[136:139], a[152:155]
v_mfma_f32_16x16x32_bf16 a[156:159], v[208:211], v[140:143], a[156:159]
s_waitcnt lgkmcnt(5)
v_mfma_f32_16x16x32_bf16 a[160:163], v[212:215], v[128:131], a[160:163]
v_mfma_f32_16x16x32_bf16 a[164:167], v[212:215], v[132:135], a[164:167]
s_waitcnt vmcnt(20)
s_waitcnt lgkmcnt(0)
v_mfma_f32_16x16x32_bf16 a[168:171], v[212:215], v[136:139], a[168:171]
s_barrier
ds_read_b128 v[144:147], v228 offset:8192
v_mfma_f32_16x16x32_bf16 a[172:175], v[212:215], v[140:143], a[172:175]
s_mov_b32 s61, 0x300000
s_add_u32 m0, s60, 0x0
ds_read_b128 v[148:151], v228 offset:9216
ds_read_b128 v[152:155], v255 offset:10240
v_mfma_f32_16x16x32_bf16 a[176:179], v[216:219], v[128:131], a[176:179]
ds_read_b128 v[156:159], v255 offset:11264
ds_read_b64_tr_b16 v[208:209], v227 offset:32768
v_mfma_f32_16x16x32_bf16 a[180:183], v[216:219], v[132:135], a[180:183]
buffer_load_dwordx4 v233, s[40:43], s61 offen offset:0  lds
v_mfma_f32_16x16x32_bf16 a[184:187], v[216:219], v[136:139], a[184:187]
ds_read_b64_tr_b16 v[210:211], v227 offset:33024
ds_read_b64_tr_b16 v[212:213], v227 offset:32776
v_mfma_f32_16x16x32_bf16 a[188:191], v[216:219], v[140:143], a[188:191]
ds_read_b64_tr_b16 v[214:215], v227 offset:33032
v_mfma_f32_16x16x32_bf16 a[192:195], v[192:195], v[128:131], a[192:195]
ds_read_b64_tr_b16 v[216:217], v227 offset:33280
v_mfma_f32_16x16x32_bf16 a[196:199], v[192:195], v[132:135], a[196:199]
s_add_u32 m0, s60, 0x380
ds_read_b64_tr_b16 v[218:219], v227 offset:33536
v_mfma_f32_16x16x32_bf16 a[200:203], v[192:195], v[136:139], a[200:203]
v_mfma_f32_16x16x32_bf16 a[204:207], v[192:195], v[140:143], a[204:207]
buffer_load_dwordx4 v233, s[40:43], s61 offen offset:128  lds
v_mfma_f32_16x16x32_bf16 a[208:211], v[196:199], v[128:131], a[208:211]
ds_read_b64_tr_b16 v[192:193], v227 offset:33288
v_mfma_f32_16x16x32_bf16 a[212:215], v[196:199], v[132:135], a[212:215]
ds_read_b64_tr_b16 v[194:195], v227 offset:33544
v_mfma_f32_16x16x32_bf16 a[216:219], v[196:199], v[136:139], a[216:219]
v_mfma_f32_16x16x32_bf16 a[220:223], v[196:199], v[140:143], a[220:223]
s_add_u32 m0, s60, 0x700
v_mfma_f32_16x16x32_bf16 a[224:227], v[200:203], v[128:131], a[224:227]
ds_read_b64_tr_b16 v[196:197], v227 offset:33792
v_mfma_f32_16x16x32_bf16 a[228:231], v[200:203], v[132:135], a[228:231]
buffer_load_dwordx4 v233, s[40:43], s61 offen offset:256  lds
v_mfma_f32_16x16x32_bf16 a[232:235], v[200:203], v[136:139], a[232:235]
ds_read_b64_tr_b16 v[198:199], v227 offset:34048
v_mfma_f32_16x16x32_bf16 a[236:239], v[200:203], v[140:143], a[236:239]
v_mfma_f32_16x16x32_bf16 a[240:243], v[204:207], v[128:131], a[240:243]
ds_read_b64_tr_b16 v[200:201], v227 offset:33800
v_mfma_f32_16x16x32_bf16 a[244:247], v[204:207], v[132:135], a[244:247]
s_add_u32 m0, s60, 0xa80
ds_read_b64_tr_b16 v[202:203], v227 offset:34056
v_mfma_f32_16x16x32_bf16 a[248:251], v[204:207], v[136:139], a[248:251]
v_mfma_f32_16x16x32_bf16 a[252:255], v[204:207], v[140:143], a[252:255]
LST9:
buffer_load_dwordx4 v233, s[40:43], s61 offen offset:384  lds
s_waitcnt lgkmcnt(4)
v_mfma_f32_16x16x32_bf16 v[0:3], v[208:211], v[144:147], v[0:3]
ds_read_b64_tr_b16 v[204:205], v227 offset:34304
v_mfma_f32_16x16x32_bf16 v[32:35], v[208:211], v[148:151], v[32:35]
ds_read_b64_tr_b16 v[206:207], v227 offset:34560
v_mfma_f32_16x16x32_bf16 v[64:67], v[208:211], v[152:155], v[64:67]
v_mfma_f32_16x16x32_bf16 v[96:99], v[208:211], v[156:159], v[96:99]
s_mov_b32 s61, 0x320000
s_add_u32 m0, s60, 0x2000
v_mfma_f32_16x16x32_bf16 v[4:7], v[212:215], v[144:147], v[4:7]
ds_read_b64_tr_b16 v[208:209], v227 offset:34312
v_mfma_f32_16x16x32_bf16 v[36:39], v[212:215], v[148:151], v[36:39]
buffer_load_dwordx4 v233, s[40:43], s61 offen offset:0  lds
v_mfma_f32_16x16x32_bf16 v[68:71], v[212:215], v[152:155], v[68:71]
ds_read_b64_tr_b16 v[210:211], v227 offset:34568
v_mfma_f32_16x16x32_bf16 v[100:103], v[212:215], v[156:159], v[100:103]
v_mfma_f32_16x16x32_bf16 v[8:11], v[216:219], v[144:147], v[8:11]
ds_read_b64_tr_b16 v[212:213], v227 offset:34816
v_mfma_f32_16x16x32_bf16 v[40:43], v[216:219], v[148:151], v[40:43]
s_add_u32 m0, s60, 0x2380
ds_read_b64_tr_b16 v[214:215], v227 offset:35072
v_mfma_f32_16x16x32_bf16 v[72:75], v[216:219], v[152:155], v[72:75]
v_mfma_f32_16x16x32_bf16 v[104:107], v[216:219], v[156:159], v[104:107]
buffer_load_dwordx4 v233, s[40:43], s61 offen offset:128  lds
v_mfma_f32_16x16x32_bf16 v[12:15], v[192:195], v[144:147], v[12:15]
ds_read_b64_tr_b16 v[216:217], v227 offset:34824
v_mfma_f32_16x16x32_bf16 v[44:47], v[192:195], v[148:151], v[44:47]
ds_read_b64_tr_b16 v[218:219], v227 offset:35080
v_mfma_f32_16x16x32_bf16 v[76:79], v[192:195], v[152:155], v[76:79]
v_mfma_f32_16x16x32_bf16 v[108:111], v[192:195], v[156:159], v[108:111]
s_add_u32 m0, s60, 0x2700
s_waitcnt lgkmcnt(5)
v_mfma_f32_16x16x32_bf16 v[16:19], v[196:199], v[144:147], v[16:19]
ds_read_b64_tr_b16 v[192:193], v227 offset:35328
v_mfma_f32_16x16x32_bf16 v[48:51], v[196:199], v[148:151], v[48:51]
buffer_load_dwordx4 v233, s[40:43], s61 offen offset:256  lds
v_mfma_f32_16x16x32_bf16 v[80:83], v[196:199], v[152:155], v[80:83]
ds_read_b64_tr_b16 v[194:195], v227 offset:35584
v_mfma_f32_16x16x32_bf16 v[112:115], v[196:199], v[156:159], v[112:115]
v_mfma_f32_16x16x32_bf16 v[20:23], v[200:203], v[144:147], v[20:23]
ds_read_b64_tr_b16 v[196:197], v227 offset:35336
v_mfma_f32_16x16x32_bf16 v[52:55], v[200:203], v[148:151], v[52:55]
s_add_u32 m0, s60, 0x2a80
ds_read_b64_tr_b16 v[198:199], v227 offset:35592
v_mfma_f32_16x16x32_bf16 v[84:87], v[200:203], v[152:155], v[84:87]
v_mfma_f32_16x16x32_bf16 v[116:119], v[200:203], v[156:159], v[116:119]
buffer_load_dwordx4 v233, s[40:43], s61 offen offset:384  lds
v_mfma_f32_16x16x32_bf16 v[24:27], v[204:207], v[144:147], v[24:27]
ds_read_b64_tr_b16 v[200:201], v227 offset:35840
v_mfma_f32_16x16x32_bf16 v[56:59], v[204:207], v[148:151], v[56:59]
ds_read_b64_tr_b16 v[202:203], v227 offset:36096
v_mfma_f32_16x16x32_bf16 v[88:91], v[204:207], v[152:155], v[88:91]
v_mfma_f32_16x16x32_bf16 v[120:123], v[204:207], v[156:159], v[120:123]
s_mov_b32 s61, 0x300
s_add_u32 m0, s34, 0x20000
s_waitcnt lgkmcnt(5)
v_mfma_f32_16x16x32_bf16 v[28:31], v[208:211], v[144:147], v[28:31]
ds_read_b64_tr_b16 v[204:205], v227 offset:35848
v_mfma_f32_16x16x32_bf16 v[60:63], v[208:211], v[148:151], v[60:63]
buffer_load_dwordx4 v234, s[36:39], s61 offen offset:0  lds
v_mfma_f32_16x16x32_bf16 v[92:95], v[208:211], v[152:155], v[92:95]
ds_read_b64_tr_b16 v[206:207], v227 offset:36104
v_mfma_f32_16x16x32_bf16 v[124:127], v[208:211], v[156:159], v[124:127]
v_mfma_f32_16x16x32_bf16 a[128:131], v[212:215], v[144:147], a[128:131]
ds_read_b64_tr_b16 v[208:209], v227 offset:36352
v_mfma_f32_16x16x32_bf16 a[132:135], v[212:215], v[148:151], a[132:135]
s_mov_b32 s61, 0x3b00
s_add_u32 m0, s34, 0x20400
ds_read_b64_tr_b16 v[210:211], v227 offset:36608
v_mfma_f32_16x16x32_bf16 a[136:139], v[212:215], v[152:155], a[136:139]
v_mfma_f32_16x16x32_bf16 a[140:143], v[212:215], v[156:159], a[140:143]
buffer_load_dwordx4 v234, s[36:39], s61 offen offset:0  lds
v_mfma_f32_16x16x32_bf16 a[144:147], v[216:219], v[144:147], a[144:147]
ds_read_b64_tr_b16 v[212:213], v227 offset:36360
v_mfma_f32_16x16x32_bf16 a[148:151], v[216:219], v[148:151], a[148:151]
ds_read_b64_tr_b16 v[214:215], v227 offset:36616
v_mfma_f32_16x16x32_bf16 a[152:155], v[216:219], v[152:155], a[152:155]
v_mfma_f32_16x16x32_bf16 a[156:159], v[216:219], v[156:159], a[156:159]
s_waitcnt lgkmcnt(5)
v_mfma_f32_16x16x32_bf16 a[160:163], v[192:195], v[144:147], a[160:163]
v_mfma_f32_16x16x32_bf16 a[164:167], v[192:195], v[148:151], a[164:167]
s_waitcnt vmcnt(20)
s_waitcnt lgkmcnt(0)
v_mfma_f32_16x16x32_bf16 a[168:171], v[192:195], v[152:155], a[168:171]
s_barrier
ds_read_b128 v[128:131], v228 offset:16384
v_mfma_f32_16x16x32_bf16 a[172:175], v[192:195], v[156:159], a[172:175]
s_mov_b32 s61, 0x340000
s_add_u32 m0, s60, 0x8000
ds_read_b128 v[132:135], v228 offset:17408
ds_read_b128 v[136:139], v255 offset:18432
v_mfma_f32_16x16x32_bf16 a[176:179], v[196:199], v[144:147], a[176:179]
ds_read_b128 v[140:143], v255 offset:19456
v_add_u32 v227, 0x10000, v227
v_mfma_f32_16x16x32_bf16 a[180:183], v[196:199], v[148:151], a[180:183]
buffer_load_dwordx4 v233, s[40:43], s61 offen offset:0  lds
v_mfma_f32_16x16x32_bf16 a[184:187], v[196:199], v[152:155], a[184:187]
ds_read_b64_tr_b16 v[216:217], v227 offset:0
ds_read_b64_tr_b16 v[218:219], v227 offset:256
v_mfma_f32_16x16x32_bf16 a[188:191], v[196:199], v[156:159], a[188:191]
ds_read_b64_tr_b16 v[192:193], v227 offset:8
ds_read_b64_tr_b16 v[194:195], v227 offset:264
v_mfma_f32_16x16x32_bf16 a[192:195], v[200:203], v[144:147], a[192:195]
ds_read_b64_tr_b16 v[196:197], v227 offset:512
v_mfma_f32_16x16x32_bf16 a[196:199], v[200:203], v[148:151], a[196:199]
s_add_u32 m0, s60, 0x8380
ds_read_b64_tr_b16 v[198:199], v227 offset:768
v_mfma_f32_16x16x32_bf16 a[200:203], v[200:203], v[152:155], a[200:203]
v_mfma_f32_16x16x32_bf16 a[204:207], v[200:203], v[156:159], a[204:207]
buffer_load_dwordx4 v233, s[40:43], s61 offen offset:128  lds
v_mfma_f32_16x16x32_bf16 a[208:211], v[204:207], v[144:147], a[208:211]
ds_read_b64_tr_b16 v[200:201], v227 offset:520
v_mfma_f32_16x16x32_bf16 a[212:215], v[204:207], v[148:151], a[212:215]
ds_read_b64_tr_b16 v[202:203], v227 offset:776
v_mfma_f32_16x16x32_bf16 a[216:219], v[204:207], v[152:155], a[216:219]
v_mfma_f32_16x16x32_bf16 a[220:223], v[204:207], v[156:159], a[220:223]
s_add_u32 m0, s60, 0x8700
v_mfma_f32_16x16x32_bf16 a[224:227], v[208:211], v[144:147], a[224:227]
ds_read_b64_tr_b16 v[204:205], v227 offset:1024
v_mfma_f32_16x16x32_bf16 a[228:231], v[208:211], v[148:151], a[228:231]
buffer_load_dwordx4 v233, s[40:43], s61 offen offset:256  lds
v_mfma_f32_16x16x32_bf16 a[232:235], v[208:211], v[152:155], a[232:235]
ds_read_b64_tr_b16 v[206:207], v227 offset:1280
v_mfma_f32_16x16x32_bf16 a[236:239], v[208:211], v[156:159], a[236:239]
v_mfma_f32_16x16x32_bf16 a[240:243], v[212:215], v[144:147], a[240:243]
ds_read_b64_tr_b16 v[208:209], v227 offset:1032
v_mfma_f32_16x16x32_bf16 a[244:247], v[212:215], v[148:151], a[244:247]
s_add_u32 m0, s60, 0x8a80
ds_read_b64_tr_b16 v[210:211], v227 offset:1288
v_mfma_f32_16x16x32_bf16 a[248:251], v[212:215], v[152:155], a[248:251]
v_mfma_f32_16x16x32_bf16 a[252:255], v[212:215], v[156:159], a[252:255]
LST10:
buffer_load_dwordx4 v233, s[40:43], s61 offen offset:384  lds
s_waitcnt lgkmcnt(4)
v_mfma_f32_16x16x32_bf16 v[0:3], v[216:219], v[128:131], v[0:3]
ds_read_b64_tr_b16 v[212:213], v227 offset:1536
v_mfma_f32_16x16x32_bf16 v[32:35], v[216:219], v[132:135], v[32:35]
ds_read_b64_tr_b16 v[214:215], v227 offset:1792
v_mfma_f32_16x16x32_bf16 v[64:67], v[216:219], v[136:139], v[64:67]
v_mfma_f32_16x16x32_bf16 v[96:99], v[216:219], v[140:143], v[96:99]
s_mov_b32 s61, 0x360000
s_add_u32 m0, s60, 0xa000
v_mfma_f32_16x16x32_bf16 v[4:7], v[192:195], v[128:131], v[4:7]
ds_read_b64_tr_b16 v[216:217], v227 offset:1544
v_mfma_f32_16x16x32_bf16 v[36:39], v[192:195], v[132:135], v[36:39]
buffer_load_dwordx4 v233, s[40:43], s61 offen offset:0  lds
v_mfma_f32_16x16x32_bf16 v[68:71], v[192:195], v[136:139], v[68:71]
ds_read_b64_tr_b16 v[218:219], v227 offset:1800
v_mfma_f32_16x16x32_bf16 v[100:103], v[192:195], v[140:143], v[100:103]
v_mfma_f32_16x16x32_bf16 v[8:11], v[196:199], v[128:131], v[8:11]
ds_read_b64_tr_b16 v[192:193], v227 offset:2048
v_mfma_f32_16x16x32_bf16 v[40:43], v[196:199], v[132:135], v[40:43]
s_add_u32 m0, s60, 0xa380
ds_read_b64_tr_b16 v[194:195], v227 offset:2304
v_mfma_f32_16x16x32_bf16 v[72:75], v[196:199], v[136:139], v[72:75]
v_mfma_f32_16x16x32_bf16 v[104:107], v[196:199], v[140:143], v[104:107]
buffer_load_dwordx4 v233, s[40:43], s61 offen offset:128  lds
v_mfma_f32_16x16x32_bf16 v[12:15], v[200:203], v[128:131], v[12:15]
ds_read_b64_tr_b16 v[196:197], v227 offset:2056
v_mfma_f32_16x16x32_bf16 v[44:47], v[200:203], v[132:135], v[44:47]
ds_read_b64_tr_b16 v[198:199], v227 offset:2312
v_mfma_f32_16x16x32_bf16 v[76:79], v[200:203], v[136:139], v[76:79]
v_mfma_f32_16x16x32_bf16 v[108:111], v[200:203], v[140:143], v[108:111]
s_add_u32 m0, s60, 0xa700
s_waitcnt lgkmcnt(5)
v_mfma_f32_16x16x32_bf16 v[16:19], v[204:207], v[128:131], v[16:19]
ds_read_b64_tr_b16 v[200:201], v227 offset:2560
v_mfma_f32_16x16x32_bf16 v[48:51], v[204:207], v[132:135], v[48:51]
buffer_load_dwordx4 v233, s[40:43], s61 offen offset:256  lds
v_mfma_f32_16x16x32_bf16 v[80:83], v[204:207], v[136:139], v[80:83]
ds_read_b64_tr_b16 v[202:203], v227 offset:2816
v_mfma_f32_16x16x32_bf16 v[112:115], v[204:207], v[140:143], v[112:115]
v_mfma_f32_16x16x32_bf16 v[20:23], v[208:211], v[128:131], v[20:23]
ds_read_b64_tr_b16 v[204:205], v227 offset:2568
v_mfma_f32_16x16x32_bf16 v[52:55], v[208:211], v[132:135], v[52:55]
s_add_u32 m0, s60, 0xaa80
ds_read_b64_tr_b16 v[206:207], v227 offset:2824
v_mfma_f32_16x16x32_bf16 v[84:87], v[208:211], v[136:139], v[84:87]
v_mfma_f32_16x16x32_bf16 v[116:119], v[208:211], v[140:143], v[116:119]
buffer_load_dwordx4 v233, s[40:43], s61 offen offset:384  lds
v_mfma_f32_16x16x32_bf16 v[24:27], v[212:215], v[128:131], v[24:27]
ds_read_b64_tr_b16 v[208:209], v227 offset:3072
v_mfma_f32_16x16x32_bf16 v[56:59], v[212:215], v[132:135], v[56:59]
ds_read_b64_tr_b16 v[210:211], v227 offset:3328
v_mfma_f32_16x16x32_bf16 v[88:91], v[212:215], v[136:139], v[88:91]
v_mfma_f32_16x16x32_bf16 v[120:123], v[212:215], v[140:143], v[120:123]
s_mov_b32 s61, 0x340
s_add_u32 m0, s34, 0x22000
s_waitcnt lgkmcnt(5)
v_mfma_f32_16x16x32_bf16 v[28:31], v[216:219], v[128:131], v[28:31]
ds_read_b64_tr_b16 v[212:213], v227 offset:3080
v_mfma_f32_16x16x32_bf16 v[60:63], v[216:219], v[132:135], v[60:63]
buffer_load_dwordx4 v234, s[36:39], s61 offen offset:0  lds
v_mfma_f32_16x16x32_bf16 v[92:95], v[216:219], v[136:139], v[92:95]
ds_read_b64_tr_b16 v[214:215], v227 offset:3336
v_mfma_f32_16x16x32_bf16 v[124:127], v[216:219], v[140:143], v[124:127]
v_mfma_f32_16x16x32_bf16 a[128:131], v[192:195], v[128:131], a[128:131]
ds_read_b64_tr_b16 v[216:217], v227 offset:3584
v_mfma_f32_16x16x32_bf16 a[132:135], v[192:195], v[132:135], a[132:135]
s_mov_b32 s61, 0x3b40
s_add_u32 m0, s34, 0x22400
ds_read_b64_tr_b16 v[218:219], v227 offset:3840
v_mfma_f32_16x16x32_bf16 a[136:139], v[192:195], v[136:139], a[136:139]
v_mfma_f32_16x16x32_bf16 a[140:143], v[192:195], v[140:143], a[140:143]
buffer_load_dwordx4 v234, s[36:39], s61 offen offset:0  lds
v_mfma_f32_16x16x32_bf16 a[144:147], v[196:199], v[128:131], a[144:147]
ds_read_b64_tr_b16 v[192:193], v227 offset:3592
v_mfma_f32_16x16x32_bf16 a[148:151], v[196:199], v[132:135], a[148:151]
ds_read_b64_tr_b16 v[194:195], v227 offset:3848
v_mfma_f32_16x16x32_bf16 a[152:155], v[196:199], v[136:139], a[152:155]
v_mfma_f32_16x16x32_bf16 a[156:159], v[196:199], v[140:143], a[156:159]
s_waitcnt lgkmcnt(5)
v_mfma_f32_16x16x32_bf16 a[160:163], v[200:203], v[128:131], a[160:163]
v_mfma_f32_16x16x32_bf16 a[164:167], v[200:203], v[132:135], a[164:167]
s_waitcnt vmcnt(20)
s_waitcnt lgkmcnt(0)
v_mfma_f32_16x16x32_bf16 a[168:171], v[200:203], v[136:139], a[168:171]
s_barrier
ds_read_b128 v[144:147], v228 offset:24576
v_mfma_f32_16x16x32_bf16 a[172:175], v[200:203], v[140:143], a[172:175]
s_add_u32 m0, s60, 0x10000
ds_read_b128 v[148:151], v228 offset:25600
ds_read_b128 v[152:155], v255 offset:26624
v_mfma_f32_16x16x32_bf16 a[176:179], v[204:207], v[128:131], a[176:179]
ds_read_b128 v[156:159], v255 offset:27648
ds_read_b64_tr_b16 v[196:197], v227 offset:32768
v_mfma_f32_16x16x32_bf16 a[180:183], v[204:207], v[132:135], a[180:183]
buffer_load_dwordx4 v231, s[44:47], s84 offen offset:0 nt lds
v_mfma_f32_16x16x32_bf16 a[184:187], v[204:207], v[136:139], a[184:187]
ds_read_b64_tr_b16 v[198:199], v227 offset:33024
ds_read_b64_tr_b16 v[200:201], v227 offset:32776
v_mfma_f32_16x16x32_bf16 a[188:191], v[204:207], v[140:143], a[188:191]
ds_read_b64_tr_b16 v[202:203], v227 offset:33032
v_mfma_f32_16x16x32_bf16 a[192:195], v[208:211], v[128:131], a[192:195]
ds_read_b64_tr_b16 v[204:205], v227 offset:33280
v_mfma_f32_16x16x32_bf16 a[196:199], v[208:211], v[132:135], a[196:199]
ds_read_b64_tr_b16 v[206:207], v227 offset:33536
v_mfma_f32_16x16x32_bf16 a[200:203], v[208:211], v[136:139], a[200:203]
v_mfma_f32_16x16x32_bf16 a[204:207], v[208:211], v[140:143], a[204:207]
v_mfma_f32_16x16x32_bf16 a[208:211], v[212:215], v[128:131], a[208:211]
buffer_load_dwordx4 v231, s[44:47], s85 offen offset:1024 nt lds
v_mfma_f32_16x16x32_bf16 a[212:215], v[212:215], v[132:135], a[212:215]
ds_read_b64_tr_b16 v[208:209], v227 offset:33288
ds_read_b64_tr_b16 v[210:211], v227 offset:33544
v_mfma_f32_16x16x32_bf16 a[216:219], v[212:215], v[136:139], a[216:219]
v_mfma_f32_16x16x32_bf16 a[220:223], v[212:215], v[140:143], a[220:223]
v_mfma_f32_16x16x32_bf16 a[224:227], v[216:219], v[128:131], a[224:227]
ds_read_b64_tr_b16 v[212:213], v227 offset:33792
v_mfma_f32_16x16x32_bf16 a[228:231], v[216:219], v[132:135], a[228:231]
ds_read_b64_tr_b16 v[214:215], v227 offset:34048
v_mfma_f32_16x16x32_bf16 a[232:235], v[216:219], v[136:139], a[232:235]
v_mfma_f32_16x16x32_bf16 a[236:239], v[216:219], v[140:143], a[236:239]
v_mfma_f32_16x16x32_bf16 a[240:243], v[192:195], v[128:131], a[240:243]
buffer_load_dwordx4 v231, s[44:47], s86 offen offset:2048 nt lds
v_mfma_f32_16x16x32_bf16 a[244:247], v[192:195], v[132:135], a[244:247]
ds_read_b64_tr_b16 v[216:217], v227 offset:33800
ds_read_b64_tr_b16 v[218:219], v227 offset:34056
v_mfma_f32_16x16x32_bf16 a[248:251], v[192:195], v[136:139], a[248:251]
v_mfma_f32_16x16x32_bf16 a[252:255], v[192:195], v[140:143], a[252:255]
LST11:
s_waitcnt lgkmcnt(4)
v_mfma_f32_16x16x32_bf16 v[0:3], v[196:199], v[144:147], v[0:3]
ds_read_b64_tr_b16 v[192:193], v227 offset:34304
v_mfma_f32_16x16x32_bf16 v[32:35], v[196:199], v[148:151], v[32:35]
ds_read_b64_tr_b16 v[194:195], v227 offset:34560
v_mfma_f32_16x16x32_bf16 v[64:67], v[196:199], v[152:155], v[64:67]
v_mfma_f32_16x16x32_bf16 v[96:99], v[196:199], v[156:159], v[96:99]
buffer_load_dwordx4 v231, s[44:47], s87 offen offset:3072 nt lds
v_mfma_f32_16x16x32_bf16 v[4:7], v[200:203], v[144:147], v[4:7]
ds_read_b64_tr_b16 v[196:197], v227 offset:34312
v_mfma_f32_16x16x32_bf16 v[36:39], v[200:203], v[148:151], v[36:39]
ds_read_b64_tr_b16 v[198:199], v227 offset:34568
v_mfma_f32_16x16x32_bf16 v[68:71], v[200:203], v[152:155], v[68:71]
v_mfma_f32_16x16x32_bf16 v[100:103], v[200:203], v[156:159], v[100:103]
v_mfma_f32_16x16x32_bf16 v[8:11], v[204:207], v[144:147], v[8:11]
ds_read_b64_tr_b16 v[200:201], v227 offset:34816
v_mfma_f32_16x16x32_bf16 v[40:43], v[204:207], v[148:151], v[40:43]
s_add_u32 m0, s60, 0x12000
ds_read_b64_tr_b16 v[202:203], v227 offset:35072
v_mfma_f32_16x16x32_bf16 v[72:75], v[204:207], v[152:155], v[72:75]
v_mfma_f32_16x16x32_bf16 v[104:107], v[204:207], v[156:159], v[104:107]
buffer_load_dwordx4 v231, s[44:47], s88 offen offset:0 nt lds
v_mfma_f32_16x16x32_bf16 v[12:15], v[208:211], v[144:147], v[12:15]
ds_read_b64_tr_b16 v[204:205], v227 offset:34824
v_mfma_f32_16x16x32_bf16 v[44:47], v[208:211], v[148:151], v[44:47]
ds_read_b64_tr_b16 v[206:207], v227 offset:35080
v_mfma_f32_16x16x32_bf16 v[76:79], v[208:211], v[152:155], v[76:79]
v_mfma_f32_16x16x32_bf16 v[108:111], v[208:211], v[156:159], v[108:111]
s_waitcnt lgkmcnt(4)
v_mfma_f32_16x16x32_bf16 v[16:19], v[212:215], v[144:147], v[16:19]
ds_read_b64_tr_b16 v[208:209], v227 offset:35328
v_mfma_f32_16x16x32_bf16 v[48:51], v[212:215], v[148:151], v[48:51]
ds_read_b64_tr_b16 v[210:211], v227 offset:35584
v_mfma_f32_16x16x32_bf16 v[80:83], v[212:215], v[152:155], v[80:83]
buffer_load_dwordx4 v231, s[44:47], s89 offen offset:1024 nt lds
v_mfma_f32_16x16x32_bf16 v[112:115], v[212:215], v[156:159], v[112:115]
v_mfma_f32_16x16x32_bf16 v[20:23], v[216:219], v[144:147], v[20:23]
ds_read_b64_tr_b16 v[212:213], v227 offset:35336
v_mfma_f32_16x16x32_bf16 v[52:55], v[216:219], v[148:151], v[52:55]
ds_read_b64_tr_b16 v[214:215], v227 offset:35592
v_mfma_f32_16x16x32_bf16 v[84:87], v[216:219], v[152:155], v[84:87]
v_mfma_f32_16x16x32_bf16 v[116:119], v[216:219], v[156:159], v[116:119]
v_mfma_f32_16x16x32_bf16 v[24:27], v[192:195], v[144:147], v[24:27]
ds_read_b64_tr_b16 v[216:217], v227 offset:35840
v_mfma_f32_16x16x32_bf16 v[56:59], v[192:195], v[148:151], v[56:59]
ds_read_b64_tr_b16 v[218:219], v227 offset:36096
v_mfma_f32_16x16x32_bf16 v[88:91], v[192:195], v[152:155], v[88:91]
buffer_load_dwordx4 v231, s[44:47], s90 offen offset:2048 nt lds
v_mfma_f32_16x16x32_bf16 v[120:123], v[192:195], v[156:159], v[120:123]
v_mfma_f32_16x16x32_bf16 v[28:31], v[196:199], v[144:147], v[28:31]
ds_read_b64_tr_b16 v[192:193], v227 offset:35848
v_mfma_f32_16x16x32_bf16 v[60:63], v[196:199], v[148:151], v[60:63]
ds_read_b64_tr_b16 v[194:195], v227 offset:36104
v_mfma_f32_16x16x32_bf16 v[92:95], v[196:199], v[152:155], v[92:95]
LH2:
s_cbranch_execz LHB0
v_mfma_f32_16x16x32_bf16 v[124:127], v[196:199], v[156:159], v[124:127]
s_waitcnt lgkmcnt(4)
v_mfma_f32_16x16x32_bf16 a[128:131], v[200:203], v[144:147], a[128:131]
ds_read_b64_tr_b16 v[196:197], v227 offset:36352
v_mfma_f32_16x16x32_bf16 a[132:135], v[200:203], v[148:151], a[132:135]
buffer_load_dwordx4 v231, s[44:47], s91 offen offset:3072 nt lds
v_mfma_f32_16x16x32_bf16 a[136:139], v[200:203], v[152:155], a[136:139]
ds_read_b64_tr_b16 v[198:199], v227 offset:36608
v_mfma_f32_16x16x32_bf16 a[140:143], v[200:203], v[156:159], a[140:143]
s_add_u32 s44, s44, 0x4000
s_addc_u32 s45, s45, 0
v_mfma_f32_16x16x32_bf16 a[144:147], v[204:207], v[144:147], a[144:147]
ds_read_b64_tr_b16 v[200:201], v227 offset:36360
v_mfma_f32_16x16x32_bf16 a[148:151], v[204:207], v[148:151], a[148:151]
ds_read_b64_tr_b16 v[202:203], v227 offset:36616
v_mfma_f32_16x16x32_bf16 a[152:155], v[204:207], v[152:155], a[152:155]
v_mfma_f32_16x16x32_bf16 a[156:159], v[204:207], v[156:159], a[156:159]
v_mfma_f32_16x16x32_bf16 a[160:163], v[208:211], v[144:147], a[160:163]
v_mfma_f32_16x16x32_bf16 a[164:167], v[208:211], v[148:151], a[164:167]
s_waitcnt vmcnt(18)
s_waitcnt lgkmcnt(0)
v_mfma_f32_16x16x32_bf16 a[168:171], v[208:211], v[152:155], a[168:171]
s_barrier
ds_read_b128 v[128:131], v228 offset:0
v_mfma_f32_16x16x32_bf16 a[172:175], v[208:211], v[156:159], a[172:175]
s_add_u32 m0, s60, 0x18000
ds_read_b128 v[132:135], v228 offset:1024
ds_read_b128 v[136:139], v255 offset:2048
v_mfma_f32_16x16x32_bf16 a[176:179], v[212:215], v[144:147], a[176:179]
ds_read_b128 v[140:143], v255 offset:3072
v_add_u32 v227, 0xffff0000, v227
v_mfma_f32_16x16x32_bf16 a[180:183], v[212:215], v[148:151], a[180:183]
buffer_load_dwordx4 v231, s[44:47], s84 offen offset:0 nt lds
v_mfma_f32_16x16x32_bf16 a[184:187], v[212:215], v[152:155], a[184:187]
ds_read_b64_tr_b16 v[204:205], v227 offset:0
ds_read_b64_tr_b16 v[206:207], v227 offset:256
v_mfma_f32_16x16x32_bf16 a[188:191], v[212:215], v[156:159], a[188:191]
ds_read_b64_tr_b16 v[208:209], v227 offset:8
ds_read_b64_tr_b16 v[210:211], v227 offset:264
v_mfma_f32_16x16x32_bf16 a[192:195], v[216:219], v[144:147], a[192:195]
ds_read_b64_tr_b16 v[212:213], v227 offset:512
v_mfma_f32_16x16x32_bf16 a[196:199], v[216:219], v[148:151], a[196:199]
ds_read_b64_tr_b16 v[214:215], v227 offset:768
v_mfma_f32_16x16x32_bf16 a[200:203], v[216:219], v[152:155], a[200:203]
v_mfma_f32_16x16x32_bf16 a[204:207], v[216:219], v[156:159], a[204:207]
v_mfma_f32_16x16x32_bf16 a[208:211], v[192:195], v[144:147], a[208:211]
buffer_load_dwordx4 v231, s[44:47], s85 offen offset:1024 nt lds
v_mfma_f32_16x16x32_bf16 a[212:215], v[192:195], v[148:151], a[212:215]
ds_read_b64_tr_b16 v[216:217], v227 offset:520
ds_read_b64_tr_b16 v[218:219], v227 offset:776
v_mfma_f32_16x16x32_bf16 a[216:219], v[192:195], v[152:155], a[216:219]
v_mfma_f32_16x16x32_bf16 a[220:223], v[192:195], v[156:159], a[220:223]
v_mfma_f32_16x16x32_bf16 a[224:227], v[196:199], v[144:147], a[224:227]
ds_read_b64_tr_b16 v[192:193], v227 offset:1024
v_mfma_f32_16x16x32_bf16 a[228:231], v[196:199], v[148:151], a[228:231]
ds_read_b64_tr_b16 v[194:195], v227 offset:1280
v_mfma_f32_16x16x32_bf16 a[232:235], v[196:199], v[152:155], a[232:235]
v_mfma_f32_16x16x32_bf16 a[236:239], v[196:199], v[156:159], a[236:239]
v_mfma_f32_16x16x32_bf16 a[240:243], v[200:203], v[144:147], a[240:243]
buffer_load_dwordx4 v231, s[44:47], s86 offen offset:2048 nt lds
v_mfma_f32_16x16x32_bf16 a[244:247], v[200:203], v[148:151], a[244:247]
ds_read_b64_tr_b16 v[196:197], v227 offset:1032
ds_read_b64_tr_b16 v[198:199], v227 offset:1288
v_mfma_f32_16x16x32_bf16 a[248:251], v[200:203], v[152:155], a[248:251]
v_mfma_f32_16x16x32_bf16 a[252:255], v[200:203], v[156:159], a[252:255]
LST12:
s_waitcnt lgkmcnt(4)
v_mfma_f32_16x16x32_bf16 v[0:3], v[204:207], v[128:131], v[0:3]
ds_read_b64_tr_b16 v[200:201], v227 offset:1536
v_mfma_f32_16x16x32_bf16 v[32:35], v[204:207], v[132:135], v[32:35]
ds_read_b64_tr_b16 v[202:203], v227 offset:1792
v_mfma_f32_16x16x32_bf16 v[64:67], v[204:207], v[136:139], v[64:67]
v_mfma_f32_16x16x32_bf16 v[96:99], v[204:207], v[140:143], v[96:99]
buffer_load_dwordx4 v231, s[44:47], s87 offen offset:3072 nt lds
v_mfma_f32_16x16x32_bf16 v[4:7], v[208:211], v[128:131], v[4:7]
ds_read_b64_tr_b16 v[204:205], v227 offset:1544
v_mfma_f32_16x16x32_bf16 v[36:39], v[208:211], v[132:135], v[36:39]
ds_read_b64_tr_b16 v[206:207], v227 offset:1800
v_mfma_f32_16x16x32_bf16 v[68:71], v[208:211], v[136:139], v[68:71]
v_mfma_f32_16x16x32_bf16 v[100:103], v[208:211], v[140:143], v[100:103]
v_mfma_f32_16x16x32_bf16 v[8:11], v[212:215], v[128:131], v[8:11]
ds_read_b64_tr_b16 v[208:209], v227 offset:2048
v_mfma_f32_16x16x32_bf16 v[40:43], v[212:215], v[132:135], v[40:43]
s_add_u32 m0, s60, 0x1a000
ds_read_b64_tr_b16 v[210:211], v227 offset:2304
v_mfma_f32_16x16x32_bf16 v[72:75], v[212:215], v[136:139], v[72:75]
v_mfma_f32_16x16x32_bf16 v[104:107], v[212:215], v[140:143], v[104:107]
buffer_load_dwordx4 v231, s[44:47], s88 offen offset:0 nt lds
v_mfma_f32_16x16x32_bf16 v[12:15], v[216:219], v[128:131], v[12:15]
ds_read_b64_tr_b16 v[212:213], v227 offset:2056
v_mfma_f32_16x16x32_bf16 v[44:47], v[216:219], v[132:135], v[44:47]
ds_read_b64_tr_b16 v[214:215], v227 offset:2312
v_mfma_f32_16x16x32_bf16 v[76:79], v[216:219], v[136:139], v[76:79]
v_mfma_f32_16x16x32_bf16 v[108:111], v[216:219], v[140:143], v[108:111]
s_waitcnt lgkmcnt(4)
v_mfma_f32_16x16x32_bf16 v[16:19], v[192:195], v[128:131], v[16:19]
ds_read_b64_tr_b16 v[216:217], v227 offset:2560
v_mfma_f32_16x16x32_bf16 v[48:51], v[192:195], v[132:135], v[48:51]
ds_read_b64_tr_b16 v[218:219], v227 offset:2816
v_mfma_f32_16x16x32_bf16 v[80:83], v[192:195], v[136:139], v[80:83]
buffer_load_dwordx4 v231, s[44:47], s89 offen offset:1024 nt lds
v_mfma_f32_16x16x32_bf16 v[112:115], v[192:195], v[140:143], v[112:115]
v_mfma_f32_16x16x32_bf16 v[20:23], v[196:199], v[128:131], v[20:23]
ds_read_b64_tr_b16 v[192:193], v227 offset:2568
v_mfma_f32_16x16x32_bf16 v[52:55], v[196:199], v[132:135], v[52:55]
ds_read_b64_tr_b16 v[194:195], v227 offset:2824
v_mfma_f32_16x16x32_bf16 v[84:87], v[196:199], v[136:139], v[84:87]
v_mfma_f32_16x16x32_bf16 v[116:119], v[196:199], v[140:143], v[116:119]
v_mfma_f32_16x16x32_bf16 v[24:27], v[200:203], v[128:131], v[24:27]
ds_read_b64_tr_b16 v[196:197], v227 offset:3072
v_mfma_f32_16x16x32_bf16 v[56:59], v[200:203], v[132:135], v[56:59]
ds_read_b64_tr_b16 v[198:199], v227 offset:3328
v_mfma_f32_16x16x32_bf16 v[88:91], v[200:203], v[136:139], v[88:91]
buffer_load_dwordx4 v231, s[44:47], s90 offen offset:2048 nt lds
v_mfma_f32_16x16x32_bf16 v[120:123], v[200:203], v[140:143], v[120:123]
v_mfma_f32_16x16x32_bf16 v[28:31], v[204:207], v[128:131], v[28:31]
ds_read_b64_tr_b16 v[200:201], v227 offset:3080
v_mfma_f32_16x16x32_bf16 v[60:63], v[204:207], v[132:135], v[60:63]
ds_read_b64_tr_b16 v[202:203], v227 offset:3336
v_mfma_f32_16x16x32_bf16 v[92:95], v[204:207], v[136:139], v[92:95]
v_mfma_f32_16x16x32_bf16 v[124:127], v[204:207], v[140:143], v[124:127]
s_waitcnt lgkmcnt(4)
v_mfma_f32_16x16x32_bf16 a[128:131], v[208:211], v[128:131], a[128:131]
ds_read_b64_tr_b16 v[204:205], v227 offset:3584
v_mfma_f32_16x16x32_bf16 a[132:135], v[208:211], v[132:135], a[132:135]
buffer_load_dwordx4 v231, s[44:47], s91 offen offset:3072 nt lds
v_mfma_f32_16x16x32_bf16 a[136:139], v[208:211], v[136:139], a[136:139]
ds_read_b64_tr_b16 v[206:207], v227 offset:3840
v_mfma_f32_16x16x32_bf16 a[140:143], v[208:211], v[140:143], a[140:143]
s_add_u32 s44, s44, 0x4000
s_addc_u32 s45, s45, 0
v_mfma_f32_16x16x32_bf16 a[144:147], v[212:215], v[128:131], a[144:147]
ds_read_b64_tr_b16 v[208:209], v227 offset:3592
v_mfma_f32_16x16x32_bf16 a[148:151], v[212:215], v[132:135], a[148:151]
ds_read_b64_tr_b16 v[210:211], v227 offset:3848
v_mfma_f32_16x16x32_bf16 a[152:155], v[212:215], v[136:139], a[152:155]
v_mfma_f32_16x16x32_bf16 a[156:159], v[212:215], v[140:143], a[156:159]
v_mfma_f32_16x16x32_bf16 a[160:163], v[216:219], v[128:131], a[160:163]
v_mfma_f32_16x16x32_bf16 a[164:167], v[216:219], v[132:135], a[164:167]
s_waitcnt vmcnt(16)
s_waitcnt lgkmcnt(0)
v_mfma_f32_16x16x32_bf16 a[168:171], v[216:219], v[136:139], a[168:171]
s_barrier
ds_read_b128 v[144:147], v228 offset:8192
v_mfma_f32_16x16x32_bf16 a[172:175], v[216:219], v[140:143], a[172:175]
s_add_u32 m0, s60, 0x0
ds_read_b128 v[148:151], v228 offset:9216
ds_read_b128 v[152:155], v255 offset:10240
v_mfma_f32_16x16x32_bf16 a[176:179], v[192:195], v[128:131], a[176:179]
ds_read_b128 v[156:159], v255 offset:11264
ds_read_b64_tr_b16 v[212:213], v227 offset:32768
v_mfma_f32_16x16x32_bf16 a[180:183], v[192:195], v[132:135], a[180:183]
buffer_load_dwordx4 v232, s[48:51], s84 offen offset:0 nt lds
v_mfma_f32_16x16x32_bf16 a[184:187], v[192:195], v[136:139], a[184:187]
ds_read_b64_tr_b16 v[214:215], v227 offset:33024
ds_read_b64_tr_b16 v[216:217], v227 offset:32776
v_mfma_f32_16x16x32_bf16 a[188:191], v[192:195], v[140:143], a[188:191]
ds_read_b64_tr_b16 v[218:219], v227 offset:33032
v_mfma_f32_16x16x32_bf16 a[192:195], v[196:199], v[128:131], a[192:195]
ds_read_b64_tr_b16 v[192:193], v227 offset:33280
v_mfma_f32_16x16x32_bf16 a[196:199], v[196:199], v[132:135], a[196:199]
ds_read_b64_tr_b16 v[194:195], v227 offset:33536
v_mfma_f32_16x16x32_bf16 a[200:203], v[196:199], v[136:139], a[200:203]
v_mfma_f32_16x16x32_bf16 a[204:207], v[196:199], v[140:143], a[204:207]
v_mfma_f32_16x16x32_bf16 a[208:211], v[200:203], v[128:131], a[208:211]
buffer_load_dwordx4 v232, s[48:51], s85 offen offset:1024 nt lds
v_mfma_f32_16x16x32_bf16 a[212:215], v[200:203], v[132:135], a[212:215]
ds_read_b64_tr_b16 v[196:197], v227 offset:33288
ds_read_b64_tr_b16 v[198:199], v227 offset:33544
v_mfma_f32_16x16x32_bf16 a[216:219], v[200:203], v[136:139], a[216:219]
v_mfma_f32_16x16x32_bf16 a[220:223], v[200:203], v[140:143], a[220:223]
v_mfma_f32_16x16x32_bf16 a[224:227], v[204:207], v[128:131], a[224:227]
ds_read_b64_tr_b16 v[200:201], v227 offset:33792
v_mfma_f32_16x16x32_bf16 a[228:231], v[204:207], v[132:135], a[228:231]
ds_read_b64_tr_b16 v[202:203], v227 offset:34048
v_mfma_f32_16x16x32_bf16 a[232:235], v[204:207], v[136:139], a[232:235]
v_mfma_f32_16x16x32_bf16 a[236:239], v[204:207], v[140:143], a[236:239]
v_mfma_f32_16x16x32_bf16 a[240:243], v[208:211], v[128:131], a[240:243]
buffer_load_dwordx4 v232, s[48:51], s86 offen offset:2048 nt lds
v_mfma_f32_16x16x32_bf16 a[244:247], v[208:211], v[132:135], a[244:247]
ds_read_b64_tr_b16 v[204:205], v227 offset:33800
ds_read_b64_tr_b16 v[206:207], v227 offset:34056
v_mfma_f32_16x16x32_bf16 a[248:251], v[208:211], v[136:139], a[248:251]
v_mfma_f32_16x16x32_bf16 a[252:255], v[208:211], v[140:143], a[252:255]
LST13:
s_waitcnt lgkmcnt(4)
v_mfma_f32_16x16x32_bf16 v[0:3], v[212:215], v[144:147], v[0:3]
ds_read_b64_tr_b16 v[208:209], v227 offset:34304
v_mfma_f32_16x16x32_bf16 v[32:35], v[212:215], v[148:151], v[32:35]
ds_read_b64_tr_b16 v[210:211], v227 offset:34560
v_mfma_f32_16x16x32_bf16 v[64:67], v[212:215], v[152:155], v[64:67]
v_mfma_f32_16x16x32_bf16 v[96:99], v[212:215], v[156:159], v[96:99]
buffer_load_dwordx4 v232, s[48:51], s87 offen offset:3072 nt lds
v_mfma_f32_16x16x32_bf16 v[4:7], v[216:219], v[144:147], v[4:7]
ds_read_b64_tr_b16 v[212:213], v227 offset:34312
v_mfma_f32_16x16x32_bf16 v[36:39], v[216:219], v[148:151], v[36:39]
ds_read_b64_tr_b16 v[214:215], v227 offset:34568
v_mfma_f32_16x16x32_bf16 v[68:71], v[216:219], v[152:155], v[68:71]
v_mfma_f32_16x16x32_bf16 v[100:103], v[216:219], v[156:159], v[100:103]
v_mfma_f32_16x16x32_bf16 v[8:11], v[192:195], v[144:147], v[8:11]
ds_read_b64_tr_b16 v[216:217], v227 offset:34816
v_mfma_f32_16x16x32_bf16 v[40:43], v[192:195], v[148:151], v[40:43]
s_add_u32 m0, s60, 0x2000
ds_read_b64_tr_b16 v[218:219], v227 offset:35072
v_mfma_f32_16x16x32_bf16 v[72:75], v[192:195], v[152:155], v[72:75]
v_cvt_pk_bf16_f32 v0, v0, v1
v_cvt_pk_bf16_f32 v1, v2, v3
v_mfma_f32_16x16x32_bf16 v[104:107], v[192:195], v[156:159], v[104:107]
buffer_load_dwordx4 v232, s[48:51], s88 offen offset:0 nt lds
v_mfma_f32_16x16x32_bf16 v[12:15], v[196:199], v[144:147], v[12:15]
ds_read_b64_tr_b16 v[192:193], v227 offset:34824
v_cvt_pk_bf16_f32 v2, v4, v5
v_mfma_f32_16x16x32_bf16 v[44:47], v[196:199], v[148:151], v[44:47]
ds_read_b64_tr_b16 v[194:195], v227 offset:35080
v_cvt_pk_bf16_f32 v3, v6, v7
v_mfma_f32_16x16x32_bf16 v[76:79], v[196:199], v[152:155], v[76:79]
v_cvt_pk_bf16_f32 v32, v32, v33
v_cvt_pk_bf16_f32 v33, v34, v35
v_mfma_f32_16x16x32_bf16 v[108:111], v[196:199], v[156:159], v[108:111]
v_cvt_pk_bf16_f32 v34, v36, v37
v_cvt_pk_bf16_f32 v35, v38, v39
s_waitcnt lgkmcnt(4)
v_mfma_f32_16x16x32_bf16 v[16:19], v[200:203], v[144:147], v[16:19]
ds_read_b64_tr_b16 v[196:197], v227 offset:35328
v_cvt_pk_bf16_f32 v64, v64, v65
v_mfma_f32_16x16x32_bf16 v[48:51], v[200:203], v[148:151], v[48:51]
ds_read_b64_tr_b16 v[198:199], v227 offset:35584
v_cvt_pk_bf16_f32 v65, v66, v67
v_mfma_f32_16x16x32_bf16 v[80:83], v[200:203], v[152:155], v[80:83]
buffer_load_dwordx4 v232, s[48:51], s89 offen offset:1024 nt lds
v_mfma_f32_16x16x32_bf16 v[112:115], v[200:203], v[156:159], v[112:115]
v_cvt_pk_bf16_f32 v66, v68, v69
v_cvt_pk_bf16_f32 v67, v70, v71
v_mfma_f32_16x16x32_bf16 v[20:23], v[204:207], v[144:147], v[20:23]
ds_read_b64_tr_b16 v[200:201], v227 offset:35336
v_cvt_pk_bf16_f32 v96, v96, v97
v_mfma_f32_16x16x32_bf16 v[52:55], v[204:207], v[148:151], v[52:55]
ds_read_b64_tr_b16 v[202:203], v227 offset:35592
v_cvt_pk_bf16_f32 v97, v98, v99
v_mfma_f32_16x16x32_bf16 v[84:87], v[204:207], v[152:155], v[84:87]
v_cvt_pk_bf16_f32 v98, v100, v101
v_cvt_pk_bf16_f32 v99, v102, v103
v_mfma_f32_16x16x32_bf16 v[116:119], v[204:207], v[156:159], v[116:119]
v_cvt_pk_bf16_f32 v4, v8, v9
v_cvt_pk_bf16_f32 v5, v10, v11
v_mfma_f32_16x16x32_bf16 v[24:27], v[208:211], v[144:147], v[24:27]
ds_read_b64_tr_b16 v[204:205], v227 offset:35840
v_cvt_pk_bf16_f32 v6, v12, v13
v_mfma_f32_16x16x32_bf16 v[56:59], v[208:211], v[148:151], v[56:59]
ds_read_b64_tr_b16 v[206:207], v227 offset:36096
v_cvt_pk_bf16_f32 v7, v14, v15
v_mfma_f32_16x16x32_bf16 v[88:91], v[208:211], v[152:155], v[88:91]
buffer_load_dwordx4 v232, s[48:51], s90 offen offset:2048 nt lds
v_mfma_f32_16x16x32_bf16 v[120:123], v[208:211], v[156:159], v[120:123]
v_cvt_pk_bf16_f32 v36, v40, v41
v_cvt_pk_bf16_f32 v37, v42, v43
v_mfma_f32_16x16x32_bf16 v[28:31], v[212:215], v[144:147], v[28:31]
ds_read_b64_tr_b16 v[208:209], v227 offset:35848
v_cvt_pk_bf16_f32 v38, v44, v45
v_mfma_f32_16x16x32_bf16 v[60:63], v[212:215], v[148:151], v[60:63]
ds_read_b64_tr_b16 v[210:211], v227 offset:36104
v_cvt_pk_bf16_f32 v39, v46, v47
v_mfma_f32_16x16x32_bf16 v[92:95], v[212:215], v[152:155], v[92:95]
v_cvt_pk_bf16_f32 v68, v72, v73
v_cvt_pk_bf16_f32 v69, v74, v75
v_mfma_f32_16x16x32_bf16 v[124:127], v[212:215], v[156:159], v[124:127]
v_cvt_pk_bf16_f32 v70, v76, v77
v_cvt_pk_bf16_f32 v71, v78, v79
s_waitcnt lgkmcnt(4)
v_mfma_f32_16x16x32_bf16 a[128:131], v[216:219], v[144:147], a[128:131]
ds_read_b64_tr_b16 v[212:213], v227 offset:36352
v_cvt_pk_bf16_f32 v100, v104, v105
v_mfma_f32_16x16x32_bf16 a[132:135], v[216:219], v[148:151], a[132:135]
buffer_load_dwordx4 v232, s[48:51], s91 offen offset:3072 nt lds
v_mfma_f32_16x16x32_bf16 a[136:139], v[216:219], v[152:155], a[136:139]
ds_read_b64_tr_b16 v[214:215], v227 offset:36608
v_cvt_pk_bf16_f32 v101, v106, v107
v_mfma_f32_16x16x32_bf16 a[140:143], v[216:219], v[156:159], a[140:143]
s_add_u32 s48, s48, 0x4000
s_addc_u32 s49, s49, 0
v_mfma_f32_16x16x32_bf16 a[144:147], v[192:195], v[144:147], a[144:147]
ds_read_b64_tr_b16 v[216:217], v227 offset:36360
v_cvt_pk_bf16_f32 v102, v108, v109
v_mfma_f32_16x16x32_bf16 a[148:151], v[192:195], v[148:151], a[148:151]
ds_read_b64_tr_b16 v[218:219], v227 offset:36616
v_cvt_pk_bf16_f32 v103, v110, v111
v_mfma_f32_16x16x32_bf16 a[152:155], v[192:195], v[152:155], a[152:155]
v_cvt_pk_bf16_f32 v8, v16, v17
v_cvt_pk_bf16_f32 v9, v18, v19
v_mfma_f32_16x16x32_bf16 a[156:159], v[192:195], v[156:159], a[156:159]
v_cvt_pk_bf16_f32 v10, v20, v21
v_cvt_pk_bf16_f32 v11, v22, v23
v_mfma_f32_16x16x32_bf16 a[160:163], v[196:199], v[144:147], a[160:163]
v_cvt_pk_bf16_f32 v40, v48, v49
v_cvt_pk_bf16_f32 v41, v50, v51
v_mfma_f32_16x16x32_bf16 a[164:167], v[196:199], v[148:151], a[164:167]
s_waitcnt vmcnt(16)
s_waitcnt lgkmcnt(0)
v_cvt_pk_bf16_f32 v42, v52, v53
v_mfma_f32_16x16x32_bf16 a[168:171], v[196:199], v[152:155], a[168:171]
s_barrier
v_add_u32 v224, 0x10000, v224
v_mfma_f32_16x16x32_bf16 a[172:175], v[196:199], v[156:159], a[172:175]
s_add_u32 m0, s60, 0x20000
ds_read_b128 v[192:195], v224 offset:0
v_cvt_pk_bf16_f32 v43, v54, v55
v_mfma_f32_16x16x32_bf16 a[176:179], v[200:203], v[144:147], a[176:179]
ds_read_b128 v[196:199], v224 offset:8192
v_cvt_pk_bf16_f32 v72, v80, v81
v_mfma_f32_16x16x32_bf16 a[180:183], v[200:203], v[148:151], a[180:183]
buffer_load_dwordx4 v231, s[44:47], s84 offen offset:0 nt lds
v_mfma_f32_16x16x32_bf16 a[184:187], v[200:203], v[152:155], a[184:187]
v_cvt_pk_bf16_f32 v73, v82, v83
v_cvt_pk_bf16_f32 v74, v84, v85
v_mfma_f32_16x16x32_bf16 a[188:191], v[200:203], v[156:159], a[188:191]
v_cvt_pk_bf16_f32 v75, v86, v87
v_cvt_pk_bf16_f32 v104, v112, v113
v_mfma_f32_16x16x32_bf16 a[192:195], v[204:207], v[144:147], a[192:195]
buffer_load_dwordx4 v231, s[44:47], s85 offen offset:1024 nt lds
v_mfma_f32_16x16x32_bf16 a[196:199], v[204:207], v[148:151], a[196:199]
ds_read_b128 v[200:203], v224 offset:512
v_cvt_pk_bf16_f32 v105, v114, v115
v_mfma_f32_16x16x32_bf16 a[200:203], v[204:207], v[152:155], a[200:203]
v_cvt_pk_bf16_f32 v106, v116, v117
v_cvt_pk_bf16_f32 v107, v118, v119
v_mfma_f32_16x16x32_bf16 a[204:207], v[204:207], v[156:159], a[204:207]
v_cvt_pk_bf16_f32 v12, v24, v25
v_cvt_pk_bf16_f32 v13, v26, v27
v_mfma_f32_16x16x32_bf16 a[208:211], v[208:211], v[144:147], a[208:211]
buffer_load_dwordx4 v231, s[44:47], s86 offen offset:2048 nt lds
v_mfma_f32_16x16x32_bf16 a[212:215], v[208:211], v[148:151], a[212:215]
ds_read_b128 v[204:207], v224 offset:8704
v_cvt_pk_bf16_f32 v14, v28, v29
v_mfma_f32_16x16x32_bf16 a[216:219], v[208:211], v[152:155], a[216:219]
v_cvt_pk_bf16_f32 v15, v30, v31
v_cvt_pk_bf16_f32 v44, v56, v57
v_mfma_f32_16x16x32_bf16 a[220:223], v[208:211], v[156:159], a[220:223]
v_cvt_pk_bf16_f32 v45, v58, v59
v_cvt_pk_bf16_f32 v46, v60, v61
v_mfma_f32_16x16x32_bf16 a[224:227], v[212:215], v[144:147], a[224:227]
buffer_load_dwordx4 v231, s[44:47], s87 offen offset:3072 nt lds
v_mfma_f32_16x16x32_bf16 a[228:231], v[212:215], v[148:151], a[228:231]
ds_read_b128 v[208:211], v224 offset:1024
v_cvt_pk_bf16_f32 v47, v62, v63
v_mfma_f32_16x16x32_bf16 a[232:235], v[212:215], v[152:155], a[232:235]
s_add_u32 m0, s60, 0x22000
v_cvt_pk_bf16_f32 v76, v88, v89
v_cvt_pk_bf16_f32 v77, v90, v91
v_mfma_f32_16x16x32_bf16 a[236:239], v[212:215], v[156:159], a[236:239]
v_cvt_pk_bf16_f32 v78, v92, v93
v_cvt_pk_bf16_f32 v79, v94, v95
v_mfma_f32_16x16x32_bf16 a[240:243], v[216:219], v[144:147], a[240:243]
buffer_load_dwordx4 v231, s[44:47], s88 offen offset:0 nt lds
v_mfma_f32_16x16x32_bf16 a[244:247], v[216:219], v[148:151], a[244:247]
ds_read_b128 v[212:215], v224 offset:9216
v_cvt_pk_bf16_f32 v108, v120, v121
v_mfma_f32_16x16x32_bf16 a[248:251], v[216:219], v[152:155], a[248:251]
v_cvt_pk_bf16_f32 v109, v122, v123
v_cvt_pk_bf16_f32 v110, v124, v125
v_mfma_f32_16x16x32_bf16 a[252:255], v[216:219], v[156:159], a[252:255]
LST14:
buffer_load_dwordx4 v231, s[44:47], s89 offen offset:1024 nt lds
s_waitcnt lgkmcnt(2)
v_mfma_f32_16x16x32_bf16 v[128:131], v[192:195], v[0:3], 0
ds_read_b128 v[216:219], v224 offset:1536
v_cvt_pk_bf16_f32 v111, v126, v127
v_mfma_f32_16x16x32_bf16 v[136:139], v[192:195], v[32:35], 0
v_accvgpr_read_b32 v160, a128
v_accvgpr_read_b32 v161, a129
v_mfma_f32_16x16x32_bf16 v[144:147], v[192:195], v[64:67], 0
v_accvgpr_read_b32 v162, a130
v_accvgpr_read_b32 v163, a131
v_mfma_f32_16x16x32_bf16 v[152:155], v[192:195], v[96:99], 0
buffer_load_dwordx4 v231, s[44:47], s90 offen offset:2048 nt lds
v_mfma_f32_16x16x32_bf16 v[132:135], v[196:199], v[0:3], 0
ds_read_b128 v[192:195], v224 offset:9728
v_cvt_pk_bf16_f32 v16, v160, v161
v_mfma_f32_16x16x32_bf16 v[140:143], v[196:199], v[32:35], 0
v_cvt_pk_bf16_f32 v17, v162, v163
v_accvgpr_read_b32 v164, a144
v_mfma_f32_16x16x32_bf16 v[148:151], v[196:199], v[64:67], 0
v_accvgpr_read_b32 v165, a145
v_accvgpr_read_b32 v166, a146
v_mfma_f32_16x16x32_bf16 v[156:159], v[196:199], v[96:99], 0
buffer_load_dwordx4 v231, s[44:47], s91 offen offset:3072 nt lds
v_mfma_f32_16x16x32_bf16 v[128:131], v[200:203], v[4:7], v[128:131]
s_add_u32 s44, s44, 0x4000
s_addc_u32 s45, s45, 0
v_mfma_f32_16x16x32_bf16 v[136:139], v[200:203], v[36:39], v[136:139]
s_add_u32 m0, s60, 0x8000
ds_read_b128 v[196:199], v224 offset:2048
v_accvgpr_read_b32 v167, a147
v_mfma_f32_16x16x32_bf16 v[144:147], v[200:203], v[68:71], v[144:147]
v_cvt_pk_bf16_f32 v18, v164, v165
v_cvt_pk_bf16_f32 v19, v166, v167
v_mfma_f32_16x16x32_bf16 v[152:155], v[200:203], v[100:103], v[152:155]
buffer_load_dwordx4 v232, s[48:51], s84 offen offset:0 nt lds
v_mfma_f32_16x16x32_bf16 v[132:135], v[204:207], v[4:7], v[132:135]
ds_read_b128 v[200:203], v224 offset:10240
v_accvgpr_read_b32 v172, a132
v_mfma_f32_16x16x32_bf16 v[140:143], v[204:207], v[36:39], v[140:143]
v_accvgpr_read_b32 v173, a133
v_accvgpr_read_b32 v174, a134
v_mfma_f32_16x16x32_bf16 v[148:151], v[204:207], v[68:71], v[148:151]
buffer_load_dwordx4 v232, s[48:51], s85 offen offset:1024 nt lds
v_mfma_f32_16x16x32_bf16 v[156:159], v[204:207], v[100:103], v[156:159]
v_accvgpr_read_b32 v175, a135
v_cvt_pk_bf16_f32 v48, v172, v173
s_waitcnt lgkmcnt(2)
v_mfma_f32_16x16x32_bf16 v[128:131], v[208:211], v[8:11], v[128:131]
ds_read_b128 v[204:207], v224 offset:2560
v_cvt_pk_bf16_f32 v49, v174, v175
v_mfma_f32_16x16x32_bf16 v[136:139], v[208:211], v[40:43], v[136:139]
v_accvgpr_read_b32 v160, a148
v_accvgpr_read_b32 v161, a149
v_mfma_f32_16x16x32_bf16 v[144:147], v[208:211], v[72:75], v[144:147]
buffer_load_dwordx4 v232, s[48:51], s86 offen offset:2048 nt lds
v_mfma_f32_16x16x32_bf16 v[152:155], v[208:211], v[104:107], v[152:155]
v_accvgpr_read_b32 v162, a150
v_accvgpr_read_b32 v163, a151
v_mfma_f32_16x16x32_bf16 v[132:135], v[212:215], v[8:11], v[132:135]
v_cvt_pk_bf16_f32 v50, v160, v161
v_cvt_pk_bf16_f32 v51, v162, v163
v_mfma_f32_16x16x32_bf16 v[140:143], v[212:215], v[40:43], v[140:143]
v_accvgpr_read_b32 v168, a136
v_accvgpr_read_b32 v169, a137
v_mfma_f32_16x16x32_bf16 v[148:151], v[212:215], v[72:75], v[148:151]
buffer_load_dwordx4 v232, s[48:51], s87 offen offset:3072 nt lds
v_mfma_f32_16x16x32_bf16 v[156:159], v[212:215], v[104:107], v[156:159]
v_accvgpr_read_b32 v170, a138
v_accvgpr_read_b32 v171, a139
v_mfma_f32_16x16x32_bf16 v[128:131], v[216:219], v[12:15], v[128:131]
s_add_u32 m0, s60, 0xa000
v_cvt_pk_bf16_f32 v80, v168, v169
v_cvt_pk_bf16_f32 v81, v170, v171
v_mfma_f32_16x16x32_bf16 v[136:139], v[216:219], v[44:47], v[136:139]
v_accvgpr_read_b32 v172, a152
v_accvgpr_read_b32 v173, a153
v_mfma_f32_16x16x32_bf16 v[144:147], v[216:219], v[76:79], v[144:147]
buffer_load_dwordx4 v232, s[48:51], s88 offen offset:0 nt lds
v_mfma_f32_16x16x32_bf16 v[152:155], v[216:219], v[108:111], v[152:155]
v_accvgpr_read_b32 v174, a154
v_accvgpr_read_b32 v175, a155
v_mfma_f32_16x16x32_bf16 v[132:135], v[192:195], v[12:15], v[132:135]
v_cvt_pk_bf16_f32 v82, v172, v173
v_cvt_pk_bf16_f32 v83, v174, v175
v_mfma_f32_16x16x32_bf16 v[140:143], v[192:195], v[44:47], v[140:143]
buffer_load_dwordx4 v232, s[48:51], s89 offen offset:1024 nt lds
v_mfma_f32_16x16x32_bf16 v[148:151], v[192:195], v[76:79], v[148:151]
v_accvgpr_read_b32 v164, a140
v_accvgpr_read_b32 v165, a141
v_mfma_f32_16x16x32_bf16 v[156:159], v[192:195], v[108:111], v[156:159]
v_accvgpr_read_b32 v166, a142
v_accvgpr_read_b32 v167, a143
s_waitcnt lgkmcnt(0)
v_mfma_f32_16x16x32_bf16 v[128:131], v[196:199], v[16:19], v[128:131]
v_cvt_pk_bf16_f32 v112, v164, v165
v_cvt_pk_bf16_f32 v113, v166, v167
v_mfma_f32_16x16x32_bf16 v[136:139], v[196:199], v[48:51], v[136:139]
v_accvgpr_read_b32 v168, a156
v_accvgpr_read_b32 v169, a157
v_accvgpr_read_b32 v170, a158
v_accvgpr_read_b32 v171, a159
v_cvt_pk_bf16_f32 v114, v168, v169
v_cvt_pk_bf16_f32 v115, v170, v171
v_mfma_f32_16x16x32_bf16 v[144:147], v[196:199], v[80:83], v[144:147]
ds_read_b128 v[208:211], v224 offset:10752
ds_read_b128 v[212:215], v224 offset:3072
v_mfma_f32_16x16x32_bf16 v[152:155], v[196:199], v[112:115], v[152:155]
buffer_load_dwordx4 v232, s[48:51], s90 offen offset:2048 nt lds
v_mfma_f32_16x16x32_bf16 v[132:135], v[200:203], v[16:19], v[132:135]
ds_read_b128 v[216:219], v224 offset:11264
ds_read_b128 v[192:195], v224 offset:3584
ds_read_b128 v[196:199], v224 offset:11776
v_mfma_f32_16x16x32_bf16 v[140:143], v[200:203], v[48:51], v[140:143]
v_accvgpr_read_b32 v160, a160
v_accvgpr_read_b32 v161, a161
v_mfma_f32_16x16x32_bf16 v[148:151], v[200:203], v[80:83], v[148:151]
v_accvgpr_read_b32 v162, a162
v_accvgpr_read_b32 v163, a163
v_cvt_pk_bf16_f32 v20, v160, v161
v_cvt_pk_bf16_f32 v21, v162, v163
v_accvgpr_read_b32 v164, a176
v_accvgpr_read_b32 v165, a177
v_accvgpr_read_b32 v166, a178
v_accvgpr_read_b32 v167, a179
v_cvt_pk_bf16_f32 v22, v164, v165
v_cvt_pk_bf16_f32 v23, v166, v167
v_mfma_f32_16x16x32_bf16 v[156:159], v[200:203], v[112:115], v[156:159]
v_accvgpr_read_b32 v172, a164
v_accvgpr_read_b32 v173, a165
v_accvgpr_read_b32 v174, a166
v_accvgpr_read_b32 v175, a167
v_cvt_pk_bf16_f32 v52, v172, v173
v_cvt_pk_bf16_f32 v53, v174, v175
v_accvgpr_read_b32 v160, a180
v_accvgpr_read_b32 v161, a181
v_accvgpr_read_b32 v162, a182
v_accvgpr_read_b32 v163, a183
v_cvt_pk_bf16_f32 v54, v160, v161
v_cvt_pk_bf16_f32 v55, v162, v163
buffer_load_dwordx4 v232, s[48:51], s91 offen offset:3072 nt lds
v_mfma_f32_16x16x32_bf16 v[128:131], v[204:207], v[20:23], v[128:131]
v_accvgpr_read_b32 v168, a168
v_accvgpr_read_b32 v169, a169
v_accvgpr_read_b32 v170, a170
v_accvgpr_read_b32 v171, a171
v_cvt_pk_bf16_f32 v84, v168, v169
v_cvt_pk_bf16_f32 v85, v170, v171
v_accvgpr_read_b32 v172, a184
v_accvgpr_read_b32 v173, a185
v_accvgpr_read_b32 v174, a186
v_accvgpr_read_b32 v175, a187
v_cvt_pk_bf16_f32 v86, v172, v173
v_cvt_pk_bf16_f32 v87, v174, v175
v_mfma_f32_16x16x32_bf16 v[136:139], v[204:207], v[52:55], v[136:139]
s_waitcnt vmcnt(24)
s_waitcnt lgkmcnt(0)
v_accvgpr_read_b32 v164, a172
v_accvgpr_read_b32 v165, a173
v_accvgpr_read_b32 v166, a174
v_accvgpr_read_b32 v167, a175
v_cvt_pk_bf16_f32 v116, v164, v165
v_cvt_pk_bf16_f32 v117, v166, v167
v_accvgpr_read_b32 v168, a188
v_accvgpr_read_b32 v169, a189
v_accvgpr_read_b32 v170, a190
LH3:
s_cbranch_execz LHB0
v_accvgpr_read_b32 v171, a191
v_cvt_pk_bf16_f32 v118, v168, v169
v_cvt_pk_bf16_f32 v119, v170, v171
v_mfma_f32_16x16x32_bf16 v[144:147], v[204:207], v[84:87], v[144:147]
s_barrier
v_accvgpr_read_b32 v160, a192
v_mfma_f32_16x16x32_bf16 v[152:155], v[204:207], v[116:119], v[152:155]
s_add_u32 s48, s48, 0x4000
s_addc_u32 s49, s49, 0
s_add_u32 m0, s60, 0x10000
v_mfma_f32_16x16x32_bf16 v[132:135], v[208:211], v[20:23], v[132:135]
v_accvgpr_read_b32 v161, a193
v_accvgpr_read_b32 v162, a194
v_mfma_f32_16x16x32_bf16 v[140:143], v[208:211], v[52:55], v[140:143]
v_accvgpr_read_b32 v163, a195
v_cvt_pk_bf16_f32 v24, v160, v161
v_mfma_f32_16x16x32_bf16 v[148:151], v[208:211], v[84:87], v[148:151]
v_cvt_pk_bf16_f32 v25, v162, v163
v_accvgpr_read_b32 v164, a208
v_accvgpr_read_b32 v165, a209
v_accvgpr_read_b32 v166, a210
v_accvgpr_read_b32 v167, a211
v_cvt_pk_bf16_f32 v26, v164, v165
v_cvt_pk_bf16_f32 v27, v166, v167
v_mfma_f32_16x16x32_bf16 v[156:159], v[208:211], v[116:119], v[156:159]
v_accvgpr_read_b32 v172, a196
v_accvgpr_read_b32 v173, a197
v_accvgpr_read_b32 v174, a198
v_accvgpr_read_b32 v175, a199
v_cvt_pk_bf16_f32 v56, v172, v173
v_cvt_pk_bf16_f32 v57, v174, v175
v_accvgpr_read_b32 v160, a212
v_accvgpr_read_b32 v161, a213
v_accvgpr_read_b32 v162, a214
v_accvgpr_read_b32 v163, a215
v_cvt_pk_bf16_f32 v58, v160, v161
v_cvt_pk_bf16_f32 v59, v162, v163
buffer_load_dwordx4 v231, s[44:47], s84 offen offset:0 nt lds
v_mfma_f32_16x16x32_bf16 v[128:131], v[212:215], v[24:27], v[128:131]
v_accvgpr_read_b32 v168, a200
v_accvgpr_read_b32 v169, a201
v_accvgpr_read_b32 v170, a202
v_accvgpr_read_b32 v171, a203
v_cvt_pk_bf16_f32 v88, v168, v169
v_cvt_pk_bf16_f32 v89, v170, v171
v_accvgpr_read_b32 v172, a216
v_accvgpr_read_b32 v173, a217
v_accvgpr_read_b32 v174, a218
v_accvgpr_read_b32 v175, a219
v_cvt_pk_bf16_f32 v90, v172, v173
v_cvt_pk_bf16_f32 v91, v174, v175
v_mfma_f32_16x16x32_bf16 v[136:139], v[212:215], v[56:59], v[136:139]
v_accvgpr_read_b32 v164, a204
v_accvgpr_read_b32 v165, a205
v_accvgpr_read_b32 v166, a206
v_accvgpr_read_b32 v167, a207
v_cvt_pk_bf16_f32 v120, v164, v165
v_cvt_pk_bf16_f32 v121, v166, v167
v_accvgpr_read_b32 v168, a220
v_accvgpr_read_b32 v169, a221
v_accvgpr_read_b32 v170, a222
v_accvgpr_read_b32 v171, a223
v_cvt_pk_bf16_f32 v122, v168, v169
v_cvt_pk_bf16_f32 v123, v170, v171
v_mfma_f32_16x16x32_bf16 v[144:147], v[212:215], v[88:91], v[144:147]
v_accvgpr_read_b32 v160, a224
v_accvgpr_read_b32 v161, a225
v_mfma_f32_16x16x32_bf16 v[152:155], v[212:215], v[120:123], v[152:155]
v_accvgpr_read_b32 v162, a226
v_accvgpr_read_b32 v163, a227
v_mfma_f32_16x16x32_bf16 v[132:135], v[216:219], v[24:27], v[132:135]
v_cvt_pk_bf16_f32 v28, v160, v161
v_cvt_pk_bf16_f32 v29, v162, v163
v_mfma_f32_16x16x32_bf16 v[140:143], v[216:219], v[56:59], v[140:143]
v_accvgpr_read_b32 v164, a240
v_accvgpr_read_b32 v165, a241
v_mfma_f32_16x16x32_bf16 v[148:151], v[216:219], v[88:91], v[148:151]
v_accvgpr_read_b32 v166, a242
v_accvgpr_read_b32 v167, a243
v_cvt_pk_bf16_f32 v30, v164, v165
v_cvt_pk_bf16_f32 v31, v166, v167
buffer_load_dwordx4 v231, s[44:47], s85 offen offset:1024 nt lds
v_mfma_f32_16x16x32_bf16 v[156:159], v[216:219], v[120:123], v[156:159]
ds_read_b128 v[200:203], v224 offset:32768
v_accvgpr_read_b32 v172, a228
v_accvgpr_read_b32 v173, a229
v_accvgpr_read_b32 v174, a230
v_accvgpr_read_b32 v175, a231
v_cvt_pk_bf16_f32 v60, v172, v173
v_cvt_pk_bf16_f32 v61, v174, v175
v_accvgpr_read_b32 v160, a244
v_accvgpr_read_b32 v161, a245
v_accvgpr_read_b32 v162, a246
v_accvgpr_read_b32 v163, a247
v_cvt_pk_bf16_f32 v62, v160, v161
v_cvt_pk_bf16_f32 v63, v162, v163
v_mfma_f32_16x16x32_bf16 v[128:131], v[192:195], v[28:31], v[128:131]
v_accvgpr_read_b32 v168, a232
v_accvgpr_read_b32 v169, a233
v_accvgpr_read_b32 v170, a234
v_accvgpr_read_b32 v171, a235
v_cvt_pk_bf16_f32 v92, v168, v169
v_cvt_pk_bf16_f32 v93, v170, v171
v_accvgpr_read_b32 v172, a248
v_accvgpr_read_b32 v173, a249
v_accvgpr_read_b32 v174, a250
v_accvgpr_read_b32 v175, a251
v_cvt_pk_bf16_f32 v94, v172, v173
v_cvt_pk_bf16_f32 v95, v174, v175
v_mfma_f32_16x16x32_bf16 v[136:139], v[192:195], v[60:63], v[136:139]
v_accvgpr_read_b32 v164, a236
v_accvgpr_read_b32 v165, a237
v_accvgpr_read_b32 v166, a238
v_accvgpr_read_b32 v167, a239
v_cvt_pk_bf16_f32 v124, v164, v165
v_cvt_pk_bf16_f32 v125, v166, v167
v_accvgpr_read_b32 v168, a252
v_accvgpr_read_b32 v169, a253
v_accvgpr_read_b32 v170, a254
v_accvgpr_read_b32 v171, a255
v_cvt_pk_bf16_f32 v126, v168, v169
v_cvt_pk_bf16_f32 v127, v170, v171
v_mfma_f32_16x16x32_bf16 v[144:147], v[192:195], v[92:95], v[144:147]
ds_read_b128 v[204:207], v224 offset:40960
ds_read_b128 v[208:211], v224 offset:33280
v_mfma_f32_16x16x32_bf16 v[152:155], v[192:195], v[124:127], v[152:155]
ds_read_b128 v[212:215], v224 offset:41472
ds_read_b128 v[216:219], v224 offset:33792
v_mfma_f32_16x16x32_bf16 v[132:135], v[196:199], v[28:31], v[132:135]
buffer_load_dwordx4 v231, s[44:47], s86 offen offset:2048 nt lds
v_mfma_f32_16x16x32_bf16 v[140:143], v[196:199], v[60:63], v[140:143]
ds_read_b128 v[192:195], v224 offset:41984
v_mfma_f32_16x16x32_bf16 v[148:151], v[196:199], v[92:95], v[148:151]
v_mfma_f32_16x16x32_bf16 v[156:159], v[196:199], v[124:127], v[156:159]
LST15:
s_waitcnt lgkmcnt(5)
v_mfma_f32_16x16x32_bf16 v[160:163], v[200:203], v[0:3], 0
ds_read_b128 v[196:199], v224 offset:34304
v_mfma_f32_16x16x32_bf16 v[168:171], v[200:203], v[32:35], 0
v_mfma_f32_16x16x32_bf16 v[176:179], v[200:203], v[64:67], 0
v_max3_f32 v247, v128, v129, v130
v_max3_f32 v248, v136, v137, v138
v_mfma_f32_16x16x32_bf16 v[184:187], v[200:203], v[96:99], 0
buffer_load_dwordx4 v231, s[44:47], s87 offen offset:3072 nt lds
s_waitcnt lgkmcnt(5)
v_mfma_f32_16x16x32_bf16 v[164:167], v[204:207], v[0:3], 0
ds_read_b128 v[200:203], v224 offset:42496
v_max3_f32 v249, v144, v145, v146
v_mfma_f32_16x16x32_bf16 v[172:175], v[204:207], v[32:35], 0
v_max3_f32 v250, v152, v153, v154
v_max3_f32 v247, v247, v131, v132
v_mfma_f32_16x16x32_bf16 v[180:183], v[204:207], v[64:67], 0
v_max3_f32 v248, v248, v139, v140
v_max3_f32 v249, v249, v147, v148
v_mfma_f32_16x16x32_bf16 v[188:191], v[204:207], v[96:99], 0
v_max3_f32 v250, v250, v155, v156
v_max3_f32 v247, v247, v133, v134
s_waitcnt lgkmcnt(2)
v_mfma_f32_16x16x32_bf16 v[160:163], v[208:211], v[4:7], v[160:163]
ds_read_b128 v[204:207], v224 offset:34816
v_max3_f32 v248, v248, v141, v142
v_mfma_f32_16x16x32_bf16 v[168:171], v[208:211], v[36:39], v[168:171]
s_add_u32 m0, s60, 0x12000
v_max3_f32 v249, v249, v149, v150
v_max3_f32 v250, v250, v157, v158
v_mfma_f32_16x16x32_bf16 v[176:179], v[208:211], v[68:71], v[176:179]
v_max_f32 v247, v247, v135
v_max_f32 v248, v248, v143
v_mfma_f32_16x16x32_bf16 v[184:187], v[208:211], v[100:103], v[184:187]
buffer_load_dwordx4 v231, s[44:47], s88 offen offset:0 nt lds
v_mfma_f32_16x16x32_bf16 v[164:167], v[212:215], v[4:7], v[164:167]
ds_read_b128 v[208:211], v224 offset:43008
v_max_f32 v249, v249, v151
v_mfma_f32_16x16x32_bf16 v[172:175], v[212:215], v[36:39], v[172:175]
v_max_f32 v250, v250, v159
v_permlane32_swap_b32 v247, v249
v_mfma_f32_16x16x32_bf16 v[180:183], v[212:215], v[68:71], v[180:183]
v_permlane32_swap_b32 v248, v250
v_max_f32 v247, v247, v249
v_mfma_f32_16x16x32_bf16 v[188:191], v[212:215], v[100:103], v[188:191]
v_max_f32 v248, v248, v250
s_nop 1
v_permlane16_swap_b32 v247, v248
v_mfma_f32_16x16x32_bf16 v[160:163], v[216:219], v[8:11], v[160:163]
ds_read_b128 v[212:215], v224 offset:35328
v_max_f32 v237, v247, v248
v_mfma_f32_16x16x32_bf16 v[168:171], v[216:219], v[40:43], v[168:171]
v_mul_f32 v239, s62, v237
v_mul_f32 v240, s62, v237
v_mfma_f32_16x16x32_bf16 v[176:179], v[216:219], v[72:75], v[176:179]
buffer_load_dwordx4 v231, s[44:47], s89 offen offset:1024 nt lds
v_mfma_f32_16x16x32_bf16 v[184:187], v[216:219], v[104:107], v[184:187]
v_add_f32 v238, s68, v237
v_permlane16_swap_b32 v239, v240
v_mfma_f32_16x16x32_bf16 v[164:167], v[192:195], v[8:11], v[164:167]
ds_read_b128 v[216:219], v224 offset:43520
v_mov_b32 v241, v239
v_mfma_f32_16x16x32_bf16 v[172:175], v[192:195], v[40:43], v[172:175]
v_mov_b32 v242, v240
v_permlane32_swap_b32 v239, v241
v_mfma_f32_16x16x32_bf16 v[180:183], v[192:195], v[72:75], v[180:183]
v_permlane32_swap_b32 v240, v242
v_mov_b32 v243, 0
v_mfma_f32_16x16x32_bf16 v[188:191], v[192:195], v[104:107], v[188:191]
v_mov_b32 v244, 0
v_mov_b32 v245, 0
s_waitcnt lgkmcnt(2)
v_mfma_f32_16x16x32_bf16 v[160:163], v[196:199], v[12:15], v[160:163]
ds_read_b128 v[192:195], v224 offset:35840
v_mov_b32 v246, 0
v_mfma_f32_16x16x32_bf16 v[168:171], v[196:199], v[44:47], v[168:171]
v_fma_f32 v128, v128, s62, -v239
v_fma_f32 v129, v129, s62, -v239
v_mfma_f32_16x16x32_bf16 v[176:179], v[196:199], v[76:79], v[176:179]
buffer_load_dwordx4 v231, s[44:47], s90 offen offset:2048 nt lds
v_mfma_f32_16x16x32_bf16 v[184:187], v[196:199], v[108:111], v[184:187]
v_exp_f32 v128, v128
v_mfma_f32_16x16x32_bf16 v[164:167], v[200:203], v[12:15], v[164:167]
ds_read_b128 v[196:199], v224 offset:44032
v_exp_f32 v129, v129
v_mfma_f32_16x16x32_bf16 v[172:175], v[200:203], v[44:47], v[172:175]
v_fma_f32 v130, v130, s62, -v239
v_fma_f32 v131, v131, s62, -v239
v_mfma_f32_16x16x32_bf16 v[180:183], v[200:203], v[76:79], v[180:183]
v_exp_f32 v130, v130
v_mfma_f32_16x16x32_bf16 v[188:191], v[200:203], v[108:111], v[188:191]
v_exp_f32 v131, v131
v_mfma_f32_16x16x32_bf16 v[160:163], v[204:207], v[16:19], v[160:163]
ds_read_b128 v[200:203], v224 offset:36352
v_add_f32 v243, v243, v128
v_mfma_f32_16x16x32_bf16 v[168:171], v[204:207], v[48:51], v[168:171]
buffer_load_dwordx4 v231, s[44:47], s91 offen offset:3072 nt lds
v_mfma_f32_16x16x32_bf16 v[176:179], v[204:207], v[80:83], v[176:179]
s_add_u32 s44, s44, 0x4000
s_addc_u32 s45, s45, 0
v_mfma_f32_16x16x32_bf16 v[184:187], v[204:207], v[112:115], v[184:187]
v_add_f32 v243, v243, v129
v_cvt_pk_bf16_f32 v128, v128, v129
v_mfma_f32_16x16x32_bf16 v[164:167], v[208:211], v[16:19], v[164:167]
ds_read_b128 v[204:207], v224 offset:44544
v_fma_f32 v132, v132, s62, -v239
v_mfma_f32_16x16x32_bf16 v[172:175], v[208:211], v[48:51], v[172:175]
v_fma_f32 v133, v133, s62, -v239
v_mfma_f32_16x16x32_bf16 v[180:183], v[208:211], v[80:83], v[180:183]
v_exp_f32 v132, v132
v_mfma_f32_16x16x32_bf16 v[188:191], v[208:211], v[112:115], v[188:191]
v_exp_f32 v133, v133
s_waitcnt lgkmcnt(2)
v_mfma_f32_16x16x32_bf16 v[160:163], v[212:215], v[20:23], v[160:163]
v_add_f32 v243, v243, v130
v_add_f32 v243, v243, v131
v_mfma_f32_16x16x32_bf16 v[168:171], v[212:215], v[52:55], v[168:171]
s_waitcnt vmcnt(24)
s_waitcnt lgkmcnt(0)
v_cvt_pk_bf16_f32 v129, v130, v131
v_mfma_f32_16x16x32_bf16 v[176:179], v[212:215], v[84:87], v[176:179]
s_barrier
ds_read_b64_tr_b16 v[208:209], v225 offset:0
v_mfma_f32_16x16x32_bf16 v[184:187], v[212:215], v[116:119], v[184:187]
s_add_u32 m0, s60, 0x18000
ds_read_b64_tr_b16 v[210:211], v225 offset:8192
v_fma_f32 v134, v134, s62, -v239
v_mfma_f32_16x16x32_bf16 v[164:167], v[216:219], v[20:23], v[164:167]
ds_read_b64_tr_b16 v[212:213], v226 offset:0
v_fma_f32 v135, v135, s62, -v239
v_mfma_f32_16x16x32_bf16 v[172:175], v[216:219], v[52:55], v[172:175]
buffer_load_dwordx4 v232, s[48:51], s84 offen offset:0 nt lds
v_mfma_f32_16x16x32_bf16 v[180:183], v[216:219], v[84:87], v[180:183]
ds_read_b64_tr_b16 v[214:215], v226 offset:8192
v_exp_f32 v134, v134
v_mfma_f32_16x16x32_bf16 v[188:191], v[216:219], v[116:119], v[188:191]
v_exp_f32 v135, v135
v_mfma_f32_16x16x32_bf16 v[160:163], v[192:195], v[24:27], v[160:163]
v_add_f32 v243, v243, v132
v_add_f32 v243, v243, v133
v_mfma_f32_16x16x32_bf16 v[168:171], v[192:195], v[56:59], v[168:171]
v_cvt_pk_bf16_f32 v130, v132, v133
v_fma_f32 v136, v136, s62, -v240
v_mfma_f32_16x16x32_bf16 v[176:179], v[192:195], v[88:91], v[176:179]
v_fma_f32 v137, v137, s62, -v240
ds_read_b64_tr_b16 v[216:217], v225 offset:512
v_mfma_f32_16x16x32_bf16 v[184:187], v[192:195], v[120:123], v[184:187]
v_exp_f32 v136, v136
ds_read_b64_tr_b16 v[218:219], v225 offset:8704
v_mfma_f32_16x16x32_bf16 v[164:167], v[196:199], v[24:27], v[164:167]
buffer_load_dwordx4 v232, s[48:51], s85 offen offset:1024 nt lds
v_mfma_f32_16x16x32_bf16 v[172:175], v[196:199], v[56:59], v[172:175]
v_exp_f32 v137, v137
ds_read_b64_tr_b16 v[192:193], v226 offset:512
v_mfma_f32_16x16x32_bf16 v[180:183], v[196:199], v[88:91], v[180:183]
v_add_f32 v243, v243, v134
v_add_f32 v243, v243, v135
v_mfma_f32_16x16x32_bf16 v[188:191], v[196:199], v[120:123], v[188:191]
v_cvt_pk_bf16_f32 v131, v134, v135
v_fma_f32 v138, v138, s62, -v240
v_mfma_f32_16x16x32_bf16 v[160:163], v[200:203], v[28:31], v[160:163]
v_fma_f32 v139, v139, s62, -v240
ds_read_b64_tr_b16 v[194:195], v226 offset:8704
v_mfma_f32_16x16x32_bf16 v[168:171], v[200:203], v[60:63], v[168:171]
v_exp_f32 v138, v138
ds_read_b64_tr_b16 v[196:197], v225 offset:1024
v_mfma_f32_16x16x32_bf16 v[176:179], v[200:203], v[92:95], v[176:179]
v_exp_f32 v139, v139
ds_read_b64_tr_b16 v[198:199], v225 offset:9216
v_mfma_f32_16x16x32_bf16 v[184:187], v[200:203], v[124:127], v[184:187]
v_add_f32 v244, v244, v136
v_add_f32 v244, v244, v137
v_mfma_f32_16x16x32_bf16 v[164:167], v[204:207], v[28:31], v[164:167]
buffer_load_dwordx4 v232, s[48:51], s86 offen offset:2048 nt lds
v_mfma_f32_16x16x32_bf16 v[172:175], v[204:207], v[60:63], v[172:175]
v_cvt_pk_bf16_f32 v136, v136, v137
v_fma_f32 v140, v140, s62, -v240
v_mfma_f32_16x16x32_bf16 v[180:183], v[204:207], v[92:95], v[180:183]
v_fma_f32 v141, v141, s62, -v240
ds_read_b64_tr_b16 v[200:201], v226 offset:1024
v_mfma_f32_16x16x32_bf16 v[188:191], v[204:207], v[124:127], v[188:191]
LST16:
v_exp_f32 v140, v140
v_exp_f32 v141, v141
v_add_f32 v244, v244, v138
v_add_f32 v244, v244, v139
v_cvt_pk_bf16_f32 v137, v138, v139
v_fma_f32 v142, v142, s62, -v240
v_fma_f32 v143, v143, s62, -v240
v_exp_f32 v142, v142
v_exp_f32 v143, v143
v_add_f32 v244, v244, v140
v_add_f32 v244, v244, v141
v_cvt_pk_bf16_f32 v138, v140, v141
v_fma_f32 v144, v144, s62, -v241
v_fma_f32 v145, v145, s62, -v241
v_exp_f32 v144, v144
v_exp_f32 v145, v145
v_add_f32 v244, v244, v142
v_add_f32 v244, v244, v143
v_cvt_pk_bf16_f32 v139, v142, v143
s_waitcnt lgkmcnt(4)
v_mfma_f32_16x16x32_bf16 a[0:3], v[208:211], v[128:131], 0
v_fma_f32 v146, v146, s62, -v241
v_fma_f32 v147, v147, s62, -v241
v_exp_f32 v146, v146
v_exp_f32 v147, v147
v_add_f32 v245, v245, v144
v_add_f32 v245, v245, v145
v_cvt_pk_bf16_f32 v144, v144, v145
v_fma_f32 v148, v148, s62, -v241
v_fma_f32 v149, v149, s62, -v241
v_exp_f32 v148, v148
v_exp_f32 v149, v149
v_add_f32 v245, v245, v146
v_add_f32 v245, v245, v147
v_cvt_pk_bf16_f32 v145, v146, v147
v_fma_f32 v150, v150, s62, -v241
v_fma_f32 v151, v151, s62, -v241
v_exp_f32 v150, v150
v_exp_f32 v151, v151
v_add_f32 v245, v245, v148
v_add_f32 v245, v245, v149
v_cvt_pk_bf16_f32 v146, v148, v149
v_fma_f32 v152, v152, s62, -v242
v_fma_f32 v153, v153, s62, -v242
v_exp_f32 v152, v152
v_exp_f32 v153, v153
v_add_f32 v245, v245, v150
v_add_f32 v245, v245, v151
v_cvt_pk_bf16_f32 v147, v150, v151
v_mfma_f32_16x16x32_bf16 a[4:7], v[208:211], v[136:139], 0
v_fma_f32 v154, v154, s62, -v242
v_fma_f32 v155, v155, s62, -v242
v_exp_f32 v154, v154
v_exp_f32 v155, v155
v_add_f32 v246, v246, v152
v_add_f32 v246, v246, v153
v_cvt_pk_bf16_f32 v152, v152, v153
v_fma_f32 v156, v156, s62, -v242
v_fma_f32 v157, v157, s62, -v242
v_exp_f32 v156, v156
v_exp_f32 v157, v157
v_add_f32 v246, v246, v154
v_add_f32 v246, v246, v155
v_cvt_pk_bf16_f32 v153, v154, v155
v_fma_f32 v158, v158, s62, -v242
v_fma_f32 v159, v159, s62, -v242
v_exp_f32 v158, v158
v_exp_f32 v159, v159
v_add_f32 v246, v246, v156
v_add_f32 v246, v246, v157
v_cvt_pk_bf16_f32 v154, v156, v157
v_add_f32 v246, v246, v158
v_add_f32 v246, v246, v159
v_cvt_pk_bf16_f32 v155, v158, v159
v_mfma_f32_16x16x32_bf16 a[8:11], v[208:211], v[144:147], 0
ds_read_b64_tr_b16 v[202:203], v226 offset:9216
ds_read_b64_tr_b16 v[204:205], v225 offset:1536
v_mfma_f32_16x16x32_bf16 a[12:15], v[208:211], v[152:155], 0
buffer_load_dwordx4 v232, s[48:51], s87 offen offset:3072 nt lds
v_mfma_f32_16x16x32_bf16 a[16:19], v[212:215], v[128:131], 0
ds_read_b64_tr_b16 v[206:207], v225 offset:9728
ds_read_b64_tr_b16 v[208:209], v226 offset:1536
v_mfma_f32_16x16x32_bf16 a[20:23], v[212:215], v[136:139], 0
ds_read_b64_tr_b16 v[210:211], v226 offset:9728
v_max3_f32 v247, v160, v161, v162
v_mfma_f32_16x16x32_bf16 a[24:27], v[212:215], v[144:147], 0
v_max3_f32 v248, v168, v169, v170
v_max3_f32 v249, v176, v177, v178
v_mfma_f32_16x16x32_bf16 a[28:31], v[212:215], v[152:155], 0
v_max3_f32 v250, v184, v185, v186
v_max3_f32 v247, v247, v163, v164
v_mfma_f32_16x16x32_bf16 a[32:35], v[216:219], v[128:131], 0
ds_read_b64_tr_b16 v[212:213], v225 offset:2048
v_max3_f32 v248, v248, v171, v172
v_mfma_f32_16x16x32_bf16 a[36:39], v[216:219], v[136:139], 0
s_add_u32 m0, s60, 0x1a000
ds_read_b64_tr_b16 v[214:215], v225 offset:10240
v_max3_f32 v249, v249, v179, v180
v_mfma_f32_16x16x32_bf16 a[40:43], v[216:219], v[144:147], 0
v_max3_f32 v250, v250, v187, v188
v_max3_f32 v247, v247, v165, v166
v_mfma_f32_16x16x32_bf16 a[44:47], v[216:219], v[152:155], 0
buffer_load_dwordx4 v232, s[48:51], s88 offen offset:0 nt lds
s_waitcnt lgkmcnt(7)
v_mfma_f32_16x16x32_bf16 a[48:51], v[192:195], v[128:131], 0
ds_read_b64_tr_b16 v[216:217], v226 offset:2048
v_max3_f32 v248, v248, v173, v174
v_mfma_f32_16x16x32_bf16 a[52:55], v[192:195], v[136:139], 0
ds_read_b64_tr_b16 v[218:219], v226 offset:10240
v_max3_f32 v249, v249, v181, v182
v_mfma_f32_16x16x32_bf16 a[56:59], v[192:195], v[144:147], 0
v_max3_f32 v250, v250, v189, v190
v_max_f32 v247, v247, v167
v_mfma_f32_16x16x32_bf16 a[60:63], v[192:195], v[152:155], 0
v_max_f32 v248, v248, v175
v_max_f32 v249, v249, v183
v_mfma_f32_16x16x32_bf16 a[64:67], v[196:199], v[128:131], 0
ds_read_b64_tr_b16 v[192:193], v225 offset:2560
v_max_f32 v250, v250, v191
v_mfma_f32_16x16x32_bf16 a[68:71], v[196:199], v[136:139], 0
ds_read_b64_tr_b16 v[194:195], v225 offset:10752
v_permlane32_swap_b32 v247, v249
v_mfma_f32_16x16x32_bf16 a[72:75], v[196:199], v[144:147], 0
buffer_load_dwordx4 v232, s[48:51], s89 offen offset:1024 nt lds
v_mfma_f32_16x16x32_bf16 a[76:79], v[196:199], v[152:155], 0
v_permlane32_swap_b32 v248, v250
v_max_f32 v247, v247, v249
s_waitcnt lgkmcnt(4)
v_mfma_f32_16x16x32_bf16 a[80:83], v[200:203], v[128:131], 0
ds_read_b64_tr_b16 v[196:197], v226 offset:2560
v_max_f32 v248, v248, v250
v_mfma_f32_16x16x32_bf16 a[84:87], v[200:203], v[136:139], 0
ds_read_b64_tr_b16 v[198:199], v226 offset:10752
v_permlane16_swap_b32 v247, v248
v_mfma_f32_16x16x32_bf16 a[88:91], v[200:203], v[144:147], 0
v_max_f32 v247, v247, v248
v_cmp_gt_f32 vcc, v247, v238
v_mfma_f32_16x16x32_bf16 a[92:95], v[200:203], v[152:155], 0
s_cbranch_vccnz LR1
LRB1:
v_fma_f32 v160, v160, s62, -v239
v_mfma_f32_16x16x32_bf16 a[96:99], v[204:207], v[128:131], 0
ds_read_b64_tr_b16 v[200:201], v225 offset:3072
v_fma_f32 v161, v161, s62, -v239
v_mfma_f32_16x16x32_bf16 a[100:103], v[204:207], v[136:139], 0
ds_read_b64_tr_b16 v[202:203], v225 offset:11264
v_exp_f32 v160, v160
v_mfma_f32_16x16x32_bf16 a[104:107], v[204:207], v[144:147], 0
buffer_load_dwordx4 v232, s[48:51], s90 offen offset:2048 nt lds
v_mfma_f32_16x16x32_bf16 a[108:111], v[204:207], v[152:155], 0
v_exp_f32 v161, v161
v_mfma_f32_16x16x32_bf16 a[112:115], v[208:211], v[128:131], 0
ds_read_b64_tr_b16 v[204:205], v226 offset:3072
v_fma_f32 v162, v162, s62, -v239
v_mfma_f32_16x16x32_bf16 a[116:119], v[208:211], v[136:139], 0
ds_read_b64_tr_b16 v[206:207], v226 offset:11264
v_fma_f32 v163, v163, s62, -v239
v_mfma_f32_16x16x32_bf16 a[120:123], v[208:211], v[144:147], 0
v_exp_f32 v162, v162
v_mfma_f32_16x16x32_bf16 a[124:127], v[208:211], v[152:155], 0
v_exp_f32 v163, v163
v_mfma_f32_16x16x32_bf16 a[128:131], v[212:215], v[128:131], 0
ds_read_b64_tr_b16 v[208:209], v225 offset:3584
v_add_f32 v243, v243, v160
v_mfma_f32_16x16x32_bf16 a[132:135], v[212:215], v[136:139], 0
buffer_load_dwordx4 v232, s[48:51], s91 offen offset:3072 nt lds
v_mfma_f32_16x16x32_bf16 a[136:139], v[212:215], v[144:147], 0
ds_read_b64_tr_b16 v[210:211], v225 offset:11776
v_add_f32 v243, v243, v161
v_mfma_f32_16x16x32_bf16 a[140:143], v[212:215], v[152:155], 0
s_add_u32 s48, s48, 0x4000
s_addc_u32 s49, s49, 0
s_waitcnt lgkmcnt(4)
v_mfma_f32_16x16x32_bf16 a[144:147], v[216:219], v[128:131], 0
ds_read_b64_tr_b16 v[212:213], v226 offset:3584
v_cvt_pk_bf16_f32 v160, v160, v161
v_mfma_f32_16x16x32_bf16 a[148:151], v[216:219], v[136:139], 0
ds_read_b64_tr_b16 v[214:215], v226 offset:11776
v_fma_f32 v164, v164, s62, -v239
v_mfma_f32_16x16x32_bf16 a[152:155], v[216:219], v[144:147], 0
v_fma_f32 v165, v165, s62, -v239
v_mfma_f32_16x16x32_bf16 a[156:159], v[216:219], v[152:155], 0
v_exp_f32 v164, v164
v_mfma_f32_16x16x32_bf16 a[160:163], v[192:195], v[128:131], 0
v_exp_f32 v165, v165
v_mfma_f32_16x16x32_bf16 a[164:167], v[192:195], v[136:139], 0
s_waitcnt vmcnt(24)
s_waitcnt lgkmcnt(0)
v_add_f32 v243, v243, v162
v_mfma_f32_16x16x32_bf16 a[168:171], v[192:195], v[144:147], 0
s_barrier
v_add_u32 v224, 0x10000, v224
v_mfma_f32_16x16x32_bf16 a[172:175], v[192:195], v[152:155], 0
s_add_u32 m0, s60, 0x0
ds_read_b128 v[216:219], v224 offset:0
v_add_f32 v243, v243, v163
v_mfma_f32_16x16x32_bf16 a[176:179], v[196:199], v[128:131], 0
ds_read_b128 v[192:195], v224 offset:8192
v_cvt_pk_bf16_f32 v161, v162, v163
v_mfma_f32_16x16x32_bf16 a[180:183], v[196:199], v[136:139], 0
buffer_load_dwordx4 v231, s[44:47], s84 offen offset:0 nt lds
v_mfma_f32_16x16x32_bf16 a[184:187], v[196:199], v[144:147], 0
v_fma_f32 v166, v166, s62, -v239
v_fma_f32 v167, v167, s62, -v239
v_mfma_f32_16x16x32_bf16 a[188:191], v[196:199], v[152:155], 0
v_exp_f32 v166, v166
v_mfma_f32_16x16x32_bf16 a[192:195], v[200:203], v[128:131], 0
ds_read_b128 v[196:199], v224 offset:512
v_exp_f32 v167, v167
v_mfma_f32_16x16x32_bf16 a[196:199], v[200:203], v[136:139], 0
v_add_f32 v243, v243, v164
v_add_f32 v243, v243, v165
v_mfma_f32_16x16x32_bf16 a[200:203], v[200:203], v[144:147], 0
v_cvt_pk_bf16_f32 v162, v164, v165
v_fma_f32 v168, v168, s62, -v240
v_mfma_f32_16x16x32_bf16 a[204:207], v[200:203], v[152:155], 0
v_fma_f32 v169, v169, s62, -v240
v_mfma_f32_16x16x32_bf16 a[208:211], v[204:207], v[128:131], 0
buffer_load_dwordx4 v231, s[44:47], s85 offen offset:1024 nt lds
v_mfma_f32_16x16x32_bf16 a[212:215], v[204:207], v[136:139], 0
ds_read_b128 v[200:203], v224 offset:8704
v_exp_f32 v168, v168
v_mfma_f32_16x16x32_bf16 a[216:219], v[204:207], v[144:147], 0
v_exp_f32 v169, v169
v_mfma_f32_16x16x32_bf16 a[220:223], v[204:207], v[152:155], 0
v_add_f32 v243, v243, v166
v_add_f32 v243, v243, v167
v_mfma_f32_16x16x32_bf16 a[224:227], v[208:211], v[128:131], 0
ds_read_b128 v[204:207], v224 offset:1024
v_cvt_pk_bf16_f32 v163, v166, v167
v_mfma_f32_16x16x32_bf16 a[228:231], v[208:211], v[136:139], 0
v_fma_f32 v170, v170, s62, -v240
v_fma_f32 v171, v171, s62, -v240
v_mfma_f32_16x16x32_bf16 a[232:235], v[208:211], v[144:147], 0
v_exp_f32 v170, v170
v_mfma_f32_16x16x32_bf16 a[236:239], v[208:211], v[152:155], 0
v_exp_f32 v171, v171
v_mfma_f32_16x16x32_bf16 a[240:243], v[212:215], v[128:131], 0
buffer_load_dwordx4 v231, s[44:47], s86 offen offset:2048 nt lds
v_mfma_f32_16x16x32_bf16 a[244:247], v[212:215], v[136:139], 0
ds_read_b128 v[208:211], v224 offset:9216
v_add_f32 v244, v244, v168
v_mfma_f32_16x16x32_bf16 a[248:251], v[212:215], v[144:147], 0
v_add_f32 v244, v244, v169
v_cvt_pk_bf16_f32 v168, v168, v169
v_mfma_f32_16x16x32_bf16 a[252:255], v[212:215], v[152:155], 0
LST17:
s_cmp_lg_u32 s63, 0
s_cbranch_scc1 LS1
LSB1:
s_waitcnt lgkmcnt(2)
v_mfma_f32_16x16x32_bf16 v[128:131], v[216:219], v[0:3], 0
ds_read_b128 v[212:215], v224 offset:1536
s_mov_b64 s[36:37], %9
s_and_b32 s37, s37, 0xffff
s_mov_b32 s38, 0x6c80
s_mov_b64 s[40:41], %10
s_and_b32 s41, s41, 0xffff
s_mov_b32 s42, 0x200
s_lshl_b32 s74, %6, 8
s_lshl_b32 s73, %11, 10
s_add_u32 s73, s73, s74
v_fma_f32 v172, v172, s62, -v240
v_mfma_f32_16x16x32_bf16 v[136:139], v[216:219], v[32:35], 0
v_fma_f32 v173, v173, s62, -v240
v_mfma_f32_16x16x32_bf16 v[144:147], v[216:219], v[64:67], 0
v_exp_f32 v172, v172
v_mfma_f32_16x16x32_bf16 v[152:155], v[216:219], v[96:99], 0
buffer_load_dwordx4 v231, s[44:47], s87 offen offset:3072 nt lds
v_mfma_f32_16x16x32_bf16 v[132:135], v[192:195], v[0:3], 0
ds_read_b128 v[216:219], v224 offset:9728
v_exp_f32 v173, v173
v_mfma_f32_16x16x32_bf16 v[140:143], v[192:195], v[32:35], 0
v_add_f32 v244, v244, v170
v_add_f32 v244, v244, v171
v_mfma_f32_16x16x32_bf16 v[148:151], v[192:195], v[64:67], 0
LH4:
s_cbranch_execz LH8
v_cvt_pk_bf16_f32 v169, v170, v171
v_fma_f32 v174, v174, s62, -v240
v_mfma_f32_16x16x32_bf16 v[156:159], v[192:195], v[96:99], 0
v_fma_f32 v175, v175, s62, -v240
v_mfma_f32_16x16x32_bf16 v[128:131], v[196:199], v[4:7], v[128:131]
ds_read_b128 v[192:195], v224 offset:2048
v_exp_f32 v174, v174
v_mfma_f32_16x16x32_bf16 v[136:139], v[196:199], v[36:39], v[136:139]
s_add_u32 m0, s60, 0x2000
v_exp_f32 v175, v175
v_mfma_f32_16x16x32_bf16 v[144:147], v[196:199], v[68:71], v[144:147]
v_add_f32 v244, v244, v172
v_add_f32 v244, v244, v173
v_mfma_f32_16x16x32_bf16 v[152:155], v[196:199], v[100:103], v[152:155]
buffer_load_dwordx4 v231, s[44:47], s88 offen offset:0 nt lds
v_mfma_f32_16x16x32_bf16 v[132:135], v[200:203], v[4:7], v[132:135]
ds_read_b128 v[196:199], v224 offset:10240
v_cvt_pk_bf16_f32 v170, v172, v173
v_mfma_f32_16x16x32_bf16 v[140:143], v[200:203], v[36:39], v[140:143]
v_fma_f32 v176, v176, s62, -v241
v_fma_f32 v177, v177, s62, -v241
v_mfma_f32_16x16x32_bf16 v[148:151], v[200:203], v[68:71], v[148:151]
v_exp_f32 v176, v176
v_mfma_f32_16x16x32_bf16 v[156:159], v[200:203], v[100:103], v[156:159]
v_exp_f32 v177, v177
s_waitcnt lgkmcnt(2)
v_mfma_f32_16x16x32_bf16 v[128:131], v[204:207], v[8:11], v[128:131]
ds_read_b128 v[200:203], v224 offset:2560
v_add_f32 v244, v244, v174
v_mfma_f32_16x16x32_bf16 v[136:139], v[204:207], v[40:43], v[136:139]
v_add_f32 v244, v244, v175
v_cvt_pk_bf16_f32 v171, v174, v175
v_mfma_f32_16x16x32_bf16 v[144:147], v[204:207], v[72:75], v[144:147]
buffer_load_dwordx4 v231, s[44:47], s89 offen offset:1024 nt lds
v_mfma_f32_16x16x32_bf16 v[152:155], v[204:207], v[104:107], v[152:155]
v_fma_f32 v178, v178, s62, -v241
v_fma_f32 v179, v179, s62, -v241
v_mfma_f32_16x16x32_bf16 v[132:135], v[208:211], v[8:11], v[132:135]
ds_read_b128 v[204:207], v224 offset:10752
v_exp_f32 v178, v178
v_mfma_f32_16x16x32_bf16 v[140:143], v[208:211], v[40:43], v[140:143]
v_exp_f32 v179, v179
v_mfma_f32_16x16x32_bf16 v[148:151], v[208:211], v[72:75], v[148:151]
v_add_f32 v245, v245, v176
v_add_f32 v245, v245, v177
v_mfma_f32_16x16x32_bf16 v[156:159], v[208:211], v[104:107], v[156:159]
v_cvt_pk_bf16_f32 v176, v176, v177
v_fma_f32 v180, v180, s62, -v241
v_mfma_f32_16x16x32_bf16 v[128:131], v[212:215], v[12:15], v[128:131]
ds_read_b128 v[208:211], v224 offset:3072
v_fma_f32 v181, v181, s62, -v241
v_mfma_f32_16x16x32_bf16 v[136:139], v[212:215], v[44:47], v[136:139]
v_exp_f32 v180, v180
v_mfma_f32_16x16x32_bf16 v[144:147], v[212:215], v[76:79], v[144:147]
buffer_load_dwordx4 v231, s[44:47], s90 offen offset:2048 nt lds
v_mfma_f32_16x16x32_bf16 v[152:155], v[212:215], v[108:111], v[152:155]
v_exp_f32 v181, v181
v_mfma_f32_16x16x32_bf16 v[132:135], v[216:219], v[12:15], v[132:135]
ds_read_b128 v[212:215], v224 offset:11264
v_add_f32 v245, v245, v178
v_mfma_f32_16x16x32_bf16 v[140:143], v[216:219], v[44:47], v[140:143]
v_add_f32 v245, v245, v179
v_cvt_pk_bf16_f32 v177, v178, v179
v_mfma_f32_16x16x32_bf16 v[148:151], v[216:219], v[76:79], v[148:151]
v_fma_f32 v182, v182, s62, -v241
v_fma_f32 v183, v183, s62, -v241
v_mfma_f32_16x16x32_bf16 v[156:159], v[216:219], v[108:111], v[156:159]
v_exp_f32 v182, v182
s_waitcnt lgkmcnt(2)
v_mfma_f32_16x16x32_bf16 v[128:131], v[192:195], v[16:19], v[128:131]
ds_read_b128 v[216:219], v224 offset:3584
v_exp_f32 v183, v183
v_mfma_f32_16x16x32_bf16 v[136:139], v[192:195], v[48:51], v[136:139]
buffer_load_dwordx4 v231, s[44:47], s91 offen offset:3072 nt lds
v_mfma_f32_16x16x32_bf16 v[144:147], v[192:195], v[80:83], v[144:147]
s_add_u32 s44, s44, 0x4000
s_addc_u32 s45, s45, 0
v_mfma_f32_16x16x32_bf16 v[152:155], v[192:195], v[112:115], v[152:155]
v_add_f32 v245, v245, v180
v_add_f32 v245, v245, v181
v_mfma_f32_16x16x32_bf16 v[132:135], v[196:199], v[16:19], v[132:135]
ds_read_b128 v[192:195], v224 offset:11776
v_cvt_pk_bf16_f32 v178, v180, v181
v_mfma_f32_16x16x32_bf16 v[140:143], v[196:199], v[48:51], v[140:143]
v_fma_f32 v184, v184, s62, -v242
v_fma_f32 v185, v185, s62, -v242
v_mfma_f32_16x16x32_bf16 v[148:151], v[196:199], v[80:83], v[148:151]
v_exp_f32 v184, v184
v_mfma_f32_16x16x32_bf16 v[156:159], v[196:199], v[112:115], v[156:159]
v_exp_f32 v185, v185
v_mfma_f32_16x16x32_bf16 v[128:131], v[200:203], v[20:23], v[128:131]
v_add_f32 v245, v245, v182
v_add_f32 v245, v245, v183
v_mfma_f32_16x16x32_bf16 v[136:139], v[200:203], v[52:55], v[136:139]
s_waitcnt vmcnt(24)
s_waitcnt lgkmcnt(0)
v_cvt_pk_bf16_f32 v179, v182, v183
v_mfma_f32_16x16x32_bf16 v[144:147], v[200:203], v[84:87], v[144:147]
s_barrier
ds_read_b64_tr_b16 v[196:197], v225 offset:32768
v_mfma_f32_16x16x32_bf16 v[152:155], v[200:203], v[116:119], v[152:155]
s_add_u32 m0, s60, 0x20000
ds_read_b64_tr_b16 v[198:199], v225 offset:40960
v_fma_f32 v186, v186, s62, -v242
v_mfma_f32_16x16x32_bf16 v[132:135], v[204:207], v[20:23], v[132:135]
ds_read_b64_tr_b16 v[200:201], v226 offset:32768
v_fma_f32 v187, v187, s62, -v242
v_mfma_f32_16x16x32_bf16 v[140:143], v[204:207], v[52:55], v[140:143]
buffer_load_dwordx4 v232, s[48:51], s84 offen offset:0 nt lds
v_mfma_f32_16x16x32_bf16 v[148:151], v[204:207], v[84:87], v[148:151]
ds_read_b64_tr_b16 v[202:203], v226 offset:40960
v_exp_f32 v186, v186
v_mfma_f32_16x16x32_bf16 v[156:159], v[204:207], v[116:119], v[156:159]
v_exp_f32 v187, v187
v_mfma_f32_16x16x32_bf16 v[128:131], v[208:211], v[24:27], v[128:131]
ds_read_b64_tr_b16 v[204:205], v225 offset:33280
v_add_f32 v246, v246, v184
v_mfma_f32_16x16x32_bf16 v[136:139], v[208:211], v[56:59], v[136:139]
ds_read_b64_tr_b16 v[206:207], v225 offset:41472
v_add_f32 v246, v246, v185
v_mfma_f32_16x16x32_bf16 v[144:147], v[208:211], v[88:91], v[144:147]
v_cvt_pk_bf16_f32 v184, v184, v185
v_fma_f32 v188, v188, s62, -v242
v_mfma_f32_16x16x32_bf16 v[152:155], v[208:211], v[120:123], v[152:155]
v_fma_f32 v189, v189, s62, -v242
v_mfma_f32_16x16x32_bf16 v[132:135], v[212:215], v[24:27], v[132:135]
buffer_load_dwordx4 v232, s[48:51], s85 offen offset:1024 nt lds
v_mfma_f32_16x16x32_bf16 v[140:143], v[212:215], v[56:59], v[140:143]
v_exp_f32 v188, v188
ds_read_b64_tr_b16 v[208:209], v226 offset:33280
v_mfma_f32_16x16x32_bf16 v[148:151], v[212:215], v[88:91], v[148:151]
v_exp_f32 v189, v189
ds_read_b64_tr_b16 v[210:211], v226 offset:41472
v_mfma_f32_16x16x32_bf16 v[156:159], v[212:215], v[120:123], v[156:159]
v_add_f32 v246, v246, v186
v_add_f32 v246, v246, v187
v_mfma_f32_16x16x32_bf16 v[128:131], v[216:219], v[28:31], v[128:131]
v_cvt_pk_bf16_f32 v185, v186, v187
v_fma_f32 v190, v190, s62, -v242
v_mfma_f32_16x16x32_bf16 v[136:139], v[216:219], v[60:63], v[136:139]
v_fma_f32 v191, v191, s62, -v242
ds_read_b64_tr_b16 v[212:213], v225 offset:33792
v_mfma_f32_16x16x32_bf16 v[144:147], v[216:219], v[92:95], v[144:147]
v_exp_f32 v190, v190
ds_read_b64_tr_b16 v[214:215], v225 offset:41984
v_mfma_f32_16x16x32_bf16 v[152:155], v[216:219], v[124:127], v[152:155]
v_exp_f32 v191, v191
v_mfma_f32_16x16x32_bf16 v[132:135], v[192:195], v[28:31], v[132:135]
buffer_load_dwordx4 v232, s[48:51], s86 offen offset:2048 nt lds
v_mfma_f32_16x16x32_bf16 v[140:143], v[192:195], v[60:63], v[140:143]
v_add_f32 v246, v246, v188
v_add_f32 v246, v246, v189
v_mfma_f32_16x16x32_bf16 v[148:151], v[192:195], v[92:95], v[148:151]
v_cvt_pk_bf16_f32 v186, v188, v189
v_add_f32 v246, v246, v190
v_mfma_f32_16x16x32_bf16 v[156:159], v[192:195], v[124:127], v[156:159]
LST18:
v_add_f32 v246, v246, v191
v_cvt_pk_bf16_f32 v187, v190, v191
s_waitcnt lgkmcnt(3)
v_mfma_f32_16x16x32_bf16 a[0:3], v[196:199], v[160:163], a[0:3]
ds_read_b64_tr_b16 v[216:217], v226 offset:33792
ds_read_b64_tr_b16 v[218:219], v226 offset:41984
v_mfma_f32_16x16x32_bf16 a[4:7], v[196:199], v[168:171], a[4:7]
ds_read_b64_tr_b16 v[192:193], v225 offset:34304
ds_read_b64_tr_b16 v[194:195], v225 offset:42496
v_mfma_f32_16x16x32_bf16 a[8:11], v[196:199], v[176:179], a[8:11]
v_max3_f32 v247, v128, v129, v130
v_max3_f32 v248, v136, v137, v138
v_mfma_f32_16x16x32_bf16 a[12:15], v[196:199], v[184:187], a[12:15]
buffer_load_dwordx4 v232, s[48:51], s87 offen offset:3072 nt lds
v_mfma_f32_16x16x32_bf16 a[16:19], v[200:203], v[160:163], a[16:19]
ds_read_b64_tr_b16 v[196:197], v226 offset:34304
v_max3_f32 v249, v144, v145, v146
v_mfma_f32_16x16x32_bf16 a[20:23], v[200:203], v[168:171], a[20:23]
ds_read_b64_tr_b16 v[198:199], v226 offset:42496
v_max3_f32 v250, v152, v153, v154
v_mfma_f32_16x16x32_bf16 a[24:27], v[200:203], v[176:179], a[24:27]
v_max3_f32 v247, v247, v131, v132
v_max3_f32 v248, v248, v139, v140
v_mfma_f32_16x16x32_bf16 a[28:31], v[200:203], v[184:187], a[28:31]
v_max3_f32 v249, v249, v147, v148
v_max3_f32 v250, v250, v155, v156
v_mfma_f32_16x16x32_bf16 a[32:35], v[204:207], v[160:163], a[32:35]
ds_read_b64_tr_b16 v[200:201], v225 offset:34816
v_max3_f32 v247, v247, v133, v134
v_mfma_f32_16x16x32_bf16 a[36:39], v[204:207], v[168:171], a[36:39]
s_add_u32 m0, s60, 0x22000
ds_read_b64_tr_b16 v[202:203], v225 offset:43008
v_max3_f32 v248, v248, v141, v142
v_mfma_f32_16x16x32_bf16 a[40:43], v[204:207], v[176:179], a[40:43]
v_max3_f32 v249, v249, v149, v150
v_max3_f32 v250, v250, v157, v158
v_mfma_f32_16x16x32_bf16 a[44:47], v[204:207], v[184:187], a[44:47]
buffer_load_dwordx4 v232, s[48:51], s88 offen offset:0 nt lds
s_waitcnt lgkmcnt(4)
v_mfma_f32_16x16x32_bf16 a[48:51], v[208:211], v[160:163], a[48:51]
ds_read_b64_tr_b16 v[204:205], v226 offset:34816
v_max_f32 v247, v247, v135
v_mfma_f32_16x16x32_bf16 a[52:55], v[208:211], v[168:171], a[52:55]
ds_read_b64_tr_b16 v[206:207], v226 offset:43008
v_max_f32 v248, v248, v143
v_mfma_f32_16x16x32_bf16 a[56:59], v[208:211], v[176:179], a[56:59]
v_max_f32 v249, v249, v151
v_max_f32 v250, v250, v159
v_mfma_f32_16x16x32_bf16 a[60:63], v[208:211], v[184:187], a[60:63]
v_permlane32_swap_b32 v247, v249
v_permlane32_swap_b32 v248, v250
v_mfma_f32_16x16x32_bf16 a[64:67], v[212:215], v[160:163], a[64:67]
ds_read_b64_tr_b16 v[208:209], v225 offset:35328
v_max_f32 v247, v247, v249
v_mfma_f32_16x16x32_bf16 a[68:71], v[212:215], v[168:171], a[68:71]
ds_read_b64_tr_b16 v[210:211], v225 offset:43520
v_max_f32 v248, v248, v250
v_mfma_f32_16x16x32_bf16 a[72:75], v[212:215], v[176:179], a[72:75]
buffer_load_dwordx4 v232, s[48:51], s89 offen offset:1024 nt lds
v_mfma_f32_16x16x32_bf16 a[76:79], v[212:215], v[184:187], a[76:79]
v_permlane16_swap_b32 v247, v248
v_max_f32 v247, v247, v248
v_mfma_f32_16x16x32_bf16 a[80:83], v[216:219], v[160:163], a[80:83]
ds_read_b64_tr_b16 v[212:213], v226 offset:35328
v_cmp_gt_f32 vcc, v247, v238
v_mfma_f32_16x16x32_bf16 a[84:87], v[216:219], v[168:171], a[84:87]
ds_read_b64_tr_b16 v[214:215], v226 offset:43520
s_cbranch_vccnz LR2
LRB2:
v_mfma_f32_16x16x32_bf16 a[88:91], v[216:219], v[176:179], a[88:91]
v_fma_f32 v128, v128, s62, -v239
v_fma_f32 v129, v129, s62, -v239
v_mfma_f32_16x16x32_bf16 a[92:95], v[216:219], v[184:187], a[92:95]
v_exp_f32 v128, v128
v_mfma_f32_16x16x32_bf16 a[96:99], v[192:195], v[160:163], a[96:99]
ds_read_b64_tr_b16 v[216:217], v225 offset:35840
v_exp_f32 v129, v129
v_mfma_f32_16x16x32_bf16 a[100:103], v[192:195], v[168:171], a[100:103]
ds_read_b64_tr_b16 v[218:219], v225 offset:44032
v_fma_f32 v130, v130, s62, -v239
v_mfma_f32_16x16x32_bf16 a[104:107], v[192:195], v[176:179], a[104:107]
buffer_load_dwordx4 v232, s[48:51], s90 offen offset:2048 nt lds
v_mfma_f32_16x16x32_bf16 a[108:111], v[192:195], v[184:187], a[108:111]
v_fma_f32 v131, v131, s62, -v239
s_waitcnt lgkmcnt(4)
v_mfma_f32_16x16x32_bf16 a[112:115], v[196:199], v[160:163], a[112:115]
ds_read_b64_tr_b16 v[192:193], v226 offset:35840
v_exp_f32 v130, v130
v_mfma_f32_16x16x32_bf16 a[116:119], v[196:199], v[168:171], a[116:119]
ds_read_b64_tr_b16 v[194:195], v226 offset:44032
v_exp_f32 v131, v131
v_mfma_f32_16x16x32_bf16 a[120:123], v[196:199], v[176:179], a[120:123]
v_add_f32 v243, v243, v128
v_add_f32 v243, v243, v129
v_mfma_f32_16x16x32_bf16 a[124:127], v[196:199], v[184:187], a[124:127]
v_cvt_pk_bf16_f32 v128, v128, v129
v_fma_f32 v132, v132, s62, -v239
v_mfma_f32_16x16x32_bf16 a[128:131], v[200:203], v[160:163], a[128:131]
ds_read_b64_tr_b16 v[196:197], v225 offset:36352
v_fma_f32 v133, v133, s62, -v239
v_mfma_f32_16x16x32_bf16 a[132:135], v[200:203], v[168:171], a[132:135]
buffer_load_dwordx4 v232, s[48:51], s91 offen offset:3072 nt lds
v_mfma_f32_16x16x32_bf16 a[136:139], v[200:203], v[176:179], a[136:139]
ds_read_b64_tr_b16 v[198:199], v225 offset:44544
v_exp_f32 v132, v132
v_mfma_f32_16x16x32_bf16 a[140:143], v[200:203], v[184:187], a[140:143]
s_add_u32 s48, s48, 0x4000
s_addc_u32 s49, s49, 0
v_mfma_f32_16x16x32_bf16 a[144:147], v[204:207], v[160:163], a[144:147]
ds_read_b64_tr_b16 v[200:201], v226 offset:36352
v_exp_f32 v133, v133
v_mfma_f32_16x16x32_bf16 a[148:151], v[204:207], v[168:171], a[148:151]
ds_read_b64_tr_b16 v[202:203], v226 offset:44544
v_add_f32 v243, v243, v130
v_mfma_f32_16x16x32_bf16 a[152:155], v[204:207], v[176:179], a[152:155]
v_add_f32 v243, v243, v131
v_cvt_pk_bf16_f32 v129, v130, v131
v_mfma_f32_16x16x32_bf16 a[156:159], v[204:207], v[184:187], a[156:159]
v_fma_f32 v134, v134, s62, -v239
v_fma_f32 v135, v135, s62, -v239
v_mfma_f32_16x16x32_bf16 a[160:163], v[208:211], v[160:163], a[160:163]
v_exp_f32 v134, v134
v_mfma_f32_16x16x32_bf16 a[164:167], v[208:211], v[168:171], a[164:167]
s_waitcnt vmcnt(24)
s_waitcnt lgkmcnt(0)
v_exp_f32 v135, v135
v_mfma_f32_16x16x32_bf16 a[168:171], v[208:211], v[176:179], a[168:171]
s_barrier
v_add_u32 v224, 0xffff0000, v224
v_mfma_f32_16x16x32_bf16 a[172:175], v[208:211], v[184:187], a[172:175]
s_add_u32 m0, s60, 0x8000
ds_read_b128 v[204:207], v224 offset:0
v_add_f32 v243, v243, v132
v_mfma_f32_16x16x32_bf16 a[176:179], v[212:215], v[160:163], a[176:179]
ds_read_b128 v[208:211], v224 offset:8192
v_add_f32 v243, v243, v133
v_mfma_f32_16x16x32_bf16 a[180:183], v[212:215], v[168:171], a[180:183]
buffer_load_dwordx4 v231, s[44:47], s84 offen offset:0 nt lds
v_mfma_f32_16x16x32_bf16 a[184:187], v[212:215], v[176:179], a[184:187]
v_cvt_pk_bf16_f32 v130, v132, v133
v_fma_f32 v136, v136, s62, -v240
v_mfma_f32_16x16x32_bf16 a[188:191], v[212:215], v[184:187], a[188:191]
v_fma_f32 v137, v137, s62, -v240
v_mfma_f32_16x16x32_bf16 a[192:195], v[216:219], v[160:163], a[192:195]
ds_read_b128 v[212:215], v224 offset:512
v_exp_f32 v136, v136
v_mfma_f32_16x16x32_bf16 a[196:199], v[216:219], v[168:171], a[196:199]
v_exp_f32 v137, v137
v_mfma_f32_16x16x32_bf16 a[200:203], v[216:219], v[176:179], a[200:203]
v_add_f32 v243, v243, v134
v_add_f32 v243, v243, v135
v_mfma_f32_16x16x32_bf16 a[204:207], v[216:219], v[184:187], a[204:207]
v_cvt_pk_bf16_f32 v131, v134, v135
v_fma_f32 v138, v138, s62, -v240
v_mfma_f32_16x16x32_bf16 a[208:211], v[192:195], v[160:163], a[208:211]
buffer_load_dwordx4 v231, s[44:47], s85 offen offset:1024 nt lds
v_mfma_f32_16x16x32_bf16 a[212:215], v[192:195], v[168:171], a[212:215]
ds_read_b128 v[216:219], v224 offset:8704
v_fma_f32 v139, v139, s62, -v240
v_mfma_f32_16x16x32_bf16 a[216:219], v[192:195], v[176:179], a[216:219]
v_exp_f32 v138, v138
v_mfma_f32_16x16x32_bf16 a[220:223], v[192:195], v[184:187], a[220:223]
v_exp_f32 v139, v139
v_mfma_f32_16x16x32_bf16 a[224:227], v[196:199], v[160:163], a[224:227]
ds_read_b128 v[192:195], v224 offset:1024
v_add_f32 v244, v244, v136
v_mfma_f32_16x16x32_bf16 a[228:231], v[196:199], v[168:171], a[228:231]
v_add_f32 v244, v244, v137
v_cvt_pk_bf16_f32 v136, v136, v137
v_mfma_f32_16x16x32_bf16 a[232:235], v[196:199], v[176:179], a[232:235]
v_fma_f32 v140, v140, s62, -v240
v_fma_f32 v141, v141, s62, -v240
v_mfma_f32_16x16x32_bf16 a[236:239], v[196:199], v[184:187], a[236:239]
v_exp_f32 v140, v140
v_mfma_f32_16x16x32_bf16 a[240:243], v[200:203], v[160:163], a[240:243]
buffer_load_dwordx4 v231, s[44:47], s86 offen offset:2048 nt lds
v_mfma_f32_16x16x32_bf16 a[244:247], v[200:203], v[168:171], a[244:247]
ds_read_b128 v[196:199], v224 offset:9216
v_exp_f32 v141, v141
v_mfma_f32_16x16x32_bf16 a[248:251], v[200:203], v[176:179], a[248:251]
v_add_f32 v244, v244, v138
v_add_f32 v244, v244, v139
v_mfma_f32_16x16x32_bf16 a[252:255], v[200:203], v[184:187], a[252:255]
LST19:
s_cmp_lg_u32 s63, 0
s_cbranch_scc1 LS2
LSB2:
s_waitcnt lgkmcnt(2)
v_mfma_f32_16x16x32_bf16 v[160:163], v[204:207], v[0:3], 0
ds_read_b128 v[200:203], v224 offset:1536
v_mbcnt_lo_u32_b32 v234, -1, 0
v_mfma_f32_16x16x32_bf16 v[168:171], v[204:207], v[32:35], 0
v_mbcnt_hi_u32_b32 v234, -1, v234
v_and_b32 v233, 15, v234
v_mfma_f32_16x16x32_bf16 v[176:179], v[204:207], v[64:67], 0
v_lshrrev_b32 v234, 4, v234
v_mul_u32_u24 v228, 0x700, v233
v_mfma_f32_16x16x32_bf16 v[184:187], v[204:207], v[96:99], 0
buffer_load_dwordx4 v231, s[44:47], s87 offen offset:3072 nt lds
v_mfma_f32_16x16x32_bf16 v[164:167], v[208:211], v[0:3], 0
ds_read_b128 v[204:207], v224 offset:9728
v_lshl_add_u32 v228, v234, 5, v228
s_lshl_b32 s74, %6, 3
v_mfma_f32_16x16x32_bf16 v[172:175], v[208:211], v[32:35], 0
v_add_u32 v228, s74, v228
v_lshrrev_b32 v227, 1, v234
v_mfma_f32_16x16x32_bf16 v[180:183], v[208:211], v[64:67], 0
v_lshl_add_u32 v228, v227, 20, v228
s_mov_b32 s74, 0x1c000
v_mul_lo_u32 v234, s74, v234
v_mfma_f32_16x16x32_bf16 v[188:191], v[208:211], v[96:99], 0
v_lshl_add_u32 v227, v233, 4, v234
s_lshr_b32 s74, %11, 1
s_lshl_b32 s74, s74, 6
v_and_b32 v234, 7, v233
v_mfma_f32_16x16x32_bf16 v[160:163], v[212:215], v[4:7], v[160:163]
ds_read_b128 v[208:211], v224 offset:2048
v_lshlrev_b32 v234, 3, v234
v_mfma_f32_16x16x32_bf16 v[168:171], v[212:215], v[36:39], v[168:171]
s_add_u32 m0, s60, 0xa000
v_add_u32 v234, s74, v234
s_lshl_b32 s74, %6, 6
s_lshl_b32 s75, %11, 3
s_add_u32 s74, s74, s75
v_mov_b32 v233, s74
v_mfma_f32_16x16x32_bf16 v[176:179], v[212:215], v[68:71], v[176:179]
v_cvt_pk_bf16_f32 v137, v138, v139
v_fma_f32 v142, v142, s62, -v240
v_mfma_f32_16x16x32_bf16 v[184:187], v[212:215], v[100:103], v[184:187]
buffer_load_dwordx4 v231, s[44:47], s88 offen offset:0 nt lds
v_mfma_f32_16x16x32_bf16 v[164:167], v[216:219], v[4:7], v[164:167]
ds_read_b128 v[212:215], v224 offset:10240
v_fma_f32 v143, v143, s62, -v240
v_mfma_f32_16x16x32_bf16 v[172:175], v[216:219], v[36:39], v[172:175]
v_exp_f32 v142, v142
v_mfma_f32_16x16x32_bf16 v[180:183], v[216:219], v[68:71], v[180:183]
v_exp_f32 v143, v143
v_mfma_f32_16x16x32_bf16 v[188:191], v[216:219], v[100:103], v[188:191]
v_add_f32 v244, v244, v140
v_add_f32 v244, v244, v141
s_waitcnt lgkmcnt(2)
v_mfma_f32_16x16x32_bf16 v[160:163], v[192:195], v[8:11], v[160:163]
ds_read_b128 v[216:219], v224 offset:2560
v_cvt_pk_bf16_f32 v138, v140, v141
v_mfma_f32_16x16x32_bf16 v[168:171], v[192:195], v[40:43], v[168:171]
v_fma_f32 v144, v144, s62, -v241
v_fma_f32 v145, v145, s62, -v241
v_mfma_f32_16x16x32_bf16 v[176:179], v[192:195], v[72:75], v[176:179]
buffer_load_dwordx4 v231, s[44:47], s89 offen offset:1024 nt lds
v_mfma_f32_16x16x32_bf16 v[184:187], v[192:195], v[104:107], v[184:187]
v_exp_f32 v144, v144
v_mfma_f32_16x16x32_bf16 v[164:167], v[196:199], v[8:11], v[164:167]
ds_read_b128 v[192:195], v224 offset:10752
v_exp_f32 v145, v145
v_mfma_f32_16x16x32_bf16 v[172:175], v[196:199], v[40:43], v[172:175]
v_add_f32 v244, v244, v142
v_add_f32 v244, v244, v143
v_mfma_f32_16x16x32_bf16 v[180:183], v[196:199], v[72:75], v[180:183]
v_cvt_pk_bf16_f32 v139, v142, v143
v_fma_f32 v146, v146, s62, -v241
v_mfma_f32_16x16x32_bf16 v[188:191], v[196:199], v[104:107], v[188:191]
v_fma_f32 v147, v147, s62, -v241
v_mfma_f32_16x16x32_bf16 v[160:163], v[200:203], v[12:15], v[160:163]
ds_read_b128 v[196:199], v224 offset:3072
v_exp_f32 v146, v146
v_mfma_f32_16x16x32_bf16 v[168:171], v[200:203], v[44:47], v[168:171]
v_exp_f32 v147, v147
v_mfma_f32_16x16x32_bf16 v[176:179], v[200:203], v[76:79], v[176:179]
buffer_load_dwordx4 v231, s[44:47], s90 offen offset:2048 nt lds
v_mfma_f32_16x16x32_bf16 v[184:187], v[200:203], v[108:111], v[184:187]
v_add_f32 v245, v245, v144
v_add_f32 v245, v245, v145
v_mfma_f32_16x16x32_bf16 v[164:167], v[204:207], v[12:15], v[164:167]
ds_read_b128 v[200:203], v224 offset:11264
v_cvt_pk_bf16_f32 v144, v144, v145
v_mfma_f32_16x16x32_bf16 v[172:175], v[204:207], v[44:47], v[172:175]
v_fma_f32 v148, v148, s62, -v241
v_fma_f32 v149, v149, s62, -v241
v_mfma_f32_16x16x32_bf16 v[180:183], v[204:207], v[76:79], v[180:183]
v_exp_f32 v148, v148
v_mfma_f32_16x16x32_bf16 v[188:191], v[204:207], v[108:111], v[188:191]
v_exp_f32 v149, v149
s_waitcnt lgkmcnt(2)
v_mfma_f32_16x16x32_bf16 v[160:163], v[208:211], v[16:19], v[160:163]
ds_read_b128 v[204:207], v224 offset:3584
v_add_f32 v245, v245, v146
v_mfma_f32_16x16x32_bf16 v[168:171], v[208:211], v[48:51], v[168:171]
buffer_load_dwordx4 v231, s[44:47], s91 offen offset:3072 nt lds
v_mfma_f32_16x16x32_bf16 v[176:179], v[208:211], v[80:83], v[176:179]
s_add_u32 s44, s44, 0x4000
s_addc_u32 s45, s45, 0
v_mfma_f32_16x16x32_bf16 v[184:187], v[208:211], v[112:115], v[184:187]
v_add_f32 v245, v245, v147
v_cvt_pk_bf16_f32 v145, v146, v147
v_mfma_f32_16x16x32_bf16 v[164:167], v[212:215], v[16:19], v[164:167]
ds_read_b128 v[208:211], v224 offset:11776
v_fma_f32 v150, v150, s62, -v241
v_mfma_f32_16x16x32_bf16 v[172:175], v[212:215], v[48:51], v[172:175]
v_fma_f32 v151, v151, s62, -v241
v_mfma_f32_16x16x32_bf16 v[180:183], v[212:215], v[80:83], v[180:183]
v_exp_f32 v150, v150
v_mfma_f32_16x16x32_bf16 v[188:191], v[212:215], v[112:115], v[188:191]
v_exp_f32 v151, v151
v_mfma_f32_16x16x32_bf16 v[160:163], v[216:219], v[20:23], v[160:163]
v_add_f32 v245, v245, v148
v_add_f32 v245, v245, v149
v_mfma_f32_16x16x32_bf16 v[168:171], v[216:219], v[52:55], v[168:171]
s_waitcnt vmcnt(24)
s_waitcnt lgkmcnt(0)
v_cvt_pk_bf16_f32 v146, v148, v149
v_mfma_f32_16x16x32_bf16 v[176:179], v[216:219], v[84:87], v[176:179]
s_barrier
v_add_u32 v225, 0x10000, v225
v_mfma_f32_16x16x32_bf16 v[184:187], v[216:219], v[116:119], v[184:187]
s_add_u32 m0, s60, 0x10000
ds_read_b64_tr_b16 v[212:213], v225 offset:32768
ds_read_b64_tr_b16 v[214:215], v225 offset:40960
v_mfma_f32_16x16x32_bf16 v[164:167], v[192:195], v[20:23], v[164:167]
v_add_u32 v226, 0x10000, v226
ds_read_b64_tr_b16 v[216:217], v226 offset:32768
v_mfma_f32_16x16x32_bf16 v[172:175], v[192:195], v[52:55], v[172:175]
buffer_load_dwordx4 v232, s[48:51], s84 offen offset:0 nt lds
v_mfma_f32_16x16x32_bf16 v[180:183], v[192:195], v[84:87], v[180:183]
ds_read_b64_tr_b16 v[218:219], v226 offset:40960
v_fma_f32 v152, v152, s62, -v242
v_mfma_f32_16x16x32_bf16 v[188:191], v[192:195], v[116:119], v[188:191]
v_fma_f32 v153, v153, s62, -v242
v_mfma_f32_16x16x32_bf16 v[160:163], v[196:199], v[24:27], v[160:163]
ds_read_b64_tr_b16 v[192:193], v225 offset:33280
v_exp_f32 v152, v152
v_mfma_f32_16x16x32_bf16 v[168:171], v[196:199], v[56:59], v[168:171]
ds_read_b64_tr_b16 v[194:195], v225 offset:41472
v_exp_f32 v153, v153
v_mfma_f32_16x16x32_bf16 v[176:179], v[196:199], v[88:91], v[176:179]
v_add_f32 v245, v245, v150
v_add_f32 v245, v245, v151
v_mfma_f32_16x16x32_bf16 v[184:187], v[196:199], v[120:123], v[184:187]
v_cvt_pk_bf16_f32 v147, v150, v151
v_fma_f32 v154, v154, s62, -v242
v_mfma_f32_16x16x32_bf16 v[164:167], v[200:203], v[24:27], v[164:167]
buffer_load_dwordx4 v232, s[48:51], s85 offen offset:1024 nt lds
v_mfma_f32_16x16x32_bf16 v[172:175], v[200:203], v[56:59], v[172:175]
v_fma_f32 v155, v155, s62, -v242
ds_read_b64_tr_b16 v[196:197], v226 offset:33280
v_mfma_f32_16x16x32_bf16 v[180:183], v[200:203], v[88:91], v[180:183]
v_exp_f32 v154, v154
ds_read_b64_tr_b16 v[198:199], v226 offset:41472
v_mfma_f32_16x16x32_bf16 v[188:191], v[200:203], v[120:123], v[188:191]
v_exp_f32 v155, v155
v_mfma_f32_16x16x32_bf16 v[160:163], v[204:207], v[28:31], v[160:163]
v_add_f32 v246, v246, v152
v_add_f32 v246, v246, v153
v_mfma_f32_16x16x32_bf16 v[168:171], v[204:207], v[60:63], v[168:171]
v_cvt_pk_bf16_f32 v152, v152, v153
v_fma_f32 v156, v156, s62, -v242
v_mfma_f32_16x16x32_bf16 v[176:179], v[204:207], v[92:95], v[176:179]
v_fma_f32 v157, v157, s62, -v242
ds_read_b64_tr_b16 v[200:201], v225 offset:33792
v_mfma_f32_16x16x32_bf16 v[184:187], v[204:207], v[124:127], v[184:187]
v_exp_f32 v156, v156
ds_read_b64_tr_b16 v[202:203], v225 offset:41984
v_mfma_f32_16x16x32_bf16 v[164:167], v[208:211], v[28:31], v[164:167]
buffer_load_dwordx4 v232, s[48:51], s86 offen offset:2048 nt lds
v_mfma_f32_16x16x32_bf16 v[172:175], v[208:211], v[60:63], v[172:175]
v_exp_f32 v157, v157
ds_read_b64_tr_b16 v[204:205], v226 offset:33792
v_mfma_f32_16x16x32_bf16 v[180:183], v[208:211], v[92:95], v[180:183]
v_add_f32 v246, v246, v154
v_add_f32 v246, v246, v155
v_mfma_f32_16x16x32_bf16 v[188:191], v[208:211], v[124:127], v[188:191]
LST20:
v_cvt_pk_bf16_f32 v153, v154, v155
v_fma_f32 v158, v158, s62, -v242
s_waitcnt lgkmcnt(4)
v_mfma_f32_16x16x32_bf16 a[0:3], v[212:215], v[128:131], a[0:3]
v_fma_f32 v159, v159, s62, -v242
ds_read_b64_tr_b16 v[206:207], v226 offset:41984
v_mfma_f32_16x16x32_bf16 a[4:7], v[212:215], v[136:139], a[4:7]
v_exp_f32 v158, v158
v_exp_f32 v159, v159
v_add_f32 v246, v246, v156
v_add_f32 v246, v246, v157
v_cvt_pk_bf16_f32 v154, v156, v157
v_add_f32 v246, v246, v158
v_add_f32 v246, v246, v159
v_cvt_pk_bf16_f32 v155, v158, v159
v_mfma_f32_16x16x32_bf16 a[8:11], v[212:215], v[144:147], a[8:11]
ds_read_b64_tr_b16 v[208:209], v225 offset:34304
ds_read_b64_tr_b16 v[210:211], v225 offset:42496
v_mfma_f32_16x16x32_bf16 a[12:15], v[212:215], v[152:155], a[12:15]
buffer_load_dwordx4 v232, s[48:51], s87 offen offset:3072 nt lds
v_mfma_f32_16x16x32_bf16 a[16:19], v[216:219], v[128:131], a[16:19]
ds_read_b64_tr_b16 v[212:213], v226 offset:34304
v_max3_f32 v247, v160, v161, v162
v_mfma_f32_16x16x32_bf16 a[20:23], v[216:219], v[136:139], a[20:23]
ds_read_b64_tr_b16 v[214:215], v226 offset:42496
v_max3_f32 v248, v168, v169, v170
v_mfma_f32_16x16x32_bf16 a[24:27], v[216:219], v[144:147], a[24:27]
v_max3_f32 v249, v176, v177, v178
v_max3_f32 v250, v184, v185, v186
v_mfma_f32_16x16x32_bf16 a[28:31], v[216:219], v[152:155], a[28:31]
v_max3_f32 v247, v247, v163, v164
v_max3_f32 v248, v248, v171, v172
v_mfma_f32_16x16x32_bf16 a[32:35], v[192:195], v[128:131], a[32:35]
ds_read_b64_tr_b16 v[216:217], v225 offset:34816
v_max3_f32 v249, v249, v179, v180
v_mfma_f32_16x16x32_bf16 a[36:39], v[192:195], v[136:139], a[36:39]
s_add_u32 m0, s60, 0x12000
ds_read_b64_tr_b16 v[218:219], v225 offset:43008
v_max3_f32 v250, v250, v187, v188
v_mfma_f32_16x16x32_bf16 a[40:43], v[192:195], v[144:147], a[40:43]
v_max3_f32 v247, v247, v165, v166
v_max3_f32 v248, v248, v173, v174
v_mfma_f32_16x16x32_bf16 a[44:47], v[192:195], v[152:155], a[44:47]
buffer_load_dwordx4 v232, s[48:51], s88 offen offset:0 nt lds
s_waitcnt lgkmcnt(6)
v_mfma_f32_16x16x32_bf16 a[48:51], v[196:199], v[128:131], a[48:51]
ds_read_b64_tr_b16 v[192:193], v226 offset:34816
v_max3_f32 v249, v249, v181, v182
v_mfma_f32_16x16x32_bf16 a[52:55], v[196:199], v[136:139], a[52:55]
ds_read_b64_tr_b16 v[194:195], v226 offset:43008
v_max3_f32 v250, v250, v189, v190
v_mfma_f32_16x16x32_bf16 a[56:59], v[196:199], v[144:147], a[56:59]
v_max_f32 v247, v247, v167
v_max_f32 v248, v248, v175
v_mfma_f32_16x16x32_bf16 a[60:63], v[196:199], v[152:155], a[60:63]
v_max_f32 v249, v249, v183
v_max_f32 v250, v250, v191
v_mfma_f32_16x16x32_bf16 a[64:67], v[200:203], v[128:131], a[64:67]
ds_read_b64_tr_b16 v[196:197], v225 offset:35328
v_permlane32_swap_b32 v247, v249
v_mfma_f32_16x16x32_bf16 a[68:71], v[200:203], v[136:139], a[68:71]
ds_read_b64_tr_b16 v[198:199], v225 offset:43520
v_permlane32_swap_b32 v248, v250
v_mfma_f32_16x16x32_bf16 a[72:75], v[200:203], v[144:147], a[72:75]
buffer_load_dwordx4 v232, s[48:51], s89 offen offset:1024 nt lds
v_mfma_f32_16x16x32_bf16 a[76:79], v[200:203], v[152:155], a[76:79]
v_max_f32 v247, v247, v249
v_max_f32 v248, v248, v250
v_mfma_f32_16x16x32_bf16 a[80:83], v[204:207], v[128:131], a[80:83]
LH5:
s_cbranch_execz LH9
ds_read_b64_tr_b16 v[200:201], v226 offset:35328
v_permlane16_swap_b32 v247, v248
v_mfma_f32_16x16x32_bf16 a[84:87], v[204:207], v[136:139], a[84:87]
ds_read_b64_tr_b16 v[202:203], v226 offset:43520
v_max_f32 v247, v247, v248
v_mfma_f32_16x16x32_bf16 a[88:91], v[204:207], v[144:147], a[88:91]
v_cmp_gt_f32 vcc, v247, v238
s_cbranch_vccnz LR3
LRB3:
v_mfma_f32_16x16x32_bf16 a[92:95], v[204:207], v[152:155], a[92:95]
v_fma_f32 v160, v160, s62, -v239
v_fma_f32 v161, v161, s62, -v239
s_waitcnt lgkmcnt(4)
v_mfma_f32_16x16x32_bf16 a[96:99], v[208:211], v[128:131], a[96:99]
ds_read_b64_tr_b16 v[204:205], v225 offset:35840
v_exp_f32 v160, v160
v_mfma_f32_16x16x32_bf16 a[100:103], v[208:211], v[136:139], a[100:103]
ds_read_b64_tr_b16 v[206:207], v225 offset:44032
v_exp_f32 v161, v161
v_mfma_f32_16x16x32_bf16 a[104:107], v[208:211], v[144:147], a[104:107]
buffer_load_dwordx4 v232, s[48:51], s90 offen offset:2048 nt lds
v_mfma_f32_16x16x32_bf16 a[108:111], v[208:211], v[152:155], a[108:111]
v_fma_f32 v162, v162, s62, -v239
v_fma_f32 v163, v163, s62, -v239
v_mfma_f32_16x16x32_bf16 a[112:115], v[212:215], v[128:131], a[112:115]
ds_read_b64_tr_b16 v[208:209], v226 offset:35840
v_exp_f32 v162, v162
v_mfma_f32_16x16x32_bf16 a[116:119], v[212:215], v[136:139], a[116:119]
ds_read_b64_tr_b16 v[210:211], v226 offset:44032
v_exp_f32 v163, v163
v_mfma_f32_16x16x32_bf16 a[120:123], v[212:215], v[144:147], a[120:123]
v_add_f32 v243, v243, v160
v_add_f32 v243, v243, v161
v_mfma_f32_16x16x32_bf16 a[124:127], v[212:215], v[152:155], a[124:127]
v_cvt_pk_bf16_f32 v160, v160, v161
v_fma_f32 v164, v164, s62, -v239
v_mfma_f32_16x16x32_bf16 a[128:131], v[216:219], v[128:131], a[128:131]
ds_read_b64_tr_b16 v[212:213], v225 offset:36352
v_fma_f32 v165, v165, s62, -v239
v_mfma_f32_16x16x32_bf16 a[132:135], v[216:219], v[136:139], a[132:135]
buffer_load_dwordx4 v232, s[48:51], s91 offen offset:3072 nt lds
v_mfma_f32_16x16x32_bf16 a[136:139], v[216:219], v[144:147], a[136:139]
ds_read_b64_tr_b16 v[214:215], v225 offset:44544
v_exp_f32 v164, v164
v_mfma_f32_16x16x32_bf16 a[140:143], v[216:219], v[152:155], a[140:143]
s_add_u32 s48, s48, 0x4000
s_addc_u32 s49, s49, 0
v_mfma_f32_16x16x32_bf16 a[144:147], v[192:195], v[128:131], a[144:147]
ds_read_b64_tr_b16 v[216:217], v226 offset:36352
v_exp_f32 v165, v165
v_mfma_f32_16x16x32_bf16 a[148:151], v[192:195], v[136:139], a[148:151]
ds_read_b64_tr_b16 v[218:219], v226 offset:44544
v_add_f32 v243, v243, v162
v_mfma_f32_16x16x32_bf16 a[152:155], v[192:195], v[144:147], a[152:155]
v_add_f32 v243, v243, v163
v_cvt_pk_bf16_f32 v161, v162, v163
v_mfma_f32_16x16x32_bf16 a[156:159], v[192:195], v[152:155], a[156:159]
v_fma_f32 v166, v166, s62, -v239
v_fma_f32 v167, v167, s62, -v239
s_waitcnt lgkmcnt(4)
v_mfma_f32_16x16x32_bf16 a[160:163], v[196:199], v[128:131], a[160:163]
v_exp_f32 v166, v166
v_mfma_f32_16x16x32_bf16 a[164:167], v[196:199], v[136:139], a[164:167]
s_waitcnt vmcnt(24)
s_waitcnt lgkmcnt(0)
v_exp_f32 v167, v167
v_mfma_f32_16x16x32_bf16 a[168:171], v[196:199], v[144:147], a[168:171]
s_barrier
v_add_u32 v224, 0xffff0000, v224
v_mfma_f32_16x16x32_bf16 a[172:175], v[196:199], v[152:155], a[172:175]
s_add_u32 m0, s60, 0x18000
ds_read_b128 v[192:195], v224 offset:0
v_add_f32 v243, v243, v164
v_mfma_f32_16x16x32_bf16 a[176:179], v[200:203], v[128:131], a[176:179]
ds_read_b128 v[196:199], v224 offset:8192
v_add_f32 v243, v243, v165
v_mfma_f32_16x16x32_bf16 a[180:183], v[200:203], v[136:139], a[180:183]
buffer_load_dwordx4 v231, s[44:47], s84 offen offset:0 nt lds
v_mfma_f32_16x16x32_bf16 a[184:187], v[200:203], v[144:147], a[184:187]
v_cvt_pk_bf16_f32 v162, v164, v165
v_fma_f32 v168, v168, s62, -v240
v_mfma_f32_16x16x32_bf16 a[188:191], v[200:203], v[152:155], a[188:191]
v_fma_f32 v169, v169, s62, -v240
v_mfma_f32_16x16x32_bf16 a[192:195], v[204:207], v[128:131], a[192:195]
ds_read_b128 v[200:203], v224 offset:512
v_exp_f32 v168, v168
v_mfma_f32_16x16x32_bf16 a[196:199], v[204:207], v[136:139], a[196:199]
v_exp_f32 v169, v169
v_mfma_f32_16x16x32_bf16 a[200:203], v[204:207], v[144:147], a[200:203]
v_add_f32 v243, v243, v166
v_add_f32 v243, v243, v167
v_mfma_f32_16x16x32_bf16 a[204:207], v[204:207], v[152:155], a[204:207]
v_cvt_pk_bf16_f32 v163, v166, v167
v_fma_f32 v170, v170, s62, -v240
v_mfma_f32_16x16x32_bf16 a[208:211], v[208:211], v[128:131], a[208:211]
buffer_load_dwordx4 v231, s[44:47], s85 offen offset:1024 nt lds
v_mfma_f32_16x16x32_bf16 a[212:215], v[208:211], v[136:139], a[212:215]
ds_read_b128 v[204:207], v224 offset:8704
v_fma_f32 v171, v171, s62, -v240
v_mfma_f32_16x16x32_bf16 a[216:219], v[208:211], v[144:147], a[216:219]
v_exp_f32 v170, v170
v_mfma_f32_16x16x32_bf16 a[220:223], v[208:211], v[152:155], a[220:223]
v_exp_f32 v171, v171
v_mfma_f32_16x16x32_bf16 a[224:227], v[212:215], v[128:131], a[224:227]
ds_read_b128 v[208:211], v224 offset:1024
v_add_f32 v244, v244, v168
v_mfma_f32_16x16x32_bf16 a[228:231], v[212:215], v[136:139], a[228:231]
v_add_f32 v244, v244, v169
v_cvt_pk_bf16_f32 v168, v168, v169
v_mfma_f32_16x16x32_bf16 a[232:235], v[212:215], v[144:147], a[232:235]
v_fma_f32 v172, v172, s62, -v240
v_fma_f32 v173, v173, s62, -v240
v_mfma_f32_16x16x32_bf16 a[236:239], v[212:215], v[152:155], a[236:239]
v_exp_f32 v172, v172
v_mfma_f32_16x16x32_bf16 a[240:243], v[216:219], v[128:131], a[240:243]
buffer_load_dwordx4 v231, s[44:47], s86 offen offset:2048 nt lds
v_mfma_f32_16x16x32_bf16 a[244:247], v[216:219], v[136:139], a[244:247]
ds_read_b128 v[212:215], v224 offset:9216
v_exp_f32 v173, v173
v_mfma_f32_16x16x32_bf16 a[248:251], v[216:219], v[144:147], a[248:251]
v_add_f32 v244, v244, v170
v_add_f32 v244, v244, v171
v_mfma_f32_16x16x32_bf16 a[252:255], v[216:219], v[152:155], a[252:255]
LST21:
s_cmp_lg_u32 s63, 0
s_cbranch_scc1 LS3
LSB3:
s_waitcnt lgkmcnt(2)
v_mfma_f32_16x16x32_bf16 v[128:131], v[192:195], v[0:3], 0
ds_read_b128 v[216:219], v224 offset:1536
v_cvt_pk_bf16_f32 v169, v170, v171
v_mfma_f32_16x16x32_bf16 v[136:139], v[192:195], v[32:35], 0
v_fma_f32 v174, v174, s62, -v240
v_fma_f32 v175, v175, s62, -v240
v_mfma_f32_16x16x32_bf16 v[144:147], v[192:195], v[64:67], 0
v_exp_f32 v174, v174
v_mfma_f32_16x16x32_bf16 v[152:155], v[192:195], v[96:99], 0
buffer_load_dwordx4 v231, s[44:47], s87 offen offset:3072 nt lds
v_mfma_f32_16x16x32_bf16 v[132:135], v[196:199], v[0:3], 0
ds_read_b128 v[192:195], v224 offset:9728
v_exp_f32 v175, v175
v_mfma_f32_16x16x32_bf16 v[140:143], v[196:199], v[32:35], 0
v_add_f32 v244, v244, v172
v_add_f32 v244, v244, v173
v_mfma_f32_16x16x32_bf16 v[148:151], v[196:199], v[64:67], 0
v_cvt_pk_bf16_f32 v170, v172, v173
v_fma_f32 v176, v176, s62, -v241
v_mfma_f32_16x16x32_bf16 v[156:159], v[196:199], v[96:99], 0
v_fma_f32 v177, v177, s62, -v241
v_mfma_f32_16x16x32_bf16 v[128:131], v[200:203], v[4:7], v[128:131]
ds_read_b128 v[196:199], v224 offset:2048
v_exp_f32 v176, v176
v_mfma_f32_16x16x32_bf16 v[136:139], v[200:203], v[36:39], v[136:139]
s_add_u32 m0, s60, 0x1a000
v_exp_f32 v177, v177
v_mfma_f32_16x16x32_bf16 v[144:147], v[200:203], v[68:71], v[144:147]
v_add_f32 v244, v244, v174
v_add_f32 v244, v244, v175
v_mfma_f32_16x16x32_bf16 v[152:155], v[200:203], v[100:103], v[152:155]
buffer_load_dwordx4 v231, s[44:47], s88 offen offset:0 nt lds
v_mfma_f32_16x16x32_bf16 v[132:135], v[204:207], v[4:7], v[132:135]
ds_read_b128 v[200:203], v224 offset:10240
v_cvt_pk_bf16_f32 v171, v174, v175
v_mfma_f32_16x16x32_bf16 v[140:143], v[204:207], v[36:39], v[140:143]
v_fma_f32 v178, v178, s62, -v241
v_fma_f32 v179, v179, s62, -v241
v_mfma_f32_16x16x32_bf16 v[148:151], v[204:207], v[68:71], v[148:151]
v_exp_f32 v178, v178
v_mfma_f32_16x16x32_bf16 v[156:159], v[204:207], v[100:103], v[156:159]
v_exp_f32 v179, v179
s_waitcnt lgkmcnt(2)
v_mfma_f32_16x16x32_bf16 v[128:131], v[208:211], v[8:11], v[128:131]
ds_read_b128 v[204:207], v224 offset:2560
v_add_f32 v245, v245, v176
v_mfma_f32_16x16x32_bf16 v[136:139], v[208:211], v[40:43], v[136:139]
v_add_f32 v245, v245, v177
v_cvt_pk_bf16_f32 v176, v176, v177
v_mfma_f32_16x16x32_bf16 v[144:147], v[208:211], v[72:75], v[144:147]
buffer_load_dwordx4 v231, s[44:47], s89 offen offset:1024 nt lds
v_mfma_f32_16x16x32_bf16 v[152:155], v[208:211], v[104:107], v[152:155]
v_fma_f32 v180, v180, s62, -v241
v_fma_f32 v181, v181, s62, -v241
v_mfma_f32_16x16x32_bf16 v[132:135], v[212:215], v[8:11], v[132:135]
ds_read_b128 v[208:211], v224 offset:10752
v_exp_f32 v180, v180
v_mfma_f32_16x16x32_bf16 v[140:143], v[212:215], v[40:43], v[140:143]
v_exp_f32 v181, v181
v_mfma_f32_16x16x32_bf16 v[148:151], v[212:215], v[72:75], v[148:151]
v_add_f32 v245, v245, v178
v_add_f32 v245, v245, v179
v_mfma_f32_16x16x32_bf16 v[156:159], v[212:215], v[104:107], v[156:159]
v_cvt_pk_bf16_f32 v177, v178, v179
v_fma_f32 v182, v182, s62, -v241
v_mfma_f32_16x16x32_bf16 v[128:131], v[216:219], v[12:15], v[128:131]
ds_read_b128 v[212:215], v224 offset:3072
v_fma_f32 v183, v183, s62, -v241
v_mfma_f32_16x16x32_bf16 v[136:139], v[216:219], v[44:47], v[136:139]
v_exp_f32 v182, v182
v_mfma_f32_16x16x32_bf16 v[144:147], v[216:219], v[76:79], v[144:147]
buffer_load_dwordx4 v231, s[44:47], s90 offen offset:2048 nt lds
v_mfma_f32_16x16x32_bf16 v[152:155], v[216:219], v[108:111], v[152:155]
v_exp_f32 v183, v183
v_mfma_f32_16x16x32_bf16 v[132:135], v[192:195], v[12:15], v[132:135]
ds_read_b128 v[216:219], v224 offset:11264
v_add_f32 v245, v245, v180
v_mfma_f32_16x16x32_bf16 v[140:143], v[192:195], v[44:47], v[140:143]
v_add_f32 v245, v245, v181
v_cvt_pk_bf16_f32 v178, v180, v181
v_mfma_f32_16x16x32_bf16 v[148:151], v[192:195], v[76:79], v[148:151]
v_fma_f32 v184, v184, s62, -v242
v_fma_f32 v185, v185, s62, -v242
v_mfma_f32_16x16x32_bf16 v[156:159], v[192:195], v[108:111], v[156:159]
v_exp_f32 v184, v184
s_waitcnt lgkmcnt(2)
v_mfma_f32_16x16x32_bf16 v[128:131], v[196:199], v[16:19], v[128:131]
ds_read_b128 v[192:195], v224 offset:3584
v_exp_f32 v185, v185
v_mfma_f32_16x16x32_bf16 v[136:139], v[196:199], v[48:51], v[136:139]
buffer_load_dwordx4 v231, s[44:47], s91 offen offset:3072 nt lds
v_mfma_f32_16x16x32_bf16 v[144:147], v[196:199], v[80:83], v[144:147]
s_add_u32 s44, s44, 0x4000
s_addc_u32 s45, s45, 0
v_mfma_f32_16x16x32_bf16 v[152:155], v[196:199], v[112:115], v[152:155]
v_add_f32 v245, v245, v182
v_add_f32 v245, v245, v183
v_mfma_f32_16x16x32_bf16 v[132:135], v[200:203], v[16:19], v[132:135]
ds_read_b128 v[196:199], v224 offset:11776
v_cvt_pk_bf16_f32 v179, v182, v183
v_mfma_f32_16x16x32_bf16 v[140:143], v[200:203], v[48:51], v[140:143]
v_fma_f32 v186, v186, s62, -v242
v_fma_f32 v187, v187, s62, -v242
v_mfma_f32_16x16x32_bf16 v[148:151], v[200:203], v[80:83], v[148:151]
v_exp_f32 v186, v186
v_mfma_f32_16x16x32_bf16 v[156:159], v[200:203], v[112:115], v[156:159]
v_exp_f32 v187, v187
v_mfma_f32_16x16x32_bf16 v[128:131], v[204:207], v[20:23], v[128:131]
v_add_f32 v246, v246, v184
v_add_f32 v246, v246, v185
v_mfma_f32_16x16x32_bf16 v[136:139], v[204:207], v[52:55], v[136:139]
s_waitcnt vmcnt(24)
s_waitcnt lgkmcnt(0)
v_cvt_pk_bf16_f32 v184, v184, v185
v_mfma_f32_16x16x32_bf16 v[144:147], v[204:207], v[84:87], v[144:147]
s_barrier
v_add_u32 v225, 0x10000, v225
v_mfma_f32_16x16x32_bf16 v[152:155], v[204:207], v[116:119], v[152:155]
s_add_u32 m0, s60, 0x0
ds_read_b64_tr_b16 v[200:201], v225 offset:0
ds_read_b64_tr_b16 v[202:203], v225 offset:8192
v_mfma_f32_16x16x32_bf16 v[132:135], v[208:211], v[20:23], v[132:135]
v_add_u32 v226, 0x10000, v226
ds_read_b64_tr_b16 v[204:205], v226 offset:0
v_mfma_f32_16x16x32_bf16 v[140:143], v[208:211], v[52:55], v[140:143]
buffer_load_dwordx4 v232, s[48:51], s84 offen offset:0 nt lds
v_mfma_f32_16x16x32_bf16 v[148:151], v[208:211], v[84:87], v[148:151]
ds_read_b64_tr_b16 v[206:207], v226 offset:8192
v_fma_f32 v188, v188, s62, -v242
v_mfma_f32_16x16x32_bf16 v[156:159], v[208:211], v[116:119], v[156:159]
v_fma_f32 v189, v189, s62, -v242
v_mfma_f32_16x16x32_bf16 v[128:131], v[212:215], v[24:27], v[128:131]
ds_read_b64_tr_b16 v[208:209], v225 offset:512
v_exp_f32 v188, v188
v_mfma_f32_16x16x32_bf16 v[136:139], v[212:215], v[56:59], v[136:139]
ds_read_b64_tr_b16 v[210:211], v225 offset:8704
v_exp_f32 v189, v189
v_mfma_f32_16x16x32_bf16 v[144:147], v[212:215], v[88:91], v[144:147]
v_add_f32 v246, v246, v186
v_add_f32 v246, v246, v187
v_mfma_f32_16x16x32_bf16 v[152:155], v[212:215], v[120:123], v[152:155]
v_cvt_pk_bf16_f32 v185, v186, v187
v_fma_f32 v190, v190, s62, -v242
v_mfma_f32_16x16x32_bf16 v[132:135], v[216:219], v[24:27], v[132:135]
buffer_load_dwordx4 v232, s[48:51], s85 offen offset:1024 nt lds
v_mfma_f32_16x16x32_bf16 v[140:143], v[216:219], v[56:59], v[140:143]
v_fma_f32 v191, v191, s62, -v242
ds_read_b64_tr_b16 v[212:213], v226 offset:512
v_mfma_f32_16x16x32_bf16 v[148:151], v[216:219], v[88:91], v[148:151]
v_exp_f32 v190, v190
ds_read_b64_tr_b16 v[214:215], v226 offset:8704
v_mfma_f32_16x16x32_bf16 v[156:159], v[216:219], v[120:123], v[156:159]
v_exp_f32 v191, v191
v_mfma_f32_16x16x32_bf16 v[128:131], v[192:195], v[28:31], v[128:131]
v_add_f32 v246, v246, v188
v_add_f32 v246, v246, v189
v_mfma_f32_16x16x32_bf16 v[136:139], v[192:195], v[60:63], v[136:139]
v_cvt_pk_bf16_f32 v186, v188, v189
v_add_f32 v246, v246, v190
v_mfma_f32_16x16x32_bf16 v[144:147], v[192:195], v[92:95], v[144:147]
v_add_f32 v246, v246, v191
v_cvt_pk_bf16_f32 v187, v190, v191
v_mfma_f32_16x16x32_bf16 v[152:155], v[192:195], v[124:127], v[152:155]
ds_read_b64_tr_b16 v[216:217], v225 offset:1024
ds_read_b64_tr_b16 v[218:219], v225 offset:9216
v_mfma_f32_16x16x32_bf16 v[132:135], v[196:199], v[28:31], v[132:135]
buffer_load_dwordx4 v232, s[48:51], s86 offen offset:2048 nt lds
v_mfma_f32_16x16x32_bf16 v[140:143], v[196:199], v[60:63], v[140:143]
ds_read_b64_tr_b16 v[192:193], v226 offset:1024
ds_read_b64_tr_b16 v[194:195], v226 offset:9216
v_mfma_f32_16x16x32_bf16 v[148:151], v[196:199], v[92:95], v[148:151]
v_mfma_f32_16x16x32_bf16 v[156:159], v[196:199], v[124:127], v[156:159]
LST22:
s_waitcnt lgkmcnt(5)
v_mfma_f32_16x16x32_bf16 a[0:3], v[200:203], v[160:163], a[0:3]
ds_read_b64_tr_b16 v[196:197], v225 offset:1536
v_mfma_f32_16x16x32_bf16 a[4:7], v[200:203], v[168:171], a[4:7]
ds_read_b64_tr_b16 v[198:199], v225 offset:9728
v_mfma_f32_16x16x32_bf16 a[8:11], v[200:203], v[176:179], a[8:11]
v_max3_f32 v247, v128, v129, v130
v_max3_f32 v248, v136, v137, v138
v_mfma_f32_16x16x32_bf16 a[12:15], v[200:203], v[184:187], a[12:15]
buffer_load_dwordx4 v232, s[48:51], s87 offen offset:3072 nt lds
v_mfma_f32_16x16x32_bf16 a[16:19], v[204:207], v[160:163], a[16:19]
ds_read_b64_tr_b16 v[200:201], v226 offset:1536
v_max3_f32 v249, v144, v145, v146
v_mfma_f32_16x16x32_bf16 a[20:23], v[204:207], v[168:171], a[20:23]
ds_read_b64_tr_b16 v[202:203], v226 offset:9728
v_max3_f32 v250, v152, v153, v154
v_mfma_f32_16x16x32_bf16 a[24:27], v[204:207], v[176:179], a[24:27]
v_max3_f32 v247, v247, v131, v132
v_max3_f32 v248, v248, v139, v140
v_mfma_f32_16x16x32_bf16 a[28:31], v[204:207], v[184:187], a[28:31]
v_max3_f32 v249, v249, v147, v148
v_max3_f32 v250, v250, v155, v156
v_mfma_f32_16x16x32_bf16 a[32:35], v[208:211], v[160:163], a[32:35]
ds_read_b64_tr_b16 v[204:205], v225 offset:2048
v_max3_f32 v247, v247, v133, v134
v_mfma_f32_16x16x32_bf16 a[36:39], v[208:211], v[168:171], a[36:39]
s_add_u32 m0, s60, 0x2000
ds_read_b64_tr_b16 v[206:207], v225 offset:10240
v_max3_f32 v248, v248, v141, v142
v_mfma_f32_16x16x32_bf16 a[40:43], v[208:211], v[176:179], a[40:43]
v_max3_f32 v249, v249, v149, v150
v_max3_f32 v250, v250, v157, v158
v_mfma_f32_16x16x32_bf16 a[44:47], v[208:211], v[184:187], a[44:47]
buffer_load_dwordx4 v232, s[48:51], s88 offen offset:0 nt lds
s_waitcnt lgkmcnt(4)
v_mfma_f32_16x16x32_bf16 a[48:51], v[212:215], v[160:163], a[48:51]
ds_read_b64_tr_b16 v[208:209], v226 offset:2048
v_max_f32 v247, v247, v135
v_mfma_f32_16x16x32_bf16 a[52:55], v[212:215], v[168:171], a[52:55]
ds_read_b64_tr_b16 v[210:211], v226 offset:10240
v_max_f32 v248, v248, v143
v_mfma_f32_16x16x32_bf16 a[56:59], v[212:215], v[176:179], a[56:59]
v_max_f32 v249, v249, v151
v_max_f32 v250, v250, v159
v_mfma_f32_16x16x32_bf16 a[60:63], v[212:215], v[184:187], a[60:63]
v_permlane32_swap_b32 v247, v249
v_permlane32_swap_b32 v248, v250
v_mfma_f32_16x16x32_bf16 a[64:67], v[216:219], v[160:163], a[64:67]
ds_read_b64_tr_b16 v[212:213], v225 offset:2560
v_max_f32 v247, v247, v249
v_mfma_f32_16x16x32_bf16 a[68:71], v[216:219], v[168:171], a[68:71]
ds_read_b64_tr_b16 v[214:215], v225 offset:10752
v_max_f32 v248, v248, v250
v_mfma_f32_16x16x32_bf16 a[72:75], v[216:219], v[176:179], a[72:75]
buffer_load_dwordx4 v232, s[48:51], s89 offen offset:1024 nt lds
v_mfma_f32_16x16x32_bf16 a[76:79], v[216:219], v[184:187], a[76:79]
v_permlane16_swap_b32 v247, v248
v_max_f32 v247, v247, v248
v_mfma_f32_16x16x32_bf16 a[80:83], v[192:195], v[160:163], a[80:83]
ds_read_b64_tr_b16 v[216:217], v226 offset:2560
v_cmp_gt_f32 vcc, v247, v238
v_mfma_f32_16x16x32_bf16 a[84:87], v[192:195], v[168:171], a[84:87]
ds_read_b64_tr_b16 v[218:219], v226 offset:10752
s_cbranch_vccnz LR4
LRB4:
v_mfma_f32_16x16x32_bf16 a[88:91], v[192:195], v[176:179], a[88:91]
v_fma_f32 v128, v128, s62, -v239
v_fma_f32 v129, v129, s62, -v239
v_mfma_f32_16x16x32_bf16 a[92:95], v[192:195], v[184:187], a[92:95]
v_exp_f32 v128, v128
v_mfma_f32_16x16x32_bf16 a[96:99], v[196:199], v[160:163], a[96:99]
ds_read_b64_tr_b16 v[192:193], v225 offset:3072
v_exp_f32 v129, v129
v_mfma_f32_16x16x32_bf16 a[100:103], v[196:199], v[168:171], a[100:103]
ds_read_b64_tr_b16 v[194:195], v225 offset:11264
v_fma_f32 v130, v130, s62, -v239
v_mfma_f32_16x16x32_bf16 a[104:107], v[196:199], v[176:179], a[104:107]
buffer_load_dwordx4 v232, s[48:51], s90 offen offset:2048 nt lds
v_mfma_f32_16x16x32_bf16 a[108:111], v[196:199], v[184:187], a[108:111]
v_fma_f32 v131, v131, s62, -v239
s_waitcnt lgkmcnt(4)
v_mfma_f32_16x16x32_bf16 a[112:115], v[200:203], v[160:163], a[112:115]
ds_read_b64_tr_b16 v[196:197], v226 offset:3072
v_exp_f32 v130, v130
v_mfma_f32_16x16x32_bf16 a[116:119], v[200:203], v[168:171], a[116:119]
ds_read_b64_tr_b16 v[198:199], v226 offset:11264
v_exp_f32 v131, v131
v_mfma_f32_16x16x32_bf16 a[120:123], v[200:203], v[176:179], a[120:123]
v_add_f32 v243, v243, v128
v_add_f32 v243, v243, v129
v_mfma_f32_16x16x32_bf16 a[124:127], v[200:203], v[184:187], a[124:127]
v_cvt_pk_bf16_f32 v128, v128, v129
v_fma_f32 v132, v132, s62, -v239
v_mfma_f32_16x16x32_bf16 a[128:131], v[204:207], v[160:163], a[128:131]
ds_read_b64_tr_b16 v[200:201], v225 offset:3584
v_fma_f32 v133, v133, s62, -v239
v_mfma_f32_16x16x32_bf16 a[132:135], v[204:207], v[168:171], a[132:135]
buffer_load_dwordx4 v232, s[48:51], s91 offen offset:3072 nt lds
v_mfma_f32_16x16x32_bf16 a[136:139], v[204:207], v[176:179], a[136:139]
ds_read_b64_tr_b16 v[202:203], v225 offset:11776
v_exp_f32 v132, v132
v_mfma_f32_16x16x32_bf16 a[140:143], v[204:207], v[184:187], a[140:143]
s_add_u32 s48, s48, 0x4000
s_addc_u32 s49, s49, 0
v_mfma_f32_16x16x32_bf16 a[144:147], v[208:211], v[160:163], a[144:147]
ds_read_b64_tr_b16 v[204:205], v226 offset:3584
v_exp_f32 v133, v133
v_mfma_f32_16x16x32_bf16 a[148:151], v[208:211], v[168:171], a[148:151]
ds_read_b64_tr_b16 v[206:207], v226 offset:11776
v_add_f32 v243, v243, v130
v_mfma_f32_16x16x32_bf16 a[152:155], v[208:211], v[176:179], a[152:155]
v_add_f32 v243, v243, v131
v_cvt_pk_bf16_f32 v129, v130, v131
v_mfma_f32_16x16x32_bf16 a[156:159], v[208:211], v[184:187], a[156:159]
v_fma_f32 v134, v134, s62, -v239
v_fma_f32 v135, v135, s62, -v239
v_mfma_f32_16x16x32_bf16 a[160:163], v[212:215], v[160:163], a[160:163]
v_exp_f32 v134, v134
v_mfma_f32_16x16x32_bf16 a[164:167], v[212:215], v[168:171], a[164:167]
s_waitcnt vmcnt(24)
s_waitcnt lgkmcnt(0)
v_exp_f32 v135, v135
v_mfma_f32_16x16x32_bf16 a[168:171], v[212:215], v[176:179], a[168:171]
s_barrier
ds_read_b128 v[208:211], v224 offset:32768
v_mfma_f32_16x16x32_bf16 a[172:175], v[212:215], v[184:187], a[172:175]
s_add_u32 m0, s60, 0x20000
v_add_f32 v243, v243, v132
v_add_f32 v243, v243, v133
v_mfma_f32_16x16x32_bf16 a[176:179], v[216:219], v[160:163], a[176:179]
ds_read_b128 v[212:215], v224 offset:40960
v_cvt_pk_bf16_f32 v130, v132, v133
v_mfma_f32_16x16x32_bf16 a[180:183], v[216:219], v[168:171], a[180:183]
buffer_load_dwordx4 v231, s[44:47], s84 offen offset:0 nt lds
v_mfma_f32_16x16x32_bf16 a[184:187], v[216:219], v[176:179], a[184:187]
v_fma_f32 v136, v136, s62, -v240
v_fma_f32 v137, v137, s62, -v240
v_mfma_f32_16x16x32_bf16 a[188:191], v[216:219], v[184:187], a[188:191]
v_exp_f32 v136, v136
v_mfma_f32_16x16x32_bf16 a[192:195], v[192:195], v[160:163], a[192:195]
ds_read_b128 v[216:219], v224 offset:33280
v_exp_f32 v137, v137
v_mfma_f32_16x16x32_bf16 a[196:199], v[192:195], v[168:171], a[196:199]
v_add_f32 v243, v243, v134
v_add_f32 v243, v243, v135
v_mfma_f32_16x16x32_bf16 a[200:203], v[192:195], v[176:179], a[200:203]
v_cvt_pk_bf16_f32 v131, v134, v135
v_fma_f32 v138, v138, s62, -v240
v_mfma_f32_16x16x32_bf16 a[204:207], v[192:195], v[184:187], a[204:207]
v_fma_f32 v139, v139, s62, -v240
v_mfma_f32_16x16x32_bf16 a[208:211], v[196:199], v[160:163], a[208:211]
buffer_load_dwordx4 v231, s[44:47], s85 offen offset:1024 nt lds
v_mfma_f32_16x16x32_bf16 a[212:215], v[196:199], v[168:171], a[212:215]
ds_read_b128 v[192:195], v224 offset:41472
v_exp_f32 v138, v138
v_mfma_f32_16x16x32_bf16 a[216:219], v[196:199], v[176:179], a[216:219]
v_exp_f32 v139, v139
v_mfma_f32_16x16x32_bf16 a[220:223], v[196:199], v[184:187], a[220:223]
v_add_f32 v244, v244, v136
v_add_f32 v244, v244, v137
v_mfma_f32_16x16x32_bf16 a[224:227], v[200:203], v[160:163], a[224:227]
ds_read_b128 v[196:199], v224 offset:33792
v_cvt_pk_bf16_f32 v136, v136, v137
v_mfma_f32_16x16x32_bf16 a[228:231], v[200:203], v[168:171], a[228:231]
v_fma_f32 v140, v140, s62, -v240
v_fma_f32 v141, v141, s62, -v240
v_mfma_f32_16x16x32_bf16 a[232:235], v[200:203], v[176:179], a[232:235]
v_exp_f32 v140, v140
v_mfma_f32_16x16x32_bf16 a[236:239], v[200:203], v[184:187], a[236:239]
v_exp_f32 v141, v141
v_mfma_f32_16x16x32_bf16 a[240:243], v[204:207], v[160:163], a[240:243]
buffer_load_dwordx4 v231, s[44:47], s86 offen offset:2048 nt lds
v_mfma_f32_16x16x32_bf16 a[244:247], v[204:207], v[168:171], a[244:247]
ds_read_b128 v[200:203], v224 offset:41984
v_add_f32 v244, v244, v138
v_mfma_f32_16x16x32_bf16 a[248:251], v[204:207], v[176:179], a[248:251]
v_add_f32 v244, v244, v139
v_cvt_pk_bf16_f32 v137, v138, v139
v_mfma_f32_16x16x32_bf16 a[252:255], v[204:207], v[184:187], a[252:255]
LST23:
s_cmp_lg_u32 s63, 0
s_cbranch_scc1 LS4
LSB4:
s_waitcnt lgkmcnt(2)
v_mfma_f32_16x16x32_bf16 v[160:163], v[208:211], v[0:3], 0
ds_read_b128 v[204:207], v224 offset:34304
v_fma_f32 v142, v142, s62, -v240
v_mfma_f32_16x16x32_bf16 v[168:171], v[208:211], v[32:35], 0
v_fma_f32 v143, v143, s62, -v240
v_mfma_f32_16x16x32_bf16 v[176:179], v[208:211], v[64:67], 0
v_exp_f32 v142, v142
v_mfma_f32_16x16x32_bf16 v[184:187], v[208:211], v[96:99], 0
buffer_load_dwordx4 v231, s[44:47], s87 offen offset:3072 nt lds
v_mfma_f32_16x16x32_bf16 v[164:167], v[212:215], v[0:3], 0
ds_read_b128 v[208:211], v224 offset:42496
v_exp_f32 v143, v143
v_mfma_f32_16x16x32_bf16 v[172:175], v[212:215], v[32:35], 0
v_add_f32 v244, v244, v140
v_add_f32 v244, v244, v141
v_mfma_f32_16x16x32_bf16 v[180:183], v[212:215], v[64:67], 0
v_cvt_pk_bf16_f32 v138, v140, v141
v_fma_f32 v144, v144, s62, -v241
v_mfma_f32_16x16x32_bf16 v[188:191], v[212:215], v[96:99], 0
v_fma_f32 v145, v145, s62, -v241
v_mfma_f32_16x16x32_bf16 v[160:163], v[216:219], v[4:7], v[160:163]
ds_read_b128 v[212:215], v224 offset:34816
v_exp_f32 v144, v144
v_mfma_f32_16x16x32_bf16 v[168:171], v[216:219], v[36:39], v[168:171]
s_add_u32 m0, s60, 0x22000
v_exp_f32 v145, v145
v_mfma_f32_16x16x32_bf16 v[176:179], v[216:219], v[68:71], v[176:179]
v_add_f32 v244, v244, v142
v_add_f32 v244, v244, v143
v_mfma_f32_16x16x32_bf16 v[184:187], v[216:219], v[100:103], v[184:187]
buffer_load_dwordx4 v231, s[44:47], s88 offen offset:0 nt lds
v_mfma_f32_16x16x32_bf16 v[164:167], v[192:195], v[4:7], v[164:167]
ds_read_b128 v[216:219], v224 offset:43008
v_cvt_pk_bf16_f32 v139, v142, v143
v_mfma_f32_16x16x32_bf16 v[172:175], v[192:195], v[36:39], v[172:175]
v_fma_f32 v146, v146, s62, -v241
v_fma_f32 v147, v147, s62, -v241
v_mfma_f32_16x16x32_bf16 v[180:183], v[192:195], v[68:71], v[180:183]
v_exp_f32 v146, v146
v_mfma_f32_16x16x32_bf16 v[188:191], v[192:195], v[100:103], v[188:191]
v_exp_f32 v147, v147
s_waitcnt lgkmcnt(2)
v_mfma_f32_16x16x32_bf16 v[160:163], v[196:199], v[8:11], v[160:163]
ds_read_b128 v[192:195], v224 offset:35328
v_add_f32 v245, v245, v144
v_mfma_f32_16x16x32_bf16 v[168:171], v[196:199], v[40:43], v[168:171]
v_add_f32 v245, v245, v145
v_cvt_pk_bf16_f32 v144, v144, v145
v_mfma_f32_16x16x32_bf16 v[176:179], v[196:199], v[72:75], v[176:179]
buffer_load_dwordx4 v231, s[44:47], s89 offen offset:1024 nt lds
v_mfma_f32_16x16x32_bf16 v[184:187], v[196:199], v[104:107], v[184:187]
v_fma_f32 v148, v148, s62, -v241
v_fma_f32 v149, v149, s62, -v241
v_mfma_f32_16x16x32_bf16 v[164:167], v[200:203], v[8:11], v[164:167]
ds_read_b128 v[196:199], v224 offset:43520
v_exp_f32 v148, v148
v_mfma_f32_16x16x32_bf16 v[172:175], v[200:203], v[40:43], v[172:175]
v_exp_f32 v149, v149
v_mfma_f32_16x16x32_bf16 v[180:183], v[200:203], v[72:75], v[180:183]
v_add_f32 v245, v245, v146
v_add_f32 v245, v245, v147
v_mfma_f32_16x16x32_bf16 v[188:191], v[200:203], v[104:107], v[188:191]
v_cvt_pk_bf16_f32 v145, v146, v147
v_fma_f32 v150, v150, s62, -v241
v_mfma_f32_16x16x32_bf16 v[160:163], v[204:207], v[12:15], v[160:163]
ds_read_b128 v[200:203], v224 offset:35840
v_fma_f32 v151, v151, s62, -v241
v_mfma_f32_16x16x32_bf16 v[168:171], v[204:207], v[44:47], v[168:171]
v_exp_f32 v150, v150
v_mfma_f32_16x16x32_bf16 v[176:179], v[204:207], v[76:79], v[176:179]
buffer_load_dwordx4 v231, s[44:47], s90 offen offset:2048 nt lds
v_mfma_f32_16x16x32_bf16 v[184:187], v[204:207], v[108:111], v[184:187]
v_exp_f32 v151, v151
v_mfma_f32_16x16x32_bf16 v[164:167], v[208:211], v[12:15], v[164:167]
ds_read_b128 v[204:207], v224 offset:44032
v_add_f32 v245, v245, v148
v_mfma_f32_16x16x32_bf16 v[172:175], v[208:211], v[44:47], v[172:175]
v_add_f32 v245, v245, v149
v_cvt_pk_bf16_f32 v146, v148, v149
v_mfma_f32_16x16x32_bf16 v[180:183], v[208:211], v[76:79], v[180:183]
v_fma_f32 v152, v152, s62, -v242
v_fma_f32 v153, v153, s62, -v242
v_mfma_f32_16x16x32_bf16 v[188:191], v[208:211], v[108:111], v[188:191]
v_exp_f32 v152, v152
s_waitcnt lgkmcnt(2)
v_mfma_f32_16x16x32_bf16 v[160:163], v[212:215], v[16:19], v[160:163]
ds_read_b128 v[208:211], v224 offset:36352
v_exp_f32 v153, v153
v_mfma_f32_16x16x32_bf16 v[168:171], v[212:215], v[48:51], v[168:171]
buffer_load_dwordx4 v231, s[44:47], s91 offen offset:3072 nt lds
v_mfma_f32_16x16x32_bf16 v[176:179], v[212:215], v[80:83], v[176:179]
s_add_u32 s44, s44, 0x4000
s_addc_u32 s45, s45, 0
v_mfma_f32_16x16x32_bf16 v[184:187], v[212:215], v[112:115], v[184:187]
v_add_f32 v245, v245, v150
v_add_f32 v245, v245, v151
v_mfma_f32_16x16x32_bf16 v[164:167], v[216:219], v[16:19], v[164:167]
ds_read_b128 v[212:215], v224 offset:44544
v_cvt_pk_bf16_f32 v147, v150, v151
v_mfma_f32_16x16x32_bf16 v[172:175], v[216:219], v[48:51], v[172:175]
v_fma_f32 v154, v154, s62, -v242
v_fma_f32 v155, v155, s62, -v242
v_mfma_f32_16x16x32_bf16 v[180:183], v[216:219], v[80:83], v[180:183]
v_exp_f32 v154, v154
v_mfma_f32_16x16x32_bf16 v[188:191], v[216:219], v[112:115], v[188:191]
v_exp_f32 v155, v155
v_mfma_f32_16x16x32_bf16 v[160:163], v[192:195], v[20:23], v[160:163]
v_add_f32 v246, v246, v152
v_add_f32 v246, v246, v153
v_mfma_f32_16x16x32_bf16 v[168:171], v[192:195], v[52:55], v[168:171]
s_waitcnt vmcnt(24)
s_waitcnt lgkmcnt(0)
v_cvt_pk_bf16_f32 v152, v152, v153
v_mfma_f32_16x16x32_bf16 v[176:179], v[192:195], v[84:87], v[176:179]
s_barrier
v_add_u32 v225, 0xffff0000, v225
LH6:
s_cbranch_execz LH10
v_mfma_f32_16x16x32_bf16 v[184:187], v[192:195], v[116:119], v[184:187]
s_add_u32 m0, s60, 0x8000
ds_read_b64_tr_b16 v[216:217], v225 offset:0
ds_read_b64_tr_b16 v[218:219], v225 offset:8192
v_mfma_f32_16x16x32_bf16 v[164:167], v[196:199], v[20:23], v[164:167]
v_add_u32 v226, 0xffff0000, v226
ds_read_b64_tr_b16 v[192:193], v226 offset:0
v_mfma_f32_16x16x32_bf16 v[172:175], v[196:199], v[52:55], v[172:175]
buffer_load_dwordx4 v232, s[48:51], s84 offen offset:0 nt lds
v_mfma_f32_16x16x32_bf16 v[180:183], v[196:199], v[84:87], v[180:183]
ds_read_b64_tr_b16 v[194:195], v226 offset:8192
v_fma_f32 v156, v156, s62, -v242
v_mfma_f32_16x16x32_bf16 v[188:191], v[196:199], v[116:119], v[188:191]
v_fma_f32 v157, v157, s62, -v242
v_mfma_f32_16x16x32_bf16 v[160:163], v[200:203], v[24:27], v[160:163]
ds_read_b64_tr_b16 v[196:197], v225 offset:512
v_exp_f32 v156, v156
v_mfma_f32_16x16x32_bf16 v[168:171], v[200:203], v[56:59], v[168:171]
ds_read_b64_tr_b16 v[198:199], v225 offset:8704
v_exp_f32 v157, v157
v_mfma_f32_16x16x32_bf16 v[176:179], v[200:203], v[88:91], v[176:179]
v_add_f32 v246, v246, v154
v_add_f32 v246, v246, v155
v_mfma_f32_16x16x32_bf16 v[184:187], v[200:203], v[120:123], v[184:187]
v_cvt_pk_bf16_f32 v153, v154, v155
v_fma_f32 v158, v158, s62, -v242
v_mfma_f32_16x16x32_bf16 v[164:167], v[204:207], v[24:27], v[164:167]
buffer_load_dwordx4 v232, s[48:51], s85 offen offset:1024 nt lds
v_mfma_f32_16x16x32_bf16 v[172:175], v[204:207], v[56:59], v[172:175]
v_fma_f32 v159, v159, s62, -v242
ds_read_b64_tr_b16 v[200:201], v226 offset:512
v_mfma_f32_16x16x32_bf16 v[180:183], v[204:207], v[88:91], v[180:183]
v_exp_f32 v158, v158
ds_read_b64_tr_b16 v[202:203], v226 offset:8704
v_mfma_f32_16x16x32_bf16 v[188:191], v[204:207], v[120:123], v[188:191]
v_exp_f32 v159, v159
v_mfma_f32_16x16x32_bf16 v[160:163], v[208:211], v[28:31], v[160:163]
v_add_f32 v246, v246, v156
v_add_f32 v246, v246, v157
v_mfma_f32_16x16x32_bf16 v[168:171], v[208:211], v[60:63], v[168:171]
v_cvt_pk_bf16_f32 v154, v156, v157
v_add_f32 v246, v246, v158
v_mfma_f32_16x16x32_bf16 v[176:179], v[208:211], v[92:95], v[176:179]
v_add_f32 v246, v246, v159
v_cvt_pk_bf16_f32 v155, v158, v159
v_mfma_f32_16x16x32_bf16 v[184:187], v[208:211], v[124:127], v[184:187]
ds_read_b64_tr_b16 v[204:205], v225 offset:1024
ds_read_b64_tr_b16 v[206:207], v225 offset:9216
v_mfma_f32_16x16x32_bf16 v[164:167], v[212:215], v[28:31], v[164:167]
buffer_load_dwordx4 v232, s[48:51], s86 offen offset:2048 nt lds
v_mfma_f32_16x16x32_bf16 v[172:175], v[212:215], v[60:63], v[172:175]
ds_read_b64_tr_b16 v[208:209], v226 offset:1024
ds_read_b64_tr_b16 v[210:211], v226 offset:9216
v_mfma_f32_16x16x32_bf16 v[180:183], v[212:215], v[92:95], v[180:183]
v_mfma_f32_16x16x32_bf16 v[188:191], v[212:215], v[124:127], v[188:191]
LST24:
s_waitcnt lgkmcnt(5)
v_mfma_f32_16x16x32_bf16 a[0:3], v[216:219], v[128:131], a[0:3]
ds_read_b64_tr_b16 v[212:213], v225 offset:1536
v_mfma_f32_16x16x32_bf16 a[4:7], v[216:219], v[136:139], a[4:7]
ds_read_b64_tr_b16 v[214:215], v225 offset:9728
v_mfma_f32_16x16x32_bf16 a[8:11], v[216:219], v[144:147], a[8:11]
v_max3_f32 v247, v160, v161, v162
v_max3_f32 v248, v168, v169, v170
v_mfma_f32_16x16x32_bf16 a[12:15], v[216:219], v[152:155], a[12:15]
buffer_load_dwordx4 v232, s[48:51], s87 offen offset:3072 nt lds
v_mfma_f32_16x16x32_bf16 a[16:19], v[192:195], v[128:131], a[16:19]
ds_read_b64_tr_b16 v[216:217], v226 offset:1536
v_max3_f32 v249, v176, v177, v178
v_mfma_f32_16x16x32_bf16 a[20:23], v[192:195], v[136:139], a[20:23]
ds_read_b64_tr_b16 v[218:219], v226 offset:9728
v_max3_f32 v250, v184, v185, v186
v_mfma_f32_16x16x32_bf16 a[24:27], v[192:195], v[144:147], a[24:27]
v_max3_f32 v247, v247, v163, v164
v_max3_f32 v248, v248, v171, v172
v_mfma_f32_16x16x32_bf16 a[28:31], v[192:195], v[152:155], a[28:31]
v_max3_f32 v249, v249, v179, v180
v_max3_f32 v250, v250, v187, v188
v_mfma_f32_16x16x32_bf16 a[32:35], v[196:199], v[128:131], a[32:35]
ds_read_b64_tr_b16 v[192:193], v225 offset:2048
v_max3_f32 v247, v247, v165, v166
v_mfma_f32_16x16x32_bf16 a[36:39], v[196:199], v[136:139], a[36:39]
s_add_u32 m0, s60, 0xa000
ds_read_b64_tr_b16 v[194:195], v225 offset:10240
v_max3_f32 v248, v248, v173, v174
v_mfma_f32_16x16x32_bf16 a[40:43], v[196:199], v[144:147], a[40:43]
v_max3_f32 v249, v249, v181, v182
v_max3_f32 v250, v250, v189, v190
v_mfma_f32_16x16x32_bf16 a[44:47], v[196:199], v[152:155], a[44:47]
buffer_load_dwordx4 v232, s[48:51], s88 offen offset:0 nt lds
s_waitcnt lgkmcnt(4)
v_mfma_f32_16x16x32_bf16 a[48:51], v[200:203], v[128:131], a[48:51]
ds_read_b64_tr_b16 v[196:197], v226 offset:2048
v_max_f32 v247, v247, v167
v_mfma_f32_16x16x32_bf16 a[52:55], v[200:203], v[136:139], a[52:55]
ds_read_b64_tr_b16 v[198:199], v226 offset:10240
v_max_f32 v248, v248, v175
v_mfma_f32_16x16x32_bf16 a[56:59], v[200:203], v[144:147], a[56:59]
v_max_f32 v249, v249, v183
v_max_f32 v250, v250, v191
v_mfma_f32_16x16x32_bf16 a[60:63], v[200:203], v[152:155], a[60:63]
v_permlane32_swap_b32 v247, v249
v_permlane32_swap_b32 v248, v250
v_mfma_f32_16x16x32_bf16 a[64:67], v[204:207], v[128:131], a[64:67]
ds_read_b64_tr_b16 v[200:201], v225 offset:2560
v_max_f32 v247, v247, v249
v_mfma_f32_16x16x32_bf16 a[68:71], v[204:207], v[136:139], a[68:71]
ds_read_b64_tr_b16 v[202:203], v225 offset:10752
v_max_f32 v248, v248, v250
v_mfma_f32_16x16x32_bf16 a[72:75], v[204:207], v[144:147], a[72:75]
buffer_load_dwordx4 v232, s[48:51], s89 offen offset:1024 nt lds
v_mfma_f32_16x16x32_bf16 a[76:79], v[204:207], v[152:155], a[76:79]
v_permlane16_swap_b32 v247, v248
v_max_f32 v247, v247, v248
v_mfma_f32_16x16x32_bf16 a[80:83], v[208:211], v[128:131], a[80:83]
ds_read_b64_tr_b16 v[204:205], v226 offset:2560
v_cmp_gt_f32 vcc, v247, v238
v_mfma_f32_16x16x32_bf16 a[84:87], v[208:211], v[136:139], a[84:87]
ds_read_b64_tr_b16 v[206:207], v226 offset:10752
s_cbranch_vccnz LR5
LRB5:
v_mfma_f32_16x16x32_bf16 a[88:91], v[208:211], v[144:147], a[88:91]
v_fma_f32 v160, v160, s62, -v239
v_fma_f32 v161, v161, s62, -v239
v_mfma_f32_16x16x32_bf16 a[92:95], v[208:211], v[152:155], a[92:95]
v_exp_f32 v160, v160
v_mfma_f32_16x16x32_bf16 a[96:99], v[212:215], v[128:131], a[96:99]
ds_read_b64_tr_b16 v[208:209], v225 offset:3072
v_exp_f32 v161, v161
v_mfma_f32_16x16x32_bf16 a[100:103], v[212:215], v[136:139], a[100:103]
ds_read_b64_tr_b16 v[210:211], v225 offset:11264
v_fma_f32 v162, v162, s62, -v239
v_mfma_f32_16x16x32_bf16 a[104:107], v[212:215], v[144:147], a[104:107]
buffer_load_dwordx4 v232, s[48:51], s90 offen offset:2048 nt lds
v_mfma_f32_16x16x32_bf16 a[108:111], v[212:215], v[152:155], a[108:111]
v_fma_f32 v163, v163, s62, -v239
s_waitcnt lgkmcnt(4)
v_mfma_f32_16x16x32_bf16 a[112:115], v[216:219], v[128:131], a[112:115]
ds_read_b64_tr_b16 v[212:213], v226 offset:3072
v_exp_f32 v162, v162
v_mfma_f32_16x16x32_bf16 a[116:119], v[216:219], v[136:139], a[116:119]
ds_read_b64_tr_b16 v[214:215], v226 offset:11264
v_exp_f32 v163, v163
v_mfma_f32_16x16x32_bf16 a[120:123], v[216:219], v[144:147], a[120:123]
v_add_f32 v243, v243, v160
v_add_f32 v243, v243, v161
v_mfma_f32_16x16x32_bf16 a[124:127], v[216:219], v[152:155], a[124:127]
v_cvt_pk_bf16_f32 v160, v160, v161
v_fma_f32 v164, v164, s62, -v239
v_mfma_f32_16x16x32_bf16 a[128:131], v[192:195], v[128:131], a[128:131]
ds_read_b64_tr_b16 v[216:217], v225 offset:3584
v_fma_f32 v165, v165, s62, -v239
v_mfma_f32_16x16x32_bf16 a[132:135], v[192:195], v[136:139], a[132:135]
buffer_load_dwordx4 v232, s[48:51], s91 offen offset:3072 nt lds
v_mfma_f32_16x16x32_bf16 a[136:139], v[192:195], v[144:147], a[136:139]
ds_read_b64_tr_b16 v[218:219], v225 offset:11776
v_exp_f32 v164, v164
v_mfma_f32_16x16x32_bf16 a[140:143], v[192:195], v[152:155], a[140:143]
s_add_u32 s48, s48, 0x4000
s_addc_u32 s49, s49, 0
v_mfma_f32_16x16x32_bf16 a[144:147], v[196:199], v[128:131], a[144:147]
ds_read_b64_tr_b16 v[192:193], v226 offset:3584
v_exp_f32 v165, v165
v_mfma_f32_16x16x32_bf16 a[148:151], v[196:199], v[136:139], a[148:151]
ds_read_b64_tr_b16 v[194:195], v226 offset:11776
v_add_f32 v243, v243, v162
v_mfma_f32_16x16x32_bf16 a[152:155], v[196:199], v[144:147], a[152:155]
v_add_f32 v243, v243, v163
v_cvt_pk_bf16_f32 v161, v162, v163
v_mfma_f32_16x16x32_bf16 a[156:159], v[196:199], v[152:155], a[156:159]
v_fma_f32 v166, v166, s62, -v239
v_fma_f32 v167, v167, s62, -v239
v_mfma_f32_16x16x32_bf16 a[160:163], v[200:203], v[128:131], a[160:163]
v_exp_f32 v166, v166
v_mfma_f32_16x16x32_bf16 a[164:167], v[200:203], v[136:139], a[164:167]
s_waitcnt vmcnt(24)
s_waitcnt lgkmcnt(0)
v_exp_f32 v167, v167
v_mfma_f32_16x16x32_bf16 a[168:171], v[200:203], v[144:147], a[168:171]
s_barrier
v_add_u32 v224, 0x10000, v224
v_mfma_f32_16x16x32_bf16 a[172:175], v[200:203], v[152:155], a[172:175]
s_add_u32 m0, s60, 0x10000
ds_read_b128 v[196:199], v224 offset:32768
v_add_f32 v243, v243, v164
v_mfma_f32_16x16x32_bf16 a[176:179], v[204:207], v[128:131], a[176:179]
ds_read_b128 v[200:203], v224 offset:40960
v_add_f32 v243, v243, v165
v_mfma_f32_16x16x32_bf16 a[180:183], v[204:207], v[136:139], a[180:183]
buffer_load_dwordx4 v231, s[44:47], s84 offen offset:0 nt lds
v_mfma_f32_16x16x32_bf16 a[184:187], v[204:207], v[144:147], a[184:187]
v_cvt_pk_bf16_f32 v162, v164, v165
v_fma_f32 v168, v168, s62, -v240
v_mfma_f32_16x16x32_bf16 a[188:191], v[204:207], v[152:155], a[188:191]
v_fma_f32 v169, v169, s62, -v240
v_mfma_f32_16x16x32_bf16 a[192:195], v[208:211], v[128:131], a[192:195]
ds_read_b128 v[204:207], v224 offset:33280
v_exp_f32 v168, v168
v_mfma_f32_16x16x32_bf16 a[196:199], v[208:211], v[136:139], a[196:199]
v_exp_f32 v169, v169
v_mfma_f32_16x16x32_bf16 a[200:203], v[208:211], v[144:147], a[200:203]
v_add_f32 v243, v243, v166
v_add_f32 v243, v243, v167
v_mfma_f32_16x16x32_bf16 a[204:207], v[208:211], v[152:155], a[204:207]
v_cvt_pk_bf16_f32 v163, v166, v167
v_fma_f32 v170, v170, s62, -v240
v_mfma_f32_16x16x32_bf16 a[208:211], v[212:215], v[128:131], a[208:211]
buffer_load_dwordx4 v231, s[44:47], s85 offen offset:1024 nt lds
v_mfma_f32_16x16x32_bf16 a[212:215], v[212:215], v[136:139], a[212:215]
ds_read_b128 v[208:211], v224 offset:41472
v_fma_f32 v171, v171, s62, -v240
v_mfma_f32_16x16x32_bf16 a[216:219], v[212:215], v[144:147], a[216:219]
v_exp_f32 v170, v170
v_mfma_f32_16x16x32_bf16 a[220:223], v[212:215], v[152:155], a[220:223]
v_exp_f32 v171, v171
v_mfma_f32_16x16x32_bf16 a[224:227], v[216:219], v[128:131], a[224:227]
ds_read_b128 v[212:215], v224 offset:33792
v_add_f32 v244, v244, v168
v_mfma_f32_16x16x32_bf16 a[228:231], v[216:219], v[136:139], a[228:231]
v_add_f32 v244, v244, v169
v_cvt_pk_bf16_f32 v168, v168, v169
v_mfma_f32_16x16x32_bf16 a[232:235], v[216:219], v[144:147], a[232:235]
v_fma_f32 v172, v172, s62, -v240
v_fma_f32 v173, v173, s62, -v240
v_mfma_f32_16x16x32_bf16 a[236:239], v[216:219], v[152:155], a[236:239]
v_exp_f32 v172, v172
v_mfma_f32_16x16x32_bf16 a[240:243], v[192:195], v[128:131], a[240:243]
buffer_load_dwordx4 v231, s[44:47], s86 offen offset:2048 nt lds
v_mfma_f32_16x16x32_bf16 a[244:247], v[192:195], v[136:139], a[244:247]
ds_read_b128 v[216:219], v224 offset:41984
v_exp_f32 v173, v173
v_mfma_f32_16x16x32_bf16 a[248:251], v[192:195], v[144:147], a[248:251]
v_add_f32 v244, v244, v170
v_add_f32 v244, v244, v171
v_mfma_f32_16x16x32_bf16 a[252:255], v[192:195], v[152:155], a[252:255]
LST25:
s_cmp_lg_u32 s63, 0
s_cbranch_scc1 LS5
LSB5:
s_waitcnt lgkmcnt(2)
v_mfma_f32_16x16x32_bf16 v[128:131], v[196:199], v[0:3], 0
ds_read_b128 v[192:195], v224 offset:34304
v_cvt_pk_bf16_f32 v169, v170, v171
v_mfma_f32_16x16x32_bf16 v[136:139], v[196:199], v[32:35], 0
v_fma_f32 v174, v174, s62, -v240
v_fma_f32 v175, v175, s62, -v240
v_mfma_f32_16x16x32_bf16 v[144:147], v[196:199], v[64:67], 0
v_exp_f32 v174, v174
v_mfma_f32_16x16x32_bf16 v[152:155], v[196:199], v[96:99], 0
buffer_load_dwordx4 v231, s[44:47], s87 offen offset:3072 nt lds
v_mfma_f32_16x16x32_bf16 v[132:135], v[200:203], v[0:3], 0
ds_read_b128 v[196:199], v224 offset:42496
v_exp_f32 v175, v175
v_mfma_f32_16x16x32_bf16 v[140:143], v[200:203], v[32:35], 0
v_add_f32 v244, v244, v172
v_add_f32 v244, v244, v173
v_mfma_f32_16x16x32_bf16 v[148:151], v[200:203], v[64:67], 0
v_cvt_pk_bf16_f32 v170, v172, v173
v_fma_f32 v176, v176, s62, -v241
v_mfma_f32_16x16x32_bf16 v[156:159], v[200:203], v[96:99], 0
v_fma_f32 v177, v177, s62, -v241
v_mfma_f32_16x16x32_bf16 v[128:131], v[204:207], v[4:7], v[128:131]
ds_read_b128 v[200:203], v224 offset:34816
v_exp_f32 v176, v176
v_mfma_f32_16x16x32_bf16 v[136:139], v[204:207], v[36:39], v[136:139]
s_add_u32 m0, s60, 0x12000
v_exp_f32 v177, v177
v_mfma_f32_16x16x32_bf16 v[144:147], v[204:207], v[68:71], v[144:147]
v_add_f32 v244, v244, v174
v_add_f32 v244, v244, v175
v_mfma_f32_16x16x32_bf16 v[152:155], v[204:207], v[100:103], v[152:155]
buffer_load_dwordx4 v231, s[44:47], s88 offen offset:0 nt lds
v_mfma_f32_16x16x32_bf16 v[132:135], v[208:211], v[4:7], v[132:135]
ds_read_b128 v[204:207], v224 offset:43008
v_cvt_pk_bf16_f32 v171, v174, v175
v_mfma_f32_16x16x32_bf16 v[140:143], v[208:211], v[36:39], v[140:143]
v_fma_f32 v178, v178, s62, -v241
v_fma_f32 v179, v179, s62, -v241
v_mfma_f32_16x16x32_bf16 v[148:151], v[208:211], v[68:71], v[148:151]
v_exp_f32 v178, v178
v_mfma_f32_16x16x32_bf16 v[156:159], v[208:211], v[100:103], v[156:159]
v_exp_f32 v179, v179
s_waitcnt lgkmcnt(2)
v_mfma_f32_16x16x32_bf16 v[128:131], v[212:215], v[8:11], v[128:131]
ds_read_b128 v[208:211], v224 offset:35328
v_add_f32 v245, v245, v176
v_mfma_f32_16x16x32_bf16 v[136:139], v[212:215], v[40:43], v[136:139]
v_add_f32 v245, v245, v177
v_cvt_pk_bf16_f32 v176, v176, v177
v_mfma_f32_16x16x32_bf16 v[144:147], v[212:215], v[72:75], v[144:147]
buffer_load_dwordx4 v231, s[44:47], s89 offen offset:1024 nt lds
v_mfma_f32_16x16x32_bf16 v[152:155], v[212:215], v[104:107], v[152:155]
v_fma_f32 v180, v180, s62, -v241
v_fma_f32 v181, v181, s62, -v241
v_mfma_f32_16x16x32_bf16 v[132:135], v[216:219], v[8:11], v[132:135]
ds_read_b128 v[212:215], v224 offset:43520
v_exp_f32 v180, v180
v_mfma_f32_16x16x32_bf16 v[140:143], v[216:219], v[40:43], v[140:143]
v_exp_f32 v181, v181
v_mfma_f32_16x16x32_bf16 v[148:151], v[216:219], v[72:75], v[148:151]
v_add_f32 v245, v245, v178
v_add_f32 v245, v245, v179
v_mfma_f32_16x16x32_bf16 v[156:159], v[216:219], v[104:107], v[156:159]
v_cvt_pk_bf16_f32 v177, v178, v179
v_fma_f32 v182, v182, s62, -v241
v_mfma_f32_16x16x32_bf16 v[128:131], v[192:195], v[12:15], v[128:131]
ds_read_b128 v[216:219], v224 offset:35840
v_fma_f32 v183, v183, s62, -v241
v_mfma_f32_16x16x32_bf16 v[136:139], v[192:195], v[44:47], v[136:139]
v_exp_f32 v182, v182
v_mfma_f32_16x16x32_bf16 v[144:147], v[192:195], v[76:79], v[144:147]
buffer_load_dwordx4 v231, s[44:47], s90 offen offset:2048 nt lds
v_mfma_f32_16x16x32_bf16 v[152:155], v[192:195], v[108:111], v[152:155]
v_exp_f32 v183, v183
v_mfma_f32_16x16x32_bf16 v[132:135], v[196:199], v[12:15], v[132:135]
ds_read_b128 v[192:195], v224 offset:44032
v_add_f32 v245, v245, v180
v_mfma_f32_16x16x32_bf16 v[140:143], v[196:199], v[44:47], v[140:143]
v_add_f32 v245, v245, v181
v_cvt_pk_bf16_f32 v178, v180, v181
v_mfma_f32_16x16x32_bf16 v[148:151], v[196:199], v[76:79], v[148:151]
v_fma_f32 v184, v184, s62, -v242
v_fma_f32 v185, v185, s62, -v242
v_mfma_f32_16x16x32_bf16 v[156:159], v[196:199], v[108:111], v[156:159]
v_exp_f32 v184, v184
s_waitcnt lgkmcnt(2)
v_mfma_f32_16x16x32_bf16 v[128:131], v[200:203], v[16:19], v[128:131]
ds_read_b128 v[196:199], v224 offset:36352
v_exp_f32 v185, v185
v_mfma_f32_16x16x32_bf16 v[136:139], v[200:203], v[48:51], v[136:139]
buffer_load_dwordx4 v231, s[44:47], s91 offen offset:3072 nt lds
v_mfma_f32_16x16x32_bf16 v[144:147], v[200:203], v[80:83], v[144:147]
s_add_u32 s44, s44, 0x4000
s_addc_u32 s45, s45, 0
v_mfma_f32_16x16x32_bf16 v[152:155], v[200:203], v[112:115], v[152:155]
v_add_f32 v245, v245, v182
v_add_f32 v245, v245, v183
v_mfma_f32_16x16x32_bf16 v[132:135], v[204:207], v[16:19], v[132:135]
ds_read_b128 v[200:203], v224 offset:44544
v_cvt_pk_bf16_f32 v179, v182, v183
v_mfma_f32_16x16x32_bf16 v[140:143], v[204:207], v[48:51], v[140:143]
v_fma_f32 v186, v186, s62, -v242
v_fma_f32 v187, v187, s62, -v242
v_mfma_f32_16x16x32_bf16 v[148:151], v[204:207], v[80:83], v[148:151]
v_exp_f32 v186, v186
v_mfma_f32_16x16x32_bf16 v[156:159], v[204:207], v[112:115], v[156:159]
v_exp_f32 v187, v187
v_mfma_f32_16x16x32_bf16 v[128:131], v[208:211], v[20:23], v[128:131]
v_add_f32 v246, v246, v184
v_add_f32 v246, v246, v185
v_mfma_f32_16x16x32_bf16 v[136:139], v[208:211], v[52:55], v[136:139]
s_waitcnt vmcnt(24)
s_waitcnt lgkmcnt(0)
v_cvt_pk_bf16_f32 v184, v184, v185
v_mfma_f32_16x16x32_bf16 v[144:147], v[208:211], v[84:87], v[144:147]
s_barrier
v_add_u32 v225, 0xffff0000, v225
v_mfma_f32_16x16x32_bf16 v[152:155], v[208:211], v[116:119], v[152:155]
s_add_u32 m0, s60, 0x18000
ds_read_b64_tr_b16 v[204:205], v225 offset:0
ds_read_b64_tr_b16 v[206:207], v225 offset:8192
v_mfma_f32_16x16x32_bf16 v[132:135], v[212:215], v[20:23], v[132:135]
v_add_u32 v226, 0xffff0000, v226
ds_read_b64_tr_b16 v[208:209], v226 offset:0
v_mfma_f32_16x16x32_bf16 v[140:143], v[212:215], v[52:55], v[140:143]
buffer_load_dwordx4 v232, s[48:51], s84 offen offset:0 nt lds
v_mfma_f32_16x16x32_bf16 v[148:151], v[212:215], v[84:87], v[148:151]
ds_read_b64_tr_b16 v[210:211], v226 offset:8192
v_fma_f32 v188, v188, s62, -v242
v_mfma_f32_16x16x32_bf16 v[156:159], v[212:215], v[116:119], v[156:159]
v_fma_f32 v189, v189, s62, -v242
v_mfma_f32_16x16x32_bf16 v[128:131], v[216:219], v[24:27], v[128:131]
ds_read_b64_tr_b16 v[212:213], v225 offset:512
v_exp_f32 v188, v188
v_mfma_f32_16x16x32_bf16 v[136:139], v[216:219], v[56:59], v[136:139]
ds_read_b64_tr_b16 v[214:215], v225 offset:8704
v_exp_f32 v189, v189
v_mfma_f32_16x16x32_bf16 v[144:147], v[216:219], v[88:91], v[144:147]
v_add_f32 v246, v246, v186
v_add_f32 v246, v246, v187
v_mfma_f32_16x16x32_bf16 v[152:155], v[216:219], v[120:123], v[152:155]
v_cvt_pk_bf16_f32 v185, v186, v187
v_fma_f32 v190, v190, s62, -v242
v_mfma_f32_16x16x32_bf16 v[132:135], v[192:195], v[24:27], v[132:135]
buffer_load_dwordx4 v232, s[48:51], s85 offen offset:1024 nt lds
v_mfma_f32_16x16x32_bf16 v[140:143], v[192:195], v[56:59], v[140:143]
v_fma_f32 v191, v191, s62, -v242
ds_read_b64_tr_b16 v[216:217], v226 offset:512
v_mfma_f32_16x16x32_bf16 v[148:151], v[192:195], v[88:91], v[148:151]
v_exp_f32 v190, v190
ds_read_b64_tr_b16 v[218:219], v226 offset:8704
v_mfma_f32_16x16x32_bf16 v[156:159], v[192:195], v[120:123], v[156:159]
v_exp_f32 v191, v191
v_mfma_f32_16x16x32_bf16 v[128:131], v[196:199], v[28:31], v[128:131]
v_add_f32 v246, v246, v188
v_add_f32 v246, v246, v189
v_mfma_f32_16x16x32_bf16 v[136:139], v[196:199], v[60:63], v[136:139]
v_cvt_pk_bf16_f32 v186, v188, v189
v_add_f32 v246, v246, v190
v_mfma_f32_16x16x32_bf16 v[144:147], v[196:199], v[92:95], v[144:147]
v_add_f32 v246, v246, v191
v_cvt_pk_bf16_f32 v187, v190, v191
v_mfma_f32_16x16x32_bf16 v[152:155], v[196:199], v[124:127], v[152:155]
ds_read_b64_tr_b16 v[192:193], v225 offset:1024
ds_read_b64_tr_b16 v[194:195], v225 offset:9216
v_mfma_f32_16x16x32_bf16 v[132:135], v[200:203], v[28:31], v[132:135]
buffer_load_dwordx4 v232, s[48:51], s86 offen offset:2048 nt lds
v_mfma_f32_16x16x32_bf16 v[140:143], v[200:203], v[60:63], v[140:143]
ds_read_b64_tr_b16 v[196:197], v226 offset:1024
ds_read_b64_tr_b16 v[198:199], v226 offset:9216
v_mfma_f32_16x16x32_bf16 v[148:151], v[200:203], v[92:95], v[148:151]
v_mfma_f32_16x16x32_bf16 v[156:159], v[200:203], v[124:127], v[156:159]
LST26:
s_waitcnt lgkmcnt(5)
v_mfma_f32_16x16x32_bf16 a[0:3], v[204:207], v[160:163], a[0:3]
ds_read_b64_tr_b16 v[200:201], v225 offset:1536
v_mfma_f32_16x16x32_bf16 a[4:7], v[204:207], v[168:171], a[4:7]
ds_read_b64_tr_b16 v[202:203], v225 offset:9728
v_mfma_f32_16x16x32_bf16 a[8:11], v[204:207], v[176:179], a[8:11]
v_max3_f32 v247, v128, v129, v130
v_max3_f32 v248, v136, v137, v138
v_mfma_f32_16x16x32_bf16 a[12:15], v[204:207], v[184:187], a[12:15]
buffer_load_dwordx4 v232, s[48:51], s87 offen offset:3072 nt lds
v_mfma_f32_16x16x32_bf16 a[16:19], v[208:211], v[160:163], a[16:19]
ds_read_b64_tr_b16 v[204:205], v226 offset:1536
v_max3_f32 v249, v144, v145, v146
v_mfma_f32_16x16x32_bf16 a[20:23], v[208:211], v[168:171], a[20:23]
ds_read_b64_tr_b16 v[206:207], v226 offset:9728
v_max3_f32 v250, v152, v153, v154
v_mfma_f32_16x16x32_bf16 a[24:27], v[208:211], v[176:179], a[24:27]
v_max3_f32 v247, v247, v131, v132
v_max3_f32 v248, v248, v139, v140
v_mfma_f32_16x16x32_bf16 a[28:31], v[208:211], v[184:187], a[28:31]
v_max3_f32 v249, v249, v147, v148
v_max3_f32 v250, v250, v155, v156
v_mfma_f32_16x16x32_bf16 a[32:35], v[212:215], v[160:163], a[32:35]
ds_read_b64_tr_b16 v[208:209], v225 offset:2048
v_max3_f32 v247, v247, v133, v134
v_mfma_f32_16x16x32_bf16 a[36:39], v[212:215], v[168:171], a[36:39]
s_add_u32 m0, s60, 0x1a000
ds_read_b64_tr_b16 v[210:211], v225 offset:10240
v_max3_f32 v248, v248, v141, v142
v_mfma_f32_16x16x32_bf16 a[40:43], v[212:215], v[176:179], a[40:43]
v_max3_f32 v249, v249, v149, v150
v_max3_f32 v250, v250, v157, v158
v_mfma_f32_16x16x32_bf16 a[44:47], v[212:215], v[184:187], a[44:47]
buffer_load_dwordx4 v232, s[48:51], s88 offen offset:0 nt lds
s_waitcnt lgkmcnt(4)
v_mfma_f32_16x16x32_bf16 a[48:51], v[216:219], v[160:163], a[48:51]
ds_read_b64_tr_b16 v[212:213], v226 offset:2048
v_max_f32 v247, v247, v135
v_mfma_f32_16x16x32_bf16 a[52:55], v[216:219], v[168:171], a[52:55]
ds_read_b64_tr_b16 v[214:215], v226 offset:10240
v_max_f32 v248, v248, v143
v_mfma_f32_16x16x32_bf16 a[56:59], v[216:219], v[176:179], a[56:59]
v_max_f32 v249, v249, v151
v_max_f32 v250, v250, v159
v_mfma_f32_16x16x32_bf16 a[60:63], v[216:219], v[184:187], a[60:63]
v_permlane32_swap_b32 v247, v249
v_permlane32_swap_b32 v248, v250
v_mfma_f32_16x16x32_bf16 a[64:67], v[192:195], v[160:163], a[64:67]
ds_read_b64_tr_b16 v[216:217], v225 offset:2560
v_max_f32 v247, v247, v249
v_mfma_f32_16x16x32_bf16 a[68:71], v[192:195], v[168:171], a[68:71]
ds_read_b64_tr_b16 v[218:219], v225 offset:10752
v_max_f32 v248, v248, v250
v_mfma_f32_16x16x32_bf16 a[72:75], v[192:195], v[176:179], a[72:75]
buffer_load_dwordx4 v232, s[48:51], s89 offen offset:1024 nt lds
v_mfma_f32_16x16x32_bf16 a[76:79], v[192:195], v[184:187], a[76:79]
v_permlane16_swap_b32 v247, v248
v_max_f32 v247, v247, v248
v_mfma_f32_16x16x32_bf16 a[80:83], v[196:199], v[160:163], a[80:83]
ds_read_b64_tr_b16 v[192:193], v226 offset:2560
v_cmp_gt_f32 vcc, v247, v238
v_mfma_f32_16x16x32_bf16 a[84:87], v[196:199], v[168:171], a[84:87]
ds_read_b64_tr_b16 v[194:195], v226 offset:10752
s_cbranch_vccnz LR6
LRB6:
v_mfma_f32_16x16x32_bf16 a[88:91], v[196:199], v[176:179], a[88:91]
v_fma_f32 v128, v128, s62, -v239
v_fma_f32 v129, v129, s62, -v239
v_mfma_f32_16x16x32_bf16 a[92:95], v[196:199], v[184:187], a[92:95]
v_exp_f32 v128, v128
v_mfma_f32_16x16x32_bf16 a[96:99], v[200:203], v[160:163], a[96:99]
ds_read_b64_tr_b16 v[196:197], v225 offset:3072
v_exp_f32 v129, v129
v_mfma_f32_16x16x32_bf16 a[100:103], v[200:203], v[168:171], a[100:103]
ds_read_b64_tr_b16 v[198:199], v225 offset:11264
v_fma_f32 v130, v130, s62, -v239
v_mfma_f32_16x16x32_bf16 a[104:107], v[200:203], v[176:179], a[104:107]
buffer_load_dwordx4 v232, s[48:51], s90 offen offset:2048 nt lds
v_mfma_f32_16x16x32_bf16 a[108:111], v[200:203], v[184:187], a[108:111]
v_fma_f32 v131, v131, s62, -v239
s_waitcnt lgkmcnt(4)
v_mfma_f32_16x16x32_bf16 a[112:115], v[204:207], v[160:163], a[112:115]
ds_read_b64_tr_b16 v[200:201], v226 offset:3072
v_exp_f32 v130, v130
v_mfma_f32_16x16x32_bf16 a[116:119], v[204:207], v[168:171], a[116:119]
ds_read_b64_tr_b16 v[202:203], v226 offset:11264
v_exp_f32 v131, v131
v_mfma_f32_16x16x32_bf16 a[120:123], v[204:207], v[176:179], a[120:123]
v_add_f32 v243, v243, v128
v_add_f32 v243, v243, v129
v_mfma_f32_16x16x32_bf16 a[124:127], v[204:207], v[184:187], a[124:127]
v_cvt_pk_bf16_f32 v128, v128, v129
v_fma_f32 v132, v132, s62, -v239
v_mfma_f32_16x16x32_bf16 a[128:131], v[208:211], v[160:163], a[128:131]
ds_read_b64_tr_b16 v[204:205], v225 offset:3584
v_fma_f32 v133, v133, s62, -v239
v_mfma_f32_16x16x32_bf16 a[132:135], v[208:211], v[168:171], a[132:135]
buffer_load_dwordx4 v232, s[48:51], s91 offen offset:3072 nt lds
v_mfma_f32_16x16x32_bf16 a[136:139], v[208:211], v[176:179], a[136:139]
ds_read_b64_tr_b16 v[206:207], v225 offset:11776
v_exp_f32 v132, v132
v_mfma_f32_16x16x32_bf16 a[140:143], v[208:211], v[184:187], a[140:143]
s_add_u32 s48, s48, 0x4000
s_addc_u32 s49, s49, 0
v_mfma_f32_16x16x32_bf16 a[144:147], v[212:215], v[160:163], a[144:147]
ds_read_b64_tr_b16 v[208:209], v226 offset:3584
v_exp_f32 v133, v133
v_mfma_f32_16x16x32_bf16 a[148:151], v[212:215], v[168:171], a[148:151]
ds_read_b64_tr_b16 v[210:211], v226 offset:11776
v_add_f32 v243, v243, v130
v_mfma_f32_16x16x32_bf16 a[152:155], v[212:215], v[176:179], a[152:155]
v_add_f32 v243, v243, v131
v_cvt_pk_bf16_f32 v129, v130, v131
v_mfma_f32_16x16x32_bf16 a[156:159], v[212:215], v[184:187], a[156:159]
v_fma_f32 v134, v134, s62, -v239
v_fma_f32 v135, v135, s62, -v239
v_mfma_f32_16x16x32_bf16 a[160:163], v[216:219], v[160:163], a[160:163]
v_exp_f32 v134, v134
v_mfma_f32_16x16x32_bf16 a[164:167], v[216:219], v[168:171], a[164:167]
s_waitcnt vmcnt(24)
s_waitcnt lgkmcnt(0)
v_exp_f32 v135, v135
v_mfma_f32_16x16x32_bf16 a[168:171], v[216:219], v[176:179], a[168:171]
s_barrier
v_add_u32 v224, 0x10000, v224
v_mfma_f32_16x16x32_bf16 a[172:175], v[216:219], v[184:187], a[172:175]
s_add_u32 m0, s60, 0x0
ds_read_b128 v[212:215], v224 offset:0
v_add_f32 v243, v243, v132
v_mfma_f32_16x16x32_bf16 a[176:179], v[192:195], v[160:163], a[176:179]
ds_read_b128 v[216:219], v224 offset:8192
v_add_f32 v243, v243, v133
v_mfma_f32_16x16x32_bf16 a[180:183], v[192:195], v[168:171], a[180:183]
buffer_load_dwordx4 v231, s[44:47], s84 offen offset:0 nt lds
v_mfma_f32_16x16x32_bf16 a[184:187], v[192:195], v[176:179], a[184:187]
v_cvt_pk_bf16_f32 v130, v132, v133
v_fma_f32 v136, v136, s62, -v240
v_mfma_f32_16x16x32_bf16 a[188:191], v[192:195], v[184:187], a[188:191]
v_fma_f32 v137, v137, s62, -v240
v_mfma_f32_16x16x32_bf16 a[192:195], v[196:199], v[160:163], a[192:195]
ds_read_b128 v[192:195], v224 offset:512
v_exp_f32 v136, v136
v_mfma_f32_16x16x32_bf16 a[196:199], v[196:199], v[168:171], a[196:199]
v_exp_f32 v137, v137
v_mfma_f32_16x16x32_bf16 a[200:203], v[196:199], v[176:179], a[200:203]
v_add_f32 v243, v243, v134
v_add_f32 v243, v243, v135
v_mfma_f32_16x16x32_bf16 a[204:207], v[196:199], v[184:187], a[204:207]
v_cvt_pk_bf16_f32 v131, v134, v135
v_fma_f32 v138, v138, s62, -v240
v_mfma_f32_16x16x32_bf16 a[208:211], v[200:203], v[160:163], a[208:211]
buffer_load_dwordx4 v231, s[44:47], s85 offen offset:1024 nt lds
v_mfma_f32_16x16x32_bf16 a[212:215], v[200:203], v[168:171], a[212:215]
ds_read_b128 v[196:199], v224 offset:8704
v_fma_f32 v139, v139, s62, -v240
v_mfma_f32_16x16x32_bf16 a[216:219], v[200:203], v[176:179], a[216:219]
v_exp_f32 v138, v138
v_mfma_f32_16x16x32_bf16 a[220:223], v[200:203], v[184:187], a[220:223]
v_exp_f32 v139, v139
v_mfma_f32_16x16x32_bf16 a[224:227], v[204:207], v[160:163], a[224:227]
ds_read_b128 v[200:203], v224 offset:1024
v_add_f32 v244, v244, v136
v_mfma_f32_16x16x32_bf16 a[228:231], v[204:207], v[168:171], a[228:231]
v_add_f32 v244, v244, v137
v_cvt_pk_bf16_f32 v136, v136, v137
v_mfma_f32_16x16x32_bf16 a[232:235], v[204:207], v[176:179], a[232:235]
v_fma_f32 v140, v140, s62, -v240
v_fma_f32 v141, v141, s62, -v240
v_mfma_f32_16x16x32_bf16 a[236:239], v[204:207], v[184:187], a[236:239]
v_exp_f32 v140, v140
v_mfma_f32_16x16x32_bf16 a[240:243], v[208:211], v[160:163], a[240:243]
buffer_load_dwordx4 v231, s[44:47], s86 offen offset:2048 nt lds
v_mfma_f32_16x16x32_bf16 a[244:247], v[208:211], v[168:171], a[244:247]
ds_read_b128 v[204:207], v224 offset:9216
v_exp_f32 v141, v141
v_mfma_f32_16x16x32_bf16 a[248:251], v[208:211], v[176:179], a[248:251]
v_add_f32 v244, v244, v138
v_add_f32 v244, v244, v139
v_mfma_f32_16x16x32_bf16 a[252:255], v[208:211], v[184:187], a[252:255]
LST27:
LH7:
s_cbranch_execz LH11
s_cmp_lg_u32 s63, 0
s_cbranch_scc1 LS6
LSB6:
s_waitcnt lgkmcnt(2)
v_mfma_f32_16x16x32_bf16 v[160:163], v[212:215], v[0:3], 0
ds_read_b128 v[208:211], v224 offset:1536
v_cvt_pk_bf16_f32 v137, v138, v139
v_mfma_f32_16x16x32_bf16 v[168:171], v[212:215], v[32:35], 0
v_fma_f32 v142, v142, s62, -v240
v_fma_f32 v143, v143, s62, -v240
v_mfma_f32_16x16x32_bf16 v[176:179], v[212:215], v[64:67], 0
v_exp_f32 v142, v142
v_mfma_f32_16x16x32_bf16 v[184:187], v[212:215], v[96:99], 0
buffer_load_dwordx4 v231, s[44:47], s87 offen offset:3072 nt lds
v_mfma_f32_16x16x32_bf16 v[164:167], v[216:219], v[0:3], 0
ds_read_b128 v[212:215], v224 offset:9728
v_exp_f32 v143, v143
v_mfma_f32_16x16x32_bf16 v[172:175], v[216:219], v[32:35], 0
v_add_f32 v244, v244, v140
v_add_f32 v244, v244, v141
v_mfma_f32_16x16x32_bf16 v[180:183], v[216:219], v[64:67], 0
v_cvt_pk_bf16_f32 v138, v140, v141
v_fma_f32 v144, v144, s62, -v241
v_mfma_f32_16x16x32_bf16 v[188:191], v[216:219], v[96:99], 0
v_fma_f32 v145, v145, s62, -v241
v_mfma_f32_16x16x32_bf16 v[160:163], v[192:195], v[4:7], v[160:163]
ds_read_b128 v[216:219], v224 offset:2048
v_exp_f32 v144, v144
v_mfma_f32_16x16x32_bf16 v[168:171], v[192:195], v[36:39], v[168:171]
s_add_u32 m0, s60, 0x2000
v_exp_f32 v145, v145
v_mfma_f32_16x16x32_bf16 v[176:179], v[192:195], v[68:71], v[176:179]
v_add_f32 v244, v244, v142
v_add_f32 v244, v244, v143
v_mfma_f32_16x16x32_bf16 v[184:187], v[192:195], v[100:103], v[184:187]
buffer_load_dwordx4 v231, s[44:47], s88 offen offset:0 nt lds
v_mfma_f32_16x16x32_bf16 v[164:167], v[196:199], v[4:7], v[164:167]
ds_read_b128 v[192:195], v224 offset:10240
v_cvt_pk_bf16_f32 v139, v142, v143
v_mfma_f32_16x16x32_bf16 v[172:175], v[196:199], v[36:39], v[172:175]
v_fma_f32 v146, v146, s62, -v241
v_fma_f32 v147, v147, s62, -v241
v_mfma_f32_16x16x32_bf16 v[180:183], v[196:199], v[68:71], v[180:183]
v_exp_f32 v146, v146
v_mfma_f32_16x16x32_bf16 v[188:191], v[196:199], v[100:103], v[188:191]
v_exp_f32 v147, v147
s_waitcnt lgkmcnt(2)
v_mfma_f32_16x16x32_bf16 v[160:163], v[200:203], v[8:11], v[160:163]
ds_read_b128 v[196:199], v224 offset:2560
v_add_f32 v245, v245, v144
v_mfma_f32_16x16x32_bf16 v[168:171], v[200:203], v[40:43], v[168:171]
v_add_f32 v245, v245, v145
v_cvt_pk_bf16_f32 v144, v144, v145
v_mfma_f32_16x16x32_bf16 v[176:179], v[200:203], v[72:75], v[176:179]
buffer_load_dwordx4 v231, s[44:47], s89 offen offset:1024 nt lds
v_mfma_f32_16x16x32_bf16 v[184:187], v[200:203], v[104:107], v[184:187]
v_fma_f32 v148, v148, s62, -v241
v_fma_f32 v149, v149, s62, -v241
v_mfma_f32_16x16x32_bf16 v[164:167], v[204:207], v[8:11], v[164:167]
ds_read_b128 v[200:203], v224 offset:10752
v_exp_f32 v148, v148
v_mfma_f32_16x16x32_bf16 v[172:175], v[204:207], v[40:43], v[172:175]
v_exp_f32 v149, v149
v_mfma_f32_16x16x32_bf16 v[180:183], v[204:207], v[72:75], v[180:183]
v_add_f32 v245, v245, v146
v_add_f32 v245, v245, v147
v_mfma_f32_16x16x32_bf16 v[188:191], v[204:207], v[104:107], v[188:191]
v_cvt_pk_bf16_f32 v145, v146, v147
v_fma_f32 v150, v150, s62, -v241
v_mfma_f32_16x16x32_bf16 v[160:163], v[208:211], v[12:15], v[160:163]
ds_read_b128 v[204:207], v224 offset:3072
v_fma_f32 v151, v151, s62, -v241
v_mfma_f32_16x16x32_bf16 v[168:171], v[208:211], v[44:47], v[168:171]
v_exp_f32 v150, v150
v_mfma_f32_16x16x32_bf16 v[176:179], v[208:211], v[76:79], v[176:179]
buffer_load_dwordx4 v231, s[44:47], s90 offen offset:2048 nt lds
v_mfma_f32_16x16x32_bf16 v[184:187], v[208:211], v[108:111], v[184:187]
v_exp_f32 v151, v151
v_mfma_f32_16x16x32_bf16 v[164:167], v[212:215], v[12:15], v[164:167]
ds_read_b128 v[208:211], v224 offset:11264
v_add_f32 v245, v245, v148
v_mfma_f32_16x16x32_bf16 v[172:175], v[212:215], v[44:47], v[172:175]
v_add_f32 v245, v245, v149
v_cvt_pk_bf16_f32 v146, v148, v149
v_mfma_f32_16x16x32_bf16 v[180:183], v[212:215], v[76:79], v[180:183]
v_fma_f32 v152, v152, s62, -v242
v_fma_f32 v153, v153, s62, -v242
v_mfma_f32_16x16x32_bf16 v[188:191], v[212:215], v[108:111], v[188:191]
v_exp_f32 v152, v152
s_waitcnt lgkmcnt(2)
v_mfma_f32_16x16x32_bf16 v[160:163], v[216:219], v[16:19], v[160:163]
ds_read_b128 v[212:215], v224 offset:3584
v_exp_f32 v153, v153
v_mfma_f32_16x16x32_bf16 v[168:171], v[216:219], v[48:51], v[168:171]
buffer_load_dwordx4 v231, s[44:47], s91 offen offset:3072 nt lds
v_mfma_f32_16x16x32_bf16 v[176:179], v[216:219], v[80:83], v[176:179]
s_add_u32 s44, s44, 0x4000
s_addc_u32 s45, s45, 0
v_mfma_f32_16x16x32_bf16 v[184:187], v[216:219], v[112:115], v[184:187]
v_add_f32 v245, v245, v150
v_add_f32 v245, v245, v151
v_mfma_f32_16x16x32_bf16 v[164:167], v[192:195], v[16:19], v[164:167]
ds_read_b128 v[216:219], v224 offset:11776
v_cvt_pk_bf16_f32 v147, v150, v151
v_mfma_f32_16x16x32_bf16 v[172:175], v[192:195], v[48:51], v[172:175]
v_fma_f32 v154, v154, s62, -v242
v_fma_f32 v155, v155, s62, -v242
v_mfma_f32_16x16x32_bf16 v[180:183], v[192:195], v[80:83], v[180:183]
v_exp_f32 v154, v154
v_mfma_f32_16x16x32_bf16 v[188:191], v[192:195], v[112:115], v[188:191]
v_exp_f32 v155, v155
v_mfma_f32_16x16x32_bf16 v[160:163], v[196:199], v[20:23], v[160:163]
v_add_f32 v246, v246, v152
v_add_f32 v246, v246, v153
v_mfma_f32_16x16x32_bf16 v[168:171], v[196:199], v[52:55], v[168:171]
s_waitcnt vmcnt(24)
s_waitcnt lgkmcnt(0)
v_cvt_pk_bf16_f32 v152, v152, v153
v_mfma_f32_16x16x32_bf16 v[176:179], v[196:199], v[84:87], v[176:179]
s_barrier
ds_read_b64_tr_b16 v[192:193], v225 offset:32768
v_mfma_f32_16x16x32_bf16 v[184:187], v[196:199], v[116:119], v[184:187]
s_add_u32 m0, s60, 0x20000
ds_read_b64_tr_b16 v[194:195], v225 offset:40960
v_fma_f32 v156, v156, s62, -v242
v_mfma_f32_16x16x32_bf16 v[164:167], v[200:203], v[20:23], v[164:167]
ds_read_b64_tr_b16 v[196:197], v226 offset:32768
v_fma_f32 v157, v157, s62, -v242
v_mfma_f32_16x16x32_bf16 v[172:175], v[200:203], v[52:55], v[172:175]
buffer_load_dwordx4 v232, s[48:51], s84 offen offset:0 nt lds
v_mfma_f32_16x16x32_bf16 v[180:183], v[200:203], v[84:87], v[180:183]
ds_read_b64_tr_b16 v[198:199], v226 offset:40960
v_exp_f32 v156, v156
v_mfma_f32_16x16x32_bf16 v[188:191], v[200:203], v[116:119], v[188:191]
v_exp_f32 v157, v157
v_mfma_f32_16x16x32_bf16 v[160:163], v[204:207], v[24:27], v[160:163]
ds_read_b64_tr_b16 v[200:201], v225 offset:33280
v_add_f32 v246, v246, v154
v_mfma_f32_16x16x32_bf16 v[168:171], v[204:207], v[56:59], v[168:171]
ds_read_b64_tr_b16 v[202:203], v225 offset:41472
v_add_f32 v246, v246, v155
v_mfma_f32_16x16x32_bf16 v[176:179], v[204:207], v[88:91], v[176:179]
v_cvt_pk_bf16_f32 v153, v154, v155
v_fma_f32 v158, v158, s62, -v242
v_mfma_f32_16x16x32_bf16 v[184:187], v[204:207], v[120:123], v[184:187]
v_fma_f32 v159, v159, s62, -v242
v_mfma_f32_16x16x32_bf16 v[164:167], v[208:211], v[24:27], v[164:167]
buffer_load_dwordx4 v232, s[48:51], s85 offen offset:1024 nt lds
v_mfma_f32_16x16x32_bf16 v[172:175], v[208:211], v[56:59], v[172:175]
v_exp_f32 v158, v158
ds_read_b64_tr_b16 v[204:205], v226 offset:33280
v_mfma_f32_16x16x32_bf16 v[180:183], v[208:211], v[88:91], v[180:183]
v_exp_f32 v159, v159
ds_read_b64_tr_b16 v[206:207], v226 offset:41472
v_mfma_f32_16x16x32_bf16 v[188:191], v[208:211], v[120:123], v[188:191]
v_add_f32 v246, v246, v156
v_add_f32 v246, v246, v157
v_mfma_f32_16x16x32_bf16 v[160:163], v[212:215], v[28:31], v[160:163]
v_cvt_pk_bf16_f32 v154, v156, v157
v_add_f32 v246, v246, v158
v_mfma_f32_16x16x32_bf16 v[168:171], v[212:215], v[60:63], v[168:171]
v_add_f32 v246, v246, v159
v_cvt_pk_bf16_f32 v155, v158, v159
v_mfma_f32_16x16x32_bf16 v[176:179], v[212:215], v[92:95], v[176:179]
ds_read_b64_tr_b16 v[208:209], v225 offset:33792
ds_read_b64_tr_b16 v[210:211], v225 offset:41984
v_mfma_f32_16x16x32_bf16 v[184:187], v[212:215], v[124:127], v[184:187]
v_mfma_f32_16x16x32_bf16 v[164:167], v[216:219], v[28:31], v[164:167]
buffer_load_dwordx4 v232, s[48:51], s86 offen offset:2048 nt lds
v_mfma_f32_16x16x32_bf16 v[172:175], v[216:219], v[60:63], v[172:175]
ds_read_b64_tr_b16 v[212:213], v226 offset:33792
ds_read_b64_tr_b16 v[214:215], v226 offset:41984
v_mfma_f32_16x16x32_bf16 v[180:183], v[216:219], v[92:95], v[180:183]
v_mfma_f32_16x16x32_bf16 v[188:191], v[216:219], v[124:127], v[188:191]
LST28:
s_waitcnt lgkmcnt(5)
v_mfma_f32_16x16x32_bf16 a[0:3], v[192:195], v[128:131], a[0:3]
ds_read_b64_tr_b16 v[216:217], v225 offset:34304
v_mfma_f32_16x16x32_bf16 a[4:7], v[192:195], v[136:139], a[4:7]
ds_read_b64_tr_b16 v[218:219], v225 offset:42496
v_mfma_f32_16x16x32_bf16 a[8:11], v[192:195], v[144:147], a[8:11]
v_max3_f32 v247, v160, v161, v162
v_max3_f32 v248, v168, v169, v170
v_mfma_f32_16x16x32_bf16 a[12:15], v[192:195], v[152:155], a[12:15]
buffer_load_dwordx4 v232, s[48:51], s87 offen offset:3072 nt lds
v_mfma_f32_16x16x32_bf16 a[16:19], v[196:199], v[128:131], a[16:19]
ds_read_b64_tr_b16 v[192:193], v226 offset:34304
v_max3_f32 v249, v176, v177, v178
v_mfma_f32_16x16x32_bf16 a[20:23], v[196:199], v[136:139], a[20:23]
ds_read_b64_tr_b16 v[194:195], v226 offset:42496
v_max3_f32 v250, v184, v185, v186
v_mfma_f32_16x16x32_bf16 a[24:27], v[196:199], v[144:147], a[24:27]
v_max3_f32 v247, v247, v163, v164
v_max3_f32 v248, v248, v171, v172
v_mfma_f32_16x16x32_bf16 a[28:31], v[196:199], v[152:155], a[28:31]
v_max3_f32 v249, v249, v179, v180
v_max3_f32 v250, v250, v187, v188
v_mfma_f32_16x16x32_bf16 a[32:35], v[200:203], v[128:131], a[32:35]
ds_read_b64_tr_b16 v[196:197], v225 offset:34816
v_max3_f32 v247, v247, v165, v166
v_mfma_f32_16x16x32_bf16 a[36:39], v[200:203], v[136:139], a[36:39]
s_add_u32 m0, s60, 0x22000
ds_read_b64_tr_b16 v[198:199], v225 offset:43008
v_max3_f32 v248, v248, v173, v174
v_mfma_f32_16x16x32_bf16 a[40:43], v[200:203], v[144:147], a[40:43]
v_max3_f32 v249, v249, v181, v182
v_max3_f32 v250, v250, v189, v190
v_mfma_f32_16x16x32_bf16 a[44:47], v[200:203], v[152:155], a[44:47]
buffer_load_dwordx4 v232, s[48:51], s88 offen offset:0 nt lds
s_waitcnt lgkmcnt(4)
v_mfma_f32_16x16x32_bf16 a[48:51], v[204:207], v[128:131], a[48:51]
ds_read_b64_tr_b16 v[200:201], v226 offset:34816
v_max_f32 v247, v247, v167
v_mfma_f32_16x16x32_bf16 a[52:55], v[204:207], v[136:139], a[52:55]
ds_read_b64_tr_b16 v[202:203], v226 offset:43008
v_max_f32 v248, v248, v175
v_mfma_f32_16x16x32_bf16 a[56:59], v[204:207], v[144:147], a[56:59]
v_max_f32 v249, v249, v183
v_max_f32 v250, v250, v191
v_mfma_f32_16x16x32_bf16 a[60:63], v[204:207], v[152:155], a[60:63]
v_permlane32_swap_b32 v247, v249
v_permlane32_swap_b32 v248, v250
v_mfma_f32_16x16x32_bf16 a[64:67], v[208:211], v[128:131], a[64:67]
ds_read_b64_tr_b16 v[204:205], v225 offset:35328
v_max_f32 v247, v247, v249
v_mfma_f32_16x16x32_bf16 a[68:71], v[208:211], v[136:139], a[68:71]
ds_read_b64_tr_b16 v[206:207], v225 offset:43520
v_max_f32 v248, v248, v250
v_mfma_f32_16x16x32_bf16 a[72:75], v[208:211], v[144:147], a[72:75]
buffer_load_dwordx4 v232, s[48:51], s89 offen offset:1024 nt lds
v_mfma_f32_16x16x32_bf16 a[76:79], v[208:211], v[152:155], a[76:79]
v_permlane16_swap_b32 v247, v248
v_max_f32 v247, v247, v248
v_mfma_f32_16x16x32_bf16 a[80:83], v[212:215], v[128:131], a[80:83]
ds_read_b64_tr_b16 v[208:209], v226 offset:35328
v_cmp_gt_f32 vcc, v247, v238
v_mfma_f32_16x16x32_bf16 a[84:87], v[212:215], v[136:139], a[84:87]
ds_read_b64_tr_b16 v[210:211], v226 offset:43520
s_cbranch_vccnz LR7
LRB7:
v_mfma_f32_16x16x32_bf16 a[88:91], v[212:215], v[144:147], a[88:91]
v_fma_f32 v160, v160, s62, -v239
v_fma_f32 v161, v161, s62, -v239
v_mfma_f32_16x16x32_bf16 a[92:95], v[212:215], v[152:155], a[92:95]
v_exp_f32 v160, v160
v_mfma_f32_16x16x32_bf16 a[96:99], v[216:219], v[128:131], a[96:99]
ds_read_b64_tr_b16 v[212:213], v225 offset:35840
v_exp_f32 v161, v161
v_mfma_f32_16x16x32_bf16 a[100:103], v[216:219], v[136:139], a[100:103]
ds_read_b64_tr_b16 v[214:215], v225 offset:44032
v_fma_f32 v162, v162, s62, -v239
v_mfma_f32_16x16x32_bf16 a[104:107], v[216:219], v[144:147], a[104:107]
buffer_load_dwordx4 v232, s[48:51], s90 offen offset:2048 nt lds
v_mfma_f32_16x16x32_bf16 a[108:111], v[216:219], v[152:155], a[108:111]
v_fma_f32 v163, v163, s62, -v239
s_waitcnt lgkmcnt(4)
v_mfma_f32_16x16x32_bf16 a[112:115], v[192:195], v[128:131], a[112:115]
ds_read_b64_tr_b16 v[216:217], v226 offset:35840
v_exp_f32 v162, v162
v_mfma_f32_16x16x32_bf16 a[116:119], v[192:195], v[136:139], a[116:119]
ds_read_b64_tr_b16 v[218:219], v226 offset:44032
v_exp_f32 v163, v163
v_mfma_f32_16x16x32_bf16 a[120:123], v[192:195], v[144:147], a[120:123]
v_add_f32 v243, v243, v160
v_add_f32 v243, v243, v161
v_mfma_f32_16x16x32_bf16 a[124:127], v[192:195], v[152:155], a[124:127]
v_cvt_pk_bf16_f32 v160, v160, v161
v_fma_f32 v164, v164, s62, -v239
v_mfma_f32_16x16x32_bf16 a[128:131], v[196:199], v[128:131], a[128:131]
ds_read_b64_tr_b16 v[192:193], v225 offset:36352
v_fma_f32 v165, v165, s62, -v239
v_mfma_f32_16x16x32_bf16 a[132:135], v[196:199], v[136:139], a[132:135]
buffer_load_dwordx4 v232, s[48:51], s91 offen offset:3072 nt lds
v_mfma_f32_16x16x32_bf16 a[136:139], v[196:199], v[144:147], a[136:139]
ds_read_b64_tr_b16 v[194:195], v225 offset:44544
v_exp_f32 v164, v164
v_mfma_f32_16x16x32_bf16 a[140:143], v[196:199], v[152:155], a[140:143]
s_add_u32 s48, s48, 0x4000
s_addc_u32 s49, s49, 0
v_mfma_f32_16x16x32_bf16 a[144:147], v[200:203], v[128:131], a[144:147]
ds_read_b64_tr_b16 v[196:197], v226 offset:36352
v_exp_f32 v165, v165
v_mfma_f32_16x16x32_bf16 a[148:151], v[200:203], v[136:139], a[148:151]
ds_read_b64_tr_b16 v[198:199], v226 offset:44544
v_add_f32 v243, v243, v162
v_mfma_f32_16x16x32_bf16 a[152:155], v[200:203], v[144:147], a[152:155]
v_add_f32 v243, v243, v163
v_cvt_pk_bf16_f32 v161, v162, v163
v_mfma_f32_16x16x32_bf16 a[156:159], v[200:203], v[152:155], a[156:159]
v_fma_f32 v166, v166, s62, -v239
v_fma_f32 v167, v167, s62, -v239
v_mfma_f32_16x16x32_bf16 a[160:163], v[204:207], v[128:131], a[160:163]
v_exp_f32 v166, v166
v_mfma_f32_16x16x32_bf16 a[164:167], v[204:207], v[136:139], a[164:167]
s_waitcnt vmcnt(24)
s_waitcnt lgkmcnt(0)
v_exp_f32 v167, v167
v_mfma_f32_16x16x32_bf16 a[168:171], v[204:207], v[144:147], a[168:171]
s_barrier
v_add_u32 v224, 0xffff0000, v224
v_mfma_f32_16x16x32_bf16 a[172:175], v[204:207], v[152:155], a[172:175]
s_add_u32 m0, s60, 0x8000
ds_read_b128 v[200:203], v224 offset:0
v_add_f32 v243, v243, v164
v_mfma_f32_16x16x32_bf16 a[176:179], v[208:211], v[128:131], a[176:179]
ds_read_b128 v[204:207], v224 offset:8192
v_add_f32 v243, v243, v165
v_mfma_f32_16x16x32_bf16 a[180:183], v[208:211], v[136:139], a[180:183]
buffer_load_dwordx4 v231, s[44:47], s84 offen offset:0 nt lds
v_mfma_f32_16x16x32_bf16 a[184:187], v[208:211], v[144:147], a[184:187]
v_cvt_pk_bf16_f32 v162, v164, v165
v_fma_f32 v168, v168, s62, -v240
v_mfma_f32_16x16x32_bf16 a[188:191], v[208:211], v[152:155], a[188:191]
v_fma_f32 v169, v169, s62, -v240
v_mfma_f32_16x16x32_bf16 a[192:195], v[212:215], v[128:131], a[192:195]
ds_read_b128 v[208:211], v224 offset:512
v_exp_f32 v168, v168
v_mfma_f32_16x16x32_bf16 a[196:199], v[212:215], v[136:139], a[196:199]
v_exp_f32 v169, v169
v_mfma_f32_16x16x32_bf16 a[200:203], v[212:215], v[144:147], a[200:203]
v_add_f32 v243, v243, v166
v_add_f32 v243, v243, v167
v_mfma_f32_16x16x32_bf16 a[204:207], v[212:215], v[152:155], a[204:207]
v_cvt_pk_bf16_f32 v163, v166, v167
v_fma_f32 v170, v170, s62, -v240
v_mfma_f32_16x16x32_bf16 a[208:211], v[216:219], v[128:131], a[208:211]
buffer_load_dwordx4 v231, s[44:47], s85 offen offset:1024 nt lds
v_mfma_f32_16x16x32_bf16 a[212:215], v[216:219], v[136:139], a[212:215]
ds_read_b128 v[212:215], v224 offset:8704
v_fma_f32 v171, v171, s62, -v240
v_mfma_f32_16x16x32_bf16 a[216:219], v[216:219], v[144:147], a[216:219]
v_exp_f32 v170, v170
v_mfma_f32_16x16x32_bf16 a[220:223], v[216:219], v[152:155], a[220:223]
v_exp_f32 v171, v171
v_mfma_f32_16x16x32_bf16 a[224:227], v[192:195], v[128:131], a[224:227]
ds_read_b128 v[216:219], v224 offset:1024
v_add_f32 v244, v244, v168
v_mfma_f32_16x16x32_bf16 a[228:231], v[192:195], v[136:139], a[228:231]
v_add_f32 v244, v244, v169
v_cvt_pk_bf16_f32 v168, v168, v169
v_mfma_f32_16x16x32_bf16 a[232:235], v[192:195], v[144:147], a[232:235]
v_fma_f32 v172, v172, s62, -v240
v_fma_f32 v173, v173, s62, -v240
v_mfma_f32_16x16x32_bf16 a[236:239], v[192:195], v[152:155], a[236:239]
v_exp_f32 v172, v172
v_mfma_f32_16x16x32_bf16 a[240:243], v[196:199], v[128:131], a[240:243]
buffer_load_dwordx4 v231, s[44:47], s86 offen offset:2048 nt lds
v_mfma_f32_16x16x32_bf16 a[244:247], v[196:199], v[136:139], a[244:247]
ds_read_b128 v[192:195], v224 offset:9216
v_exp_f32 v173, v173
v_mfma_f32_16x16x32_bf16 a[248:251], v[196:199], v[144:147], a[248:251]
v_add_f32 v244, v244, v170
v_add_f32 v244, v244, v171
v_mfma_f32_16x16x32_bf16 a[252:255], v[196:199], v[152:155], a[252:255]
LST29:
s_cmp_lg_u32 s63, 0
s_cbranch_scc1 LS7
LSB7:
s_waitcnt lgkmcnt(2)
v_mfma_f32_16x16x32_bf16 v[128:131], v[200:203], v[0:3], 0
ds_read_b128 v[196:199], v224 offset:1536
v_cvt_pk_bf16_f32 v169, v170, v171
v_mfma_f32_16x16x32_bf16 v[136:139], v[200:203], v[32:35], 0
v_fma_f32 v174, v174, s62, -v240
v_fma_f32 v175, v175, s62, -v240
v_mfma_f32_16x16x32_bf16 v[144:147], v[200:203], v[64:67], 0
v_exp_f32 v174, v174
v_mfma_f32_16x16x32_bf16 v[152:155], v[200:203], v[96:99], 0
buffer_load_dwordx4 v231, s[44:47], s87 offen offset:3072 nt lds
v_mfma_f32_16x16x32_bf16 v[132:135], v[204:207], v[0:3], 0
ds_read_b128 v[200:203], v224 offset:9728
v_exp_f32 v175, v175
v_mfma_f32_16x16x32_bf16 v[140:143], v[204:207], v[32:35], 0
v_add_f32 v244, v244, v172
v_add_f32 v244, v244, v173
v_mfma_f32_16x16x32_bf16 v[148:151], v[204:207], v[64:67], 0
v_cvt_pk_bf16_f32 v170, v172, v173
v_fma_f32 v176, v176, s62, -v241
v_mfma_f32_16x16x32_bf16 v[156:159], v[204:207], v[96:99], 0
v_fma_f32 v177, v177, s62, -v241
v_mfma_f32_16x16x32_bf16 v[128:131], v[208:211], v[4:7], v[128:131]
ds_read_b128 v[204:207], v224 offset:2048
v_exp_f32 v176, v176
v_mfma_f32_16x16x32_bf16 v[136:139], v[208:211], v[36:39], v[136:139]
s_add_u32 m0, s60, 0xa000
v_exp_f32 v177, v177
v_mfma_f32_16x16x32_bf16 v[144:147], v[208:211], v[68:71], v[144:147]
v_add_f32 v244, v244, v174
v_add_f32 v244, v244, v175
v_mfma_f32_16x16x32_bf16 v[152:155], v[208:211], v[100:103], v[152:155]
buffer_load_dwordx4 v231, s[44:47], s88 offen offset:0 nt lds
v_mfma_f32_16x16x32_bf16 v[132:135], v[212:215], v[4:7], v[132:135]
ds_read_b128 v[208:211], v224 offset:10240
v_cvt_pk_bf16_f32 v171, v174, v175
v_mfma_f32_16x16x32_bf16 v[140:143], v[212:215], v[36:39], v[140:143]
v_fma_f32 v178, v178, s62, -v241
v_fma_f32 v179, v179, s62, -v241
v_mfma_f32_16x16x32_bf16 v[148:151], v[212:215], v[68:71], v[148:151]
v_exp_f32 v178, v178
v_mfma_f32_16x16x32_bf16 v[156:159], v[212:215], v[100:103], v[156:159]
v_exp_f32 v179, v179
s_waitcnt lgkmcnt(2)
v_mfma_f32_16x16x32_bf16 v[128:131], v[216:219], v[8:11], v[128:131]
ds_read_b128 v[212:215], v224 offset:2560
v_add_f32 v245, v245, v176
v_mfma_f32_16x16x32_bf16 v[136:139], v[216:219], v[40:43], v[136:139]
v_add_f32 v245, v245, v177
v_cvt_pk_bf16_f32 v176, v176, v177
v_mfma_f32_16x16x32_bf16 v[144:147], v[216:219], v[72:75], v[144:147]
buffer_load_dwordx4 v231, s[44:47], s89 offen offset:1024 nt lds
v_mfma_f32_16x16x32_bf16 v[152:155], v[216:219], v[104:107], v[152:155]
v_fma_f32 v180, v180, s62, -v241
v_fma_f32 v181, v181, s62, -v241
v_mfma_f32_16x16x32_bf16 v[132:135], v[192:195], v[8:11], v[132:135]
ds_read_b128 v[216:219], v224 offset:10752
v_exp_f32 v180, v180
v_mfma_f32_16x16x32_bf16 v[140:143], v[192:195], v[40:43], v[140:143]
v_exp_f32 v181, v181
v_mfma_f32_16x16x32_bf16 v[148:151], v[192:195], v[72:75], v[148:151]
v_add_f32 v245, v245, v178
v_add_f32 v245, v245, v179
v_mfma_f32_16x16x32_bf16 v[156:159], v[192:195], v[104:107], v[156:159]
v_cvt_pk_bf16_f32 v177, v178, v179
v_fma_f32 v182, v182, s62, -v241
v_mfma_f32_16x16x32_bf16 v[128:131], v[196:199], v[12:15], v[128:131]
ds_read_b128 v[192:195], v224 offset:3072
v_fma_f32 v183, v183, s62, -v241
v_mfma_f32_16x16x32_bf16 v[136:139], v[196:199], v[44:47], v[136:139]
v_exp_f32 v182, v182
v_mfma_f32_16x16x32_bf16 v[144:147], v[196:199], v[76:79], v[144:147]
buffer_load_dwordx4 v231, s[44:47], s90 offen offset:2048 nt lds
v_mfma_f32_16x16x32_bf16 v[152:155], v[196:199], v[108:111], v[152:155]
v_exp_f32 v183, v183
v_mfma_f32_16x16x32_bf16 v[132:135], v[200:203], v[12:15], v[132:135]
ds_read_b128 v[196:199], v224 offset:11264
v_add_f32 v245, v245, v180
v_mfma_f32_16x16x32_bf16 v[140:143], v[200:203], v[44:47], v[140:143]
v_add_f32 v245, v245, v181
v_cvt_pk_bf16_f32 v178, v180, v181
v_mfma_f32_16x16x32_bf16 v[148:151], v[200:203], v[76:79], v[148:151]
v_fma_f32 v184, v184, s62, -v242
v_fma_f32 v185, v185, s62, -v242
v_mfma_f32_16x16x32_bf16 v[156:159], v[200:203], v[108:111], v[156:159]
v_exp_f32 v184, v184
s_waitcnt lgkmcnt(2)
v_mfma_f32_16x16x32_bf16 v[128:131], v[204:207], v[16:19], v[128:131]
ds_read_b128 v[200:203], v224 offset:3584
v_exp_f32 v185, v185
v_mfma_f32_16x16x32_bf16 v[136:139], v[204:207], v[48:51], v[136:139]
buffer_load_dwordx4 v231, s[44:47], s91 offen offset:3072 nt lds
v_mfma_f32_16x16x32_bf16 v[144:147], v[204:207], v[80:83], v[144:147]
s_add_u32 s44, s44, 0x4000
s_addc_u32 s45, s45, 0
v_mfma_f32_16x16x32_bf16 v[152:155], v[204:207], v[112:115], v[152:155]
v_add_f32 v245, v245, v182
v_add_f32 v245, v245, v183
v_mfma_f32_16x16x32_bf16 v[132:135], v[208:211], v[16:19], v[132:135]
ds_read_b128 v[204:207], v224 offset:11776
v_cvt_pk_bf16_f32 v179, v182, v183
v_mfma_f32_16x16x32_bf16 v[140:143], v[208:211], v[48:51], v[140:143]
v_fma_f32 v186, v186, s62, -v242
v_fma_f32 v187, v187, s62, -v242
v_mfma_f32_16x16x32_bf16 v[148:151], v[208:211], v[80:83], v[148:151]
v_exp_f32 v186, v186
v_mfma_f32_16x16x32_bf16 v[156:159], v[208:211], v[112:115], v[156:159]
v_exp_f32 v187, v187
v_mfma_f32_16x16x32_bf16 v[128:131], v[212:215], v[20:23], v[128:131]
v_add_f32 v246, v246, v184
v_add_f32 v246, v246, v185
v_mfma_f32_16x16x32_bf16 v[136:139], v[212:215], v[52:55], v[136:139]
s_waitcnt vmcnt(24)
s_waitcnt lgkmcnt(0)
v_cvt_pk_bf16_f32 v184, v184, v185
v_mfma_f32_16x16x32_bf16 v[144:147], v[212:215], v[84:87], v[144:147]
s_barrier
v_add_u32 v225, 0x10000, v225
v_mfma_f32_16x16x32_bf16 v[152:155], v[212:215], v[116:119], v[152:155]
s_add_u32 m0, s60, 0x10000
ds_read_b64_tr_b16 v[208:209], v225 offset:32768
ds_read_b64_tr_b16 v[210:211], v225 offset:40960
v_mfma_f32_16x16x32_bf16 v[132:135], v[216:219], v[20:23], v[132:135]
v_add_u32 v226, 0x10000, v226
ds_read_b64_tr_b16 v[212:213], v226 offset:32768
v_mfma_f32_16x16x32_bf16 v[140:143], v[216:219], v[52:55], v[140:143]
buffer_load_dwordx4 v232, s[48:51], s84 offen offset:0 nt lds
v_mfma_f32_16x16x32_bf16 v[148:151], v[216:219], v[84:87], v[148:151]
ds_read_b64_tr_b16 v[214:215], v226 offset:40960
v_fma_f32 v188, v188, s62, -v242
v_mfma_f32_16x16x32_bf16 v[156:159], v[216:219], v[116:119], v[156:159]
v_fma_f32 v189, v189, s62, -v242
v_mfma_f32_16x16x32_bf16 v[128:131], v[192:195], v[24:27], v[128:131]
ds_read_b64_tr_b16 v[216:217], v225 offset:33280
v_exp_f32 v188, v188
v_mfma_f32_16x16x32_bf16 v[136:139], v[192:195], v[56:59], v[136:139]
ds_read_b64_tr_b16 v[218:219], v225 offset:41472
v_exp_f32 v189, v189
v_mfma_f32_16x16x32_bf16 v[144:147], v[192:195], v[88:91], v[144:147]
v_add_f32 v246, v246, v186
v_add_f32 v246, v246, v187
v_mfma_f32_16x16x32_bf16 v[152:155], v[192:195], v[120:123], v[152:155]
v_cvt_pk_bf16_f32 v185, v186, v187
v_fma_f32 v190, v190, s62, -v242
v_mfma_f32_16x16x32_bf16 v[132:135], v[196:199], v[24:27], v[132:135]
buffer_load_dwordx4 v232, s[48:51], s85 offen offset:1024 nt lds
v_mfma_f32_16x16x32_bf16 v[140:143], v[196:199], v[56:59], v[140:143]
v_fma_f32 v191, v191, s62, -v242
ds_read_b64_tr_b16 v[192:193], v226 offset:33280
v_mfma_f32_16x16x32_bf16 v[148:151], v[196:199], v[88:91], v[148:151]
v_exp_f32 v190, v190
ds_read_b64_tr_b16 v[194:195], v226 offset:41472
v_mfma_f32_16x16x32_bf16 v[156:159], v[196:199], v[120:123], v[156:159]
v_exp_f32 v191, v191
v_mfma_f32_16x16x32_bf16 v[128:131], v[200:203], v[28:31], v[128:131]
v_add_f32 v246, v246, v188
v_add_f32 v246, v246, v189
v_mfma_f32_16x16x32_bf16 v[136:139], v[200:203], v[60:63], v[136:139]
v_cvt_pk_bf16_f32 v186, v188, v189
v_add_f32 v246, v246, v190
v_mfma_f32_16x16x32_bf16 v[144:147], v[200:203], v[92:95], v[144:147]
v_add_f32 v246, v246, v191
v_cvt_pk_bf16_f32 v187, v190, v191
v_mfma_f32_16x16x32_bf16 v[152:155], v[200:203], v[124:127], v[152:155]
ds_read_b64_tr_b16 v[196:197], v225 offset:33792
ds_read_b64_tr_b16 v[198:199], v225 offset:41984
v_mfma_f32_16x16x32_bf16 v[132:135], v[204:207], v[28:31], v[132:135]
buffer_load_dwordx4 v232, s[48:51], s86 offen offset:2048 nt lds
v_mfma_f32_16x16x32_bf16 v[140:143], v[204:207], v[60:63], v[140:143]
ds_read_b64_tr_b16 v[200:201], v226 offset:33792
ds_read_b64_tr_b16 v[202:203], v226 offset:41984
v_mfma_f32_16x16x32_bf16 v[148:151], v[204:207], v[92:95], v[148:151]
v_mfma_f32_16x16x32_bf16 v[156:159], v[204:207], v[124:127], v[156:159]
LST30:
s_waitcnt lgkmcnt(5)
v_mfma_f32_16x16x32_bf16 a[0:3], v[208:211], v[160:163], a[0:3]
ds_read_b64_tr_b16 v[204:205], v225 offset:34304
v_mfma_f32_16x16x32_bf16 a[4:7], v[208:211], v[168:171], a[4:7]
ds_read_b64_tr_b16 v[206:207], v225 offset:42496
v_mfma_f32_16x16x32_bf16 a[8:11], v[208:211], v[176:179], a[8:11]
v_max3_f32 v247, v128, v129, v130
v_max3_f32 v248, v136, v137, v138
v_mfma_f32_16x16x32_bf16 a[12:15], v[208:211], v[184:187], a[12:15]
buffer_load_dwordx4 v232, s[48:51], s87 offen offset:3072 nt lds
v_mfma_f32_16x16x32_bf16 a[16:19], v[212:215], v[160:163], a[16:19]
ds_read_b64_tr_b16 v[208:209], v226 offset:34304
v_max3_f32 v249, v144, v145, v146
v_mfma_f32_16x16x32_bf16 a[20:23], v[212:215], v[168:171], a[20:23]
ds_read_b64_tr_b16 v[210:211], v226 offset:42496
v_max3_f32 v250, v152, v153, v154
v_mfma_f32_16x16x32_bf16 a[24:27], v[212:215], v[176:179], a[24:27]
v_max3_f32 v247, v247, v131, v132
v_max3_f32 v248, v248, v139, v140
v_mfma_f32_16x16x32_bf16 a[28:31], v[212:215], v[184:187], a[28:31]
v_max3_f32 v249, v249, v147, v148
v_max3_f32 v250, v250, v155, v156
v_mfma_f32_16x16x32_bf16 a[32:35], v[216:219], v[160:163], a[32:35]
ds_read_b64_tr_b16 v[212:213], v225 offset:34816
v_max3_f32 v247, v247, v133, v134
v_mfma_f32_16x16x32_bf16 a[36:39], v[216:219], v[168:171], a[36:39]
s_add_u32 m0, s60, 0x12000
ds_read_b64_tr_b16 v[214:215], v225 offset:43008
v_max3_f32 v248, v248, v141, v142
v_mfma_f32_16x16x32_bf16 a[40:43], v[216:219], v[176:179], a[40:43]
v_max3_f32 v249, v249, v149, v150
v_max3_f32 v250, v250, v157, v158
v_mfma_f32_16x16x32_bf16 a[44:47], v[216:219], v[184:187], a[44:47]
buffer_load_dwordx4 v232, s[48:51], s88 offen offset:0 nt lds
s_waitcnt lgkmcnt(4)
v_mfma_f32_16x16x32_bf16 a[48:51], v[192:195], v[160:163], a[48:51]
ds_read_b64_tr_b16 v[216:217], v226 offset:34816
v_max_f32 v247, v247, v135
v_mfma_f32_16x16x32_bf16 a[52:55], v[192:195], v[168:171], a[52:55]
ds_read_b64_tr_b16 v[218:219], v226 offset:43008
v_max_f32 v248, v248, v143
v_mfma_f32_16x16x32_bf16 a[56:59], v[192:195], v[176:179], a[56:59]
v_max_f32 v249, v249, v151
v_max_f32 v250, v250, v159
v_mfma_f32_16x16x32_bf16 a[60:63], v[192:195], v[184:187], a[60:63]
v_permlane32_swap_b32 v247, v249
v_permlane32_swap_b32 v248, v250
v_mfma_f32_16x16x32_bf16 a[64:67], v[196:199], v[160:163], a[64:67]
ds_read_b64_tr_b16 v[192:193], v225 offset:35328
v_max_f32 v247, v247, v249
v_mfma_f32_16x16x32_bf16 a[68:71], v[196:199], v[168:171], a[68:71]
ds_read_b64_tr_b16 v[194:195], v225 offset:43520
v_max_f32 v248, v248, v250
v_mfma_f32_16x16x32_bf16 a[72:75], v[196:199], v[176:179], a[72:75]
buffer_load_dwordx4 v232, s[48:51], s89 offen offset:1024 nt lds
v_mfma_f32_16x16x32_bf16 a[76:79], v[196:199], v[184:187], a[76:79]
v_permlane16_swap_b32 v247, v248
v_max_f32 v247, v247, v248
v_mfma_f32_16x16x32_bf16 a[80:83], v[200:203], v[160:163], a[80:83]
ds_read_b64_tr_b16 v[196:197], v226 offset:35328
v_cmp_gt_f32 vcc, v247, v238
LH8:
s_cbranch_execz LH12
v_mfma_f32_16x16x32_bf16 a[84:87], v[200:203], v[168:171], a[84:87]
ds_read_b64_tr_b16 v[198:199], v226 offset:43520
s_cbranch_vccnz LR8
LRB8:
v_mfma_f32_16x16x32_bf16 a[88:91], v[200:203], v[176:179], a[88:91]
v_fma_f32 v128, v128, s62, -v239
v_fma_f32 v129, v129, s62, -v239
v_mfma_f32_16x16x32_bf16 a[92:95], v[200:203], v[184:187], a[92:95]
v_exp_f32 v128, v128
v_mfma_f32_16x16x32_bf16 a[96:99], v[204:207], v[160:163], a[96:99]
ds_read_b64_tr_b16 v[200:201], v225 offset:35840
v_exp_f32 v129, v129
v_mfma_f32_16x16x32_bf16 a[100:103], v[204:207], v[168:171], a[100:103]
ds_read_b64_tr_b16 v[202:203], v225 offset:44032
v_fma_f32 v130, v130, s62, -v239
v_mfma_f32_16x16x32_bf16 a[104:107], v[204:207], v[176:179], a[104:107]
buffer_load_dwordx4 v232, s[48:51], s90 offen offset:2048 nt lds
v_mfma_f32_16x16x32_bf16 a[108:111], v[204:207], v[184:187], a[108:111]
v_fma_f32 v131, v131, s62, -v239
s_waitcnt lgkmcnt(4)
v_mfma_f32_16x16x32_bf16 a[112:115], v[208:211], v[160:163], a[112:115]
ds_read_b64_tr_b16 v[204:205], v226 offset:35840
v_exp_f32 v130, v130
v_mfma_f32_16x16x32_bf16 a[116:119], v[208:211], v[168:171], a[116:119]
ds_read_b64_tr_b16 v[206:207], v226 offset:44032
v_exp_f32 v131, v131
v_mfma_f32_16x16x32_bf16 a[120:123], v[208:211], v[176:179], a[120:123]
v_add_f32 v243, v243, v128
v_add_f32 v243, v243, v129
v_mfma_f32_16x16x32_bf16 a[124:127], v[208:211], v[184:187], a[124:127]
v_cvt_pk_bf16_f32 v128, v128, v129
v_fma_f32 v132, v132, s62, -v239
v_mfma_f32_16x16x32_bf16 a[128:131], v[212:215], v[160:163], a[128:131]
ds_read_b64_tr_b16 v[208:209], v225 offset:36352
v_fma_f32 v133, v133, s62, -v239
v_mfma_f32_16x16x32_bf16 a[132:135], v[212:215], v[168:171], a[132:135]
buffer_load_dwordx4 v232, s[48:51], s91 offen offset:3072 nt lds
v_mfma_f32_16x16x32_bf16 a[136:139], v[212:215], v[176:179], a[136:139]
ds_read_b64_tr_b16 v[210:211], v225 offset:44544
v_exp_f32 v132, v132
v_mfma_f32_16x16x32_bf16 a[140:143], v[212:215], v[184:187], a[140:143]
s_add_u32 s48, s48, 0x4000
s_addc_u32 s49, s49, 0
v_mfma_f32_16x16x32_bf16 a[144:147], v[216:219], v[160:163], a[144:147]
ds_read_b64_tr_b16 v[212:213], v226 offset:36352
v_exp_f32 v133, v133
v_mfma_f32_16x16x32_bf16 a[148:151], v[216:219], v[168:171], a[148:151]
ds_read_b64_tr_b16 v[214:215], v226 offset:44544
v_add_f32 v243, v243, v130
v_mfma_f32_16x16x32_bf16 a[152:155], v[216:219], v[176:179], a[152:155]
v_add_f32 v243, v243, v131
v_cvt_pk_bf16_f32 v129, v130, v131
v_mfma_f32_16x16x32_bf16 a[156:159], v[216:219], v[184:187], a[156:159]
v_fma_f32 v134, v134, s62, -v239
v_fma_f32 v135, v135, s62, -v239
v_mfma_f32_16x16x32_bf16 a[160:163], v[192:195], v[160:163], a[160:163]
v_exp_f32 v134, v134
v_mfma_f32_16x16x32_bf16 a[164:167], v[192:195], v[168:171], a[164:167]
s_waitcnt vmcnt(24)
s_waitcnt lgkmcnt(0)
v_exp_f32 v135, v135
v_mfma_f32_16x16x32_bf16 a[168:171], v[192:195], v[176:179], a[168:171]
s_barrier
v_add_u32 v224, 0xffff0000, v224
v_mfma_f32_16x16x32_bf16 a[172:175], v[192:195], v[184:187], a[172:175]
s_add_u32 m0, s60, 0x18000
ds_read_b128 v[216:219], v224 offset:0
v_add_f32 v243, v243, v132
v_mfma_f32_16x16x32_bf16 a[176:179], v[196:199], v[160:163], a[176:179]
ds_read_b128 v[192:195], v224 offset:8192
v_add_f32 v243, v243, v133
v_mfma_f32_16x16x32_bf16 a[180:183], v[196:199], v[168:171], a[180:183]
buffer_load_dwordx4 v231, s[44:47], s84 offen offset:0 nt lds
v_mfma_f32_16x16x32_bf16 a[184:187], v[196:199], v[176:179], a[184:187]
v_cvt_pk_bf16_f32 v130, v132, v133
v_fma_f32 v136, v136, s62, -v240
v_mfma_f32_16x16x32_bf16 a[188:191], v[196:199], v[184:187], a[188:191]
v_fma_f32 v137, v137, s62, -v240
v_mfma_f32_16x16x32_bf16 a[192:195], v[200:203], v[160:163], a[192:195]
ds_read_b128 v[196:199], v224 offset:512
v_exp_f32 v136, v136
v_mfma_f32_16x16x32_bf16 a[196:199], v[200:203], v[168:171], a[196:199]
v_exp_f32 v137, v137
v_mfma_f32_16x16x32_bf16 a[200:203], v[200:203], v[176:179], a[200:203]
v_add_f32 v243, v243, v134
v_add_f32 v243, v243, v135
v_mfma_f32_16x16x32_bf16 a[204:207], v[200:203], v[184:187], a[204:207]
v_cvt_pk_bf16_f32 v131, v134, v135
v_fma_f32 v138, v138, s62, -v240
v_mfma_f32_16x16x32_bf16 a[208:211], v[204:207], v[160:163], a[208:211]
buffer_load_dwordx4 v231, s[44:47], s85 offen offset:1024 nt lds
v_mfma_f32_16x16x32_bf16 a[212:215], v[204:207], v[168:171], a[212:215]
ds_read_b128 v[200:203], v224 offset:8704
v_fma_f32 v139, v139, s62, -v240
v_mfma_f32_16x16x32_bf16 a[216:219], v[204:207], v[176:179], a[216:219]
v_exp_f32 v138, v138
v_mfma_f32_16x16x32_bf16 a[220:223], v[204:207], v[184:187], a[220:223]
v_exp_f32 v139, v139
v_mfma_f32_16x16x32_bf16 a[224:227], v[208:211], v[160:163], a[224:227]
ds_read_b128 v[204:207], v224 offset:1024
v_add_f32 v244, v244, v136
v_mfma_f32_16x16x32_bf16 a[228:231], v[208:211], v[168:171], a[228:231]
v_add_f32 v244, v244, v137
v_cvt_pk_bf16_f32 v136, v136, v137
v_mfma_f32_16x16x32_bf16 a[232:235], v[208:211], v[176:179], a[232:235]
v_fma_f32 v140, v140, s62, -v240
v_fma_f32 v141, v141, s62, -v240
v_mfma_f32_16x16x32_bf16 a[236:239], v[208:211], v[184:187], a[236:239]
v_exp_f32 v140, v140
v_mfma_f32_16x16x32_bf16 a[240:243], v[212:215], v[160:163], a[240:243]
buffer_load_dwordx4 v231, s[44:47], s86 offen offset:2048 nt lds
v_mfma_f32_16x16x32_bf16 a[244:247], v[212:215], v[168:171], a[244:247]
ds_read_b128 v[208:211], v224 offset:9216
v_exp_f32 v141, v141
v_mfma_f32_16x16x32_bf16 a[248:251], v[212:215], v[176:179], a[248:251]
v_add_f32 v244, v244, v138
v_add_f32 v244, v244, v139
v_mfma_f32_16x16x32_bf16 a[252:255], v[212:215], v[184:187], a[252:255]
LST31:
s_cmp_lg_u32 s63, 0
s_cbranch_scc1 LS8
LSB8:
s_waitcnt lgkmcnt(2)
v_mfma_f32_16x16x32_bf16 v[160:163], v[216:219], v[0:3], 0
ds_read_b128 v[212:215], v224 offset:1536
v_cvt_pk_bf16_f32 v137, v138, v139
v_mfma_f32_16x16x32_bf16 v[168:171], v[216:219], v[32:35], 0
v_fma_f32 v142, v142, s62, -v240
v_fma_f32 v143, v143, s62, -v240
v_mfma_f32_16x16x32_bf16 v[176:179], v[216:219], v[64:67], 0
v_exp_f32 v142, v142
v_mfma_f32_16x16x32_bf16 v[184:187], v[216:219], v[96:99], 0
buffer_load_dwordx4 v231, s[44:47], s87 offen offset:3072 nt lds
v_mfma_f32_16x16x32_bf16 v[164:167], v[192:195], v[0:3], 0
ds_read_b128 v[216:219], v224 offset:9728
v_exp_f32 v143, v143
v_mfma_f32_16x16x32_bf16 v[172:175], v[192:195], v[32:35], 0
v_add_f32 v244, v244, v140
v_add_f32 v244, v244, v141
v_mfma_f32_16x16x32_bf16 v[180:183], v[192:195], v[64:67], 0
v_cvt_pk_bf16_f32 v138, v140, v141
v_fma_f32 v144, v144, s62, -v241
v_mfma_f32_16x16x32_bf16 v[188:191], v[192:195], v[96:99], 0
v_fma_f32 v145, v145, s62, -v241
v_mfma_f32_16x16x32_bf16 v[160:163], v[196:199], v[4:7], v[160:163]
ds_read_b128 v[192:195], v224 offset:2048
v_exp_f32 v144, v144
v_mfma_f32_16x16x32_bf16 v[168:171], v[196:199], v[36:39], v[168:171]
s_add_u32 m0, s60, 0x1a000
v_exp_f32 v145, v145
v_mfma_f32_16x16x32_bf16 v[176:179], v[196:199], v[68:71], v[176:179]
v_add_f32 v244, v244, v142
v_add_f32 v244, v244, v143
v_mfma_f32_16x16x32_bf16 v[184:187], v[196:199], v[100:103], v[184:187]
buffer_load_dwordx4 v231, s[44:47], s88 offen offset:0 nt lds
v_mfma_f32_16x16x32_bf16 v[164:167], v[200:203], v[4:7], v[164:167]
ds_read_b128 v[196:199], v224 offset:10240
v_cvt_pk_bf16_f32 v139, v142, v143
v_mfma_f32_16x16x32_bf16 v[172:175], v[200:203], v[36:39], v[172:175]
v_fma_f32 v146, v146, s62, -v241
v_fma_f32 v147, v147, s62, -v241
v_mfma_f32_16x16x32_bf16 v[180:183], v[200:203], v[68:71], v[180:183]
v_exp_f32 v146, v146
v_mfma_f32_16x16x32_bf16 v[188:191], v[200:203], v[100:103], v[188:191]
v_exp_f32 v147, v147
s_waitcnt lgkmcnt(2)
v_mfma_f32_16x16x32_bf16 v[160:163], v[204:207], v[8:11], v[160:163]
ds_read_b128 v[200:203], v224 offset:2560
v_add_f32 v245, v245, v144
v_mfma_f32_16x16x32_bf16 v[168:171], v[204:207], v[40:43], v[168:171]
v_add_f32 v245, v245, v145
v_cvt_pk_bf16_f32 v144, v144, v145
v_mfma_f32_16x16x32_bf16 v[176:179], v[204:207], v[72:75], v[176:179]
buffer_load_dwordx4 v231, s[44:47], s89 offen offset:1024 nt lds
v_mfma_f32_16x16x32_bf16 v[184:187], v[204:207], v[104:107], v[184:187]
v_fma_f32 v148, v148, s62, -v241
v_fma_f32 v149, v149, s62, -v241
v_mfma_f32_16x16x32_bf16 v[164:167], v[208:211], v[8:11], v[164:167]
ds_read_b128 v[204:207], v224 offset:10752
v_exp_f32 v148, v148
v_mfma_f32_16x16x32_bf16 v[172:175], v[208:211], v[40:43], v[172:175]
v_exp_f32 v149, v149
v_mfma_f32_16x16x32_bf16 v[180:183], v[208:211], v[72:75], v[180:183]
v_add_f32 v245, v245, v146
v_add_f32 v245, v245, v147
v_mfma_f32_16x16x32_bf16 v[188:191], v[208:211], v[104:107], v[188:191]
v_cvt_pk_bf16_f32 v145, v146, v147
v_fma_f32 v150, v150, s62, -v241
v_mfma_f32_16x16x32_bf16 v[160:163], v[212:215], v[12:15], v[160:163]
ds_read_b128 v[208:211], v224 offset:3072
v_fma_f32 v151, v151, s62, -v241
v_mfma_f32_16x16x32_bf16 v[168:171], v[212:215], v[44:47], v[168:171]
v_exp_f32 v150, v150
v_mfma_f32_16x16x32_bf16 v[176:179], v[212:215], v[76:79], v[176:179]
buffer_load_dwordx4 v231, s[44:47], s90 offen offset:2048 nt lds
v_mfma_f32_16x16x32_bf16 v[184:187], v[212:215], v[108:111], v[184:187]
v_exp_f32 v151, v151
v_mfma_f32_16x16x32_bf16 v[164:167], v[216:219], v[12:15], v[164:167]
ds_read_b128 v[212:215], v224 offset:11264
v_add_f32 v245, v245, v148
v_mfma_f32_16x16x32_bf16 v[172:175], v[216:219], v[44:47], v[172:175]
v_add_f32 v245, v245, v149
v_cvt_pk_bf16_f32 v146, v148, v149
v_mfma_f32_16x16x32_bf16 v[180:183], v[216:219], v[76:79], v[180:183]
v_fma_f32 v152, v152, s62, -v242
v_fma_f32 v153, v153, s62, -v242
v_mfma_f32_16x16x32_bf16 v[188:191], v[216:219], v[108:111], v[188:191]
v_exp_f32 v152, v152
s_waitcnt lgkmcnt(2)
v_mfma_f32_16x16x32_bf16 v[160:163], v[192:195], v[16:19], v[160:163]
ds_read_b128 v[216:219], v224 offset:3584
v_exp_f32 v153, v153
v_mfma_f32_16x16x32_bf16 v[168:171], v[192:195], v[48:51], v[168:171]
buffer_load_dwordx4 v231, s[44:47], s91 offen offset:3072 nt lds
v_mfma_f32_16x16x32_bf16 v[176:179], v[192:195], v[80:83], v[176:179]
s_add_u32 s44, s44, 0x4000
s_addc_u32 s45, s45, 0
v_mfma_f32_16x16x32_bf16 v[184:187], v[192:195], v[112:115], v[184:187]
v_add_f32 v245, v245, v150
v_add_f32 v245, v245, v151
v_mfma_f32_16x16x32_bf16 v[164:167], v[196:199], v[16:19], v[164:167]
ds_read_b128 v[192:195], v224 offset:11776
v_cvt_pk_bf16_f32 v147, v150, v151
v_mfma_f32_16x16x32_bf16 v[172:175], v[196:199], v[48:51], v[172:175]
v_fma_f32 v154, v154, s62, -v242
v_fma_f32 v155, v155, s62, -v242
v_mfma_f32_16x16x32_bf16 v[180:183], v[196:199], v[80:83], v[180:183]
v_exp_f32 v154, v154
v_mfma_f32_16x16x32_bf16 v[188:191], v[196:199], v[112:115], v[188:191]
v_exp_f32 v155, v155
v_mfma_f32_16x16x32_bf16 v[160:163], v[200:203], v[20:23], v[160:163]
v_add_f32 v246, v246, v152
v_add_f32 v246, v246, v153
v_mfma_f32_16x16x32_bf16 v[168:171], v[200:203], v[52:55], v[168:171]
s_waitcnt vmcnt(24)
s_waitcnt lgkmcnt(0)
v_cvt_pk_bf16_f32 v152, v152, v153
v_mfma_f32_16x16x32_bf16 v[176:179], v[200:203], v[84:87], v[176:179]
s_barrier
v_add_u32 v225, 0x10000, v225
v_mfma_f32_16x16x32_bf16 v[184:187], v[200:203], v[116:119], v[184:187]
s_add_u32 m0, s60, 0x0
ds_read_b64_tr_b16 v[196:197], v225 offset:0
ds_read_b64_tr_b16 v[198:199], v225 offset:8192
v_mfma_f32_16x16x32_bf16 v[164:167], v[204:207], v[20:23], v[164:167]
v_add_u32 v226, 0x10000, v226
ds_read_b64_tr_b16 v[200:201], v226 offset:0
v_mfma_f32_16x16x32_bf16 v[172:175], v[204:207], v[52:55], v[172:175]
buffer_load_dwordx4 v232, s[48:51], s84 offen offset:0 nt lds
v_mfma_f32_16x16x32_bf16 v[180:183], v[204:207], v[84:87], v[180:183]
ds_read_b64_tr_b16 v[202:203], v226 offset:8192
v_fma_f32 v156, v156, s62, -v242
v_mfma_f32_16x16x32_bf16 v[188:191], v[204:207], v[116:119], v[188:191]
v_fma_f32 v157, v157, s62, -v242
v_mfma_f32_16x16x32_bf16 v[160:163], v[208:211], v[24:27], v[160:163]
ds_read_b64_tr_b16 v[204:205], v225 offset:512
v_exp_f32 v156, v156
v_mfma_f32_16x16x32_bf16 v[168:171], v[208:211], v[56:59], v[168:171]
ds_read_b64_tr_b16 v[206:207], v225 offset:8704
v_exp_f32 v157, v157
v_mfma_f32_16x16x32_bf16 v[176:179], v[208:211], v[88:91], v[176:179]
v_add_f32 v246, v246, v154
v_add_f32 v246, v246, v155
v_mfma_f32_16x16x32_bf16 v[184:187], v[208:211], v[120:123], v[184:187]
v_cvt_pk_bf16_f32 v153, v154, v155
v_fma_f32 v158, v158, s62, -v242
v_mfma_f32_16x16x32_bf16 v[164:167], v[212:215], v[24:27], v[164:167]
buffer_load_dwordx4 v232, s[48:51], s85 offen offset:1024 nt lds
v_mfma_f32_16x16x32_bf16 v[172:175], v[212:215], v[56:59], v[172:175]
v_fma_f32 v159, v159, s62, -v242
ds_read_b64_tr_b16 v[208:209], v226 offset:512
v_mfma_f32_16x16x32_bf16 v[180:183], v[212:215], v[88:91], v[180:183]
v_exp_f32 v158, v158
ds_read_b64_tr_b16 v[210:211], v226 offset:8704
v_mfma_f32_16x16x32_bf16 v[188:191], v[212:215], v[120:123], v[188:191]
v_exp_f32 v159, v159
v_mfma_f32_16x16x32_bf16 v[160:163], v[216:219], v[28:31], v[160:163]
v_add_f32 v246, v246, v156
v_add_f32 v246, v246, v157
v_mfma_f32_16x16x32_bf16 v[168:171], v[216:219], v[60:63], v[168:171]
v_cvt_pk_bf16_f32 v154, v156, v157
v_add_f32 v246, v246, v158
v_mfma_f32_16x16x32_bf16 v[176:179], v[216:219], v[92:95], v[176:179]
v_add_f32 v246, v246, v159
v_cvt_pk_bf16_f32 v155, v158, v159
v_mfma_f32_16x16x32_bf16 v[184:187], v[216:219], v[124:127], v[184:187]
ds_read_b64_tr_b16 v[212:213], v225 offset:1024
ds_read_b64_tr_b16 v[214:215], v225 offset:9216
v_mfma_f32_16x16x32_bf16 v[164:167], v[192:195], v[28:31], v[164:167]
buffer_load_dwordx4 v232, s[48:51], s86 offen offset:2048 nt lds
v_mfma_f32_16x16x32_bf16 v[172:175], v[192:195], v[60:63], v[172:175]
ds_read_b64_tr_b16 v[216:217], v226 offset:1024
ds_read_b64_tr_b16 v[218:219], v226 offset:9216
v_mfma_f32_16x16x32_bf16 v[180:183], v[192:195], v[92:95], v[180:183]
v_mfma_f32_16x16x32_bf16 v[188:191], v[192:195], v[124:127], v[188:191]
LST32:
s_waitcnt lgkmcnt(5)
v_mfma_f32_16x16x32_bf16 a[0:3], v[196:199], v[128:131], a[0:3]
ds_read_b64_tr_b16 v[192:193], v225 offset:1536
v_mfma_f32_16x16x32_bf16 a[4:7], v[196:199], v[136:139], a[4:7]
ds_read_b64_tr_b16 v[194:195], v225 offset:9728
v_mfma_f32_16x16x32_bf16 a[8:11], v[196:199], v[144:147], a[8:11]
v_max3_f32 v247, v160, v161, v162
v_max3_f32 v248, v168, v169, v170
v_mfma_f32_16x16x32_bf16 a[12:15], v[196:199], v[152:155], a[12:15]
buffer_load_dwordx4 v232, s[48:51], s87 offen offset:3072 nt lds
v_mfma_f32_16x16x32_bf16 a[16:19], v[200:203], v[128:131], a[16:19]
ds_read_b64_tr_b16 v[196:197], v226 offset:1536
v_max3_f32 v249, v176, v177, v178
v_mfma_f32_16x16x32_bf16 a[20:23], v[200:203], v[136:139], a[20:23]
ds_read_b64_tr_b16 v[198:199], v226 offset:9728
v_max3_f32 v250, v184, v185, v186
v_mfma_f32_16x16x32_bf16 a[24:27], v[200:203], v[144:147], a[24:27]
v_max3_f32 v247, v247, v163, v164
v_max3_f32 v248, v248, v171, v172
v_mfma_f32_16x16x32_bf16 a[28:31], v[200:203], v[152:155], a[28:31]
v_max3_f32 v249, v249, v179, v180
v_max3_f32 v250, v250, v187, v188
v_mfma_f32_16x16x32_bf16 a[32:35], v[204:207], v[128:131], a[32:35]
ds_read_b64_tr_b16 v[200:201], v225 offset:2048
v_max3_f32 v247, v247, v165, v166
v_mfma_f32_16x16x32_bf16 a[36:39], v[204:207], v[136:139], a[36:39]
s_add_u32 m0, s60, 0x2000
ds_read_b64_tr_b16 v[202:203], v225 offset:10240
v_max3_f32 v248, v248, v173, v174
v_mfma_f32_16x16x32_bf16 a[40:43], v[204:207], v[144:147], a[40:43]
v_max3_f32 v249, v249, v181, v182
v_max3_f32 v250, v250, v189, v190
v_mfma_f32_16x16x32_bf16 a[44:47], v[204:207], v[152:155], a[44:47]
buffer_load_dwordx4 v232, s[48:51], s88 offen offset:0 nt lds
s_waitcnt lgkmcnt(4)
v_mfma_f32_16x16x32_bf16 a[48:51], v[208:211], v[128:131], a[48:51]
ds_read_b64_tr_b16 v[204:205], v226 offset:2048
v_max_f32 v247, v247, v167
v_mfma_f32_16x16x32_bf16 a[52:55], v[208:211], v[136:139], a[52:55]
ds_read_b64_tr_b16 v[206:207], v226 offset:10240
v_max_f32 v248, v248, v175
v_mfma_f32_16x16x32_bf16 a[56:59], v[208:211], v[144:147], a[56:59]
v_max_f32 v249, v249, v183
v_max_f32 v250, v250, v191
v_mfma_f32_16x16x32_bf16 a[60:63], v[208:211], v[152:155], a[60:63]
v_permlane32_swap_b32 v247, v249
v_permlane32_swap_b32 v248, v250
v_mfma_f32_16x16x32_bf16 a[64:67], v[212:215], v[128:131], a[64:67]
ds_read_b64_tr_b16 v[208:209], v225 offset:2560
v_max_f32 v247, v247, v249
v_mfma_f32_16x16x32_bf16 a[68:71], v[212:215], v[136:139], a[68:71]
ds_read_b64_tr_b16 v[210:211], v225 offset:10752
v_max_f32 v248, v248, v250
v_mfma_f32_16x16x32_bf16 a[72:75], v[212:215], v[144:147], a[72:75]
buffer_load_dwordx4 v232, s[48:51], s89 offen offset:1024 nt lds
v_mfma_f32_16x16x32_bf16 a[76:79], v[212:215], v[152:155], a[76:79]
v_permlane16_swap_b32 v247, v248
v_max_f32 v247, v247, v248
v_mfma_f32_16x16x32_bf16 a[80:83], v[216:219], v[128:131], a[80:83]
ds_read_b64_tr_b16 v[212:213], v226 offset:2560
v_cmp_gt_f32 vcc, v247, v238
v_mfma_f32_16x16x32_bf16 a[84:87], v[216:219], v[136:139], a[84:87]
ds_read_b64_tr_b16 v[214:215], v226 offset:10752
s_cbranch_vccnz LR9
LRB9:
v_mfma_f32_16x16x32_bf16 a[88:91], v[216:219], v[144:147], a[88:91]
v_fma_f32 v160, v160, s62, -v239
v_fma_f32 v161, v161, s62, -v239
v_mfma_f32_16x16x32_bf16 a[92:95], v[216:219], v[152:155], a[92:95]
v_exp_f32 v160, v160
v_mfma_f32_16x16x32_bf16 a[96:99], v[192:195], v[128:131], a[96:99]
ds_read_b64_tr_b16 v[216:217], v225 offset:3072
v_exp_f32 v161, v161
v_mfma_f32_16x16x32_bf16 a[100:103], v[192:195], v[136:139], a[100:103]
ds_read_b64_tr_b16 v[218:219], v225 offset:11264
v_fma_f32 v162, v162, s62, -v239
v_mfma_f32_16x16x32_bf16 a[104:107], v[192:195], v[144:147], a[104:107]
buffer_load_dwordx4 v232, s[48:51], s90 offen offset:2048 nt lds
v_mfma_f32_16x16x32_bf16 a[108:111], v[192:195], v[152:155], a[108:111]
v_fma_f32 v163, v163, s62, -v239
s_waitcnt lgkmcnt(4)
v_mfma_f32_16x16x32_bf16 a[112:115], v[196:199], v[128:131], a[112:115]
ds_read_b64_tr_b16 v[192:193], v226 offset:3072
v_exp_f32 v162, v162
v_mfma_f32_16x16x32_bf16 a[116:119], v[196:199], v[136:139], a[116:119]
ds_read_b64_tr_b16 v[194:195], v226 offset:11264
v_exp_f32 v163, v163
v_mfma_f32_16x16x32_bf16 a[120:123], v[196:199], v[144:147], a[120:123]
v_add_f32 v243, v243, v160
v_add_f32 v243, v243, v161
v_mfma_f32_16x16x32_bf16 a[124:127], v[196:199], v[152:155], a[124:127]
v_cvt_pk_bf16_f32 v160, v160, v161
v_fma_f32 v164, v164, s62, -v239
v_mfma_f32_16x16x32_bf16 a[128:131], v[200:203], v[128:131], a[128:131]
ds_read_b64_tr_b16 v[196:197], v225 offset:3584
v_fma_f32 v165, v165, s62, -v239
v_mfma_f32_16x16x32_bf16 a[132:135], v[200:203], v[136:139], a[132:135]
buffer_load_dwordx4 v232, s[48:51], s91 offen offset:3072 nt lds
v_mfma_f32_16x16x32_bf16 a[136:139], v[200:203], v[144:147], a[136:139]
ds_read_b64_tr_b16 v[198:199], v225 offset:11776
v_exp_f32 v164, v164
v_mfma_f32_16x16x32_bf16 a[140:143], v[200:203], v[152:155], a[140:143]
s_add_u32 s48, s48, 0x4000
s_addc_u32 s49, s49, 0
v_mfma_f32_16x16x32_bf16 a[144:147], v[204:207], v[128:131], a[144:147]
ds_read_b64_tr_b16 v[200:201], v226 offset:3584
v_exp_f32 v165, v165
v_mfma_f32_16x16x32_bf16 a[148:151], v[204:207], v[136:139], a[148:151]
ds_read_b64_tr_b16 v[202:203], v226 offset:11776
v_add_f32 v243, v243, v162
v_mfma_f32_16x16x32_bf16 a[152:155], v[204:207], v[144:147], a[152:155]
v_add_f32 v243, v243, v163
v_cvt_pk_bf16_f32 v161, v162, v163
v_mfma_f32_16x16x32_bf16 a[156:159], v[204:207], v[152:155], a[156:159]
v_fma_f32 v166, v166, s62, -v239
v_fma_f32 v167, v167, s62, -v239
v_mfma_f32_16x16x32_bf16 a[160:163], v[208:211], v[128:131], a[160:163]
v_exp_f32 v166, v166
v_mfma_f32_16x16x32_bf16 a[164:167], v[208:211], v[136:139], a[164:167]
s_waitcnt vmcnt(24)
s_waitcnt lgkmcnt(0)
v_exp_f32 v167, v167
v_mfma_f32_16x16x32_bf16 a[168:171], v[208:211], v[144:147], a[168:171]
s_barrier
ds_read_b128 v[204:207], v224 offset:32768
v_mfma_f32_16x16x32_bf16 a[172:175], v[208:211], v[152:155], a[172:175]
s_add_u32 m0, s60, 0x20000
v_add_f32 v243, v243, v164
v_add_f32 v243, v243, v165
v_mfma_f32_16x16x32_bf16 a[176:179], v[212:215], v[128:131], a[176:179]
ds_read_b128 v[208:211], v224 offset:40960
v_cvt_pk_bf16_f32 v162, v164, v165
v_mfma_f32_16x16x32_bf16 a[180:183], v[212:215], v[136:139], a[180:183]
buffer_load_dwordx4 v231, s[44:47], s84 offen offset:0 nt lds
v_mfma_f32_16x16x32_bf16 a[184:187], v[212:215], v[144:147], a[184:187]
v_fma_f32 v168, v168, s62, -v240
v_fma_f32 v169, v169, s62, -v240
v_mfma_f32_16x16x32_bf16 a[188:191], v[212:215], v[152:155], a[188:191]
v_exp_f32 v168, v168
v_mfma_f32_16x16x32_bf16 a[192:195], v[216:219], v[128:131], a[192:195]
ds_read_b128 v[212:215], v224 offset:33280
v_exp_f32 v169, v169
v_mfma_f32_16x16x32_bf16 a[196:199], v[216:219], v[136:139], a[196:199]
v_add_f32 v243, v243, v166
v_add_f32 v243, v243, v167
v_mfma_f32_16x16x32_bf16 a[200:203], v[216:219], v[144:147], a[200:203]
v_cvt_pk_bf16_f32 v163, v166, v167
v_fma_f32 v170, v170, s62, -v240
v_mfma_f32_16x16x32_bf16 a[204:207], v[216:219], v[152:155], a[204:207]
v_fma_f32 v171, v171, s62, -v240
v_mfma_f32_16x16x32_bf16 a[208:211], v[192:195], v[128:131], a[208:211]
buffer_load_dwordx4 v231, s[44:47], s85 offen offset:1024 nt lds
v_mfma_f32_16x16x32_bf16 a[212:215], v[192:195], v[136:139], a[212:215]
ds_read_b128 v[216:219], v224 offset:41472
v_exp_f32 v170, v170
v_mfma_f32_16x16x32_bf16 a[216:219], v[192:195], v[144:147], a[216:219]
v_exp_f32 v171, v171
v_mfma_f32_16x16x32_bf16 a[220:223], v[192:195], v[152:155], a[220:223]
v_add_f32 v244, v244, v168
v_add_f32 v244, v244, v169
v_mfma_f32_16x16x32_bf16 a[224:227], v[196:199], v[128:131], a[224:227]
ds_read_b128 v[192:195], v224 offset:33792
v_cvt_pk_bf16_f32 v168, v168, v169
v_mfma_f32_16x16x32_bf16 a[228:231], v[196:199], v[136:139], a[228:231]
v_fma_f32 v172, v172, s62, -v240
v_fma_f32 v173, v173, s62, -v240
v_mfma_f32_16x16x32_bf16 a[232:235], v[196:199], v[144:147], a[232:235]
v_exp_f32 v172, v172
v_mfma_f32_16x16x32_bf16 a[236:239], v[196:199], v[152:155], a[236:239]
v_exp_f32 v173, v173
v_mfma_f32_16x16x32_bf16 a[240:243], v[200:203], v[128:131], a[240:243]
buffer_load_dwordx4 v231, s[44:47], s86 offen offset:2048 nt lds
v_mfma_f32_16x16x32_bf16 a[244:247], v[200:203], v[136:139], a[244:247]
ds_read_b128 v[196:199], v224 offset:41984
v_add_f32 v244, v244, v170
v_mfma_f32_16x16x32_bf16 a[248:251], v[200:203], v[144:147], a[248:251]
v_add_f32 v244, v244, v171
v_cvt_pk_bf16_f32 v169, v170, v171
v_mfma_f32_16x16x32_bf16 a[252:255], v[200:203], v[152:155], a[252:255]
LST33:
s_cmp_lg_u32 s63, 0
s_cbranch_scc1 LS9
LSB9:
s_waitcnt lgkmcnt(2)
v_mfma_f32_16x16x32_bf16 v[128:131], v[204:207], v[0:3], 0
ds_read_b128 v[200:203], v224 offset:34304
v_fma_f32 v174, v174, s62, -v240
v_mfma_f32_16x16x32_bf16 v[136:139], v[204:207], v[32:35], 0
v_fma_f32 v175, v175, s62, -v240
v_mfma_f32_16x16x32_bf16 v[144:147], v[204:207], v[64:67], 0
v_exp_f32 v174, v174
v_mfma_f32_16x16x32_bf16 v[152:155], v[204:207], v[96:99], 0
buffer_load_dwordx4 v231, s[44:47], s87 offen offset:3072 nt lds
v_mfma_f32_16x16x32_bf16 v[132:135], v[208:211], v[0:3], 0
ds_read_b128 v[204:207], v224 offset:42496
v_exp_f32 v175, v175
v_mfma_f32_16x16x32_bf16 v[140:143], v[208:211], v[32:35], 0
v_add_f32 v244, v244, v172
v_add_f32 v244, v244, v173
v_mfma_f32_16x16x32_bf16 v[148:151], v[208:211], v[64:67], 0
v_cvt_pk_bf16_f32 v170, v172, v173
v_fma_f32 v176, v176, s62, -v241
v_mfma_f32_16x16x32_bf16 v[156:159], v[208:211], v[96:99], 0
v_fma_f32 v177, v177, s62, -v241
v_mfma_f32_16x16x32_bf16 v[128:131], v[212:215], v[4:7], v[128:131]
ds_read_b128 v[208:211], v224 offset:34816
v_exp_f32 v176, v176
v_mfma_f32_16x16x32_bf16 v[136:139], v[212:215], v[36:39], v[136:139]
s_add_u32 m0, s60, 0x22000
v_exp_f32 v177, v177
v_mfma_f32_16x16x32_bf16 v[144:147], v[212:215], v[68:71], v[144:147]
v_add_f32 v244, v244, v174
v_add_f32 v244, v244, v175
v_mfma_f32_16x16x32_bf16 v[152:155], v[212:215], v[100:103], v[152:155]
buffer_load_dwordx4 v231, s[44:47], s88 offen offset:0 nt lds
v_mfma_f32_16x16x32_bf16 v[132:135], v[216:219], v[4:7], v[132:135]
ds_read_b128 v[212:215], v224 offset:43008
v_cvt_pk_bf16_f32 v171, v174, v175
v_mfma_f32_16x16x32_bf16 v[140:143], v[216:219], v[36:39], v[140:143]
v_fma_f32 v178, v178, s62, -v241
v_fma_f32 v179, v179, s62, -v241
v_mfma_f32_16x16x32_bf16 v[148:151], v[216:219], v[68:71], v[148:151]
v_exp_f32 v178, v178
v_mfma_f32_16x16x32_bf16 v[156:159], v[216:219], v[100:103], v[156:159]
v_exp_f32 v179, v179
s_waitcnt lgkmcnt(2)
v_mfma_f32_16x16x32_bf16 v[128:131], v[192:195], v[8:11], v[128:131]
ds_read_b128 v[216:219], v224 offset:35328
v_add_f32 v245, v245, v176
v_mfma_f32_16x16x32_bf16 v[136:139], v[192:195], v[40:43], v[136:139]
v_add_f32 v245, v245, v177
v_cvt_pk_bf16_f32 v176, v176, v177
v_mfma_f32_16x16x32_bf16 v[144:147], v[192:195], v[72:75], v[144:147]
buffer_load_dwordx4 v231, s[44:47], s89 offen offset:1024 nt lds
v_mfma_f32_16x16x32_bf16 v[152:155], v[192:195], v[104:107], v[152:155]
v_fma_f32 v180, v180, s62, -v241
v_fma_f32 v181, v181, s62, -v241
v_mfma_f32_16x16x32_bf16 v[132:135], v[196:199], v[8:11], v[132:135]
ds_read_b128 v[192:195], v224 offset:43520
v_exp_f32 v180, v180
v_mfma_f32_16x16x32_bf16 v[140:143], v[196:199], v[40:43], v[140:143]
v_exp_f32 v181, v181
v_mfma_f32_16x16x32_bf16 v[148:151], v[196:199], v[72:75], v[148:151]
v_add_f32 v245, v245, v178
v_add_f32 v245, v245, v179
v_mfma_f32_16x16x32_bf16 v[156:159], v[196:199], v[104:107], v[156:159]
v_cvt_pk_bf16_f32 v177, v178, v179
v_fma_f32 v182, v182, s62, -v241
v_mfma_f32_16x16x32_bf16 v[128:131], v[200:203], v[12:15], v[128:131]
ds_read_b128 v[196:199], v224 offset:35840
v_fma_f32 v183, v183, s62, -v241
v_mfma_f32_16x16x32_bf16 v[136:139], v[200:203], v[44:47], v[136:139]
v_exp_f32 v182, v182
v_mfma_f32_16x16x32_bf16 v[144:147], v[200:203], v[76:79], v[144:147]
buffer_load_dwordx4 v231, s[44:47], s90 offen offset:2048 nt lds
v_mfma_f32_16x16x32_bf16 v[152:155], v[200:203], v[108:111], v[152:155]
v_exp_f32 v183, v183
v_mfma_f32_16x16x32_bf16 v[132:135], v[204:207], v[12:15], v[132:135]
ds_read_b128 v[200:203], v224 offset:44032
v_add_f32 v245, v245, v180
v_mfma_f32_16x16x32_bf16 v[140:143], v[204:207], v[44:47], v[140:143]
v_add_f32 v245, v245, v181
v_cvt_pk_bf16_f32 v178, v180, v181
v_mfma_f32_16x16x32_bf16 v[148:151], v[204:207], v[76:79], v[148:151]
v_fma_f32 v184, v184, s62, -v242
v_fma_f32 v185, v185, s62, -v242
v_mfma_f32_16x16x32_bf16 v[156:159], v[204:207], v[108:111], v[156:159]
v_exp_f32 v184, v184
s_waitcnt lgkmcnt(2)
v_mfma_f32_16x16x32_bf16 v[128:131], v[208:211], v[16:19], v[128:131]
ds_read_b128 v[204:207], v224 offset:36352
v_exp_f32 v185, v185
v_mfma_f32_16x16x32_bf16 v[136:139], v[208:211], v[48:51], v[136:139]
buffer_load_dwordx4 v231, s[44:47], s91 offen offset:3072 nt lds
v_mfma_f32_16x16x32_bf16 v[144:147], v[208:211], v[80:83], v[144:147]
s_add_u32 s44, s44, 0x4000
s_addc_u32 s45, s45, 0
v_mfma_f32_16x16x32_bf16 v[152:155], v[208:211], v[112:115], v[152:155]
v_add_f32 v245, v245, v182
v_add_f32 v245, v245, v183
v_mfma_f32_16x16x32_bf16 v[132:135], v[212:215], v[16:19], v[132:135]
ds_read_b128 v[208:211], v224 offset:44544
v_cvt_pk_bf16_f32 v179, v182, v183
v_mfma_f32_16x16x32_bf16 v[140:143], v[212:215], v[48:51], v[140:143]
v_fma_f32 v186, v186, s62, -v242
v_fma_f32 v187, v187, s62, -v242
v_mfma_f32_16x16x32_bf16 v[148:151], v[212:215], v[80:83], v[148:151]
v_exp_f32 v186, v186
v_mfma_f32_16x16x32_bf16 v[156:159], v[212:215], v[112:115], v[156:159]
v_exp_f32 v187, v187
v_mfma_f32_16x16x32_bf16 v[128:131], v[216:219], v[20:23], v[128:131]
v_add_f32 v246, v246, v184
v_add_f32 v246, v246, v185
v_mfma_f32_16x16x32_bf16 v[136:139], v[216:219], v[52:55], v[136:139]
s_waitcnt vmcnt(24)
s_waitcnt lgkmcnt(0)
v_cvt_pk_bf16_f32 v184, v184, v185
v_mfma_f32_16x16x32_bf16 v[144:147], v[216:219], v[84:87], v[144:147]
s_barrier
v_add_u32 v225, 0xffff0000, v225
v_mfma_f32_16x16x32_bf16 v[152:155], v[216:219], v[116:119], v[152:155]
s_add_u32 m0, s60, 0x8000
ds_read_b64_tr_b16 v[212:213], v225 offset:0
ds_read_b64_tr_b16 v[214:215], v225 offset:8192
LH9:
s_cbranch_execz LH13
v_mfma_f32_16x16x32_bf16 v[132:135], v[192:195], v[20:23], v[132:135]
v_add_u32 v226, 0xffff0000, v226
ds_read_b64_tr_b16 v[216:217], v226 offset:0
v_mfma_f32_16x16x32_bf16 v[140:143], v[192:195], v[52:55], v[140:143]
buffer_load_dwordx4 v232, s[48:51], s84 offen offset:0 nt lds
v_mfma_f32_16x16x32_bf16 v[148:151], v[192:195], v[84:87], v[148:151]
ds_read_b64_tr_b16 v[218:219], v226 offset:8192
v_fma_f32 v188, v188, s62, -v242
v_mfma_f32_16x16x32_bf16 v[156:159], v[192:195], v[116:119], v[156:159]
v_fma_f32 v189, v189, s62, -v242
v_mfma_f32_16x16x32_bf16 v[128:131], v[196:199], v[24:27], v[128:131]
ds_read_b64_tr_b16 v[192:193], v225 offset:512
v_exp_f32 v188, v188
v_mfma_f32_16x16x32_bf16 v[136:139], v[196:199], v[56:59], v[136:139]
ds_read_b64_tr_b16 v[194:195], v225 offset:8704
v_exp_f32 v189, v189
v_mfma_f32_16x16x32_bf16 v[144:147], v[196:199], v[88:91], v[144:147]
v_add_f32 v246, v246, v186
v_add_f32 v246, v246, v187
v_mfma_f32_16x16x32_bf16 v[152:155], v[196:199], v[120:123], v[152:155]
v_cvt_pk_bf16_f32 v185, v186, v187
v_fma_f32 v190, v190, s62, -v242
v_mfma_f32_16x16x32_bf16 v[132:135], v[200:203], v[24:27], v[132:135]
buffer_load_dwordx4 v232, s[48:51], s85 offen offset:1024 nt lds
v_mfma_f32_16x16x32_bf16 v[140:143], v[200:203], v[56:59], v[140:143]
v_fma_f32 v191, v191, s62, -v242
ds_read_b64_tr_b16 v[196:197], v226 offset:512
v_mfma_f32_16x16x32_bf16 v[148:151], v[200:203], v[88:91], v[148:151]
v_exp_f32 v190, v190
ds_read_b64_tr_b16 v[198:199], v226 offset:8704
v_mfma_f32_16x16x32_bf16 v[156:159], v[200:203], v[120:123], v[156:159]
v_exp_f32 v191, v191
v_mfma_f32_16x16x32_bf16 v[128:131], v[204:207], v[28:31], v[128:131]
v_add_f32 v246, v246, v188
v_add_f32 v246, v246, v189
v_mfma_f32_16x16x32_bf16 v[136:139], v[204:207], v[60:63], v[136:139]
v_cvt_pk_bf16_f32 v186, v188, v189
v_add_f32 v246, v246, v190
v_mfma_f32_16x16x32_bf16 v[144:147], v[204:207], v[92:95], v[144:147]
v_add_f32 v246, v246, v191
v_cvt_pk_bf16_f32 v187, v190, v191
v_mfma_f32_16x16x32_bf16 v[152:155], v[204:207], v[124:127], v[152:155]
ds_read_b64_tr_b16 v[200:201], v225 offset:1024
ds_read_b64_tr_b16 v[202:203], v225 offset:9216
v_mfma_f32_16x16x32_bf16 v[132:135], v[208:211], v[28:31], v[132:135]
buffer_load_dwordx4 v232, s[48:51], s86 offen offset:2048 nt lds
v_mfma_f32_16x16x32_bf16 v[140:143], v[208:211], v[60:63], v[140:143]
ds_read_b64_tr_b16 v[204:205], v226 offset:1024
ds_read_b64_tr_b16 v[206:207], v226 offset:9216
v_mfma_f32_16x16x32_bf16 v[148:151], v[208:211], v[92:95], v[148:151]
v_mfma_f32_16x16x32_bf16 v[156:159], v[208:211], v[124:127], v[156:159]
LST34:
s_waitcnt lgkmcnt(5)
v_mfma_f32_16x16x32_bf16 a[0:3], v[212:215], v[160:163], a[0:3]
ds_read_b64_tr_b16 v[208:209], v225 offset:1536
v_mfma_f32_16x16x32_bf16 a[4:7], v[212:215], v[168:171], a[4:7]
ds_read_b64_tr_b16 v[210:211], v225 offset:9728
v_mfma_f32_16x16x32_bf16 a[8:11], v[212:215], v[176:179], a[8:11]
v_max3_f32 v247, v128, v129, v130
v_max3_f32 v248, v136, v137, v138
v_mfma_f32_16x16x32_bf16 a[12:15], v[212:215], v[184:187], a[12:15]
buffer_load_dwordx4 v232, s[48:51], s87 offen offset:3072 nt lds
v_mfma_f32_16x16x32_bf16 a[16:19], v[216:219], v[160:163], a[16:19]
ds_read_b64_tr_b16 v[212:213], v226 offset:1536
v_max3_f32 v249, v144, v145, v146
v_mfma_f32_16x16x32_bf16 a[20:23], v[216:219], v[168:171], a[20:23]
ds_read_b64_tr_b16 v[214:215], v226 offset:9728
v_max3_f32 v250, v152, v153, v154
v_mfma_f32_16x16x32_bf16 a[24:27], v[216:219], v[176:179], a[24:27]
v_max3_f32 v247, v247, v131, v132
v_max3_f32 v248, v248, v139, v140
v_mfma_f32_16x16x32_bf16 a[28:31], v[216:219], v[184:187], a[28:31]
v_max3_f32 v249, v249, v147, v148
v_max3_f32 v250, v250, v155, v156
v_mfma_f32_16x16x32_bf16 a[32:35], v[192:195], v[160:163], a[32:35]
ds_read_b64_tr_b16 v[216:217], v225 offset:2048
v_max3_f32 v247, v247, v133, v134
v_mfma_f32_16x16x32_bf16 a[36:39], v[192:195], v[168:171], a[36:39]
s_add_u32 m0, s60, 0xa000
ds_read_b64_tr_b16 v[218:219], v225 offset:10240
v_max3_f32 v248, v248, v141, v142
v_mfma_f32_16x16x32_bf16 a[40:43], v[192:195], v[176:179], a[40:43]
v_max3_f32 v249, v249, v149, v150
v_max3_f32 v250, v250, v157, v158
v_mfma_f32_16x16x32_bf16 a[44:47], v[192:195], v[184:187], a[44:47]
buffer_load_dwordx4 v232, s[48:51], s88 offen offset:0 nt lds
s_waitcnt lgkmcnt(4)
v_mfma_f32_16x16x32_bf16 a[48:51], v[196:199], v[160:163], a[48:51]
ds_read_b64_tr_b16 v[192:193], v226 offset:2048
v_max_f32 v247, v247, v135
v_mfma_f32_16x16x32_bf16 a[52:55], v[196:199], v[168:171], a[52:55]
ds_read_b64_tr_b16 v[194:195], v226 offset:10240
v_max_f32 v248, v248, v143
v_mfma_f32_16x16x32_bf16 a[56:59], v[196:199], v[176:179], a[56:59]
v_max_f32 v249, v249, v151
v_max_f32 v250, v250, v159
v_mfma_f32_16x16x32_bf16 a[60:63], v[196:199], v[184:187], a[60:63]
v_permlane32_swap_b32 v247, v249
v_permlane32_swap_b32 v248, v250
v_mfma_f32_16x16x32_bf16 a[64:67], v[200:203], v[160:163], a[64:67]
ds_read_b64_tr_b16 v[196:197], v225 offset:2560
v_max_f32 v247, v247, v249
v_mfma_f32_16x16x32_bf16 a[68:71], v[200:203], v[168:171], a[68:71]
ds_read_b64_tr_b16 v[198:199], v225 offset:10752
v_max_f32 v248, v248, v250
v_mfma_f32_16x16x32_bf16 a[72:75], v[200:203], v[176:179], a[72:75]
buffer_load_dwordx4 v232, s[48:51], s89 offen offset:1024 nt lds
v_mfma_f32_16x16x32_bf16 a[76:79], v[200:203], v[184:187], a[76:79]
v_permlane16_swap_b32 v247, v248
v_max_f32 v247, v247, v248
v_mfma_f32_16x16x32_bf16 a[80:83], v[204:207], v[160:163], a[80:83]
ds_read_b64_tr_b16 v[200:201], v226 offset:2560
v_cmp_gt_f32 vcc, v247, v238
v_mfma_f32_16x16x32_bf16 a[84:87], v[204:207], v[168:171], a[84:87]
ds_read_b64_tr_b16 v[202:203], v226 offset:10752
s_cbranch_vccnz LR10
LRB10:
v_mfma_f32_16x16x32_bf16 a[88:91], v[204:207], v[176:179], a[88:91]
v_fma_f32 v128, v128, s62, -v239
v_fma_f32 v129, v129, s62, -v239
v_mfma_f32_16x16x32_bf16 a[92:95], v[204:207], v[184:187], a[92:95]
v_exp_f32 v128, v128
v_mfma_f32_16x16x32_bf16 a[96:99], v[208:211], v[160:163], a[96:99]
ds_read_b64_tr_b16 v[204:205], v225 offset:3072
v_exp_f32 v129, v129
v_mfma_f32_16x16x32_bf16 a[100:103], v[208:211], v[168:171], a[100:103]
ds_read_b64_tr_b16 v[206:207], v225 offset:11264
v_fma_f32 v130, v130, s62, -v239
v_mfma_f32_16x16x32_bf16 a[104:107], v[208:211], v[176:179], a[104:107]
buffer_load_dwordx4 v232, s[48:51], s90 offen offset:2048 nt lds
v_mfma_f32_16x16x32_bf16 a[108:111], v[208:211], v[184:187], a[108:111]
v_fma_f32 v131, v131, s62, -v239
s_waitcnt lgkmcnt(4)
v_mfma_f32_16x16x32_bf16 a[112:115], v[212:215], v[160:163], a[112:115]
ds_read_b64_tr_b16 v[208:209], v226 offset:3072
v_exp_f32 v130, v130
v_mfma_f32_16x16x32_bf16 a[116:119], v[212:215], v[168:171], a[116:119]
ds_read_b64_tr_b16 v[210:211], v226 offset:11264
v_exp_f32 v131, v131
v_mfma_f32_16x16x32_bf16 a[120:123], v[212:215], v[176:179], a[120:123]
v_add_f32 v243, v243, v128
v_add_f32 v243, v243, v129
v_mfma_f32_16x16x32_bf16 a[124:127], v[212:215], v[184:187], a[124:127]
v_cvt_pk_bf16_f32 v128, v128, v129
v_fma_f32 v132, v132, s62, -v239
v_mfma_f32_16x16x32_bf16 a[128:131], v[216:219], v[160:163], a[128:131]
ds_read_b64_tr_b16 v[212:213], v225 offset:3584
v_fma_f32 v133, v133, s62, -v239
v_mfma_f32_16x16x32_bf16 a[132:135], v[216:219], v[168:171], a[132:135]
buffer_load_dwordx4 v232, s[48:51], s91 offen offset:3072 nt lds
v_mfma_f32_16x16x32_bf16 a[136:139], v[216:219], v[176:179], a[136:139]
ds_read_b64_tr_b16 v[214:215], v225 offset:11776
v_exp_f32 v132, v132
v_mfma_f32_16x16x32_bf16 a[140:143], v[216:219], v[184:187], a[140:143]
s_add_u32 s48, s48, 0x4000
s_addc_u32 s49, s49, 0
v_mfma_f32_16x16x32_bf16 a[144:147], v[192:195], v[160:163], a[144:147]
ds_read_b64_tr_b16 v[216:217], v226 offset:3584
v_exp_f32 v133, v133
v_mfma_f32_16x16x32_bf16 a[148:151], v[192:195], v[168:171], a[148:151]
ds_read_b64_tr_b16 v[218:219], v226 offset:11776
v_add_f32 v243, v243, v130
v_mfma_f32_16x16x32_bf16 a[152:155], v[192:195], v[176:179], a[152:155]
v_add_f32 v243, v243, v131
v_cvt_pk_bf16_f32 v129, v130, v131
v_mfma_f32_16x16x32_bf16 a[156:159], v[192:195], v[184:187], a[156:159]
v_fma_f32 v134, v134, s62, -v239
v_fma_f32 v135, v135, s62, -v239
v_mfma_f32_16x16x32_bf16 a[160:163], v[196:199], v[160:163], a[160:163]
v_exp_f32 v134, v134
v_mfma_f32_16x16x32_bf16 a[164:167], v[196:199], v[168:171], a[164:167]
s_waitcnt vmcnt(24)
s_waitcnt lgkmcnt(0)
v_exp_f32 v135, v135
v_mfma_f32_16x16x32_bf16 a[168:171], v[196:199], v[176:179], a[168:171]
s_barrier
v_add_u32 v224, 0x10000, v224
v_mfma_f32_16x16x32_bf16 a[172:175], v[196:199], v[184:187], a[172:175]
s_add_u32 m0, s60, 0x10000
ds_read_b128 v[192:195], v224 offset:32768
v_add_f32 v243, v243, v132
v_mfma_f32_16x16x32_bf16 a[176:179], v[200:203], v[160:163], a[176:179]
ds_read_b128 v[196:199], v224 offset:40960
v_add_f32 v243, v243, v133
v_mfma_f32_16x16x32_bf16 a[180:183], v[200:203], v[168:171], a[180:183]
buffer_load_dwordx4 v231, s[44:47], s84 offen offset:0 nt lds
v_mfma_f32_16x16x32_bf16 a[184:187], v[200:203], v[176:179], a[184:187]
v_cvt_pk_bf16_f32 v130, v132, v133
v_fma_f32 v136, v136, s62, -v240
v_mfma_f32_16x16x32_bf16 a[188:191], v[200:203], v[184:187], a[188:191]
v_fma_f32 v137, v137, s62, -v240
v_mfma_f32_16x16x32_bf16 a[192:195], v[204:207], v[160:163], a[192:195]
ds_read_b128 v[200:203], v224 offset:33280
v_exp_f32 v136, v136
v_mfma_f32_16x16x32_bf16 a[196:199], v[204:207], v[168:171], a[196:199]
v_exp_f32 v137, v137
v_mfma_f32_16x16x32_bf16 a[200:203], v[204:207], v[176:179], a[200:203]
v_add_f32 v243, v243, v134
v_add_f32 v243, v243, v135
v_mfma_f32_16x16x32_bf16 a[204:207], v[204:207], v[184:187], a[204:207]
v_cvt_pk_bf16_f32 v131, v134, v135
v_fma_f32 v138, v138, s62, -v240
v_mfma_f32_16x16x32_bf16 a[208:211], v[208:211], v[160:163], a[208:211]
buffer_load_dwordx4 v231, s[44:47], s85 offen offset:1024 nt lds
v_mfma_f32_16x16x32_bf16 a[212:215], v[208:211], v[168:171], a[212:215]
ds_read_b128 v[204:207], v224 offset:41472
v_fma_f32 v139, v139, s62, -v240
v_mfma_f32_16x16x32_bf16 a[216:219], v[208:211], v[176:179], a[216:219]
v_exp_f32 v138, v138
v_mfma_f32_16x16x32_bf16 a[220:223], v[208:211], v[184:187], a[220:223]
v_exp_f32 v139, v139
v_mfma_f32_16x16x32_bf16 a[224:227], v[212:215], v[160:163], a[224:227]
ds_read_b128 v[208:211], v224 offset:33792
v_add_f32 v244, v244, v136
v_mfma_f32_16x16x32_bf16 a[228:231], v[212:215], v[168:171], a[228:231]
v_add_f32 v244, v244, v137
v_cvt_pk_bf16_f32 v136, v136, v137
v_mfma_f32_16x16x32_bf16 a[232:235], v[212:215], v[176:179], a[232:235]
v_fma_f32 v140, v140, s62, -v240
v_fma_f32 v141, v141, s62, -v240
v_mfma_f32_16x16x32_bf16 a[236:239], v[212:215], v[184:187], a[236:239]
v_exp_f32 v140, v140
v_mfma_f32_16x16x32_bf16 a[240:243], v[216:219], v[160:163], a[240:243]
buffer_load_dwordx4 v231, s[44:47], s86 offen offset:2048 nt lds
v_mfma_f32_16x16x32_bf16 a[244:247], v[216:219], v[168:171], a[244:247]
ds_read_b128 v[212:215], v224 offset:41984
v_exp_f32 v141, v141
v_mfma_f32_16x16x32_bf16 a[248:251], v[216:219], v[176:179], a[248:251]
v_add_f32 v244, v244, v138
v_add_f32 v244, v244, v139
v_mfma_f32_16x16x32_bf16 a[252:255], v[216:219], v[184:187], a[252:255]
LST35:
s_cmp_lg_u32 s63, 0
s_cbranch_scc1 LS10
LSB10:
s_waitcnt lgkmcnt(2)
v_mfma_f32_16x16x32_bf16 v[160:163], v[192:195], v[0:3], 0
ds_read_b128 v[216:219], v224 offset:34304
v_cvt_pk_bf16_f32 v137, v138, v139
v_mfma_f32_16x16x32_bf16 v[168:171], v[192:195], v[32:35], 0
v_fma_f32 v142, v142, s62, -v240
v_fma_f32 v143, v143, s62, -v240
v_mfma_f32_16x16x32_bf16 v[176:179], v[192:195], v[64:67], 0
v_exp_f32 v142, v142
v_mfma_f32_16x16x32_bf16 v[184:187], v[192:195], v[96:99], 0
buffer_load_dwordx4 v231, s[44:47], s87 offen offset:3072 nt lds
v_mfma_f32_16x16x32_bf16 v[164:167], v[196:199], v[0:3], 0
ds_read_b128 v[192:195], v224 offset:42496
v_exp_f32 v143, v143
v_mfma_f32_16x16x32_bf16 v[172:175], v[196:199], v[32:35], 0
v_add_f32 v244, v244, v140
v_add_f32 v244, v244, v141
v_mfma_f32_16x16x32_bf16 v[180:183], v[196:199], v[64:67], 0
v_cvt_pk_bf16_f32 v138, v140, v141
v_fma_f32 v144, v144, s62, -v241
v_mfma_f32_16x16x32_bf16 v[188:191], v[196:199], v[96:99], 0
v_fma_f32 v145, v145, s62, -v241
v_mfma_f32_16x16x32_bf16 v[160:163], v[200:203], v[4:7], v[160:163]
ds_read_b128 v[196:199], v224 offset:34816
v_exp_f32 v144, v144
v_mfma_f32_16x16x32_bf16 v[168:171], v[200:203], v[36:39], v[168:171]
s_add_u32 m0, s60, 0x12000
v_exp_f32 v145, v145
v_mfma_f32_16x16x32_bf16 v[176:179], v[200:203], v[68:71], v[176:179]
v_add_f32 v244, v244, v142
v_add_f32 v244, v244, v143
v_mfma_f32_16x16x32_bf16 v[184:187], v[200:203], v[100:103], v[184:187]
buffer_load_dwordx4 v231, s[44:47], s88 offen offset:0 nt lds
v_mfma_f32_16x16x32_bf16 v[164:167], v[204:207], v[4:7], v[164:167]
ds_read_b128 v[200:203], v224 offset:43008
v_cvt_pk_bf16_f32 v139, v142, v143
v_mfma_f32_16x16x32_bf16 v[172:175], v[204:207], v[36:39], v[172:175]
v_fma_f32 v146, v146, s62, -v241
v_fma_f32 v147, v147, s62, -v241
v_mfma_f32_16x16x32_bf16 v[180:183], v[204:207], v[68:71], v[180:183]
v_exp_f32 v146, v146
v_mfma_f32_16x16x32_bf16 v[188:191], v[204:207], v[100:103], v[188:191]
v_exp_f32 v147, v147
s_waitcnt lgkmcnt(2)
v_mfma_f32_16x16x32_bf16 v[160:163], v[208:211], v[8:11], v[160:163]
ds_read_b128 v[204:207], v224 offset:35328
v_add_f32 v245, v245, v144
v_mfma_f32_16x16x32_bf16 v[168:171], v[208:211], v[40:43], v[168:171]
v_add_f32 v245, v245, v145
v_cvt_pk_bf16_f32 v144, v144, v145
v_mfma_f32_16x16x32_bf16 v[176:179], v[208:211], v[72:75], v[176:179]
buffer_load_dwordx4 v231, s[44:47], s89 offen offset:1024 nt lds
v_mfma_f32_16x16x32_bf16 v[184:187], v[208:211], v[104:107], v[184:187]
v_fma_f32 v148, v148, s62, -v241
v_fma_f32 v149, v149, s62, -v241
v_mfma_f32_16x16x32_bf16 v[164:167], v[212:215], v[8:11], v[164:167]
ds_read_b128 v[208:211], v224 offset:43520
v_exp_f32 v148, v148
v_mfma_f32_16x16x32_bf16 v[172:175], v[212:215], v[40:43], v[172:175]
v_exp_f32 v149, v149
v_mfma_f32_16x16x32_bf16 v[180:183], v[212:215], v[72:75], v[180:183]
v_add_f32 v245, v245, v146
v_add_f32 v245, v245, v147
v_mfma_f32_16x16x32_bf16 v[188:191], v[212:215], v[104:107], v[188:191]
v_cvt_pk_bf16_f32 v145, v146, v147
v_fma_f32 v150, v150, s62, -v241
v_mfma_f32_16x16x32_bf16 v[160:163], v[216:219], v[12:15], v[160:163]
ds_read_b128 v[212:215], v224 offset:35840
v_fma_f32 v151, v151, s62, -v241
v_mfma_f32_16x16x32_bf16 v[168:171], v[216:219], v[44:47], v[168:171]
v_exp_f32 v150, v150
v_mfma_f32_16x16x32_bf16 v[176:179], v[216:219], v[76:79], v[176:179]
buffer_load_dwordx4 v231, s[44:47], s90 offen offset:2048 nt lds
v_mfma_f32_16x16x32_bf16 v[184:187], v[216:219], v[108:111], v[184:187]
v_exp_f32 v151, v151
v_mfma_f32_16x16x32_bf16 v[164:167], v[192:195], v[12:15], v[164:167]
ds_read_b128 v[216:219], v224 offset:44032
v_add_f32 v245, v245, v148
v_mfma_f32_16x16x32_bf16 v[172:175], v[192:195], v[44:47], v[172:175]
v_add_f32 v245, v245, v149
v_cvt_pk_bf16_f32 v146, v148, v149
v_mfma_f32_16x16x32_bf16 v[180:183], v[192:195], v[76:79], v[180:183]
v_fma_f32 v152, v152, s62, -v242
v_fma_f32 v153, v153, s62, -v242
v_mfma_f32_16x16x32_bf16 v[188:191], v[192:195], v[108:111], v[188:191]
v_exp_f32 v152, v152
s_waitcnt lgkmcnt(2)
v_mfma_f32_16x16x32_bf16 v[160:163], v[196:199], v[16:19], v[160:163]
ds_read_b128 v[192:195], v224 offset:36352
v_exp_f32 v153, v153
v_mfma_f32_16x16x32_bf16 v[168:171], v[196:199], v[48:51], v[168:171]
buffer_load_dwordx4 v231, s[44:47], s91 offen offset:3072 nt lds
v_mfma_f32_16x16x32_bf16 v[176:179], v[196:199], v[80:83], v[176:179]
s_add_u32 s44, s44, 0x4000
s_addc_u32 s45, s45, 0
v_mfma_f32_16x16x32_bf16 v[184:187], v[196:199], v[112:115], v[184:187]
v_add_f32 v245, v245, v150
v_add_f32 v245, v245, v151
v_mfma_f32_16x16x32_bf16 v[164:167], v[200:203], v[16:19], v[164:167]
ds_read_b128 v[196:199], v224 offset:44544
v_cvt_pk_bf16_f32 v147, v150, v151
v_mfma_f32_16x16x32_bf16 v[172:175], v[200:203], v[48:51], v[172:175]
v_fma_f32 v154, v154, s62, -v242
v_fma_f32 v155, v155, s62, -v242
v_mfma_f32_16x16x32_bf16 v[180:183], v[200:203], v[80:83], v[180:183]
v_exp_f32 v154, v154
v_mfma_f32_16x16x32_bf16 v[188:191], v[200:203], v[112:115], v[188:191]
v_exp_f32 v155, v155
v_mfma_f32_16x16x32_bf16 v[160:163], v[204:207], v[20:23], v[160:163]
v_add_f32 v246, v246, v152
v_add_f32 v246, v246, v153
v_mfma_f32_16x16x32_bf16 v[168:171], v[204:207], v[52:55], v[168:171]
s_waitcnt vmcnt(24)
s_waitcnt lgkmcnt(0)
v_cvt_pk_bf16_f32 v152, v152, v153
v_mfma_f32_16x16x32_bf16 v[176:179], v[204:207], v[84:87], v[176:179]
s_barrier
v_add_u32 v225, 0xffff0000, v225
v_mfma_f32_16x16x32_bf16 v[184:187], v[204:207], v[116:119], v[184:187]
s_add_u32 m0, s60, 0x18000
ds_read_b64_tr_b16 v[200:201], v225 offset:0
ds_read_b64_tr_b16 v[202:203], v225 offset:8192
v_mfma_f32_16x16x32_bf16 v[164:167], v[208:211], v[20:23], v[164:167]
v_add_u32 v226, 0xffff0000, v226
ds_read_b64_tr_b16 v[204:205], v226 offset:0
v_mfma_f32_16x16x32_bf16 v[172:175], v[208:211], v[52:55], v[172:175]
buffer_load_dwordx4 v232, s[48:51], s84 offen offset:0 nt lds
v_mfma_f32_16x16x32_bf16 v[180:183], v[208:211], v[84:87], v[180:183]
ds_read_b64_tr_b16 v[206:207], v226 offset:8192
v_fma_f32 v156, v156, s62, -v242
v_mfma_f32_16x16x32_bf16 v[188:191], v[208:211], v[116:119], v[188:191]
v_fma_f32 v157, v157, s62, -v242
v_mfma_f32_16x16x32_bf16 v[160:163], v[212:215], v[24:27], v[160:163]
ds_read_b64_tr_b16 v[208:209], v225 offset:512
v_exp_f32 v156, v156
v_mfma_f32_16x16x32_bf16 v[168:171], v[212:215], v[56:59], v[168:171]
ds_read_b64_tr_b16 v[210:211], v225 offset:8704
v_exp_f32 v157, v157
v_mfma_f32_16x16x32_bf16 v[176:179], v[212:215], v[88:91], v[176:179]
v_add_f32 v246, v246, v154
v_add_f32 v246, v246, v155
v_mfma_f32_16x16x32_bf16 v[184:187], v[212:215], v[120:123], v[184:187]
v_cvt_pk_bf16_f32 v153, v154, v155
v_fma_f32 v158, v158, s62, -v242
v_mfma_f32_16x16x32_bf16 v[164:167], v[216:219], v[24:27], v[164:167]
buffer_load_dwordx4 v232, s[48:51], s85 offen offset:1024 nt lds
v_mfma_f32_16x16x32_bf16 v[172:175], v[216:219], v[56:59], v[172:175]
v_fma_f32 v159, v159, s62, -v242
ds_read_b64_tr_b16 v[212:213], v226 offset:512
v_mfma_f32_16x16x32_bf16 v[180:183], v[216:219], v[88:91], v[180:183]
v_exp_f32 v158, v158
ds_read_b64_tr_b16 v[214:215], v226 offset:8704
v_mfma_f32_16x16x32_bf16 v[188:191], v[216:219], v[120:123], v[188:191]
v_exp_f32 v159, v159
v_mfma_f32_16x16x32_bf16 v[160:163], v[192:195], v[28:31], v[160:163]
v_add_f32 v246, v246, v156
v_add_f32 v246, v246, v157
v_mfma_f32_16x16x32_bf16 v[168:171], v[192:195], v[60:63], v[168:171]
v_cvt_pk_bf16_f32 v154, v156, v157
v_add_f32 v246, v246, v158
v_mfma_f32_16x16x32_bf16 v[176:179], v[192:195], v[92:95], v[176:179]
v_add_f32 v246, v246, v159
v_cvt_pk_bf16_f32 v155, v158, v159
v_mfma_f32_16x16x32_bf16 v[184:187], v[192:195], v[124:127], v[184:187]
ds_read_b64_tr_b16 v[216:217], v225 offset:1024
ds_read_b64_tr_b16 v[218:219], v225 offset:9216
v_mfma_f32_16x16x32_bf16 v[164:167], v[196:199], v[28:31], v[164:167]
buffer_load_dwordx4 v232, s[48:51], s86 offen offset:2048 nt lds
v_mfma_f32_16x16x32_bf16 v[172:175], v[196:199], v[60:63], v[172:175]
ds_read_b64_tr_b16 v[192:193], v226 offset:1024
ds_read_b64_tr_b16 v[194:195], v226 offset:9216
v_mfma_f32_16x16x32_bf16 v[180:183], v[196:199], v[92:95], v[180:183]
v_mfma_f32_16x16x32_bf16 v[188:191], v[196:199], v[124:127], v[188:191]
LST36:
s_waitcnt lgkmcnt(5)
v_mfma_f32_16x16x32_bf16 a[0:3], v[200:203], v[128:131], a[0:3]
ds_read_b64_tr_b16 v[196:197], v225 offset:1536
v_mfma_f32_16x16x32_bf16 a[4:7], v[200:203], v[136:139], a[4:7]
ds_read_b64_tr_b16 v[198:199], v225 offset:9728
v_mfma_f32_16x16x32_bf16 a[8:11], v[200:203], v[144:147], a[8:11]
v_max3_f32 v247, v160, v161, v162
v_max3_f32 v248, v168, v169, v170
v_mfma_f32_16x16x32_bf16 a[12:15], v[200:203], v[152:155], a[12:15]
buffer_load_dwordx4 v232, s[48:51], s87 offen offset:3072 nt lds
v_mfma_f32_16x16x32_bf16 a[16:19], v[204:207], v[128:131], a[16:19]
ds_read_b64_tr_b16 v[200:201], v226 offset:1536
v_max3_f32 v249, v176, v177, v178
v_mfma_f32_16x16x32_bf16 a[20:23], v[204:207], v[136:139], a[20:23]
ds_read_b64_tr_b16 v[202:203], v226 offset:9728
v_max3_f32 v250, v184, v185, v186
v_mfma_f32_16x16x32_bf16 a[24:27], v[204:207], v[144:147], a[24:27]
v_max3_f32 v247, v247, v163, v164
v_max3_f32 v248, v248, v171, v172
v_mfma_f32_16x16x32_bf16 a[28:31], v[204:207], v[152:155], a[28:31]
v_max3_f32 v249, v249, v179, v180
v_max3_f32 v250, v250, v187, v188
v_mfma_f32_16x16x32_bf16 a[32:35], v[208:211], v[128:131], a[32:35]
ds_read_b64_tr_b16 v[204:205], v225 offset:2048
v_max3_f32 v247, v247, v165, v166
v_mfma_f32_16x16x32_bf16 a[36:39], v[208:211], v[136:139], a[36:39]
s_add_u32 m0, s60, 0x1a000
ds_read_b64_tr_b16 v[206:207], v225 offset:10240
v_max3_f32 v248, v248, v173, v174
v_mfma_f32_16x16x32_bf16 a[40:43], v[208:211], v[144:147], a[40:43]
v_max3_f32 v249, v249, v181, v182
v_max3_f32 v250, v250, v189, v190
v_mfma_f32_16x16x32_bf16 a[44:47], v[208:211], v[152:155], a[44:47]
buffer_load_dwordx4 v232, s[48:51], s88 offen offset:0 nt lds
s_waitcnt lgkmcnt(4)
v_mfma_f32_16x16x32_bf16 a[48:51], v[212:215], v[128:131], a[48:51]
ds_read_b64_tr_b16 v[208:209], v226 offset:2048
v_max_f32 v247, v247, v167
v_mfma_f32_16x16x32_bf16 a[52:55], v[212:215], v[136:139], a[52:55]
ds_read_b64_tr_b16 v[210:211], v226 offset:10240
v_max_f32 v248, v248, v175
v_mfma_f32_16x16x32_bf16 a[56:59], v[212:215], v[144:147], a[56:59]
v_max_f32 v249, v249, v183
v_max_f32 v250, v250, v191
v_mfma_f32_16x16x32_bf16 a[60:63], v[212:215], v[152:155], a[60:63]
v_permlane32_swap_b32 v247, v249
v_permlane32_swap_b32 v248, v250
v_mfma_f32_16x16x32_bf16 a[64:67], v[216:219], v[128:131], a[64:67]
ds_read_b64_tr_b16 v[212:213], v225 offset:2560
v_max_f32 v247, v247, v249
v_mfma_f32_16x16x32_bf16 a[68:71], v[216:219], v[136:139], a[68:71]
ds_read_b64_tr_b16 v[214:215], v225 offset:10752
v_max_f32 v248, v248, v250
v_mfma_f32_16x16x32_bf16 a[72:75], v[216:219], v[144:147], a[72:75]
buffer_load_dwordx4 v232, s[48:51], s89 offen offset:1024 nt lds
v_mfma_f32_16x16x32_bf16 a[76:79], v[216:219], v[152:155], a[76:79]
v_permlane16_swap_b32 v247, v248
v_max_f32 v247, v247, v248
v_mfma_f32_16x16x32_bf16 a[80:83], v[192:195], v[128:131], a[80:83]
ds_read_b64_tr_b16 v[216:217], v226 offset:2560
v_cmp_gt_f32 vcc, v247, v238
v_mfma_f32_16x16x32_bf16 a[84:87], v[192:195], v[136:139], a[84:87]
ds_read_b64_tr_b16 v[218:219], v226 offset:10752
s_cbranch_vccnz LR11
LRB11:
v_mfma_f32_16x16x32_bf16 a[88:91], v[192:195], v[144:147], a[88:91]
v_fma_f32 v160, v160, s62, -v239
v_fma_f32 v161, v161, s62, -v239
v_mfma_f32_16x16x32_bf16 a[92:95], v[192:195], v[152:155], a[92:95]
v_exp_f32 v160, v160
v_mfma_f32_16x16x32_bf16 a[96:99], v[196:199], v[128:131], a[96:99]
ds_read_b64_tr_b16 v[192:193], v225 offset:3072
v_exp_f32 v161, v161
v_mfma_f32_16x16x32_bf16 a[100:103], v[196:199], v[136:139], a[100:103]
ds_read_b64_tr_b16 v[194:195], v225 offset:11264
v_fma_f32 v162, v162, s62, -v239
v_mfma_f32_16x16x32_bf16 a[104:107], v[196:199], v[144:147], a[104:107]
buffer_load_dwordx4 v232, s[48:51], s90 offen offset:2048 nt lds
v_mfma_f32_16x16x32_bf16 a[108:111], v[196:199], v[152:155], a[108:111]
v_fma_f32 v163, v163, s62, -v239
s_waitcnt lgkmcnt(4)
v_mfma_f32_16x16x32_bf16 a[112:115], v[200:203], v[128:131], a[112:115]
ds_read_b64_tr_b16 v[196:197], v226 offset:3072
v_exp_f32 v162, v162
v_mfma_f32_16x16x32_bf16 a[116:119], v[200:203], v[136:139], a[116:119]
ds_read_b64_tr_b16 v[198:199], v226 offset:11264
v_exp_f32 v163, v163
v_mfma_f32_16x16x32_bf16 a[120:123], v[200:203], v[144:147], a[120:123]
v_add_f32 v243, v243, v160
v_add_f32 v243, v243, v161
v_mfma_f32_16x16x32_bf16 a[124:127], v[200:203], v[152:155], a[124:127]
v_cvt_pk_bf16_f32 v160, v160, v161
v_fma_f32 v164, v164, s62, -v239
v_mfma_f32_16x16x32_bf16 a[128:131], v[204:207], v[128:131], a[128:131]
ds_read_b64_tr_b16 v[200:201], v225 offset:3584
v_fma_f32 v165, v165, s62, -v239
v_mfma_f32_16x16x32_bf16 a[132:135], v[204:207], v[136:139], a[132:135]
buffer_load_dwordx4 v232, s[48:51], s91 offen offset:3072 nt lds
v_mfma_f32_16x16x32_bf16 a[136:139], v[204:207], v[144:147], a[136:139]
ds_read_b64_tr_b16 v[202:203], v225 offset:11776
v_exp_f32 v164, v164
v_mfma_f32_16x16x32_bf16 a[140:143], v[204:207], v[152:155], a[140:143]
s_add_u32 s48, s48, 0x4000
s_addc_u32 s49, s49, 0
v_mfma_f32_16x16x32_bf16 a[144:147], v[208:211], v[128:131], a[144:147]
ds_read_b64_tr_b16 v[204:205], v226 offset:3584
v_exp_f32 v165, v165
v_mfma_f32_16x16x32_bf16 a[148:151], v[208:211], v[136:139], a[148:151]
ds_read_b64_tr_b16 v[206:207], v226 offset:11776
v_add_f32 v243, v243, v162
v_mfma_f32_16x16x32_bf16 a[152:155], v[208:211], v[144:147], a[152:155]
v_add_f32 v243, v243, v163
v_cvt_pk_bf16_f32 v161, v162, v163
v_mfma_f32_16x16x32_bf16 a[156:159], v[208:211], v[152:155], a[156:159]
v_fma_f32 v166, v166, s62, -v239
v_fma_f32 v167, v167, s62, -v239
v_mfma_f32_16x16x32_bf16 a[160:163], v[212:215], v[128:131], a[160:163]
v_exp_f32 v166, v166
v_mfma_f32_16x16x32_bf16 a[164:167], v[212:215], v[136:139], a[164:167]
s_waitcnt vmcnt(24)
s_waitcnt lgkmcnt(0)
v_exp_f32 v167, v167
v_mfma_f32_16x16x32_bf16 a[168:171], v[212:215], v[144:147], a[168:171]
s_barrier
v_add_u32 v224, 0x10000, v224
v_mfma_f32_16x16x32_bf16 a[172:175], v[212:215], v[152:155], a[172:175]
s_add_u32 m0, s60, 0x0
ds_read_b128 v[208:211], v224 offset:0
v_add_f32 v243, v243, v164
v_mfma_f32_16x16x32_bf16 a[176:179], v[216:219], v[128:131], a[176:179]
ds_read_b128 v[212:215], v224 offset:8192
v_add_f32 v243, v243, v165
v_mfma_f32_16x16x32_bf16 a[180:183], v[216:219], v[136:139], a[180:183]
buffer_load_dwordx4 v231, s[44:47], s84 offen offset:0 nt lds
v_mfma_f32_16x16x32_bf16 a[184:187], v[216:219], v[144:147], a[184:187]
v_cvt_pk_bf16_f32 v162, v164, v165
v_fma_f32 v168, v168, s62, -v240
v_mfma_f32_16x16x32_bf16 a[188:191], v[216:219], v[152:155], a[188:191]
v_fma_f32 v169, v169, s62, -v240
v_mfma_f32_16x16x32_bf16 a[192:195], v[192:195], v[128:131], a[192:195]
ds_read_b128 v[216:219], v224 offset:512
v_exp_f32 v168, v168
v_mfma_f32_16x16x32_bf16 a[196:199], v[192:195], v[136:139], a[196:199]
v_exp_f32 v169, v169
v_mfma_f32_16x16x32_bf16 a[200:203], v[192:195], v[144:147], a[200:203]
v_add_f32 v243, v243, v166
v_add_f32 v243, v243, v167
v_mfma_f32_16x16x32_bf16 a[204:207], v[192:195], v[152:155], a[204:207]
v_cvt_pk_bf16_f32 v163, v166, v167
v_fma_f32 v170, v170, s62, -v240
v_mfma_f32_16x16x32_bf16 a[208:211], v[196:199], v[128:131], a[208:211]
buffer_load_dwordx4 v231, s[44:47], s85 offen offset:1024 nt lds
v_mfma_f32_16x16x32_bf16 a[212:215], v[196:199], v[136:139], a[212:215]
ds_read_b128 v[192:195], v224 offset:8704
v_fma_f32 v171, v171, s62, -v240
v_mfma_f32_16x16x32_bf16 a[216:219], v[196:199], v[144:147], a[216:219]
v_exp_f32 v170, v170
v_mfma_f32_16x16x32_bf16 a[220:223], v[196:199], v[152:155], a[220:223]
v_exp_f32 v171, v171
v_mfma_f32_16x16x32_bf16 a[224:227], v[200:203], v[128:131], a[224:227]
ds_read_b128 v[196:199], v224 offset:1024
v_add_f32 v244, v244, v168
v_mfma_f32_16x16x32_bf16 a[228:231], v[200:203], v[136:139], a[228:231]
v_add_f32 v244, v244, v169
v_cvt_pk_bf16_f32 v168, v168, v169
v_mfma_f32_16x16x32_bf16 a[232:235], v[200:203], v[144:147], a[232:235]
v_fma_f32 v172, v172, s62, -v240
v_fma_f32 v173, v173, s62, -v240
v_mfma_f32_16x16x32_bf16 a[236:239], v[200:203], v[152:155], a[236:239]
v_exp_f32 v172, v172
v_mfma_f32_16x16x32_bf16 a[240:243], v[204:207], v[128:131], a[240:243]
buffer_load_dwordx4 v231, s[44:47], s86 offen offset:2048 nt lds
v_mfma_f32_16x16x32_bf16 a[244:247], v[204:207], v[136:139], a[244:247]
ds_read_b128 v[200:203], v224 offset:9216
v_exp_f32 v173, v173
v_mfma_f32_16x16x32_bf16 a[248:251], v[204:207], v[144:147], a[248:251]
v_add_f32 v244, v244, v170
v_add_f32 v244, v244, v171
v_mfma_f32_16x16x32_bf16 a[252:255], v[204:207], v[152:155], a[252:255]
LST37:
s_cmp_lg_u32 s63, 0
s_cbranch_scc1 LS11
LSB11:
s_waitcnt lgkmcnt(2)
v_mfma_f32_16x16x32_bf16 v[128:131], v[208:211], v[0:3], 0
ds_read_b128 v[204:207], v224 offset:1536
LH10:
s_cbranch_execz LH14
v_cvt_pk_bf16_f32 v169, v170, v171
v_mfma_f32_16x16x32_bf16 v[136:139], v[208:211], v[32:35], 0
v_fma_f32 v174, v174, s62, -v240
v_fma_f32 v175, v175, s62, -v240
v_mfma_f32_16x16x32_bf16 v[144:147], v[208:211], v[64:67], 0
v_exp_f32 v174, v174
v_mfma_f32_16x16x32_bf16 v[152:155], v[208:211], v[96:99], 0
buffer_load_dwordx4 v231, s[44:47], s87 offen offset:3072 nt lds
v_mfma_f32_16x16x32_bf16 v[132:135], v[212:215], v[0:3], 0
ds_read_b128 v[208:211], v224 offset:9728
v_exp_f32 v175, v175
v_mfma_f32_16x16x32_bf16 v[140:143], v[212:215], v[32:35], 0
v_add_f32 v244, v244, v172
v_add_f32 v244, v244, v173
v_mfma_f32_16x16x32_bf16 v[148:151], v[212:215], v[64:67], 0
v_cvt_pk_bf16_f32 v170, v172, v173
v_fma_f32 v176, v176, s62, -v241
v_mfma_f32_16x16x32_bf16 v[156:159], v[212:215], v[96:99], 0
v_fma_f32 v177, v177, s62, -v241
v_mfma_f32_16x16x32_bf16 v[128:131], v[216:219], v[4:7], v[128:131]
ds_read_b128 v[212:215], v224 offset:2048
v_exp_f32 v176, v176
v_mfma_f32_16x16x32_bf16 v[136:139], v[216:219], v[36:39], v[136:139]
s_add_u32 m0, s60, 0x2000
v_exp_f32 v177, v177
v_mfma_f32_16x16x32_bf16 v[144:147], v[216:219], v[68:71], v[144:147]
v_add_f32 v244, v244, v174
v_add_f32 v244, v244, v175
v_mfma_f32_16x16x32_bf16 v[152:155], v[216:219], v[100:103], v[152:155]
buffer_load_dwordx4 v231, s[44:47], s88 offen offset:0 nt lds
v_mfma_f32_16x16x32_bf16 v[132:135], v[192:195], v[4:7], v[132:135]
ds_read_b128 v[216:219], v224 offset:10240
v_cvt_pk_bf16_f32 v171, v174, v175
v_mfma_f32_16x16x32_bf16 v[140:143], v[192:195], v[36:39], v[140:143]
v_fma_f32 v178, v178, s62, -v241
v_fma_f32 v179, v179, s62, -v241
v_mfma_f32_16x16x32_bf16 v[148:151], v[192:195], v[68:71], v[148:151]
v_exp_f32 v178, v178
v_mfma_f32_16x16x32_bf16 v[156:159], v[192:195], v[100:103], v[156:159]
v_exp_f32 v179, v179
s_waitcnt lgkmcnt(2)
v_mfma_f32_16x16x32_bf16 v[128:131], v[196:199], v[8:11], v[128:131]
ds_read_b128 v[192:195], v224 offset:2560
v_add_f32 v245, v245, v176
v_mfma_f32_16x16x32_bf16 v[136:139], v[196:199], v[40:43], v[136:139]
v_add_f32 v245, v245, v177
v_cvt_pk_bf16_f32 v176, v176, v177
v_mfma_f32_16x16x32_bf16 v[144:147], v[196:199], v[72:75], v[144:147]
buffer_load_dwordx4 v231, s[44:47], s89 offen offset:1024 nt lds
v_mfma_f32_16x16x32_bf16 v[152:155], v[196:199], v[104:107], v[152:155]
v_fma_f32 v180, v180, s62, -v241
v_fma_f32 v181, v181, s62, -v241
v_mfma_f32_16x16x32_bf16 v[132:135], v[200:203], v[8:11], v[132:135]
ds_read_b128 v[196:199], v224 offset:10752
v_exp_f32 v180, v180
v_mfma_f32_16x16x32_bf16 v[140:143], v[200:203], v[40:43], v[140:143]
v_exp_f32 v181, v181
v_mfma_f32_16x16x32_bf16 v[148:151], v[200:203], v[72:75], v[148:151]
v_add_f32 v245, v245, v178
v_add_f32 v245, v245, v179
v_mfma_f32_16x16x32_bf16 v[156:159], v[200:203], v[104:107], v[156:159]
v_cvt_pk_bf16_f32 v177, v178, v179
v_fma_f32 v182, v182, s62, -v241
v_mfma_f32_16x16x32_bf16 v[128:131], v[204:207], v[12:15], v[128:131]
ds_read_b128 v[200:203], v224 offset:3072
v_fma_f32 v183, v183, s62, -v241
v_mfma_f32_16x16x32_bf16 v[136:139], v[204:207], v[44:47], v[136:139]
v_exp_f32 v182, v182
v_mfma_f32_16x16x32_bf16 v[144:147], v[204:207], v[76:79], v[144:147]
buffer_load_dwordx4 v231, s[44:47], s90 offen offset:2048 nt lds
v_mfma_f32_16x16x32_bf16 v[152:155], v[204:207], v[108:111], v[152:155]
v_exp_f32 v183, v183
v_mfma_f32_16x16x32_bf16 v[132:135], v[208:211], v[12:15], v[132:135]
ds_read_b128 v[204:207], v224 offset:11264
v_add_f32 v245, v245, v180
v_mfma_f32_16x16x32_bf16 v[140:143], v[208:211], v[44:47], v[140:143]
v_add_f32 v245, v245, v181
v_cvt_pk_bf16_f32 v178, v180, v181
v_mfma_f32_16x16x32_bf16 v[148:151], v[208:211], v[76:79], v[148:151]
v_fma_f32 v184, v184, s62, -v242
v_fma_f32 v185, v185, s62, -v242
v_mfma_f32_16x16x32_bf16 v[156:159], v[208:211], v[108:111], v[156:159]
v_exp_f32 v184, v184
s_waitcnt lgkmcnt(2)
v_mfma_f32_16x16x32_bf16 v[128:131], v[212:215], v[16:19], v[128:131]
ds_read_b128 v[208:211], v224 offset:3584
v_exp_f32 v185, v185
v_mfma_f32_16x16x32_bf16 v[136:139], v[212:215], v[48:51], v[136:139]
buffer_load_dwordx4 v231, s[44:47], s91 offen offset:3072 nt lds
v_mfma_f32_16x16x32_bf16 v[144:147], v[212:215], v[80:83], v[144:147]
s_add_u32 s44, s44, 0x4000
s_addc_u32 s45, s45, 0
v_mfma_f32_16x16x32_bf16 v[152:155], v[212:215], v[112:115], v[152:155]
v_add_f32 v245, v245, v182
v_add_f32 v245, v245, v183
v_mfma_f32_16x16x32_bf16 v[132:135], v[216:219], v[16:19], v[132:135]
ds_read_b128 v[212:215], v224 offset:11776
v_cvt_pk_bf16_f32 v179, v182, v183
v_mfma_f32_16x16x32_bf16 v[140:143], v[216:219], v[48:51], v[140:143]
v_fma_f32 v186, v186, s62, -v242
v_fma_f32 v187, v187, s62, -v242
v_mfma_f32_16x16x32_bf16 v[148:151], v[216:219], v[80:83], v[148:151]
v_exp_f32 v186, v186
v_mfma_f32_16x16x32_bf16 v[156:159], v[216:219], v[112:115], v[156:159]
v_exp_f32 v187, v187
v_mfma_f32_16x16x32_bf16 v[128:131], v[192:195], v[20:23], v[128:131]
v_add_f32 v246, v246, v184
v_add_f32 v246, v246, v185
v_mfma_f32_16x16x32_bf16 v[136:139], v[192:195], v[52:55], v[136:139]
s_waitcnt vmcnt(24)
s_waitcnt lgkmcnt(0)
v_cvt_pk_bf16_f32 v184, v184, v185
v_mfma_f32_16x16x32_bf16 v[144:147], v[192:195], v[84:87], v[144:147]
s_barrier
ds_read_b64_tr_b16 v[216:217], v225 offset:32768
v_mfma_f32_16x16x32_bf16 v[152:155], v[192:195], v[116:119], v[152:155]
s_add_u32 m0, s60, 0x20000
ds_read_b64_tr_b16 v[218:219], v225 offset:40960
v_fma_f32 v188, v188, s62, -v242
v_mfma_f32_16x16x32_bf16 v[132:135], v[196:199], v[20:23], v[132:135]
ds_read_b64_tr_b16 v[192:193], v226 offset:32768
v_fma_f32 v189, v189, s62, -v242
v_mfma_f32_16x16x32_bf16 v[140:143], v[196:199], v[52:55], v[140:143]
buffer_load_dwordx4 v232, s[48:51], s84 offen offset:0 nt lds
v_mfma_f32_16x16x32_bf16 v[148:151], v[196:199], v[84:87], v[148:151]
ds_read_b64_tr_b16 v[194:195], v226 offset:40960
v_exp_f32 v188, v188
v_mfma_f32_16x16x32_bf16 v[156:159], v[196:199], v[116:119], v[156:159]
v_exp_f32 v189, v189
v_mfma_f32_16x16x32_bf16 v[128:131], v[200:203], v[24:27], v[128:131]
ds_read_b64_tr_b16 v[196:197], v225 offset:33280
v_add_f32 v246, v246, v186
v_mfma_f32_16x16x32_bf16 v[136:139], v[200:203], v[56:59], v[136:139]
ds_read_b64_tr_b16 v[198:199], v225 offset:41472
v_add_f32 v246, v246, v187
v_mfma_f32_16x16x32_bf16 v[144:147], v[200:203], v[88:91], v[144:147]
v_cvt_pk_bf16_f32 v185, v186, v187
v_fma_f32 v190, v190, s62, -v242
v_mfma_f32_16x16x32_bf16 v[152:155], v[200:203], v[120:123], v[152:155]
v_fma_f32 v191, v191, s62, -v242
v_mfma_f32_16x16x32_bf16 v[132:135], v[204:207], v[24:27], v[132:135]
buffer_load_dwordx4 v232, s[48:51], s85 offen offset:1024 nt lds
v_mfma_f32_16x16x32_bf16 v[140:143], v[204:207], v[56:59], v[140:143]
v_exp_f32 v190, v190
ds_read_b64_tr_b16 v[200:201], v226 offset:33280
v_mfma_f32_16x16x32_bf16 v[148:151], v[204:207], v[88:91], v[148:151]
v_exp_f32 v191, v191
ds_read_b64_tr_b16 v[202:203], v226 offset:41472
v_mfma_f32_16x16x32_bf16 v[156:159], v[204:207], v[120:123], v[156:159]
v_add_f32 v246, v246, v188
v_add_f32 v246, v246, v189
v_mfma_f32_16x16x32_bf16 v[128:131], v[208:211], v[28:31], v[128:131]
v_cvt_pk_bf16_f32 v186, v188, v189
v_add_f32 v246, v246, v190
v_mfma_f32_16x16x32_bf16 v[136:139], v[208:211], v[60:63], v[136:139]
v_add_f32 v246, v246, v191
v_cvt_pk_bf16_f32 v187, v190, v191
v_mfma_f32_16x16x32_bf16 v[144:147], v[208:211], v[92:95], v[144:147]
ds_read_b64_tr_b16 v[204:205], v225 offset:33792
ds_read_b64_tr_b16 v[206:207], v225 offset:41984
v_mfma_f32_16x16x32_bf16 v[152:155], v[208:211], v[124:127], v[152:155]
v_mfma_f32_16x16x32_bf16 v[132:135], v[212:215], v[28:31], v[132:135]
buffer_load_dwordx4 v232, s[48:51], s86 offen offset:2048 nt lds
v_mfma_f32_16x16x32_bf16 v[140:143], v[212:215], v[60:63], v[140:143]
ds_read_b64_tr_b16 v[208:209], v226 offset:33792
ds_read_b64_tr_b16 v[210:211], v226 offset:41984
v_mfma_f32_16x16x32_bf16 v[148:151], v[212:215], v[92:95], v[148:151]
v_mfma_f32_16x16x32_bf16 v[156:159], v[212:215], v[124:127], v[156:159]
LST38:
s_waitcnt lgkmcnt(5)
v_mfma_f32_16x16x32_bf16 a[0:3], v[216:219], v[160:163], a[0:3]
ds_read_b64_tr_b16 v[212:213], v225 offset:34304
v_mfma_f32_16x16x32_bf16 a[4:7], v[216:219], v[168:171], a[4:7]
ds_read_b64_tr_b16 v[214:215], v225 offset:42496
v_mfma_f32_16x16x32_bf16 a[8:11], v[216:219], v[176:179], a[8:11]
v_max3_f32 v247, v128, v129, v130
v_max3_f32 v248, v136, v137, v138
v_mfma_f32_16x16x32_bf16 a[12:15], v[216:219], v[184:187], a[12:15]
buffer_load_dwordx4 v232, s[48:51], s87 offen offset:3072 nt lds
v_mfma_f32_16x16x32_bf16 a[16:19], v[192:195], v[160:163], a[16:19]
ds_read_b64_tr_b16 v[216:217], v226 offset:34304
v_max3_f32 v249, v144, v145, v146
v_mfma_f32_16x16x32_bf16 a[20:23], v[192:195], v[168:171], a[20:23]
ds_read_b64_tr_b16 v[218:219], v226 offset:42496
v_max3_f32 v250, v152, v153, v154
v_mfma_f32_16x16x32_bf16 a[24:27], v[192:195], v[176:179], a[24:27]
v_max3_f32 v247, v247, v131, v132
v_max3_f32 v248, v248, v139, v140
v_mfma_f32_16x16x32_bf16 a[28:31], v[192:195], v[184:187], a[28:31]
v_max3_f32 v249, v249, v147, v148
v_max3_f32 v250, v250, v155, v156
v_mfma_f32_16x16x32_bf16 a[32:35], v[196:199], v[160:163], a[32:35]
ds_read_b64_tr_b16 v[192:193], v225 offset:34816
v_max3_f32 v247, v247, v133, v134
v_mfma_f32_16x16x32_bf16 a[36:39], v[196:199], v[168:171], a[36:39]
s_add_u32 m0, s60, 0x22000
ds_read_b64_tr_b16 v[194:195], v225 offset:43008
v_max3_f32 v248, v248, v141, v142
v_mfma_f32_16x16x32_bf16 a[40:43], v[196:199], v[176:179], a[40:43]
v_max3_f32 v249, v249, v149, v150
v_max3_f32 v250, v250, v157, v158
v_mfma_f32_16x16x32_bf16 a[44:47], v[196:199], v[184:187], a[44:47]
buffer_load_dwordx4 v232, s[48:51], s88 offen offset:0 nt lds
s_waitcnt lgkmcnt(4)
v_mfma_f32_16x16x32_bf16 a[48:51], v[200:203], v[160:163], a[48:51]
ds_read_b64_tr_b16 v[196:197], v226 offset:34816
v_max_f32 v247, v247, v135
v_mfma_f32_16x16x32_bf16 a[52:55], v[200:203], v[168:171], a[52:55]
ds_read_b64_tr_b16 v[198:199], v226 offset:43008
v_max_f32 v248, v248, v143
v_mfma_f32_16x16x32_bf16 a[56:59], v[200:203], v[176:179], a[56:59]
v_max_f32 v249, v249, v151
v_max_f32 v250, v250, v159
v_mfma_f32_16x16x32_bf16 a[60:63], v[200:203], v[184:187], a[60:63]
v_permlane32_swap_b32 v247, v249
v_permlane32_swap_b32 v248, v250
v_mfma_f32_16x16x32_bf16 a[64:67], v[204:207], v[160:163], a[64:67]
ds_read_b64_tr_b16 v[200:201], v225 offset:35328
v_max_f32 v247, v247, v249
v_mfma_f32_16x16x32_bf16 a[68:71], v[204:207], v[168:171], a[68:71]
ds_read_b64_tr_b16 v[202:203], v225 offset:43520
v_max_f32 v248, v248, v250
v_mfma_f32_16x16x32_bf16 a[72:75], v[204:207], v[176:179], a[72:75]
buffer_load_dwordx4 v232, s[48:51], s89 offen offset:1024 nt lds
v_mfma_f32_16x16x32_bf16 a[76:79], v[204:207], v[184:187], a[76:79]
v_permlane16_swap_b32 v247, v248
v_max_f32 v247, v247, v248
v_mfma_f32_16x16x32_bf16 a[80:83], v[208:211], v[160:163], a[80:83]
ds_read_b64_tr_b16 v[204:205], v226 offset:35328
v_cmp_gt_f32 vcc, v247, v238
v_mfma_f32_16x16x32_bf16 a[84:87], v[208:211], v[168:171], a[84:87]
ds_read_b64_tr_b16 v[206:207], v226 offset:43520
s_cbranch_vccnz LR12
LRB12:
v_mfma_f32_16x16x32_bf16 a[88:91], v[208:211], v[176:179], a[88:91]
v_fma_f32 v128, v128, s62, -v239
v_fma_f32 v129, v129, s62, -v239
v_mfma_f32_16x16x32_bf16 a[92:95], v[208:211], v[184:187], a[92:95]
v_exp_f32 v128, v128
v_mfma_f32_16x16x32_bf16 a[96:99], v[212:215], v[160:163], a[96:99]
ds_read_b64_tr_b16 v[208:209], v225 offset:35840
v_exp_f32 v129, v129
v_mfma_f32_16x16x32_bf16 a[100:103], v[212:215], v[168:171], a[100:103]
ds_read_b64_tr_b16 v[210:211], v225 offset:44032
v_fma_f32 v130, v130, s62, -v239
v_mfma_f32_16x16x32_bf16 a[104:107], v[212:215], v[176:179], a[104:107]
buffer_load_dwordx4 v232, s[48:51], s90 offen offset:2048 nt lds
v_mfma_f32_16x16x32_bf16 a[108:111], v[212:215], v[184:187], a[108:111]
v_fma_f32 v131, v131, s62, -v239
s_waitcnt lgkmcnt(4)
v_mfma_f32_16x16x32_bf16 a[112:115], v[216:219], v[160:163], a[112:115]
ds_read_b64_tr_b16 v[212:213], v226 offset:35840
v_exp_f32 v130, v130
v_mfma_f32_16x16x32_bf16 a[116:119], v[216:219], v[168:171], a[116:119]
ds_read_b64_tr_b16 v[214:215], v226 offset:44032
v_exp_f32 v131, v131
v_mfma_f32_16x16x32_bf16 a[120:123], v[216:219], v[176:179], a[120:123]
v_add_f32 v243, v243, v128
v_add_f32 v243, v243, v129
v_mfma_f32_16x16x32_bf16 a[124:127], v[216:219], v[184:187], a[124:127]
v_cvt_pk_bf16_f32 v128, v128, v129
v_fma_f32 v132, v132, s62, -v239
v_mfma_f32_16x16x32_bf16 a[128:131], v[192:195], v[160:163], a[128:131]
ds_read_b64_tr_b16 v[216:217], v225 offset:36352
v_fma_f32 v133, v133, s62, -v239
v_mfma_f32_16x16x32_bf16 a[132:135], v[192:195], v[168:171], a[132:135]
buffer_load_dwordx4 v232, s[48:51], s91 offen offset:3072 nt lds
v_mfma_f32_16x16x32_bf16 a[136:139], v[192:195], v[176:179], a[136:139]
ds_read_b64_tr_b16 v[218:219], v225 offset:44544
v_exp_f32 v132, v132
v_mfma_f32_16x16x32_bf16 a[140:143], v[192:195], v[184:187], a[140:143]
s_add_u32 s48, s48, 0x4000
s_addc_u32 s49, s49, 0
v_mfma_f32_16x16x32_bf16 a[144:147], v[196:199], v[160:163], a[144:147]
ds_read_b64_tr_b16 v[192:193], v226 offset:36352
v_exp_f32 v133, v133
v_mfma_f32_16x16x32_bf16 a[148:151], v[196:199], v[168:171], a[148:151]
ds_read_b64_tr_b16 v[194:195], v226 offset:44544
v_add_f32 v243, v243, v130
v_mfma_f32_16x16x32_bf16 a[152:155], v[196:199], v[176:179], a[152:155]
v_add_f32 v243, v243, v131
v_cvt_pk_bf16_f32 v129, v130, v131
v_mfma_f32_16x16x32_bf16 a[156:159], v[196:199], v[184:187], a[156:159]
v_fma_f32 v134, v134, s62, -v239
v_fma_f32 v135, v135, s62, -v239
v_mfma_f32_16x16x32_bf16 a[160:163], v[200:203], v[160:163], a[160:163]
v_exp_f32 v134, v134
v_mfma_f32_16x16x32_bf16 a[164:167], v[200:203], v[168:171], a[164:167]
s_waitcnt vmcnt(24)
s_waitcnt lgkmcnt(0)
v_exp_f32 v135, v135
v_mfma_f32_16x16x32_bf16 a[168:171], v[200:203], v[176:179], a[168:171]
s_barrier
v_add_u32 v224, 0xffff0000, v224
v_mfma_f32_16x16x32_bf16 a[172:175], v[200:203], v[184:187], a[172:175]
s_add_u32 m0, s60, 0x8000
ds_read_b128 v[196:199], v224 offset:0
v_add_f32 v243, v243, v132
v_mfma_f32_16x16x32_bf16 a[176:179], v[204:207], v[160:163], a[176:179]
ds_read_b128 v[200:203], v224 offset:8192
v_add_f32 v243, v243, v133
v_mfma_f32_16x16x32_bf16 a[180:183], v[204:207], v[168:171], a[180:183]
buffer_load_dwordx4 v231, s[44:47], s84 offen offset:0 nt lds
v_mfma_f32_16x16x32_bf16 a[184:187], v[204:207], v[176:179], a[184:187]
v_cvt_pk_bf16_f32 v130, v132, v133
v_fma_f32 v136, v136, s62, -v240
v_mfma_f32_16x16x32_bf16 a[188:191], v[204:207], v[184:187], a[188:191]
v_fma_f32 v137, v137, s62, -v240
v_mfma_f32_16x16x32_bf16 a[192:195], v[208:211], v[160:163], a[192:195]
ds_read_b128 v[204:207], v224 offset:512
v_exp_f32 v136, v136
v_mfma_f32_16x16x32_bf16 a[196:199], v[208:211], v[168:171], a[196:199]
v_exp_f32 v137, v137
v_mfma_f32_16x16x32_bf16 a[200:203], v[208:211], v[176:179], a[200:203]
v_add_f32 v243, v243, v134
v_add_f32 v243, v243, v135
v_mfma_f32_16x16x32_bf16 a[204:207], v[208:211], v[184:187], a[204:207]
v_cvt_pk_bf16_f32 v131, v134, v135
v_fma_f32 v138, v138, s62, -v240
v_mfma_f32_16x16x32_bf16 a[208:211], v[212:215], v[160:163], a[208:211]
buffer_load_dwordx4 v231, s[44:47], s85 offen offset:1024 nt lds
v_mfma_f32_16x16x32_bf16 a[212:215], v[212:215], v[168:171], a[212:215]
ds_read_b128 v[208:211], v224 offset:8704
v_fma_f32 v139, v139, s62, -v240
v_mfma_f32_16x16x32_bf16 a[216:219], v[212:215], v[176:179], a[216:219]
v_exp_f32 v138, v138
v_mfma_f32_16x16x32_bf16 a[220:223], v[212:215], v[184:187], a[220:223]
v_exp_f32 v139, v139
v_mfma_f32_16x16x32_bf16 a[224:227], v[216:219], v[160:163], a[224:227]
ds_read_b128 v[212:215], v224 offset:1024
v_add_f32 v244, v244, v136
v_mfma_f32_16x16x32_bf16 a[228:231], v[216:219], v[168:171], a[228:231]
v_add_f32 v244, v244, v137
v_cvt_pk_bf16_f32 v136, v136, v137
v_mfma_f32_16x16x32_bf16 a[232:235], v[216:219], v[176:179], a[232:235]
v_fma_f32 v140, v140, s62, -v240
v_fma_f32 v141, v141, s62, -v240
v_mfma_f32_16x16x32_bf16 a[236:239], v[216:219], v[184:187], a[236:239]
v_exp_f32 v140, v140
v_mfma_f32_16x16x32_bf16 a[240:243], v[192:195], v[160:163], a[240:243]
buffer_load_dwordx4 v231, s[44:47], s86 offen offset:2048 nt lds
v_mfma_f32_16x16x32_bf16 a[244:247], v[192:195], v[168:171], a[244:247]
ds_read_b128 v[216:219], v224 offset:9216
v_exp_f32 v141, v141
v_mfma_f32_16x16x32_bf16 a[248:251], v[192:195], v[176:179], a[248:251]
v_add_f32 v244, v244, v138
v_add_f32 v244, v244, v139
v_mfma_f32_16x16x32_bf16 a[252:255], v[192:195], v[184:187], a[252:255]
LST39:
s_cmp_lg_u32 s63, 0
s_cbranch_scc1 LS12
LSB12:
s_waitcnt lgkmcnt(2)
v_mfma_f32_16x16x32_bf16 v[160:163], v[196:199], v[0:3], 0
ds_read_b128 v[192:195], v224 offset:1536
v_cvt_pk_bf16_f32 v137, v138, v139
v_mfma_f32_16x16x32_bf16 v[168:171], v[196:199], v[32:35], 0
v_fma_f32 v142, v142, s62, -v240
v_fma_f32 v143, v143, s62, -v240
v_mfma_f32_16x16x32_bf16 v[176:179], v[196:199], v[64:67], 0
v_exp_f32 v142, v142
v_mfma_f32_16x16x32_bf16 v[184:187], v[196:199], v[96:99], 0
buffer_load_dwordx4 v231, s[44:47], s87 offen offset:3072 nt lds
v_mfma_f32_16x16x32_bf16 v[164:167], v[200:203], v[0:3], 0
ds_read_b128 v[196:199], v224 offset:9728
v_exp_f32 v143, v143
v_mfma_f32_16x16x32_bf16 v[172:175], v[200:203], v[32:35], 0
v_add_f32 v244, v244, v140
v_add_f32 v244, v244, v141
v_mfma_f32_16x16x32_bf16 v[180:183], v[200:203], v[64:67], 0
v_cvt_pk_bf16_f32 v138, v140, v141
v_fma_f32 v144, v144, s62, -v241
v_mfma_f32_16x16x32_bf16 v[188:191], v[200:203], v[96:99], 0
v_fma_f32 v145, v145, s62, -v241
v_mfma_f32_16x16x32_bf16 v[160:163], v[204:207], v[4:7], v[160:163]
ds_read_b128 v[200:203], v224 offset:2048
v_exp_f32 v144, v144
v_mfma_f32_16x16x32_bf16 v[168:171], v[204:207], v[36:39], v[168:171]
s_add_u32 m0, s60, 0xa000
v_exp_f32 v145, v145
v_mfma_f32_16x16x32_bf16 v[176:179], v[204:207], v[68:71], v[176:179]
v_add_f32 v244, v244, v142
v_add_f32 v244, v244, v143
v_mfma_f32_16x16x32_bf16 v[184:187], v[204:207], v[100:103], v[184:187]
buffer_load_dwordx4 v231, s[44:47], s88 offen offset:0 nt lds
v_mfma_f32_16x16x32_bf16 v[164:167], v[208:211], v[4:7], v[164:167]
ds_read_b128 v[204:207], v224 offset:10240
v_cvt_pk_bf16_f32 v139, v142, v143
v_mfma_f32_16x16x32_bf16 v[172:175], v[208:211], v[36:39], v[172:175]
v_fma_f32 v146, v146, s62, -v241
v_fma_f32 v147, v147, s62, -v241
v_mfma_f32_16x16x32_bf16 v[180:183], v[208:211], v[68:71], v[180:183]
v_exp_f32 v146, v146
v_mfma_f32_16x16x32_bf16 v[188:191], v[208:211], v[100:103], v[188:191]
v_exp_f32 v147, v147
s_waitcnt lgkmcnt(2)
v_mfma_f32_16x16x32_bf16 v[160:163], v[212:215], v[8:11], v[160:163]
ds_read_b128 v[208:211], v224 offset:2560
v_add_f32 v245, v245, v144
v_mfma_f32_16x16x32_bf16 v[168:171], v[212:215], v[40:43], v[168:171]
v_add_f32 v245, v245, v145
v_cvt_pk_bf16_f32 v144, v144, v145
v_mfma_f32_16x16x32_bf16 v[176:179], v[212:215], v[72:75], v[176:179]
buffer_load_dwordx4 v231, s[44:47], s89 offen offset:1024 nt lds
v_mfma_f32_16x16x32_bf16 v[184:187], v[212:215], v[104:107], v[184:187]
v_fma_f32 v148, v148, s62, -v241
v_fma_f32 v149, v149, s62, -v241
v_mfma_f32_16x16x32_bf16 v[164:167], v[216:219], v[8:11], v[164:167]
ds_read_b128 v[212:215], v224 offset:10752
v_exp_f32 v148, v148
v_mfma_f32_16x16x32_bf16 v[172:175], v[216:219], v[40:43], v[172:175]
v_exp_f32 v149, v149
v_mfma_f32_16x16x32_bf16 v[180:183], v[216:219], v[72:75], v[180:183]
v_add_f32 v245, v245, v146
v_add_f32 v245, v245, v147
v_mfma_f32_16x16x32_bf16 v[188:191], v[216:219], v[104:107], v[188:191]
v_cvt_pk_bf16_f32 v145, v146, v147
v_fma_f32 v150, v150, s62, -v241
v_mfma_f32_16x16x32_bf16 v[160:163], v[192:195], v[12:15], v[160:163]
ds_read_b128 v[216:219], v224 offset:3072
v_fma_f32 v151, v151, s62, -v241
v_mfma_f32_16x16x32_bf16 v[168:171], v[192:195], v[44:47], v[168:171]
v_exp_f32 v150, v150
v_mfma_f32_16x16x32_bf16 v[176:179], v[192:195], v[76:79], v[176:179]
buffer_load_dwordx4 v231, s[44:47], s90 offen offset:2048 nt lds
v_mfma_f32_16x16x32_bf16 v[184:187], v[192:195], v[108:111], v[184:187]
v_exp_f32 v151, v151
v_mfma_f32_16x16x32_bf16 v[164:167], v[196:199], v[12:15], v[164:167]
ds_read_b128 v[192:195], v224 offset:11264
v_add_f32 v245, v245, v148
v_mfma_f32_16x16x32_bf16 v[172:175], v[196:199], v[44:47], v[172:175]
v_add_f32 v245, v245, v149
v_cvt_pk_bf16_f32 v146, v148, v149
v_mfma_f32_16x16x32_bf16 v[180:183], v[196:199], v[76:79], v[180:183]
v_fma_f32 v152, v152, s62, -v242
v_fma_f32 v153, v153, s62, -v242
v_mfma_f32_16x16x32_bf16 v[188:191], v[196:199], v[108:111], v[188:191]
v_exp_f32 v152, v152
s_waitcnt lgkmcnt(2)
v_mfma_f32_16x16x32_bf16 v[160:163], v[200:203], v[16:19], v[160:163]
ds_read_b128 v[196:199], v224 offset:3584
v_exp_f32 v153, v153
v_mfma_f32_16x16x32_bf16 v[168:171], v[200:203], v[48:51], v[168:171]
buffer_load_dwordx4 v231, s[44:47], s91 offen offset:3072 nt lds
v_mfma_f32_16x16x32_bf16 v[176:179], v[200:203], v[80:83], v[176:179]
v_add_f32 v245, v245, v150
v_add_f32 v245, v245, v151
v_mfma_f32_16x16x32_bf16 v[184:187], v[200:203], v[112:115], v[184:187]
v_cvt_pk_bf16_f32 v147, v150, v151
v_fma_f32 v154, v154, s62, -v242
v_mfma_f32_16x16x32_bf16 v[164:167], v[204:207], v[16:19], v[164:167]
ds_read_b128 v[200:203], v224 offset:11776
v_fma_f32 v155, v155, s62, -v242
v_mfma_f32_16x16x32_bf16 v[172:175], v[204:207], v[48:51], v[172:175]
v_exp_f32 v154, v154
v_mfma_f32_16x16x32_bf16 v[180:183], v[204:207], v[80:83], v[180:183]
v_exp_f32 v155, v155
v_mfma_f32_16x16x32_bf16 v[188:191], v[204:207], v[112:115], v[188:191]
v_add_f32 v246, v246, v152
v_add_f32 v246, v246, v153
v_mfma_f32_16x16x32_bf16 v[160:163], v[208:211], v[20:23], v[160:163]
v_cvt_pk_bf16_f32 v152, v152, v153
v_fma_f32 v156, v156, s62, -v242
v_mfma_f32_16x16x32_bf16 v[168:171], v[208:211], v[52:55], v[168:171]
s_waitcnt vmcnt(24)
s_waitcnt lgkmcnt(0)
v_fma_f32 v157, v157, s62, -v242
v_mfma_f32_16x16x32_bf16 v[176:179], v[208:211], v[84:87], v[176:179]
s_barrier
v_add_u32 v225, 0x10000, v225
v_mfma_f32_16x16x32_bf16 v[184:187], v[208:211], v[116:119], v[184:187]
s_add_u32 m0, s60, 0x10000
ds_read_b64_tr_b16 v[204:205], v225 offset:32768
ds_read_b64_tr_b16 v[206:207], v225 offset:40960
v_mfma_f32_16x16x32_bf16 v[164:167], v[212:215], v[20:23], v[164:167]
v_add_u32 v226, 0x10000, v226
ds_read_b64_tr_b16 v[208:209], v226 offset:32768
v_mfma_f32_16x16x32_bf16 v[172:175], v[212:215], v[52:55], v[172:175]
buffer_load_dwordx4 v232, s[48:51], s84 offen offset:0 nt lds
v_mfma_f32_16x16x32_bf16 v[180:183], v[212:215], v[84:87], v[180:183]
ds_read_b64_tr_b16 v[210:211], v226 offset:40960
v_exp_f32 v156, v156
v_mfma_f32_16x16x32_bf16 v[188:191], v[212:215], v[116:119], v[188:191]
v_exp_f32 v157, v157
v_mfma_f32_16x16x32_bf16 v[160:163], v[216:219], v[24:27], v[160:163]
ds_read_b64_tr_b16 v[212:213], v225 offset:33280
v_add_f32 v246, v246, v154
v_mfma_f32_16x16x32_bf16 v[168:171], v[216:219], v[56:59], v[168:171]
ds_read_b64_tr_b16 v[214:215], v225 offset:41472
v_add_f32 v246, v246, v155
v_mfma_f32_16x16x32_bf16 v[176:179], v[216:219], v[88:91], v[176:179]
v_cvt_pk_bf16_f32 v153, v154, v155
v_fma_f32 v158, v158, s62, -v242
v_mfma_f32_16x16x32_bf16 v[184:187], v[216:219], v[120:123], v[184:187]
v_fma_f32 v159, v159, s62, -v242
v_mfma_f32_16x16x32_bf16 v[164:167], v[192:195], v[24:27], v[164:167]
buffer_load_dwordx4 v232, s[48:51], s85 offen offset:1024 nt lds
v_mfma_f32_16x16x32_bf16 v[172:175], v[192:195], v[56:59], v[172:175]
v_exp_f32 v158, v158
ds_read_b64_tr_b16 v[216:217], v226 offset:33280
v_mfma_f32_16x16x32_bf16 v[180:183], v[192:195], v[88:91], v[180:183]
v_exp_f32 v159, v159
ds_read_b64_tr_b16 v[218:219], v226 offset:41472
v_mfma_f32_16x16x32_bf16 v[188:191], v[192:195], v[120:123], v[188:191]
v_add_f32 v246, v246, v156
v_add_f32 v246, v246, v157
v_mfma_f32_16x16x32_bf16 v[160:163], v[196:199], v[28:31], v[160:163]
v_cvt_pk_bf16_f32 v154, v156, v157
v_add_f32 v246, v246, v158
v_mfma_f32_16x16x32_bf16 v[168:171], v[196:199], v[60:63], v[168:171]
v_add_f32 v246, v246, v159
v_cvt_pk_bf16_f32 v155, v158, v159
v_mfma_f32_16x16x32_bf16 v[176:179], v[196:199], v[92:95], v[176:179]
ds_read_b64_tr_b16 v[192:193], v225 offset:33792
ds_read_b64_tr_b16 v[194:195], v225 offset:41984
v_mfma_f32_16x16x32_bf16 v[184:187], v[196:199], v[124:127], v[184:187]
v_mfma_f32_16x16x32_bf16 v[164:167], v[200:203], v[28:31], v[164:167]
buffer_load_dwordx4 v232, s[48:51], s86 offen offset:2048 nt lds
v_mfma_f32_16x16x32_bf16 v[172:175], v[200:203], v[60:63], v[172:175]
ds_read_b64_tr_b16 v[196:197], v226 offset:33792
ds_read_b64_tr_b16 v[198:199], v226 offset:41984
v_mfma_f32_16x16x32_bf16 v[180:183], v[200:203], v[92:95], v[180:183]
v_mfma_f32_16x16x32_bf16 v[188:191], v[200:203], v[124:127], v[188:191]
LST40:
s_waitcnt lgkmcnt(5)
v_mfma_f32_16x16x32_bf16 a[0:3], v[204:207], v[128:131], a[0:3]
ds_read_b64_tr_b16 v[200:201], v225 offset:34304
v_mfma_f32_16x16x32_bf16 a[4:7], v[204:207], v[136:139], a[4:7]
ds_read_b64_tr_b16 v[202:203], v225 offset:42496
v_mfma_f32_16x16x32_bf16 a[8:11], v[204:207], v[144:147], a[8:11]
v_max3_f32 v247, v160, v161, v162
v_max3_f32 v248, v168, v169, v170
v_mfma_f32_16x16x32_bf16 a[12:15], v[204:207], v[152:155], a[12:15]
buffer_load_dwordx4 v232, s[48:51], s87 offen offset:3072 nt lds
v_mfma_f32_16x16x32_bf16 a[16:19], v[208:211], v[128:131], a[16:19]
ds_read_b64_tr_b16 v[204:205], v226 offset:34304
v_max3_f32 v249, v176, v177, v178
v_mfma_f32_16x16x32_bf16 a[20:23], v[208:211], v[136:139], a[20:23]
ds_read_b64_tr_b16 v[206:207], v226 offset:42496
v_max3_f32 v250, v184, v185, v186
v_mfma_f32_16x16x32_bf16 a[24:27], v[208:211], v[144:147], a[24:27]
v_max3_f32 v247, v247, v163, v164
v_max3_f32 v248, v248, v171, v172
v_mfma_f32_16x16x32_bf16 a[28:31], v[208:211], v[152:155], a[28:31]
v_max3_f32 v249, v249, v179, v180
v_max3_f32 v250, v250, v187, v188
v_mfma_f32_16x16x32_bf16 a[32:35], v[212:215], v[128:131], a[32:35]
ds_read_b64_tr_b16 v[208:209], v225 offset:34816
v_max3_f32 v247, v247, v165, v166
v_mfma_f32_16x16x32_bf16 a[36:39], v[212:215], v[136:139], a[36:39]
s_add_u32 m0, s60, 0x12000
ds_read_b64_tr_b16 v[210:211], v225 offset:43008
v_max3_f32 v248, v248, v173, v174
v_mfma_f32_16x16x32_bf16 a[40:43], v[212:215], v[144:147], a[40:43]
v_max3_f32 v249, v249, v181, v182
v_max3_f32 v250, v250, v189, v190
v_mfma_f32_16x16x32_bf16 a[44:47], v[212:215], v[152:155], a[44:47]
buffer_load_dwordx4 v232, s[48:51], s88 offen offset:0 nt lds
s_waitcnt lgkmcnt(4)
v_mfma_f32_16x16x32_bf16 a[48:51], v[216:219], v[128:131], a[48:51]
ds_read_b64_tr_b16 v[212:213], v226 offset:34816
v_max_f32 v247, v247, v167
v_mfma_f32_16x16x32_bf16 a[52:55], v[216:219], v[136:139], a[52:55]
ds_read_b64_tr_b16 v[214:215], v226 offset:43008
v_max_f32 v248, v248, v175
v_mfma_f32_16x16x32_bf16 a[56:59], v[216:219], v[144:147], a[56:59]
v_max_f32 v249, v249, v183
v_max_f32 v250, v250, v191
v_mfma_f32_16x16x32_bf16 a[60:63], v[216:219], v[152:155], a[60:63]
v_permlane32_swap_b32 v247, v249
v_permlane32_swap_b32 v248, v250
v_mfma_f32_16x16x32_bf16 a[64:67], v[192:195], v[128:131], a[64:67]
ds_read_b64_tr_b16 v[216:217], v225 offset:35328
v_max_f32 v247, v247, v249
v_mfma_f32_16x16x32_bf16 a[68:71], v[192:195], v[136:139], a[68:71]
ds_read_b64_tr_b16 v[218:219], v225 offset:43520
v_max_f32 v248, v248, v250
v_mfma_f32_16x16x32_bf16 a[72:75], v[192:195], v[144:147], a[72:75]
buffer_load_dwordx4 v232, s[48:51], s89 offen offset:1024 nt lds
v_mfma_f32_16x16x32_bf16 a[76:79], v[192:195], v[152:155], a[76:79]
v_permlane16_swap_b32 v247, v248
v_max_f32 v247, v247, v248
v_mfma_f32_16x16x32_bf16 a[80:83], v[196:199], v[128:131], a[80:83]
ds_read_b64_tr_b16 v[192:193], v226 offset:35328
v_cmp_gt_f32 vcc, v247, v238
v_mfma_f32_16x16x32_bf16 a[84:87], v[196:199], v[136:139], a[84:87]
ds_read_b64_tr_b16 v[194:195], v226 offset:43520
s_cbranch_vccnz LR13
LRB13:
v_mfma_f32_16x16x32_bf16 a[88:91], v[196:199], v[144:147], a[88:91]
v_fma_f32 v160, v160, s62, -v239
v_fma_f32 v161, v161, s62, -v239
LH11:
s_cbranch_execz LH15
v_mfma_f32_16x16x32_bf16 a[92:95], v[196:199], v[152:155], a[92:95]
v_exp_f32 v160, v160
v_mfma_f32_16x16x32_bf16 a[96:99], v[200:203], v[128:131], a[96:99]
ds_read_b64_tr_b16 v[196:197], v225 offset:35840
v_exp_f32 v161, v161
v_mfma_f32_16x16x32_bf16 a[100:103], v[200:203], v[136:139], a[100:103]
ds_read_b64_tr_b16 v[198:199], v225 offset:44032
v_fma_f32 v162, v162, s62, -v239
v_mfma_f32_16x16x32_bf16 a[104:107], v[200:203], v[144:147], a[104:107]
buffer_load_dwordx4 v232, s[48:51], s90 offen offset:2048 nt lds
v_mfma_f32_16x16x32_bf16 a[108:111], v[200:203], v[152:155], a[108:111]
v_fma_f32 v163, v163, s62, -v239
s_waitcnt lgkmcnt(4)
v_mfma_f32_16x16x32_bf16 a[112:115], v[204:207], v[128:131], a[112:115]
ds_read_b64_tr_b16 v[200:201], v226 offset:35840
v_exp_f32 v162, v162
v_mfma_f32_16x16x32_bf16 a[116:119], v[204:207], v[136:139], a[116:119]
ds_read_b64_tr_b16 v[202:203], v226 offset:44032
v_exp_f32 v163, v163
v_mfma_f32_16x16x32_bf16 a[120:123], v[204:207], v[144:147], a[120:123]
v_add_f32 v243, v243, v160
v_add_f32 v243, v243, v161
v_mfma_f32_16x16x32_bf16 a[124:127], v[204:207], v[152:155], a[124:127]
v_cvt_pk_bf16_f32 v160, v160, v161
v_fma_f32 v164, v164, s62, -v239
v_mfma_f32_16x16x32_bf16 a[128:131], v[208:211], v[128:131], a[128:131]
ds_read_b64_tr_b16 v[204:205], v225 offset:36352
v_fma_f32 v165, v165, s62, -v239
v_mfma_f32_16x16x32_bf16 a[132:135], v[208:211], v[136:139], a[132:135]
buffer_load_dwordx4 v232, s[48:51], s91 offen offset:3072 nt lds
v_mfma_f32_16x16x32_bf16 a[136:139], v[208:211], v[144:147], a[136:139]
ds_read_b64_tr_b16 v[206:207], v225 offset:44544
v_exp_f32 v164, v164
v_mfma_f32_16x16x32_bf16 a[140:143], v[208:211], v[152:155], a[140:143]
s_add_u32 s48, s48, 0x4000
s_addc_u32 s49, s49, 0
v_mfma_f32_16x16x32_bf16 a[144:147], v[212:215], v[128:131], a[144:147]
ds_read_b64_tr_b16 v[208:209], v226 offset:36352
v_exp_f32 v165, v165
v_mfma_f32_16x16x32_bf16 a[148:151], v[212:215], v[136:139], a[148:151]
ds_read_b64_tr_b16 v[210:211], v226 offset:44544
v_add_f32 v243, v243, v162
v_mfma_f32_16x16x32_bf16 a[152:155], v[212:215], v[144:147], a[152:155]
v_add_f32 v243, v243, v163
v_cvt_pk_bf16_f32 v161, v162, v163
v_mfma_f32_16x16x32_bf16 a[156:159], v[212:215], v[152:155], a[156:159]
v_fma_f32 v166, v166, s62, -v239
v_fma_f32 v167, v167, s62, -v239
v_mfma_f32_16x16x32_bf16 a[160:163], v[216:219], v[128:131], a[160:163]
v_exp_f32 v166, v166
v_mfma_f32_16x16x32_bf16 a[164:167], v[216:219], v[136:139], a[164:167]
s_waitcnt vmcnt(24)
s_waitcnt lgkmcnt(0)
v_exp_f32 v167, v167
v_mfma_f32_16x16x32_bf16 a[168:171], v[216:219], v[144:147], a[168:171]
s_barrier
v_add_u32 v224, 0xffff0000, v224
v_mfma_f32_16x16x32_bf16 a[172:175], v[216:219], v[152:155], a[172:175]
s_add_u32 m0, s60, 0x18000
ds_read_b128 v[212:215], v224 offset:0
v_add_f32 v243, v243, v164
v_mfma_f32_16x16x32_bf16 a[176:179], v[192:195], v[128:131], a[176:179]
ds_read_b128 v[216:219], v224 offset:8192
v_add_f32 v243, v243, v165
v_mfma_f32_16x16x32_bf16 a[180:183], v[192:195], v[136:139], a[180:183]
buffer_load_dwordx4 v232, s[48:51], s84 offen offset:0 nt lds
v_mfma_f32_16x16x32_bf16 a[184:187], v[192:195], v[144:147], a[184:187]
v_cvt_pk_bf16_f32 v162, v164, v165
v_fma_f32 v168, v168, s62, -v240
v_mfma_f32_16x16x32_bf16 a[188:191], v[192:195], v[152:155], a[188:191]
v_fma_f32 v169, v169, s62, -v240
v_mfma_f32_16x16x32_bf16 a[192:195], v[196:199], v[128:131], a[192:195]
ds_read_b128 v[192:195], v224 offset:512
v_exp_f32 v168, v168
v_mfma_f32_16x16x32_bf16 a[196:199], v[196:199], v[136:139], a[196:199]
v_exp_f32 v169, v169
v_mfma_f32_16x16x32_bf16 a[200:203], v[196:199], v[144:147], a[200:203]
v_add_f32 v243, v243, v166
v_add_f32 v243, v243, v167
v_mfma_f32_16x16x32_bf16 a[204:207], v[196:199], v[152:155], a[204:207]
v_cvt_pk_bf16_f32 v163, v166, v167
v_fma_f32 v170, v170, s62, -v240
v_mfma_f32_16x16x32_bf16 a[208:211], v[200:203], v[128:131], a[208:211]
buffer_load_dwordx4 v232, s[48:51], s85 offen offset:1024 nt lds
v_mfma_f32_16x16x32_bf16 a[212:215], v[200:203], v[136:139], a[212:215]
ds_read_b128 v[196:199], v224 offset:8704
v_fma_f32 v171, v171, s62, -v240
v_mfma_f32_16x16x32_bf16 a[216:219], v[200:203], v[144:147], a[216:219]
v_exp_f32 v170, v170
v_mfma_f32_16x16x32_bf16 a[220:223], v[200:203], v[152:155], a[220:223]
v_exp_f32 v171, v171
v_mfma_f32_16x16x32_bf16 a[224:227], v[204:207], v[128:131], a[224:227]
ds_read_b128 v[200:203], v224 offset:1024
v_add_f32 v244, v244, v168
v_mfma_f32_16x16x32_bf16 a[228:231], v[204:207], v[136:139], a[228:231]
v_add_f32 v244, v244, v169
v_cvt_pk_bf16_f32 v168, v168, v169
v_mfma_f32_16x16x32_bf16 a[232:235], v[204:207], v[144:147], a[232:235]
v_fma_f32 v172, v172, s62, -v240
v_fma_f32 v173, v173, s62, -v240
v_mfma_f32_16x16x32_bf16 a[236:239], v[204:207], v[152:155], a[236:239]
v_exp_f32 v172, v172
v_mfma_f32_16x16x32_bf16 a[240:243], v[208:211], v[128:131], a[240:243]
buffer_load_dwordx4 v232, s[48:51], s86 offen offset:2048 nt lds
v_mfma_f32_16x16x32_bf16 a[244:247], v[208:211], v[136:139], a[244:247]
ds_read_b128 v[204:207], v224 offset:9216
v_exp_f32 v173, v173
v_mfma_f32_16x16x32_bf16 a[248:251], v[208:211], v[144:147], a[248:251]
v_add_f32 v244, v244, v170
v_add_f32 v244, v244, v171
v_mfma_f32_16x16x32_bf16 a[252:255], v[208:211], v[152:155], a[252:255]
LST41:
s_cmp_lg_u32 s63, 0
s_cbranch_scc1 LS13
LSB13:
s_waitcnt lgkmcnt(2)
v_mfma_f32_16x16x32_bf16 v[128:131], v[212:215], v[0:3], 0
ds_read_b128 v[208:211], v224 offset:1536
v_cvt_pk_bf16_f32 v169, v170, v171
v_mfma_f32_16x16x32_bf16 v[136:139], v[212:215], v[32:35], 0
v_fma_f32 v174, v174, s62, -v240
v_fma_f32 v175, v175, s62, -v240
v_mfma_f32_16x16x32_bf16 v[144:147], v[212:215], v[64:67], 0
v_exp_f32 v174, v174
v_mfma_f32_16x16x32_bf16 v[152:155], v[212:215], v[96:99], 0
buffer_load_dwordx4 v232, s[48:51], s87 offen offset:3072 nt lds
v_mfma_f32_16x16x32_bf16 v[132:135], v[216:219], v[0:3], 0
ds_read_b128 v[212:215], v224 offset:9728
v_exp_f32 v175, v175
v_mfma_f32_16x16x32_bf16 v[140:143], v[216:219], v[32:35], 0
v_add_f32 v244, v244, v172
v_add_f32 v244, v244, v173
v_mfma_f32_16x16x32_bf16 v[148:151], v[216:219], v[64:67], 0
v_cvt_pk_bf16_f32 v170, v172, v173
v_fma_f32 v176, v176, s62, -v241
v_mfma_f32_16x16x32_bf16 v[156:159], v[216:219], v[96:99], 0
v_fma_f32 v177, v177, s62, -v241
v_mfma_f32_16x16x32_bf16 v[128:131], v[192:195], v[4:7], v[128:131]
ds_read_b128 v[216:219], v224 offset:2048
v_exp_f32 v176, v176
v_mfma_f32_16x16x32_bf16 v[136:139], v[192:195], v[36:39], v[136:139]
s_add_u32 m0, s60, 0x1a000
v_exp_f32 v177, v177
v_mfma_f32_16x16x32_bf16 v[144:147], v[192:195], v[68:71], v[144:147]
v_add_f32 v244, v244, v174
v_add_f32 v244, v244, v175
v_mfma_f32_16x16x32_bf16 v[152:155], v[192:195], v[100:103], v[152:155]
buffer_load_dwordx4 v232, s[48:51], s88 offen offset:0 nt lds
v_mfma_f32_16x16x32_bf16 v[132:135], v[196:199], v[4:7], v[132:135]
ds_read_b128 v[192:195], v224 offset:10240
v_cvt_pk_bf16_f32 v171, v174, v175
v_mfma_f32_16x16x32_bf16 v[140:143], v[196:199], v[36:39], v[140:143]
v_fma_f32 v178, v178, s62, -v241
v_fma_f32 v179, v179, s62, -v241
v_mfma_f32_16x16x32_bf16 v[148:151], v[196:199], v[68:71], v[148:151]
v_exp_f32 v178, v178
v_mfma_f32_16x16x32_bf16 v[156:159], v[196:199], v[100:103], v[156:159]
v_exp_f32 v179, v179
s_waitcnt lgkmcnt(2)
v_mfma_f32_16x16x32_bf16 v[128:131], v[200:203], v[8:11], v[128:131]
ds_read_b128 v[196:199], v224 offset:2560
v_add_f32 v245, v245, v176
v_mfma_f32_16x16x32_bf16 v[136:139], v[200:203], v[40:43], v[136:139]
v_add_f32 v245, v245, v177
v_cvt_pk_bf16_f32 v176, v176, v177
v_mfma_f32_16x16x32_bf16 v[144:147], v[200:203], v[72:75], v[144:147]
buffer_load_dwordx4 v232, s[48:51], s89 offen offset:1024 nt lds
v_mfma_f32_16x16x32_bf16 v[152:155], v[200:203], v[104:107], v[152:155]
v_fma_f32 v180, v180, s62, -v241
v_fma_f32 v181, v181, s62, -v241
v_mfma_f32_16x16x32_bf16 v[132:135], v[204:207], v[8:11], v[132:135]
ds_read_b128 v[200:203], v224 offset:10752
v_exp_f32 v180, v180
v_mfma_f32_16x16x32_bf16 v[140:143], v[204:207], v[40:43], v[140:143]
v_exp_f32 v181, v181
v_mfma_f32_16x16x32_bf16 v[148:151], v[204:207], v[72:75], v[148:151]
v_add_f32 v245, v245, v178
v_add_f32 v245, v245, v179
v_mfma_f32_16x16x32_bf16 v[156:159], v[204:207], v[104:107], v[156:159]
v_cvt_pk_bf16_f32 v177, v178, v179
v_fma_f32 v182, v182, s62, -v241
v_mfma_f32_16x16x32_bf16 v[128:131], v[208:211], v[12:15], v[128:131]
ds_read_b128 v[204:207], v224 offset:3072
v_fma_f32 v183, v183, s62, -v241
v_mfma_f32_16x16x32_bf16 v[136:139], v[208:211], v[44:47], v[136:139]
v_exp_f32 v182, v182
v_mfma_f32_16x16x32_bf16 v[144:147], v[208:211], v[76:79], v[144:147]
buffer_load_dwordx4 v232, s[48:51], s90 offen offset:2048 nt lds
v_mfma_f32_16x16x32_bf16 v[152:155], v[208:211], v[108:111], v[152:155]
v_exp_f32 v183, v183
v_mfma_f32_16x16x32_bf16 v[132:135], v[212:215], v[12:15], v[132:135]
ds_read_b128 v[208:211], v224 offset:11264
v_add_f32 v245, v245, v180
v_mfma_f32_16x16x32_bf16 v[140:143], v[212:215], v[44:47], v[140:143]
v_add_f32 v245, v245, v181
v_cvt_pk_bf16_f32 v178, v180, v181
v_mfma_f32_16x16x32_bf16 v[148:151], v[212:215], v[76:79], v[148:151]
v_fma_f32 v184, v184, s62, -v242
v_fma_f32 v185, v185, s62, -v242
v_mfma_f32_16x16x32_bf16 v[156:159], v[212:215], v[108:111], v[156:159]
v_exp_f32 v184, v184
s_waitcnt lgkmcnt(2)
v_mfma_f32_16x16x32_bf16 v[128:131], v[216:219], v[16:19], v[128:131]
ds_read_b128 v[212:215], v224 offset:3584
v_exp_f32 v185, v185
v_mfma_f32_16x16x32_bf16 v[136:139], v[216:219], v[48:51], v[136:139]
buffer_load_dwordx4 v232, s[48:51], s91 offen offset:3072 nt lds
v_mfma_f32_16x16x32_bf16 v[144:147], v[216:219], v[80:83], v[144:147]
v_add_f32 v245, v245, v182
v_add_f32 v245, v245, v183
v_mfma_f32_16x16x32_bf16 v[152:155], v[216:219], v[112:115], v[152:155]
v_cvt_pk_bf16_f32 v179, v182, v183
v_fma_f32 v186, v186, s62, -v242
v_mfma_f32_16x16x32_bf16 v[132:135], v[192:195], v[16:19], v[132:135]
ds_read_b128 v[216:219], v224 offset:11776
v_fma_f32 v187, v187, s62, -v242
v_mfma_f32_16x16x32_bf16 v[140:143], v[192:195], v[48:51], v[140:143]
v_exp_f32 v186, v186
v_mfma_f32_16x16x32_bf16 v[148:151], v[192:195], v[80:83], v[148:151]
v_exp_f32 v187, v187
v_mfma_f32_16x16x32_bf16 v[156:159], v[192:195], v[112:115], v[156:159]
v_add_f32 v246, v246, v184
v_add_f32 v246, v246, v185
v_mfma_f32_16x16x32_bf16 v[128:131], v[196:199], v[20:23], v[128:131]
v_cvt_pk_bf16_f32 v184, v184, v185
v_fma_f32 v188, v188, s62, -v242
v_mfma_f32_16x16x32_bf16 v[136:139], v[196:199], v[52:55], v[136:139]
s_waitcnt vmcnt(24)
s_waitcnt lgkmcnt(0)
v_fma_f32 v189, v189, s62, -v242
v_mfma_f32_16x16x32_bf16 v[144:147], v[196:199], v[84:87], v[144:147]
s_barrier
v_add_u32 v225, 0x10000, v225
v_mfma_f32_16x16x32_bf16 v[152:155], v[196:199], v[116:119], v[152:155]
s_mov_b32 s61, 0x0
s_add_u32 m0, s35, 0x0
ds_read_b64_tr_b16 v[192:193], v225 offset:0
ds_read_b64_tr_b16 v[194:195], v225 offset:8192
v_mfma_f32_16x16x32_bf16 v[132:135], v[200:203], v[20:23], v[132:135]
v_add_u32 v226, 0x10000, v226
ds_read_b64_tr_b16 v[196:197], v226 offset:0
v_mfma_f32_16x16x32_bf16 v[140:143], v[200:203], v[52:55], v[140:143]
buffer_load_dwordx4 v235, s[52:55], s61 offen offset:0  lds
v_mfma_f32_16x16x32_bf16 v[148:151], v[200:203], v[84:87], v[148:151]
ds_read_b64_tr_b16 v[198:199], v226 offset:8192
v_exp_f32 v188, v188
v_mfma_f32_16x16x32_bf16 v[156:159], v[200:203], v[116:119], v[156:159]
v_exp_f32 v189, v189
v_mfma_f32_16x16x32_bf16 v[128:131], v[204:207], v[24:27], v[128:131]
ds_read_b64_tr_b16 v[200:201], v225 offset:512
v_add_f32 v246, v246, v186
v_mfma_f32_16x16x32_bf16 v[136:139], v[204:207], v[56:59], v[136:139]
ds_read_b64_tr_b16 v[202:203], v225 offset:8704
v_add_f32 v246, v246, v187
v_mfma_f32_16x16x32_bf16 v[144:147], v[204:207], v[88:91], v[144:147]
s_mov_b32 s61, 0x3800
s_add_u32 m0, s35, 0x400
v_cvt_pk_bf16_f32 v185, v186, v187
v_fma_f32 v190, v190, s62, -v242
v_mfma_f32_16x16x32_bf16 v[152:155], v[204:207], v[120:123], v[152:155]
v_fma_f32 v191, v191, s62, -v242
v_mfma_f32_16x16x32_bf16 v[132:135], v[208:211], v[24:27], v[132:135]
buffer_load_dwordx4 v235, s[52:55], s61 offen offset:0  lds
v_mfma_f32_16x16x32_bf16 v[140:143], v[208:211], v[56:59], v[140:143]
v_exp_f32 v190, v190
ds_read_b64_tr_b16 v[204:205], v226 offset:512
v_mfma_f32_16x16x32_bf16 v[148:151], v[208:211], v[88:91], v[148:151]
v_exp_f32 v191, v191
ds_read_b64_tr_b16 v[206:207], v226 offset:8704
v_mfma_f32_16x16x32_bf16 v[156:159], v[208:211], v[120:123], v[156:159]
v_add_f32 v246, v246, v188
v_add_f32 v246, v246, v189
v_mfma_f32_16x16x32_bf16 v[128:131], v[212:215], v[28:31], v[128:131]
v_cvt_pk_bf16_f32 v186, v188, v189
v_add_f32 v246, v246, v190
v_mfma_f32_16x16x32_bf16 v[136:139], v[212:215], v[60:63], v[136:139]
v_add_f32 v246, v246, v191
v_cvt_pk_bf16_f32 v187, v190, v191
v_mfma_f32_16x16x32_bf16 v[144:147], v[212:215], v[92:95], v[144:147]
s_mov_b32 s61, 0x7000
s_add_u32 m0, s35, 0x800
ds_read_b64_tr_b16 v[208:209], v225 offset:1024
ds_read_b64_tr_b16 v[210:211], v225 offset:9216
v_mfma_f32_16x16x32_bf16 v[152:155], v[212:215], v[124:127], v[152:155]
v_mfma_f32_16x16x32_bf16 v[132:135], v[216:219], v[28:31], v[132:135]
buffer_load_dwordx4 v235, s[52:55], s61 offen offset:0  lds
v_mfma_f32_16x16x32_bf16 v[140:143], v[216:219], v[60:63], v[140:143]
ds_read_b64_tr_b16 v[212:213], v226 offset:1024
ds_read_b64_tr_b16 v[214:215], v226 offset:9216
v_mfma_f32_16x16x32_bf16 v[148:151], v[216:219], v[92:95], v[148:151]
v_mfma_f32_16x16x32_bf16 v[156:159], v[216:219], v[124:127], v[156:159]
LST42:
s_waitcnt lgkmcnt(5)
v_mfma_f32_16x16x32_bf16 a[0:3], v[192:195], v[160:163], a[0:3]
ds_read_b64_tr_b16 v[216:217], v225 offset:1536
v_mfma_f32_16x16x32_bf16 a[4:7], v[192:195], v[168:171], a[4:7]
s_mov_b32 s61, 0xa800
s_add_u32 m0, s35, 0xc00
ds_read_b64_tr_b16 v[218:219], v225 offset:9728
v_mfma_f32_16x16x32_bf16 a[8:11], v[192:195], v[176:179], a[8:11]
v_max3_f32 v247, v128, v129, v130
v_max3_f32 v248, v136, v137, v138
v_mfma_f32_16x16x32_bf16 a[12:15], v[192:195], v[184:187], a[12:15]
buffer_load_dwordx4 v235, s[52:55], s61 offen offset:0  lds
v_mfma_f32_16x16x32_bf16 a[16:19], v[196:199], v[160:163], a[16:19]
ds_read_b64_tr_b16 v[192:193], v226 offset:1536
v_max3_f32 v249, v144, v145, v146
v_mfma_f32_16x16x32_bf16 a[20:23], v[196:199], v[168:171], a[20:23]
ds_read_b64_tr_b16 v[194:195], v226 offset:9728
v_max3_f32 v250, v152, v153, v154
v_mfma_f32_16x16x32_bf16 a[24:27], v[196:199], v[176:179], a[24:27]
v_max3_f32 v247, v247, v131, v132
v_max3_f32 v248, v248, v139, v140
v_mfma_f32_16x16x32_bf16 a[28:31], v[196:199], v[184:187], a[28:31]
v_max3_f32 v249, v249, v147, v148
v_max3_f32 v250, v250, v155, v156
v_mfma_f32_16x16x32_bf16 a[32:35], v[200:203], v[160:163], a[32:35]
ds_read_b64_tr_b16 v[196:197], v225 offset:2048
v_max3_f32 v247, v247, v133, v134
v_mfma_f32_16x16x32_bf16 a[36:39], v[200:203], v[168:171], a[36:39]
s_mov_b32 s61, 0xe000
s_add_u32 m0, s35, 0x1000
ds_read_b64_tr_b16 v[198:199], v225 offset:10240
v_max3_f32 v248, v248, v141, v142
v_mfma_f32_16x16x32_bf16 a[40:43], v[200:203], v[176:179], a[40:43]
v_max3_f32 v249, v249, v149, v150
v_max3_f32 v250, v250, v157, v158
v_mfma_f32_16x16x32_bf16 a[44:47], v[200:203], v[184:187], a[44:47]
buffer_load_dwordx4 v235, s[52:55], s61 offen offset:0  lds
s_waitcnt lgkmcnt(4)
v_mfma_f32_16x16x32_bf16 a[48:51], v[204:207], v[160:163], a[48:51]
ds_read_b64_tr_b16 v[200:201], v226 offset:2048
v_max_f32 v247, v247, v135
v_mfma_f32_16x16x32_bf16 a[52:55], v[204:207], v[168:171], a[52:55]
ds_read_b64_tr_b16 v[202:203], v226 offset:10240
v_max_f32 v248, v248, v143
v_mfma_f32_16x16x32_bf16 a[56:59], v[204:207], v[176:179], a[56:59]
v_max_f32 v249, v249, v151
v_max_f32 v250, v250, v159
v_mfma_f32_16x16x32_bf16 a[60:63], v[204:207], v[184:187], a[60:63]
v_permlane32_swap_b32 v247, v249
v_permlane32_swap_b32 v248, v250
v_mfma_f32_16x16x32_bf16 a[64:67], v[208:211], v[160:163], a[64:67]
s_mov_b32 s61, 0x11800
s_add_u32 m0, s35, 0x1400
ds_read_b64_tr_b16 v[204:205], v225 offset:2560
v_max_f32 v247, v247, v249
v_mfma_f32_16x16x32_bf16 a[68:71], v[208:211], v[168:171], a[68:71]
ds_read_b64_tr_b16 v[206:207], v225 offset:10752
v_max_f32 v248, v248, v250
v_mfma_f32_16x16x32_bf16 a[72:75], v[208:211], v[176:179], a[72:75]
buffer_load_dwordx4 v235, s[52:55], s61 offen offset:0  lds
v_mfma_f32_16x16x32_bf16 a[76:79], v[208:211], v[184:187], a[76:79]
v_permlane16_swap_b32 v247, v248
v_max_f32 v247, v247, v248
v_mfma_f32_16x16x32_bf16 a[80:83], v[212:215], v[160:163], a[80:83]
ds_read_b64_tr_b16 v[208:209], v226 offset:2560
v_cmp_gt_f32 vcc, v247, v238
v_mfma_f32_16x16x32_bf16 a[84:87], v[212:215], v[168:171], a[84:87]
ds_read_b64_tr_b16 v[210:211], v226 offset:10752
s_cbranch_vccnz LR14
LRB14:
v_mfma_f32_16x16x32_bf16 a[88:91], v[212:215], v[176:179], a[88:91]
v_fma_f32 v128, v128, s62, -v239
v_fma_f32 v129, v129, s62, -v239
v_mfma_f32_16x16x32_bf16 a[92:95], v[212:215], v[184:187], a[92:95]
v_exp_f32 v128, v128
v_mfma_f32_16x16x32_bf16 a[96:99], v[216:219], v[160:163], a[96:99]
s_mov_b32 s61, 0x15000
s_add_u32 m0, s35, 0x1800
ds_read_b64_tr_b16 v[212:213], v225 offset:3072
v_exp_f32 v129, v129
v_mfma_f32_16x16x32_bf16 a[100:103], v[216:219], v[168:171], a[100:103]
ds_read_b64_tr_b16 v[214:215], v225 offset:11264
v_fma_f32 v130, v130, s62, -v239
v_mfma_f32_16x16x32_bf16 a[104:107], v[216:219], v[176:179], a[104:107]
buffer_load_dwordx4 v235, s[52:55], s61 offen offset:0  lds
v_mfma_f32_16x16x32_bf16 a[108:111], v[216:219], v[184:187], a[108:111]
v_fma_f32 v131, v131, s62, -v239
s_waitcnt lgkmcnt(4)
v_mfma_f32_16x16x32_bf16 a[112:115], v[192:195], v[160:163], a[112:115]
ds_read_b64_tr_b16 v[216:217], v226 offset:3072
v_exp_f32 v130, v130
v_mfma_f32_16x16x32_bf16 a[116:119], v[192:195], v[168:171], a[116:119]
ds_read_b64_tr_b16 v[218:219], v226 offset:11264
v_exp_f32 v131, v131
v_mfma_f32_16x16x32_bf16 a[120:123], v[192:195], v[176:179], a[120:123]
v_add_f32 v243, v243, v128
v_add_f32 v243, v243, v129
v_mfma_f32_16x16x32_bf16 a[124:127], v[192:195], v[184:187], a[124:127]
s_mov_b32 s61, 0x18800
s_add_u32 m0, s35, 0x1c00
v_cvt_pk_bf16_f32 v128, v128, v129
v_fma_f32 v132, v132, s62, -v239
v_mfma_f32_16x16x32_bf16 a[128:131], v[196:199], v[160:163], a[128:131]
ds_read_b64_tr_b16 v[192:193], v225 offset:3584
v_fma_f32 v133, v133, s62, -v239
v_mfma_f32_16x16x32_bf16 a[132:135], v[196:199], v[168:171], a[132:135]
buffer_load_dwordx4 v235, s[52:55], s61 offen offset:0  lds
v_mfma_f32_16x16x32_bf16 a[136:139], v[196:199], v[176:179], a[136:139]
ds_read_b64_tr_b16 v[194:195], v225 offset:11776
v_exp_f32 v132, v132
v_mfma_f32_16x16x32_bf16 a[140:143], v[196:199], v[184:187], a[140:143]
v_exp_f32 v133, v133
v_mfma_f32_16x16x32_bf16 a[144:147], v[200:203], v[160:163], a[144:147]
ds_read_b64_tr_b16 v[196:197], v226 offset:3584
v_add_f32 v243, v243, v130
v_mfma_f32_16x16x32_bf16 a[148:151], v[200:203], v[168:171], a[148:151]
ds_read_b64_tr_b16 v[198:199], v226 offset:11776
v_add_f32 v243, v243, v131
v_mfma_f32_16x16x32_bf16 a[152:155], v[200:203], v[176:179], a[152:155]
v_cvt_pk_bf16_f32 v129, v130, v131
v_fma_f32 v134, v134, s62, -v239
v_mfma_f32_16x16x32_bf16 a[156:159], v[200:203], v[184:187], a[156:159]
v_fma_f32 v135, v135, s62, -v239
v_mfma_f32_16x16x32_bf16 a[160:163], v[204:207], v[160:163], a[160:163]
v_exp_f32 v134, v134
v_mfma_f32_16x16x32_bf16 a[164:167], v[204:207], v[168:171], a[164:167]
s_waitcnt vmcnt(24)
s_waitcnt lgkmcnt(0)
v_exp_f32 v135, v135
v_mfma_f32_16x16x32_bf16 a[168:171], v[204:207], v[176:179], a[168:171]
s_barrier
ds_read_b128 v[200:203], v224 offset:32768
v_mfma_f32_16x16x32_bf16 a[172:175], v[204:207], v[184:187], a[172:175]
v_add_f32 v243, v243, v132
v_add_f32 v243, v243, v133
v_mfma_f32_16x16x32_bf16 a[176:179], v[208:211], v[160:163], a[176:179]
ds_read_b128 v[204:207], v224 offset:40960
v_cvt_pk_bf16_f32 v130, v132, v133
v_mfma_f32_16x16x32_bf16 a[180:183], v[208:211], v[168:171], a[180:183]
v_fma_f32 v136, v136, s62, -v240
v_fma_f32 v137, v137, s62, -v240
v_mfma_f32_16x16x32_bf16 a[184:187], v[208:211], v[176:179], a[184:187]
v_exp_f32 v136, v136
v_mfma_f32_16x16x32_bf16 a[188:191], v[208:211], v[184:187], a[188:191]
v_exp_f32 v137, v137
v_mfma_f32_16x16x32_bf16 a[192:195], v[212:215], v[160:163], a[192:195]
ds_read_b128 v[208:211], v224 offset:33280
v_add_f32 v243, v243, v134
v_mfma_f32_16x16x32_bf16 a[196:199], v[212:215], v[168:171], a[196:199]
v_add_f32 v243, v243, v135
v_cvt_pk_bf16_f32 v131, v134, v135
v_mfma_f32_16x16x32_bf16 a[200:203], v[212:215], v[176:179], a[200:203]
v_fma_f32 v138, v138, s62, -v240
v_fma_f32 v139, v139, s62, -v240
v_mfma_f32_16x16x32_bf16 a[204:207], v[212:215], v[184:187], a[204:207]
v_exp_f32 v138, v138
v_mfma_f32_16x16x32_bf16 a[208:211], v[216:219], v[160:163], a[208:211]
ds_read_b128 v[212:215], v224 offset:41472
v_exp_f32 v139, v139
v_mfma_f32_16x16x32_bf16 a[212:215], v[216:219], v[168:171], a[212:215]
v_add_f32 v244, v244, v136
v_add_f32 v244, v244, v137
v_mfma_f32_16x16x32_bf16 a[216:219], v[216:219], v[176:179], a[216:219]
v_cvt_pk_bf16_f32 v136, v136, v137
v_fma_f32 v140, v140, s62, -v240
v_mfma_f32_16x16x32_bf16 a[220:223], v[216:219], v[184:187], a[220:223]
v_fma_f32 v141, v141, s62, -v240
v_mfma_f32_16x16x32_bf16 a[224:227], v[192:195], v[160:163], a[224:227]
ds_read_b128 v[216:219], v224 offset:33792
v_exp_f32 v140, v140
v_mfma_f32_16x16x32_bf16 a[228:231], v[192:195], v[168:171], a[228:231]
v_exp_f32 v141, v141
v_mfma_f32_16x16x32_bf16 a[232:235], v[192:195], v[176:179], a[232:235]
v_add_f32 v244, v244, v138
v_add_f32 v244, v244, v139
v_mfma_f32_16x16x32_bf16 a[236:239], v[192:195], v[184:187], a[236:239]
v_cvt_pk_bf16_f32 v137, v138, v139
v_mfma_f32_16x16x32_bf16 a[240:243], v[196:199], v[160:163], a[240:243]
ds_read_b128 v[192:195], v224 offset:41984
v_fma_f32 v142, v142, s62, -v240
v_mfma_f32_16x16x32_bf16 a[244:247], v[196:199], v[168:171], a[244:247]
v_fma_f32 v143, v143, s62, -v240
v_mfma_f32_16x16x32_bf16 a[248:251], v[196:199], v[176:179], a[248:251]
v_exp_f32 v142, v142
v_mfma_f32_16x16x32_bf16 a[252:255], v[196:199], v[184:187], a[252:255]
LST43:
s_cmp_lg_u32 s63, 0
s_cbranch_scc1 LS14
LSB14:
s_waitcnt lgkmcnt(2)
v_mfma_f32_16x16x32_bf16 v[160:163], v[200:203], v[0:3], 0
ds_read_b128 v[196:199], v224 offset:34304
v_exp_f32 v143, v143
v_mfma_f32_16x16x32_bf16 v[168:171], v[200:203], v[32:35], 0
v_add_f32 v244, v244, v140
v_add_f32 v244, v244, v141
v_mfma_f32_16x16x32_bf16 v[176:179], v[200:203], v[64:67], 0
v_cvt_pk_bf16_f32 v138, v140, v141
v_fma_f32 v144, v144, s62, -v241
v_mfma_f32_16x16x32_bf16 v[184:187], v[200:203], v[96:99], 0
v_fma_f32 v145, v145, s62, -v241
v_mfma_f32_16x16x32_bf16 v[164:167], v[204:207], v[0:3], 0
ds_read_b128 v[200:203], v224 offset:42496
v_exp_f32 v144, v144
v_mfma_f32_16x16x32_bf16 v[172:175], v[204:207], v[32:35], 0
v_exp_f32 v145, v145
v_mfma_f32_16x16x32_bf16 v[180:183], v[204:207], v[64:67], 0
v_add_f32 v244, v244, v142
v_mfma_f32_16x16x32_bf16 v[188:191], v[204:207], v[96:99], 0
v_add_f32 v244, v244, v143
v_cvt_pk_bf16_f32 v139, v142, v143
v_mfma_f32_16x16x32_bf16 v[160:163], v[208:211], v[4:7], v[160:163]
ds_read_b128 v[204:207], v224 offset:34816
v_fma_f32 v146, v146, s62, -v241
v_mfma_f32_16x16x32_bf16 v[168:171], v[208:211], v[36:39], v[168:171]
v_fma_f32 v147, v147, s62, -v241
v_mfma_f32_16x16x32_bf16 v[176:179], v[208:211], v[68:71], v[176:179]
v_exp_f32 v146, v146
v_mfma_f32_16x16x32_bf16 v[184:187], v[208:211], v[100:103], v[184:187]
v_exp_f32 v147, v147
v_mfma_f32_16x16x32_bf16 v[164:167], v[212:215], v[4:7], v[164:167]
ds_read_b128 v[208:211], v224 offset:43008
v_add_f32 v245, v245, v144
v_mfma_f32_16x16x32_bf16 v[172:175], v[212:215], v[36:39], v[172:175]
v_add_f32 v245, v245, v145
v_cvt_pk_bf16_f32 v144, v144, v145
v_mfma_f32_16x16x32_bf16 v[180:183], v[212:215], v[68:71], v[180:183]
v_fma_f32 v148, v148, s62, -v241
v_mfma_f32_16x16x32_bf16 v[188:191], v[212:215], v[100:103], v[188:191]
v_fma_f32 v149, v149, s62, -v241
s_waitcnt lgkmcnt(2)
v_mfma_f32_16x16x32_bf16 v[160:163], v[216:219], v[8:11], v[160:163]
ds_read_b128 v[212:215], v224 offset:35328
v_exp_f32 v148, v148
v_mfma_f32_16x16x32_bf16 v[168:171], v[216:219], v[40:43], v[168:171]
v_exp_f32 v149, v149
v_mfma_f32_16x16x32_bf16 v[176:179], v[216:219], v[72:75], v[176:179]
v_add_f32 v245, v245, v146
v_mfma_f32_16x16x32_bf16 v[184:187], v[216:219], v[104:107], v[184:187]
v_add_f32 v245, v245, v147
v_mfma_f32_16x16x32_bf16 v[164:167], v[192:195], v[8:11], v[164:167]
ds_read_b128 v[216:219], v224 offset:43520
v_cvt_pk_bf16_f32 v145, v146, v147
v_mfma_f32_16x16x32_bf16 v[172:175], v[192:195], v[40:43], v[172:175]
v_fma_f32 v150, v150, s62, -v241
v_fma_f32 v151, v151, s62, -v241
v_mfma_f32_16x16x32_bf16 v[180:183], v[192:195], v[72:75], v[180:183]
v_exp_f32 v150, v150
v_mfma_f32_16x16x32_bf16 v[188:191], v[192:195], v[104:107], v[188:191]
v_exp_f32 v151, v151
v_mfma_f32_16x16x32_bf16 v[160:163], v[196:199], v[12:15], v[160:163]
ds_read_b128 v[192:195], v224 offset:35840
v_mfma_f32_16x16x32_bf16 v[168:171], v[196:199], v[44:47], v[168:171]
v_add_f32 v245, v245, v148
v_add_f32 v245, v245, v149
v_mfma_f32_16x16x32_bf16 v[176:179], v[196:199], v[76:79], v[176:179]
v_cvt_pk_bf16_f32 v146, v148, v149
v_mfma_f32_16x16x32_bf16 v[184:187], v[196:199], v[108:111], v[184:187]
v_fma_f32 v152, v152, s62, -v242
v_fma_f32 v153, v153, s62, -v242
v_mfma_f32_16x16x32_bf16 v[164:167], v[200:203], v[12:15], v[164:167]
ds_read_b128 v[196:199], v224 offset:44032
v_exp_f32 v152, v152
v_mfma_f32_16x16x32_bf16 v[172:175], v[200:203], v[44:47], v[172:175]
v_exp_f32 v153, v153
v_mfma_f32_16x16x32_bf16 v[180:183], v[200:203], v[76:79], v[180:183]
v_mfma_f32_16x16x32_bf16 v[188:191], v[200:203], v[108:111], v[188:191]
v_add_f32 v245, v245, v150
v_add_f32 v245, v245, v151
s_waitcnt lgkmcnt(2)
v_mfma_f32_16x16x32_bf16 v[160:163], v[204:207], v[16:19], v[160:163]
ds_read_b128 v[200:203], v224 offset:36352
v_cvt_pk_bf16_f32 v147, v150, v151
v_mfma_f32_16x16x32_bf16 v[168:171], v[204:207], v[48:51], v[168:171]
v_fma_f32 v154, v154, s62, -v242
v_fma_f32 v155, v155, s62, -v242
v_mfma_f32_16x16x32_bf16 v[176:179], v[204:207], v[80:83], v[176:179]
v_exp_f32 v154, v154
v_mfma_f32_16x16x32_bf16 v[184:187], v[204:207], v[112:115], v[184:187]
v_mfma_f32_16x16x32_bf16 v[164:167], v[208:211], v[16:19], v[164:167]
ds_read_b128 v[204:207], v224 offset:44544
v_exp_f32 v155, v155
v_mfma_f32_16x16x32_bf16 v[172:175], v[208:211], v[48:51], v[172:175]
v_add_f32 v246, v246, v152
v_mfma_f32_16x16x32_bf16 v[180:183], v[208:211], v[80:83], v[180:183]
v_add_f32 v246, v246, v153
v_cvt_pk_bf16_f32 v152, v152, v153
v_mfma_f32_16x16x32_bf16 v[188:191], v[208:211], v[112:115], v[188:191]
v_fma_f32 v156, v156, s62, -v242
v_mfma_f32_16x16x32_bf16 v[160:163], v[212:215], v[20:23], v[160:163]
v_fma_f32 v157, v157, s62, -v242
v_mfma_f32_16x16x32_bf16 v[168:171], v[212:215], v[52:55], v[168:171]
s_waitcnt vmcnt(16)
s_waitcnt lgkmcnt(0)
v_exp_f32 v156, v156
v_mfma_f32_16x16x32_bf16 v[176:179], v[212:215], v[84:87], v[176:179]
s_barrier
v_add_u32 v225, 0xffff0000, v225
v_mfma_f32_16x16x32_bf16 v[184:187], v[212:215], v[116:119], v[184:187]
s_mov_b32 s61, 0x40
s_add_u32 m0, s35, 0x8000
ds_read_b64_tr_b16 v[208:209], v225 offset:0
ds_read_b64_tr_b16 v[210:211], v225 offset:8192
v_mfma_f32_16x16x32_bf16 v[164:167], v[216:219], v[20:23], v[164:167]
v_add_u32 v226, 0xffff0000, v226
ds_read_b64_tr_b16 v[212:213], v226 offset:0
LH12:
s_cbranch_execz LHB1
v_mfma_f32_16x16x32_bf16 v[172:175], v[216:219], v[52:55], v[172:175]
buffer_load_dwordx4 v235, s[52:55], s61 offen offset:0  lds
v_mfma_f32_16x16x32_bf16 v[180:183], v[216:219], v[84:87], v[180:183]
ds_read_b64_tr_b16 v[214:215], v226 offset:8192
v_exp_f32 v157, v157
v_mfma_f32_16x16x32_bf16 v[188:191], v[216:219], v[116:119], v[188:191]
v_add_f32 v246, v246, v154
v_add_f32 v246, v246, v155
v_mfma_f32_16x16x32_bf16 v[160:163], v[192:195], v[24:27], v[160:163]
ds_read_b64_tr_b16 v[216:217], v225 offset:512
v_cvt_pk_bf16_f32 v153, v154, v155
v_mfma_f32_16x16x32_bf16 v[168:171], v[192:195], v[56:59], v[168:171]
ds_read_b64_tr_b16 v[218:219], v225 offset:8704
v_fma_f32 v158, v158, s62, -v242
v_mfma_f32_16x16x32_bf16 v[176:179], v[192:195], v[88:91], v[176:179]
s_mov_b32 s61, 0x3840
s_add_u32 m0, s35, 0x8400
v_fma_f32 v159, v159, s62, -v242
v_mfma_f32_16x16x32_bf16 v[184:187], v[192:195], v[120:123], v[184:187]
v_exp_f32 v158, v158
v_mfma_f32_16x16x32_bf16 v[164:167], v[196:199], v[24:27], v[164:167]
buffer_load_dwordx4 v235, s[52:55], s61 offen offset:0  lds
v_mfma_f32_16x16x32_bf16 v[172:175], v[196:199], v[56:59], v[172:175]
v_exp_f32 v159, v159
ds_read_b64_tr_b16 v[192:193], v226 offset:512
v_mfma_f32_16x16x32_bf16 v[180:183], v[196:199], v[88:91], v[180:183]
v_add_f32 v246, v246, v156
v_add_f32 v246, v246, v157
v_mfma_f32_16x16x32_bf16 v[188:191], v[196:199], v[120:123], v[188:191]
v_cvt_pk_bf16_f32 v154, v156, v157
v_add_f32 v246, v246, v158
v_mfma_f32_16x16x32_bf16 v[160:163], v[200:203], v[28:31], v[160:163]
v_add_f32 v246, v246, v159
v_cvt_pk_bf16_f32 v155, v158, v159
v_mfma_f32_16x16x32_bf16 v[168:171], v[200:203], v[60:63], v[168:171]
ds_read_b64_tr_b16 v[194:195], v226 offset:8704
ds_read_b64_tr_b16 v[196:197], v225 offset:1024
v_mfma_f32_16x16x32_bf16 v[176:179], v[200:203], v[92:95], v[176:179]
s_mov_b32 s61, 0x7040
s_add_u32 m0, s35, 0x8800
ds_read_b64_tr_b16 v[198:199], v225 offset:9216
v_mfma_f32_16x16x32_bf16 v[184:187], v[200:203], v[124:127], v[184:187]
v_mfma_f32_16x16x32_bf16 v[164:167], v[204:207], v[28:31], v[164:167]
buffer_load_dwordx4 v235, s[52:55], s61 offen offset:0  lds
v_mfma_f32_16x16x32_bf16 v[172:175], v[204:207], v[60:63], v[172:175]
ds_read_b64_tr_b16 v[200:201], v226 offset:1024
ds_read_b64_tr_b16 v[202:203], v226 offset:9216
v_mfma_f32_16x16x32_bf16 v[180:183], v[204:207], v[92:95], v[180:183]
v_mfma_f32_16x16x32_bf16 v[188:191], v[204:207], v[124:127], v[188:191]
LST44:
s_waitcnt lgkmcnt(5)
v_mfma_f32_16x16x32_bf16 a[0:3], v[208:211], v[128:131], a[0:3]
ds_read_b64_tr_b16 v[204:205], v225 offset:1536
v_mfma_f32_16x16x32_bf16 a[4:7], v[208:211], v[136:139], a[4:7]
s_mov_b32 s61, 0xa840
s_add_u32 m0, s35, 0x8c00
ds_read_b64_tr_b16 v[206:207], v225 offset:9728
v_mfma_f32_16x16x32_bf16 a[8:11], v[208:211], v[144:147], a[8:11]
v_max3_f32 v247, v160, v161, v162
v_max3_f32 v248, v168, v169, v170
v_mfma_f32_16x16x32_bf16 a[12:15], v[208:211], v[152:155], a[12:15]
buffer_load_dwordx4 v235, s[52:55], s61 offen offset:0  lds
v_mfma_f32_16x16x32_bf16 a[16:19], v[212:215], v[128:131], a[16:19]
ds_read_b64_tr_b16 v[208:209], v226 offset:1536
v_max3_f32 v249, v176, v177, v178
v_mfma_f32_16x16x32_bf16 a[20:23], v[212:215], v[136:139], a[20:23]
ds_read_b64_tr_b16 v[210:211], v226 offset:9728
v_max3_f32 v250, v184, v185, v186
v_mfma_f32_16x16x32_bf16 a[24:27], v[212:215], v[144:147], a[24:27]
v_max3_f32 v247, v247, v163, v164
v_max3_f32 v248, v248, v171, v172
v_mfma_f32_16x16x32_bf16 a[28:31], v[212:215], v[152:155], a[28:31]
v_max3_f32 v249, v249, v179, v180
v_max3_f32 v250, v250, v187, v188
v_mfma_f32_16x16x32_bf16 a[32:35], v[216:219], v[128:131], a[32:35]
ds_read_b64_tr_b16 v[212:213], v225 offset:2048
v_max3_f32 v247, v247, v165, v166
v_mfma_f32_16x16x32_bf16 a[36:39], v[216:219], v[136:139], a[36:39]
s_mov_b32 s61, 0xe040
s_add_u32 m0, s35, 0x9000
ds_read_b64_tr_b16 v[214:215], v225 offset:10240
v_max3_f32 v248, v248, v173, v174
v_mfma_f32_16x16x32_bf16 a[40:43], v[216:219], v[144:147], a[40:43]
v_max3_f32 v249, v249, v181, v182
v_max3_f32 v250, v250, v189, v190
v_mfma_f32_16x16x32_bf16 a[44:47], v[216:219], v[152:155], a[44:47]
buffer_load_dwordx4 v235, s[52:55], s61 offen offset:0  lds
s_waitcnt lgkmcnt(4)
v_mfma_f32_16x16x32_bf16 a[48:51], v[192:195], v[128:131], a[48:51]
ds_read_b64_tr_b16 v[216:217], v226 offset:2048
v_max_f32 v247, v247, v167
v_mfma_f32_16x16x32_bf16 a[52:55], v[192:195], v[136:139], a[52:55]
ds_read_b64_tr_b16 v[218:219], v226 offset:10240
v_max_f32 v248, v248, v175
v_mfma_f32_16x16x32_bf16 a[56:59], v[192:195], v[144:147], a[56:59]
v_max_f32 v249, v249, v183
v_max_f32 v250, v250, v191
v_mfma_f32_16x16x32_bf16 a[60:63], v[192:195], v[152:155], a[60:63]
v_permlane32_swap_b32 v247, v249
v_permlane32_swap_b32 v248, v250
v_mfma_f32_16x16x32_bf16 a[64:67], v[196:199], v[128:131], a[64:67]
s_mov_b32 s61, 0x11840
s_add_u32 m0, s35, 0x9400
ds_read_b64_tr_b16 v[192:193], v225 offset:2560
v_max_f32 v247, v247, v249
v_mfma_f32_16x16x32_bf16 a[68:71], v[196:199], v[136:139], a[68:71]
ds_read_b64_tr_b16 v[194:195], v225 offset:10752
v_max_f32 v248, v248, v250
v_mfma_f32_16x16x32_bf16 a[72:75], v[196:199], v[144:147], a[72:75]
buffer_load_dwordx4 v235, s[52:55], s61 offen offset:0  lds
v_mfma_f32_16x16x32_bf16 a[76:79], v[196:199], v[152:155], a[76:79]
v_permlane16_swap_b32 v247, v248
v_max_f32 v247, v247, v248
v_mfma_f32_16x16x32_bf16 a[80:83], v[200:203], v[128:131], a[80:83]
s_mov_b32 s69, 0x0
ds_read_b64_tr_b16 v[196:197], v226 offset:2560
v_cmp_gt_f32 vcc, v247, v238
v_mfma_f32_16x16x32_bf16 a[84:87], v[200:203], v[136:139], a[84:87]
buffer_load_dword v221, v220, s[52:55], s69 offen 
v_mfma_f32_16x16x32_bf16 a[88:91], v[200:203], v[144:147], a[88:91]
ds_read_b64_tr_b16 v[198:199], v226 offset:10752
s_cbranch_vccnz LR15
LRB15:
v_mfma_f32_16x16x32_bf16 a[92:95], v[200:203], v[152:155], a[92:95]
v_fma_f32 v160, v160, s62, -v239
v_fma_f32 v161, v161, s62, -v239
v_mfma_f32_16x16x32_bf16 a[96:99], v[204:207], v[128:131], a[96:99]
s_mov_b32 s61, 0x15040
s_add_u32 m0, s35, 0x9800
ds_read_b64_tr_b16 v[200:201], v225 offset:3072
v_exp_f32 v160, v160
v_mfma_f32_16x16x32_bf16 a[100:103], v[204:207], v[136:139], a[100:103]
ds_read_b64_tr_b16 v[202:203], v225 offset:11264
v_exp_f32 v161, v161
v_mfma_f32_16x16x32_bf16 a[104:107], v[204:207], v[144:147], a[104:107]
buffer_load_dwordx4 v235, s[52:55], s61 offen offset:0  lds
v_mfma_f32_16x16x32_bf16 a[108:111], v[204:207], v[152:155], a[108:111]
v_fma_f32 v162, v162, s62, -v239
v_fma_f32 v163, v163, s62, -v239
s_waitcnt lgkmcnt(4)
v_mfma_f32_16x16x32_bf16 a[112:115], v[208:211], v[128:131], a[112:115]
ds_read_b64_tr_b16 v[204:205], v226 offset:3072
v_exp_f32 v162, v162
v_mfma_f32_16x16x32_bf16 a[116:119], v[208:211], v[136:139], a[116:119]
ds_read_b64_tr_b16 v[206:207], v226 offset:11264
v_exp_f32 v163, v163
v_mfma_f32_16x16x32_bf16 a[120:123], v[208:211], v[144:147], a[120:123]
v_add_f32 v243, v243, v160
v_add_f32 v243, v243, v161
v_mfma_f32_16x16x32_bf16 a[124:127], v[208:211], v[152:155], a[124:127]
s_mov_b32 s61, 0x18840
s_add_u32 m0, s35, 0x9c00
v_cvt_pk_bf16_f32 v160, v160, v161
v_fma_f32 v164, v164, s62, -v239
v_mfma_f32_16x16x32_bf16 a[128:131], v[212:215], v[128:131], a[128:131]
ds_read_b64_tr_b16 v[208:209], v225 offset:3584
v_fma_f32 v165, v165, s62, -v239
v_mfma_f32_16x16x32_bf16 a[132:135], v[212:215], v[136:139], a[132:135]
buffer_load_dwordx4 v235, s[52:55], s61 offen offset:0  lds
v_mfma_f32_16x16x32_bf16 a[136:139], v[212:215], v[144:147], a[136:139]
ds_read_b64_tr_b16 v[210:211], v225 offset:11776
v_exp_f32 v164, v164
v_mfma_f32_16x16x32_bf16 a[140:143], v[212:215], v[152:155], a[140:143]
v_exp_f32 v165, v165
v_mfma_f32_16x16x32_bf16 a[144:147], v[216:219], v[128:131], a[144:147]
ds_read_b64_tr_b16 v[212:213], v226 offset:3584
v_add_f32 v243, v243, v162
v_mfma_f32_16x16x32_bf16 a[148:151], v[216:219], v[136:139], a[148:151]
ds_read_b64_tr_b16 v[214:215], v226 offset:11776
v_add_f32 v243, v243, v163
v_mfma_f32_16x16x32_bf16 a[152:155], v[216:219], v[144:147], a[152:155]
v_cvt_pk_bf16_f32 v161, v162, v163
v_fma_f32 v166, v166, s62, -v239
v_mfma_f32_16x16x32_bf16 a[156:159], v[216:219], v[152:155], a[156:159]
v_fma_f32 v167, v167, s62, -v239
v_mfma_f32_16x16x32_bf16 a[160:163], v[192:195], v[128:131], a[160:163]
v_exp_f32 v166, v166
v_mfma_f32_16x16x32_bf16 a[164:167], v[192:195], v[136:139], a[164:167]
s_waitcnt vmcnt(17)
s_waitcnt lgkmcnt(0)
v_exp_f32 v167, v167
v_mfma_f32_16x16x32_bf16 a[168:171], v[192:195], v[144:147], a[168:171]
s_barrier
ds_read_b64_tr_b16 v[216:217], v225 offset:32768
v_mfma_f32_16x16x32_bf16 a[172:175], v[192:195], v[152:155], a[172:175]
s_mov_b32 s61, 0x80
s_add_u32 m0, s35, 0x10000
ds_read_b64_tr_b16 v[218:219], v225 offset:40960
v_add_f32 v243, v243, v164
v_mfma_f32_16x16x32_bf16 a[176:179], v[196:199], v[128:131], a[176:179]
ds_read_b64_tr_b16 v[192:193], v226 offset:32768
v_add_f32 v243, v243, v165
v_mfma_f32_16x16x32_bf16 a[180:183], v[196:199], v[136:139], a[180:183]
buffer_load_dwordx4 v235, s[52:55], s61 offen offset:0  lds
v_mfma_f32_16x16x32_bf16 a[184:187], v[196:199], v[144:147], a[184:187]
ds_read_b64_tr_b16 v[194:195], v226 offset:40960
v_cvt_pk_bf16_f32 v162, v164, v165
v_mfma_f32_16x16x32_bf16 a[188:191], v[196:199], v[152:155], a[188:191]
v_fma_f32 v168, v168, s62, -v240
v_fma_f32 v169, v169, s62, -v240
v_mfma_f32_16x16x32_bf16 a[192:195], v[200:203], v[128:131], a[192:195]
v_exp_f32 v168, v168
ds_read_b64_tr_b16 v[196:197], v225 offset:33280
v_mfma_f32_16x16x32_bf16 a[196:199], v[200:203], v[136:139], a[196:199]
v_exp_f32 v169, v169
ds_read_b64_tr_b16 v[198:199], v225 offset:41472
v_mfma_f32_16x16x32_bf16 a[200:203], v[200:203], v[144:147], a[200:203]
s_mov_b32 s61, 0x3880
s_add_u32 m0, s35, 0x10400
v_add_f32 v243, v243, v166
v_add_f32 v243, v243, v167
v_mfma_f32_16x16x32_bf16 a[204:207], v[200:203], v[152:155], a[204:207]
v_cvt_pk_bf16_f32 v163, v166, v167
v_fma_f32 v170, v170, s62, -v240
v_mfma_f32_16x16x32_bf16 a[208:211], v[204:207], v[128:131], a[208:211]
buffer_load_dwordx4 v235, s[52:55], s61 offen offset:0  lds
v_mfma_f32_16x16x32_bf16 a[212:215], v[204:207], v[136:139], a[212:215]
v_fma_f32 v171, v171, s62, -v240
ds_read_b64_tr_b16 v[200:201], v226 offset:33280
v_mfma_f32_16x16x32_bf16 a[216:219], v[204:207], v[144:147], a[216:219]
v_exp_f32 v170, v170
ds_read_b64_tr_b16 v[202:203], v226 offset:41472
v_mfma_f32_16x16x32_bf16 a[220:223], v[204:207], v[152:155], a[220:223]
v_exp_f32 v171, v171
v_mfma_f32_16x16x32_bf16 a[224:227], v[208:211], v[128:131], a[224:227]
v_add_f32 v244, v244, v168
v_add_f32 v244, v244, v169
v_mfma_f32_16x16x32_bf16 a[228:231], v[208:211], v[136:139], a[228:231]
v_cvt_pk_bf16_f32 v168, v168, v169
v_fma_f32 v172, v172, s62, -v240
v_mfma_f32_16x16x32_bf16 a[232:235], v[208:211], v[144:147], a[232:235]
s_mov_b32 s61, 0x7080
s_add_u32 m0, s35, 0x10800
v_fma_f32 v173, v173, s62, -v240
ds_read_b64_tr_b16 v[204:205], v225 offset:33792
v_mfma_f32_16x16x32_bf16 a[236:239], v[208:211], v[152:155], a[236:239]
v_exp_f32 v172, v172
ds_read_b64_tr_b16 v[206:207], v225 offset:41984
v_mfma_f32_16x16x32_bf16 a[240:243], v[212:215], v[128:131], a[240:243]
buffer_load_dwordx4 v235, s[52:55], s61 offen offset:0  lds
v_mfma_f32_16x16x32_bf16 a[244:247], v[212:215], v[136:139], a[244:247]
v_exp_f32 v173, v173
ds_read_b64_tr_b16 v[208:209], v226 offset:33792
v_mfma_f32_16x16x32_bf16 a[248:251], v[212:215], v[144:147], a[248:251]
v_add_f32 v244, v244, v170
v_add_f32 v244, v244, v171
v_mfma_f32_16x16x32_bf16 a[252:255], v[212:215], v[152:155], a[252:255]
LST45:
s_cmp_lg_u32 s63, 0
s_cbranch_scc1 LS15
LSB15:
v_cvt_pk_bf16_f32 v169, v170, v171
v_fma_f32 v174, v174, s62, -v240
v_fma_f32 v175, v175, s62, -v240
v_exp_f32 v174, v174
v_exp_f32 v175, v175
v_add_f32 v244, v244, v172
v_add_f32 v244, v244, v173
v_cvt_pk_bf16_f32 v170, v172, v173
v_fma_f32 v176, v176, s62, -v241
v_fma_f32 v177, v177, s62, -v241
v_exp_f32 v176, v176
v_exp_f32 v177, v177
v_add_f32 v244, v244, v174
v_add_f32 v244, v244, v175
v_cvt_pk_bf16_f32 v171, v174, v175
s_waitcnt lgkmcnt(4)
v_mfma_f32_16x16x32_bf16 a[0:3], v[216:219], v[160:163], a[0:3]
v_fma_f32 v178, v178, s62, -v241
v_fma_f32 v179, v179, s62, -v241
v_exp_f32 v178, v178
v_exp_f32 v179, v179
v_add_f32 v245, v245, v176
v_add_f32 v245, v245, v177
v_cvt_pk_bf16_f32 v176, v176, v177
v_fma_f32 v180, v180, s62, -v241
v_fma_f32 v181, v181, s62, -v241
v_exp_f32 v180, v180
v_exp_f32 v181, v181
v_add_f32 v245, v245, v178
v_add_f32 v245, v245, v179
v_cvt_pk_bf16_f32 v177, v178, v179
v_fma_f32 v182, v182, s62, -v241
v_fma_f32 v183, v183, s62, -v241
v_exp_f32 v182, v182
v_exp_f32 v183, v183
v_add_f32 v245, v245, v180
v_add_f32 v245, v245, v181
v_cvt_pk_bf16_f32 v178, v180, v181
v_fma_f32 v184, v184, s62, -v242
v_fma_f32 v185, v185, s62, -v242
v_exp_f32 v184, v184
v_exp_f32 v185, v185
v_add_f32 v245, v245, v182
v_add_f32 v245, v245, v183
v_cvt_pk_bf16_f32 v179, v182, v183
v_mfma_f32_16x16x32_bf16 a[4:7], v[216:219], v[168:171], a[4:7]
v_fma_f32 v186, v186, s62, -v242
v_fma_f32 v187, v187, s62, -v242
v_exp_f32 v186, v186
v_exp_f32 v187, v187
v_add_f32 v246, v246, v184
v_add_f32 v246, v246, v185
v_cvt_pk_bf16_f32 v184, v184, v185
v_fma_f32 v188, v188, s62, -v242
v_fma_f32 v189, v189, s62, -v242
v_exp_f32 v188, v188
v_exp_f32 v189, v189
v_add_f32 v246, v246, v186
v_add_f32 v246, v246, v187
v_cvt_pk_bf16_f32 v185, v186, v187
v_fma_f32 v190, v190, s62, -v242
v_fma_f32 v191, v191, s62, -v242
v_exp_f32 v190, v190
v_exp_f32 v191, v191
v_add_f32 v246, v246, v188
v_add_f32 v246, v246, v189
v_cvt_pk_bf16_f32 v186, v188, v189
v_add_f32 v246, v246, v190
v_add_f32 v246, v246, v191
v_cvt_pk_bf16_f32 v187, v190, v191
v_mfma_f32_16x16x32_bf16 a[8:11], v[216:219], v[176:179], a[8:11]
s_mov_b32 s61, 0xa880
s_add_u32 m0, s35, 0x10c00
ds_read_b64_tr_b16 v[210:211], v226 offset:41984
ds_read_b64_tr_b16 v[212:213], v225 offset:34304
v_mfma_f32_16x16x32_bf16 a[12:15], v[216:219], v[184:187], a[12:15]
buffer_load_dwordx4 v235, s[52:55], s61 offen offset:0  lds
v_mfma_f32_16x16x32_bf16 a[16:19], v[192:195], v[160:163], a[16:19]
ds_read_b64_tr_b16 v[214:215], v225 offset:42496
ds_read_b64_tr_b16 v[216:217], v226 offset:34304
v_mfma_f32_16x16x32_bf16 a[20:23], v[192:195], v[168:171], a[20:23]
ds_read_b64_tr_b16 v[218:219], v226 offset:42496
v_permlane32_swap_b32 v243, v245
v_mfma_f32_16x16x32_bf16 a[24:27], v[192:195], v[176:179], a[24:27]
v_permlane32_swap_b32 v244, v246
v_add_f32 v243, v243, v245
v_mfma_f32_16x16x32_bf16 a[28:31], v[192:195], v[184:187], a[28:31]
v_add_f32 v244, v244, v246
s_nop 1
v_permlane16_swap_b32 v243, v244
v_mfma_f32_16x16x32_bf16 a[32:35], v[196:199], v[160:163], a[32:35]
ds_read_b64_tr_b16 v[192:193], v225 offset:34816
v_add_f32 v255, v243, v244
v_mfma_f32_16x16x32_bf16 a[36:39], v[196:199], v[168:171], a[36:39]
s_mov_b32 s61, 0xe080
s_add_u32 m0, s35, 0x11000
ds_read_b64_tr_b16 v[194:195], v225 offset:43008
v_rcp_f32 v251, v255
v_mfma_f32_16x16x32_bf16 a[40:43], v[196:199], v[176:179], a[40:43]
v_accvgpr_read_b32 v164, a0
v_accvgpr_read_b32 v165, a1
v_mfma_f32_16x16x32_bf16 a[44:47], v[196:199], v[184:187], a[44:47]
buffer_load_dwordx4 v235, s[52:55], s61 offen offset:0  lds
s_waitcnt lgkmcnt(7)
v_mfma_f32_16x16x32_bf16 a[48:51], v[200:203], v[160:163], a[48:51]
ds_read_b64_tr_b16 v[196:197], v226 offset:34816
v_accvgpr_read_b32 v166, a2
v_mfma_f32_16x16x32_bf16 a[52:55], v[200:203], v[168:171], a[52:55]
ds_read_b64_tr_b16 v[198:199], v226 offset:43008
v_accvgpr_read_b32 v167, a3
v_mfma_f32_16x16x32_bf16 a[56:59], v[200:203], v[176:179], a[56:59]
v_cvt_pk_bf16_f32 v0, v164, v165
v_cvt_pk_bf16_f32 v1, v166, v167
v_mfma_f32_16x16x32_bf16 a[60:63], v[200:203], v[184:187], a[60:63]
v_accvgpr_read_b32 v172, a16
v_accvgpr_read_b32 v173, a17
v_mfma_f32_16x16x32_bf16 a[64:67], v[204:207], v[160:163], a[64:67]
s_mov_b32 s61, 0x11880
s_add_u32 m0, s35, 0x11400
ds_read_b64_tr_b16 v[200:201], v225 offset:35328
v_accvgpr_read_b32 v174, a18
v_mfma_f32_16x16x32_bf16 a[68:71], v[204:207], v[168:171], a[68:71]
ds_read_b64_tr_b16 v[202:203], v225 offset:43520
v_accvgpr_read_b32 v175, a19
v_mfma_f32_16x16x32_bf16 a[72:75], v[204:207], v[176:179], a[72:75]
buffer_load_dwordx4 v235, s[52:55], s61 offen offset:0  lds
v_mfma_f32_16x16x32_bf16 a[76:79], v[204:207], v[184:187], a[76:79]
v_cvt_pk_bf16_f32 v2, v172, v173
v_cvt_pk_bf16_f32 v3, v174, v175
s_waitcnt lgkmcnt(4)
v_mfma_f32_16x16x32_bf16 a[80:83], v[208:211], v[160:163], a[80:83]
ds_read_b64_tr_b16 v[204:205], v226 offset:35328
v_accvgpr_read_b32 v188, a4
v_mfma_f32_16x16x32_bf16 a[84:87], v[208:211], v[168:171], a[84:87]
ds_read_b64_tr_b16 v[206:207], v226 offset:43520
v_accvgpr_read_b32 v189, a5
v_mfma_f32_16x16x32_bf16 a[88:91], v[208:211], v[176:179], a[88:91]
v_accvgpr_read_b32 v190, a6
v_accvgpr_read_b32 v191, a7
v_mfma_f32_16x16x32_bf16 a[92:95], v[208:211], v[184:187], a[92:95]
v_cvt_pk_bf16_f32 v32, v188, v189
v_cvt_pk_bf16_f32 v33, v190, v191
v_mfma_f32_16x16x32_bf16 a[96:99], v[212:215], v[160:163], a[96:99]
s_mov_b32 s61, 0x15080
s_add_u32 m0, s35, 0x11800
ds_read_b64_tr_b16 v[208:209], v225 offset:35840
v_accvgpr_read_b32 v164, a20
v_mfma_f32_16x16x32_bf16 a[100:103], v[212:215], v[168:171], a[100:103]
ds_read_b64_tr_b16 v[210:211], v225 offset:44032
v_accvgpr_read_b32 v165, a21
v_mfma_f32_16x16x32_bf16 a[104:107], v[212:215], v[176:179], a[104:107]
buffer_load_dwordx4 v235, s[52:55], s61 offen offset:0  lds
v_mfma_f32_16x16x32_bf16 a[108:111], v[212:215], v[184:187], a[108:111]
v_accvgpr_read_b32 v166, a22
v_accvgpr_read_b32 v167, a23
v_mfma_f32_16x16x32_bf16 a[112:115], v[216:219], v[160:163], a[112:115]
ds_read_b64_tr_b16 v[212:213], v226 offset:35840
v_cvt_pk_bf16_f32 v34, v164, v165
v_mfma_f32_16x16x32_bf16 a[116:119], v[216:219], v[168:171], a[116:119]
ds_read_b64_tr_b16 v[214:215], v226 offset:44032
v_cvt_pk_bf16_f32 v35, v166, v167
v_mfma_f32_16x16x32_bf16 a[120:123], v[216:219], v[176:179], a[120:123]
v_accvgpr_read_b32 v180, a8
v_accvgpr_read_b32 v181, a9
v_mfma_f32_16x16x32_bf16 a[124:127], v[216:219], v[184:187], a[124:127]
s_mov_b32 s61, 0x18880
s_add_u32 m0, s35, 0x11c00
v_accvgpr_read_b32 v182, a10
v_accvgpr_read_b32 v183, a11
v_mfma_f32_16x16x32_bf16 a[128:131], v[192:195], v[160:163], a[128:131]
ds_read_b64_tr_b16 v[216:217], v225 offset:36352
v_cvt_pk_bf16_f32 v64, v180, v181
v_mfma_f32_16x16x32_bf16 a[132:135], v[192:195], v[168:171], a[132:135]
buffer_load_dwordx4 v235, s[52:55], s61 offen offset:0  lds
v_mfma_f32_16x16x32_bf16 a[136:139], v[192:195], v[176:179], a[136:139]
ds_read_b64_tr_b16 v[218:219], v225 offset:44544
v_cvt_pk_bf16_f32 v65, v182, v183
v_mfma_f32_16x16x32_bf16 a[140:143], v[192:195], v[184:187], a[140:143]
v_accvgpr_read_b32 v188, a24
v_accvgpr_read_b32 v189, a25
s_waitcnt lgkmcnt(4)
v_mfma_f32_16x16x32_bf16 a[144:147], v[196:199], v[160:163], a[144:147]
ds_read_b64_tr_b16 v[192:193], v226 offset:36352
v_accvgpr_read_b32 v190, a26
v_mfma_f32_16x16x32_bf16 a[148:151], v[196:199], v[168:171], a[148:151]
ds_read_b64_tr_b16 v[194:195], v226 offset:44544
v_accvgpr_read_b32 v191, a27
v_mfma_f32_16x16x32_bf16 a[152:155], v[196:199], v[176:179], a[152:155]
v_cvt_pk_bf16_f32 v66, v188, v189
v_cvt_pk_bf16_f32 v67, v190, v191
v_mfma_f32_16x16x32_bf16 a[156:159], v[196:199], v[184:187], a[156:159]
v_accvgpr_read_b32 v172, a12
v_accvgpr_read_b32 v173, a13
v_mfma_f32_16x16x32_bf16 a[160:163], v[200:203], v[160:163], a[160:163]
v_accvgpr_read_b32 v174, a14
v_accvgpr_read_b32 v175, a15
v_mfma_f32_16x16x32_bf16 a[164:167], v[200:203], v[168:171], a[164:167]
s_waitcnt vmcnt(17)
s_waitcnt lgkmcnt(0)
v_cvt_pk_bf16_f32 v96, v172, v173
v_mfma_f32_16x16x32_bf16 a[168:171], v[200:203], v[176:179], a[168:171]
s_barrier
ds_read_b64_tr_b16 v[196:197], v229 offset:0
v_mfma_f32_16x16x32_bf16 a[172:175], v[200:203], v[184:187], a[172:175]
s_mov_b32 s61, 0xc0
s_add_u32 m0, s35, 0x18000
ds_read_b64_tr_b16 v[198:199], v229 offset:1024
v_cvt_pk_bf16_f32 v97, v174, v175
v_mfma_f32_16x16x32_bf16 a[176:179], v[204:207], v[160:163], a[176:179]
ds_read_b64_tr_b16 v[200:201], v230 offset:0
v_accvgpr_read_b32 v180, a28
v_mfma_f32_16x16x32_bf16 a[180:183], v[204:207], v[168:171], a[180:183]
buffer_load_dwordx4 v235, s[52:55], s61 offen offset:0  lds
v_mfma_f32_16x16x32_bf16 a[184:187], v[204:207], v[176:179], a[184:187]
ds_read_b64_tr_b16 v[202:203], v230 offset:1024
v_accvgpr_read_b32 v181, a29
v_mfma_f32_16x16x32_bf16 a[188:191], v[204:207], v[184:187], a[188:191]
v_accvgpr_read_b32 v182, a30
v_accvgpr_read_b32 v183, a31
v_mfma_f32_16x16x32_bf16 a[192:195], v[208:211], v[160:163], a[192:195]
ds_read_b64_tr_b16 v[204:205], v229 offset:2048
v_cvt_pk_bf16_f32 v98, v180, v181
v_mfma_f32_16x16x32_bf16 a[196:199], v[208:211], v[168:171], a[196:199]
ds_read_b64_tr_b16 v[206:207], v229 offset:3072
v_cvt_pk_bf16_f32 v99, v182, v183
v_mfma_f32_16x16x32_bf16 a[200:203], v[208:211], v[176:179], a[200:203]
s_mov_b32 s61, 0x38c0
s_add_u32 m0, s35, 0x18400
v_accvgpr_read_b32 v164, a32
v_accvgpr_read_b32 v165, a33
v_mfma_f32_16x16x32_bf16 a[204:207], v[208:211], v[184:187], a[204:207]
v_accvgpr_read_b32 v166, a34
v_accvgpr_read_b32 v167, a35
v_mfma_f32_16x16x32_bf16 a[208:211], v[212:215], v[160:163], a[208:211]
buffer_load_dwordx4 v235, s[52:55], s61 offen offset:0  lds
v_mfma_f32_16x16x32_bf16 a[212:215], v[212:215], v[168:171], a[212:215]
ds_read_b64_tr_b16 v[208:209], v230 offset:2048
ds_read_b64_tr_b16 v[210:211], v230 offset:3072
v_mfma_f32_16x16x32_bf16 a[216:219], v[212:215], v[176:179], a[216:219]
v_cvt_pk_bf16_f32 v4, v164, v165
v_cvt_pk_bf16_f32 v5, v166, v167
v_mfma_f32_16x16x32_bf16 a[220:223], v[212:215], v[184:187], a[220:223]
v_accvgpr_read_b32 v172, a48
v_accvgpr_read_b32 v173, a49
v_mfma_f32_16x16x32_bf16 a[224:227], v[216:219], v[160:163], a[224:227]
v_accvgpr_read_b32 v174, a50
v_accvgpr_read_b32 v175, a51
v_mfma_f32_16x16x32_bf16 a[228:231], v[216:219], v[168:171], a[228:231]
v_cvt_pk_bf16_f32 v6, v172, v173
v_cvt_pk_bf16_f32 v7, v174, v175
v_mfma_f32_16x16x32_bf16 a[232:235], v[216:219], v[176:179], a[232:235]
s_mov_b32 s61, 0x70c0
s_add_u32 m0, s35, 0x18800
ds_read_b64_tr_b16 v[212:213], v229 offset:4096
ds_read_b64_tr_b16 v[214:215], v229 offset:5120
v_mfma_f32_16x16x32_bf16 a[236:239], v[216:219], v[184:187], a[236:239]
v_accvgpr_read_b32 v188, a36
v_accvgpr_read_b32 v189, a37
v_mfma_f32_16x16x32_bf16 a[240:243], v[192:195], v[160:163], a[240:243]
buffer_load_dwordx4 v235, s[52:55], s61 offen offset:0  lds
v_mfma_f32_16x16x32_bf16 a[244:247], v[192:195], v[168:171], a[244:247]
v_accvgpr_read_b32 v190, a38
v_accvgpr_read_b32 v191, a39
v_mfma_f32_16x16x32_bf16 a[248:251], v[192:195], v[176:179], a[248:251]
v_cvt_pk_bf16_f32 v36, v188, v189
v_cvt_pk_bf16_f32 v37, v190, v191
v_mfma_f32_16x16x32_bf16 a[252:255], v[192:195], v[184:187], a[252:255]
LST46:
v_accvgpr_read_b32 v164, a52
v_accvgpr_read_b32 v165, a53
s_waitcnt lgkmcnt(2)
v_mfma_f32_16x16x32_bf16 v[128:131], v[196:199], v[0:3], 0
v_accvgpr_read_b32 v166, a54
v_accvgpr_read_b32 v167, a55
v_mfma_f32_16x16x32_bf16 v[136:139], v[196:199], v[32:35], 0
s_mov_b32 s61, 0xa8c0
s_add_u32 m0, s35, 0x18c00
v_cvt_pk_bf16_f32 v38, v164, v165
v_cvt_pk_bf16_f32 v39, v166, v167
v_mfma_f32_16x16x32_bf16 v[144:147], v[196:199], v[64:67], 0
v_accvgpr_read_b32 v180, a40
v_accvgpr_read_b32 v181, a41
v_mfma_f32_16x16x32_bf16 v[152:155], v[196:199], v[96:99], 0
buffer_load_dwordx4 v235, s[52:55], s61 offen offset:0  lds
v_mfma_f32_16x16x32_bf16 v[132:135], v[200:203], v[0:3], 0
v_accvgpr_read_b32 v182, a42
v_accvgpr_read_b32 v183, a43
v_mfma_f32_16x16x32_bf16 v[140:143], v[200:203], v[32:35], 0
v_cvt_pk_bf16_f32 v68, v180, v181
v_cvt_pk_bf16_f32 v69, v182, v183
v_mfma_f32_16x16x32_bf16 v[148:151], v[200:203], v[64:67], 0
v_accvgpr_read_b32 v188, a56
v_accvgpr_read_b32 v189, a57
v_mfma_f32_16x16x32_bf16 v[156:159], v[200:203], v[96:99], 0
v_accvgpr_read_b32 v190, a58
v_accvgpr_read_b32 v191, a59
v_mfma_f32_16x16x32_bf16 v[128:131], v[204:207], v[4:7], v[128:131]
v_cvt_pk_bf16_f32 v70, v188, v189
v_cvt_pk_bf16_f32 v71, v190, v191
v_mfma_f32_16x16x32_bf16 v[136:139], v[204:207], v[36:39], v[136:139]
v_accvgpr_read_b32 v172, a44
v_accvgpr_read_b32 v173, a45
v_accvgpr_read_b32 v174, a46
v_accvgpr_read_b32 v175, a47
v_cvt_pk_bf16_f32 v100, v172, v173
v_cvt_pk_bf16_f32 v101, v174, v175
v_accvgpr_read_b32 v180, a60
v_accvgpr_read_b32 v181, a61
v_accvgpr_read_b32 v182, a62
v_accvgpr_read_b32 v183, a63
v_cvt_pk_bf16_f32 v102, v180, v181
v_cvt_pk_bf16_f32 v103, v182, v183
v_mfma_f32_16x16x32_bf16 v[144:147], v[204:207], v[68:71], v[144:147]
s_mov_b32 s61, 0xe0c0
s_add_u32 m0, s35, 0x19000
ds_read_b64_tr_b16 v[216:217], v230 offset:4096
ds_read_b64_tr_b16 v[218:219], v230 offset:5120
v_mfma_f32_16x16x32_bf16 v[152:155], v[204:207], v[100:103], v[152:155]
buffer_load_dwordx4 v235, s[52:55], s61 offen offset:0  lds
v_mfma_f32_16x16x32_bf16 v[132:135], v[208:211], v[4:7], v[132:135]
LH13:
s_cbranch_execz LHB1
v_accvgpr_read_b32 v164, a64
v_accvgpr_read_b32 v165, a65
v_mfma_f32_16x16x32_bf16 v[140:143], v[208:211], v[36:39], v[140:143]
v_accvgpr_read_b32 v166, a66
v_accvgpr_read_b32 v167, a67
v_mfma_f32_16x16x32_bf16 v[148:151], v[208:211], v[68:71], v[148:151]
v_cvt_pk_bf16_f32 v8, v164, v165
v_cvt_pk_bf16_f32 v9, v166, v167
v_accvgpr_read_b32 v172, a80
v_accvgpr_read_b32 v173, a81
v_accvgpr_read_b32 v174, a82
v_accvgpr_read_b32 v175, a83
v_cvt_pk_bf16_f32 v10, v172, v173
v_cvt_pk_bf16_f32 v11, v174, v175
v_mfma_f32_16x16x32_bf16 v[156:159], v[208:211], v[100:103], v[156:159]
ds_read_b64_tr_b16 v[192:193], v229 offset:6144
ds_read_b64_tr_b16 v[194:195], v229 offset:7168
v_accvgpr_read_b32 v188, a68
v_accvgpr_read_b32 v189, a69
v_accvgpr_read_b32 v190, a70
v_accvgpr_read_b32 v191, a71
v_cvt_pk_bf16_f32 v40, v188, v189
v_cvt_pk_bf16_f32 v41, v190, v191
v_accvgpr_read_b32 v164, a84
v_accvgpr_read_b32 v165, a85
v_accvgpr_read_b32 v166, a86
v_accvgpr_read_b32 v167, a87
v_cvt_pk_bf16_f32 v42, v164, v165
v_cvt_pk_bf16_f32 v43, v166, v167
s_waitcnt lgkmcnt(4)
v_mfma_f32_16x16x32_bf16 v[128:131], v[212:215], v[8:11], v[128:131]
v_accvgpr_read_b32 v180, a72
v_accvgpr_read_b32 v181, a73
v_accvgpr_read_b32 v182, a74
v_accvgpr_read_b32 v183, a75
v_cvt_pk_bf16_f32 v72, v180, v181
v_cvt_pk_bf16_f32 v73, v182, v183
v_accvgpr_read_b32 v188, a88
v_accvgpr_read_b32 v189, a89
v_accvgpr_read_b32 v190, a90
v_accvgpr_read_b32 v191, a91
v_cvt_pk_bf16_f32 v74, v188, v189
v_cvt_pk_bf16_f32 v75, v190, v191
v_mfma_f32_16x16x32_bf16 v[136:139], v[212:215], v[40:43], v[136:139]
v_accvgpr_read_b32 v172, a76
v_accvgpr_read_b32 v173, a77
v_accvgpr_read_b32 v174, a78
v_accvgpr_read_b32 v175, a79
v_cvt_pk_bf16_f32 v104, v172, v173
v_cvt_pk_bf16_f32 v105, v174, v175
v_accvgpr_read_b32 v180, a92
v_accvgpr_read_b32 v181, a93
v_accvgpr_read_b32 v182, a94
v_accvgpr_read_b32 v183, a95
v_cvt_pk_bf16_f32 v106, v180, v181
v_cvt_pk_bf16_f32 v107, v182, v183
s_mov_b32 s61, 0x118c0
s_add_u32 m0, s35, 0x19400
v_mfma_f32_16x16x32_bf16 v[144:147], v[212:215], v[72:75], v[144:147]
ds_read_b64_tr_b16 v[196:197], v230 offset:6144
ds_read_b64_tr_b16 v[198:199], v230 offset:7168
v_mfma_f32_16x16x32_bf16 v[152:155], v[212:215], v[104:107], v[152:155]
buffer_load_dwordx4 v235, s[52:55], s61 offen offset:0  lds
s_waitcnt lgkmcnt(4)
v_mfma_f32_16x16x32_bf16 v[132:135], v[216:219], v[8:11], v[132:135]
v_accvgpr_read_b32 v164, a96
v_accvgpr_read_b32 v165, a97
v_mfma_f32_16x16x32_bf16 v[140:143], v[216:219], v[40:43], v[140:143]
v_accvgpr_read_b32 v166, a98
v_accvgpr_read_b32 v167, a99
v_mfma_f32_16x16x32_bf16 v[148:151], v[216:219], v[72:75], v[148:151]
v_cvt_pk_bf16_f32 v12, v164, v165
v_cvt_pk_bf16_f32 v13, v166, v167
v_accvgpr_read_b32 v172, a112
v_accvgpr_read_b32 v173, a113
v_accvgpr_read_b32 v174, a114
v_accvgpr_read_b32 v175, a115
v_cvt_pk_bf16_f32 v14, v172, v173
v_cvt_pk_bf16_f32 v15, v174, v175
v_mfma_f32_16x16x32_bf16 v[156:159], v[216:219], v[104:107], v[156:159]
ds_read_b64_tr_b16 v[200:201], v229 offset:8192
ds_read_b64_tr_b16 v[202:203], v229 offset:9216
v_accvgpr_read_b32 v188, a100
v_accvgpr_read_b32 v189, a101
v_accvgpr_read_b32 v190, a102
v_accvgpr_read_b32 v191, a103
v_cvt_pk_bf16_f32 v44, v188, v189
v_cvt_pk_bf16_f32 v45, v190, v191
v_accvgpr_read_b32 v164, a116
v_accvgpr_read_b32 v165, a117
v_accvgpr_read_b32 v166, a118
v_accvgpr_read_b32 v167, a119
v_cvt_pk_bf16_f32 v46, v164, v165
v_cvt_pk_bf16_f32 v47, v166, v167
s_waitcnt lgkmcnt(4)
v_mfma_f32_16x16x32_bf16 v[128:131], v[192:195], v[12:15], v[128:131]
v_accvgpr_read_b32 v180, a104
v_accvgpr_read_b32 v181, a105
v_accvgpr_read_b32 v182, a106
v_accvgpr_read_b32 v183, a107
v_cvt_pk_bf16_f32 v76, v180, v181
v_cvt_pk_bf16_f32 v77, v182, v183
v_accvgpr_read_b32 v188, a120
v_accvgpr_read_b32 v189, a121
v_accvgpr_read_b32 v190, a122
v_accvgpr_read_b32 v191, a123
v_cvt_pk_bf16_f32 v78, v188, v189
v_cvt_pk_bf16_f32 v79, v190, v191
v_mfma_f32_16x16x32_bf16 v[136:139], v[192:195], v[44:47], v[136:139]
v_accvgpr_read_b32 v172, a108
v_accvgpr_read_b32 v173, a109
v_accvgpr_read_b32 v174, a110
v_accvgpr_read_b32 v175, a111
v_cvt_pk_bf16_f32 v108, v172, v173
v_cvt_pk_bf16_f32 v109, v174, v175
v_accvgpr_read_b32 v180, a124
v_accvgpr_read_b32 v181, a125
v_accvgpr_read_b32 v182, a126
v_accvgpr_read_b32 v183, a127
v_cvt_pk_bf16_f32 v110, v180, v181
v_cvt_pk_bf16_f32 v111, v182, v183
s_mov_b32 s61, 0x150c0
s_add_u32 m0, s35, 0x19800
v_mfma_f32_16x16x32_bf16 v[144:147], v[192:195], v[76:79], v[144:147]
ds_read_b64_tr_b16 v[204:205], v230 offset:8192
ds_read_b64_tr_b16 v[206:207], v230 offset:9216
v_mfma_f32_16x16x32_bf16 v[152:155], v[192:195], v[108:111], v[152:155]
buffer_load_dwordx4 v235, s[52:55], s61 offen offset:0  lds
s_waitcnt lgkmcnt(4)
v_mfma_f32_16x16x32_bf16 v[132:135], v[196:199], v[12:15], v[132:135]
v_accvgpr_read_b32 v164, a128
v_accvgpr_read_b32 v165, a129
v_mfma_f32_16x16x32_bf16 v[140:143], v[196:199], v[44:47], v[140:143]
v_accvgpr_read_b32 v166, a130
v_accvgpr_read_b32 v167, a131
v_mfma_f32_16x16x32_bf16 v[148:151], v[196:199], v[76:79], v[148:151]
v_cvt_pk_bf16_f32 v16, v164, v165
v_cvt_pk_bf16_f32 v17, v166, v167
v_accvgpr_read_b32 v172, a144
v_accvgpr_read_b32 v173, a145
v_accvgpr_read_b32 v174, a146
v_accvgpr_read_b32 v175, a147
v_cvt_pk_bf16_f32 v18, v172, v173
v_cvt_pk_bf16_f32 v19, v174, v175
v_mfma_f32_16x16x32_bf16 v[156:159], v[196:199], v[108:111], v[156:159]
ds_read_b64_tr_b16 v[208:209], v229 offset:10240
ds_read_b64_tr_b16 v[210:211], v229 offset:11264
v_accvgpr_read_b32 v188, a132
v_accvgpr_read_b32 v189, a133
v_accvgpr_read_b32 v190, a134
v_accvgpr_read_b32 v191, a135
v_cvt_pk_bf16_f32 v48, v188, v189
v_cvt_pk_bf16_f32 v49, v190, v191
v_accvgpr_read_b32 v164, a148
v_accvgpr_read_b32 v165, a149
v_accvgpr_read_b32 v166, a150
v_accvgpr_read_b32 v167, a151
v_cvt_pk_bf16_f32 v50, v164, v165
v_cvt_pk_bf16_f32 v51, v166, v167
s_waitcnt lgkmcnt(4)
v_mfma_f32_16x16x32_bf16 v[128:131], v[200:203], v[16:19], v[128:131]
v_accvgpr_read_b32 v180, a136
v_accvgpr_read_b32 v181, a137
v_accvgpr_read_b32 v182, a138
v_accvgpr_read_b32 v183, a139
v_cvt_pk_bf16_f32 v80, v180, v181
v_cvt_pk_bf16_f32 v81, v182, v183
v_accvgpr_read_b32 v188, a152
v_accvgpr_read_b32 v189, a153
v_accvgpr_read_b32 v190, a154
v_accvgpr_read_b32 v191, a155
v_cvt_pk_bf16_f32 v82, v188, v189
v_cvt_pk_bf16_f32 v83, v190, v191
s_mov_b32 s61, 0x188c0
s_add_u32 m0, s35, 0x19c00
v_mfma_f32_16x16x32_bf16 v[136:139], v[200:203], v[48:51], v[136:139]
v_accvgpr_read_b32 v172, a140
v_accvgpr_read_b32 v173, a141
v_accvgpr_read_b32 v174, a142
v_accvgpr_read_b32 v175, a143
v_cvt_pk_bf16_f32 v112, v172, v173
v_cvt_pk_bf16_f32 v113, v174, v175
v_accvgpr_read_b32 v180, a156
v_accvgpr_read_b32 v181, a157
v_accvgpr_read_b32 v182, a158
v_accvgpr_read_b32 v183, a159
v_cvt_pk_bf16_f32 v114, v180, v181
v_cvt_pk_bf16_f32 v115, v182, v183
v_mfma_f32_16x16x32_bf16 v[144:147], v[200:203], v[80:83], v[144:147]
ds_read_b64_tr_b16 v[212:213], v230 offset:10240
ds_read_b64_tr_b16 v[214:215], v230 offset:11264
v_mfma_f32_16x16x32_bf16 v[152:155], v[200:203], v[112:115], v[152:155]
buffer_load_dwordx4 v235, s[52:55], s61 offen offset:0  lds
s_waitcnt lgkmcnt(4)
v_mfma_f32_16x16x32_bf16 v[132:135], v[204:207], v[16:19], v[132:135]
ds_read_b64_tr_b16 v[216:217], v229 offset:12288
ds_read_b64_tr_b16 v[218:219], v229 offset:13312
ds_read_b64_tr_b16 v[192:193], v230 offset:12288
ds_read_b64_tr_b16 v[194:195], v230 offset:13312
ds_read_b64_tr_b16 v[196:197], v229 offset:14336
ds_read_b64_tr_b16 v[198:199], v229 offset:15360
ds_read_b64_tr_b16 v[200:201], v230 offset:14336
v_mfma_f32_16x16x32_bf16 v[140:143], v[204:207], v[48:51], v[140:143]
ds_read_b64_tr_b16 v[202:203], v230 offset:15360
v_accvgpr_read_b32 v164, a160
v_mfma_f32_16x16x32_bf16 v[148:151], v[204:207], v[80:83], v[148:151]
v_accvgpr_read_b32 v165, a161
v_accvgpr_read_b32 v166, a162
v_accvgpr_read_b32 v167, a163
v_cvt_pk_bf16_f32 v20, v164, v165
v_cvt_pk_bf16_f32 v21, v166, v167
v_accvgpr_read_b32 v172, a176
v_accvgpr_read_b32 v173, a177
v_accvgpr_read_b32 v174, a178
v_accvgpr_read_b32 v175, a179
v_cvt_pk_bf16_f32 v22, v172, v173
v_cvt_pk_bf16_f32 v23, v174, v175
v_mfma_f32_16x16x32_bf16 v[156:159], v[204:207], v[112:115], v[156:159]
v_accvgpr_read_b32 v188, a164
v_accvgpr_read_b32 v189, a165
v_accvgpr_read_b32 v190, a166
v_accvgpr_read_b32 v191, a167
v_cvt_pk_bf16_f32 v52, v188, v189
v_cvt_pk_bf16_f32 v53, v190, v191
v_accvgpr_read_b32 v164, a180
v_accvgpr_read_b32 v165, a181
v_accvgpr_read_b32 v166, a182
v_accvgpr_read_b32 v167, a183
v_cvt_pk_bf16_f32 v54, v164, v165
v_cvt_pk_bf16_f32 v55, v166, v167
v_rcp_f32 v252, v255
s_nop 1
v_permlane16_swap_b32 v251, v252
v_mov_b32 v253, v251
v_mov_b32 v254, v252
s_nop 0
v_permlane32_swap_b32 v251, v253
v_permlane32_swap_b32 v252, v254
s_waitcnt lgkmcnt(10)
v_mfma_f32_16x16x32_bf16 v[128:131], v[208:211], v[20:23], v[128:131]
v_accvgpr_read_b32 v180, a168
v_accvgpr_read_b32 v181, a169
v_accvgpr_read_b32 v182, a170
v_accvgpr_read_b32 v183, a171
v_cvt_pk_bf16_f32 v84, v180, v181
v_cvt_pk_bf16_f32 v85, v182, v183
v_accvgpr_read_b32 v188, a184
v_accvgpr_read_b32 v189, a185
v_accvgpr_read_b32 v190, a186
v_accvgpr_read_b32 v191, a187
v_cvt_pk_bf16_f32 v86, v188, v189
v_cvt_pk_bf16_f32 v87, v190, v191
v_mfma_f32_16x16x32_bf16 v[136:139], v[208:211], v[52:55], v[136:139]
s_waitcnt vmcnt(16)
s_waitcnt lgkmcnt(0)
v_accvgpr_read_b32 v172, a172
v_accvgpr_read_b32 v173, a173
v_accvgpr_read_b32 v174, a174
v_accvgpr_read_b32 v175, a175
v_cvt_pk_bf16_f32 v116, v172, v173
v_cvt_pk_bf16_f32 v117, v174, v175
v_accvgpr_read_b32 v180, a188
v_accvgpr_read_b32 v181, a189
v_accvgpr_read_b32 v182, a190
v_accvgpr_read_b32 v183, a191
v_cvt_pk_bf16_f32 v118, v180, v181
v_cvt_pk_bf16_f32 v119, v182, v183
v_mfma_f32_16x16x32_bf16 v[144:147], v[208:211], v[84:87], v[144:147]
s_barrier
v_accvgpr_read_b32 v164, a192
v_mfma_f32_16x16x32_bf16 v[152:155], v[208:211], v[116:119], v[152:155]
s_mov_b32 s61, 0x100
s_add_u32 m0, s35, 0x0
v_accvgpr_read_b32 v165, a193
v_accvgpr_read_b32 v166, a194
v_mfma_f32_16x16x32_bf16 v[132:135], v[212:215], v[20:23], v[132:135]
v_accvgpr_read_b32 v167, a195
v_cvt_pk_bf16_f32 v24, v164, v165
v_mfma_f32_16x16x32_bf16 v[140:143], v[212:215], v[52:55], v[140:143]
v_cvt_pk_bf16_f32 v25, v166, v167
v_accvgpr_read_b32 v172, a208
v_mfma_f32_16x16x32_bf16 v[148:151], v[212:215], v[84:87], v[148:151]
v_accvgpr_read_b32 v173, a209
v_accvgpr_read_b32 v174, a210
v_accvgpr_read_b32 v175, a211
v_cvt_pk_bf16_f32 v26, v172, v173
v_cvt_pk_bf16_f32 v27, v174, v175
v_mfma_f32_16x16x32_bf16 v[156:159], v[212:215], v[116:119], v[156:159]
v_accvgpr_read_b32 v188, a196
v_accvgpr_read_b32 v189, a197
v_accvgpr_read_b32 v190, a198
v_accvgpr_read_b32 v191, a199
v_cvt_pk_bf16_f32 v56, v188, v189
v_cvt_pk_bf16_f32 v57, v190, v191
v_accvgpr_read_b32 v164, a212
v_accvgpr_read_b32 v165, a213
v_accvgpr_read_b32 v166, a214
v_accvgpr_read_b32 v167, a215
v_cvt_pk_bf16_f32 v58, v164, v165
v_cvt_pk_bf16_f32 v59, v166, v167
buffer_load_dwordx4 v235, s[52:55], s61 offen offset:0  lds
v_mfma_f32_16x16x32_bf16 v[128:131], v[216:219], v[24:27], v[128:131]
v_accvgpr_read_b32 v180, a200
v_accvgpr_read_b32 v181, a201
v_accvgpr_read_b32 v182, a202
v_accvgpr_read_b32 v183, a203
v_cvt_pk_bf16_f32 v88, v180, v181
v_cvt_pk_bf16_f32 v89, v182, v183
v_accvgpr_read_b32 v188, a216
v_accvgpr_read_b32 v189, a217
v_accvgpr_read_b32 v190, a218
v_accvgpr_read_b32 v191, a219
v_cvt_pk_bf16_f32 v90, v188, v189
v_cvt_pk_bf16_f32 v91, v190, v191
v_mfma_f32_16x16x32_bf16 v[136:139], v[216:219], v[56:59], v[136:139]
v_accvgpr_read_b32 v172, a204
v_accvgpr_read_b32 v173, a205
v_accvgpr_read_b32 v174, a206
v_accvgpr_read_b32 v175, a207
v_cvt_pk_bf16_f32 v120, v172, v173
v_cvt_pk_bf16_f32 v121, v174, v175
v_accvgpr_read_b32 v180, a220
v_accvgpr_read_b32 v181, a221
v_accvgpr_read_b32 v182, a222
v_accvgpr_read_b32 v183, a223
v_cvt_pk_bf16_f32 v122, v180, v181
v_cvt_pk_bf16_f32 v123, v182, v183
v_mfma_f32_16x16x32_bf16 v[144:147], v[216:219], v[88:91], v[144:147]
s_mov_b32 s61, 0x3900
s_add_u32 m0, s35, 0x400
v_accvgpr_read_b32 v164, a224
v_accvgpr_read_b32 v165, a225
v_mfma_f32_16x16x32_bf16 v[152:155], v[216:219], v[120:123], v[152:155]
v_accvgpr_read_b32 v166, a226
v_accvgpr_read_b32 v167, a227
v_mfma_f32_16x16x32_bf16 v[132:135], v[192:195], v[24:27], v[132:135]
v_cvt_pk_bf16_f32 v28, v164, v165
v_cvt_pk_bf16_f32 v29, v166, v167
v_mfma_f32_16x16x32_bf16 v[140:143], v[192:195], v[56:59], v[140:143]
v_accvgpr_read_b32 v172, a240
v_accvgpr_read_b32 v173, a241
v_mfma_f32_16x16x32_bf16 v[148:151], v[192:195], v[88:91], v[148:151]
v_accvgpr_read_b32 v174, a242
v_accvgpr_read_b32 v175, a243
v_cvt_pk_bf16_f32 v30, v172, v173
v_cvt_pk_bf16_f32 v31, v174, v175
buffer_load_dwordx4 v235, s[52:55], s61 offen offset:0  lds
v_mfma_f32_16x16x32_bf16 v[156:159], v[192:195], v[120:123], v[156:159]
ds_read_b64_tr_b16 v[204:205], v229 offset:32768
ds_read_b64_tr_b16 v[206:207], v229 offset:33792
v_accvgpr_read_b32 v188, a228
v_accvgpr_read_b32 v189, a229
v_accvgpr_read_b32 v190, a230
v_accvgpr_read_b32 v191, a231
v_cvt_pk_bf16_f32 v60, v188, v189
v_cvt_pk_bf16_f32 v61, v190, v191
v_accvgpr_read_b32 v164, a244
v_accvgpr_read_b32 v165, a245
v_accvgpr_read_b32 v166, a246
v_accvgpr_read_b32 v167, a247
v_cvt_pk_bf16_f32 v62, v164, v165
v_cvt_pk_bf16_f32 v63, v166, v167
v_mfma_f32_16x16x32_bf16 v[128:131], v[196:199], v[28:31], v[128:131]
v_accvgpr_read_b32 v180, a232
v_accvgpr_read_b32 v181, a233
v_accvgpr_read_b32 v182, a234
v_accvgpr_read_b32 v183, a235
v_cvt_pk_bf16_f32 v92, v180, v181
v_cvt_pk_bf16_f32 v93, v182, v183
v_accvgpr_read_b32 v188, a248
v_accvgpr_read_b32 v189, a249
v_accvgpr_read_b32 v190, a250
v_accvgpr_read_b32 v191, a251
v_cvt_pk_bf16_f32 v94, v188, v189
v_cvt_pk_bf16_f32 v95, v190, v191
v_mfma_f32_16x16x32_bf16 v[136:139], v[196:199], v[60:63], v[136:139]
v_accvgpr_read_b32 v172, a236
v_accvgpr_read_b32 v173, a237
v_accvgpr_read_b32 v174, a238
v_accvgpr_read_b32 v175, a239
v_cvt_pk_bf16_f32 v124, v172, v173
v_cvt_pk_bf16_f32 v125, v174, v175
v_accvgpr_read_b32 v180, a252
v_accvgpr_read_b32 v181, a253
v_accvgpr_read_b32 v182, a254
v_accvgpr_read_b32 v183, a255
v_cvt_pk_bf16_f32 v126, v180, v181
v_cvt_pk_bf16_f32 v127, v182, v183
v_mfma_f32_16x16x32_bf16 v[144:147], v[196:199], v[92:95], v[144:147]
ds_read_b64_tr_b16 v[208:209], v230 offset:32768
ds_read_b64_tr_b16 v[210:211], v230 offset:33792
s_mov_b32 s61, 0x7100
s_add_u32 m0, s35, 0x800
v_mfma_f32_16x16x32_bf16 v[152:155], v[196:199], v[124:127], v[152:155]
v_mbcnt_lo_u32_b32 v226, -1, 0
v_mbcnt_hi_u32_b32 v226, -1, v226
v_lshlrev_b32 v226, 4, v226
s_lshl_b32 s74, %6, 11
v_add_u32 v236, s74, v226
s_lshl_b32 s74, %6, 12
s_add_u32 s74, s74, 0x20000
s_xor_b32 s75, s74, 0x2000
s_add_u32 s74, s74, %7
s_add_u32 s75, s75, %7
v_add_u32 v225, s74, v226
v_add_u32 v226, s75, v226
s_lshl_b32 s74, %6, 1
s_add_u32 s74, s74, 0
s_cmp_eq_u32 s74, %11
s_cselect_b32 s74, 0x40000000, 0
v_add_u32 v222, s74, v236
s_lshl_b32 s74, %6, 1
s_add_u32 s74, s74, 1
s_cmp_eq_u32 s74, %11
s_cselect_b32 s74, 0x40000000, 0
v_add_u32 v223, s74, v236
s_lshr_b32 s74, %11, 1
s_lshl_b32 s74, s74, 12
s_xor_b32 s74, s74, 0x2000
s_add_u32 s74, s74, 0x20000
s_add_u32 s74, s74, %7
s_lshl_b32 s75, %6, 8
s_add_u32 s74, s74, s75
s_and_b32 s75, %11, 1
s_lshl_b32 s75, s75, 10
s_add_u32 s74, s74, s75
v_and_b32 v250, 0xf0, v236
v_add_u32 v250, s74, v250
v_mfma_f32_16x16x32_bf16 v[132:135], v[200:203], v[28:31], v[132:135]
buffer_load_dwordx4 v235, s[52:55], s61 offen offset:0  lds
v_mfma_f32_16x16x32_bf16 v[140:143], v[200:203], v[60:63], v[140:143]
ds_read_b64_tr_b16 v[212:213], v229 offset:34816
ds_read_b64_tr_b16 v[214:215], v229 offset:35840
v_mfma_f32_16x16x32_bf16 v[148:151], v[200:203], v[92:95], v[148:151]
ds_read_b64_tr_b16 v[216:217], v230 offset:34816
ds_read_b64_tr_b16 v[218:219], v230 offset:35840
v_mfma_f32_16x16x32_bf16 v[156:159], v[200:203], v[124:127], v[156:159]
LST47:
ds_read_b64_tr_b16 v[192:193], v229 offset:36864
ds_read_b64_tr_b16 v[194:195], v229 offset:37888
s_waitcnt lgkmcnt(8)
v_mfma_f32_16x16x32_bf16 v[160:163], v[204:207], v[0:3], 0
ds_read_b64_tr_b16 v[196:197], v230 offset:36864
ds_read_b64_tr_b16 v[198:199], v230 offset:37888
v_mfma_f32_16x16x32_bf16 v[168:171], v[204:207], v[32:35], 0
s_mov_b32 s61, 0xa900
s_add_u32 m0, s35, 0xc00
ds_read_b64_tr_b16 v[200:201], v229 offset:38912
ds_read_b64_tr_b16 v[202:203], v229 offset:39936
v_mfma_f32_16x16x32_bf16 v[176:179], v[204:207], v[64:67], 0
s_add_u32 s69, s72, 0x0
v_mul_f32 v144, v144, v253
v_mov_b32 v232, v228
v_mfma_f32_16x16x32_bf16 v[184:187], v[204:207], v[96:99], 0
buffer_load_dwordx4 v235, s[52:55], s61 offen offset:0  lds
s_waitcnt lgkmcnt(10)
v_mfma_f32_16x16x32_bf16 v[164:167], v[208:211], v[0:3], 0
ds_read_b64_tr_b16 v[204:205], v230 offset:38912
v_mul_f32 v145, v145, v253
v_mfma_f32_16x16x32_bf16 v[172:175], v[208:211], v[32:35], 0
ds_read_b64_tr_b16 v[206:207], v230 offset:39936
v_mul_f32 v146, v146, v253
v_mfma_f32_16x16x32_bf16 v[180:183], v[208:211], v[64:67], 0
v_mul_f32 v147, v147, v253
v_mov_b32 v224, v227
v_mfma_f32_16x16x32_bf16 v[188:191], v[208:211], v[96:99], 0
ds_write_b128 v225, v[144:147] offset:0
s_waitcnt lgkmcnt(11)
v_mfma_f32_16x16x32_bf16 v[160:163], v[212:215], v[4:7], v[160:163]
ds_read_b64_tr_b16 v[208:209], v229 offset:40960
v_mul_f32 v148, v148, v253
v_mfma_f32_16x16x32_bf16 v[168:171], v[212:215], v[36:39], v[168:171]
s_mov_b32 s61, 0xe100
s_add_u32 m0, s35, 0x1000
ds_read_b64_tr_b16 v[210:211], v229 offset:41984
v_mul_f32 v149, v149, v253
v_mfma_f32_16x16x32_bf16 v[176:179], v[212:215], v[68:71], v[176:179]
v_mul_f32 v150, v150, v253
v_mul_f32 v151, v151, v253
v_mfma_f32_16x16x32_bf16 v[184:187], v[212:215], v[100:103], v[184:187]
buffer_load_dwordx4 v235, s[52:55], s61 offen offset:0  lds
s_waitcnt lgkmcnt(5)
v_mfma_f32_16x16x32_bf16 v[164:167], v[216:219], v[4:7], v[164:167]
ds_write_b128 v225, v[148:151] offset:1024
v_mfma_f32_16x16x32_bf16 v[172:175], v[216:219], v[36:39], v[172:175]
v_mul_f32 v152, v152, v254
v_mul_f32 v153, v153, v254
v_mfma_f32_16x16x32_bf16 v[180:183], v[216:219], v[68:71], v[180:183]
v_mul_f32 v154, v154, v254
ds_read_b64_tr_b16 v[212:213], v230 offset:40960
v_mfma_f32_16x16x32_bf16 v[188:191], v[216:219], v[100:103], v[188:191]
v_mul_f32 v155, v155, v254
ds_read_b64_tr_b16 v[214:215], v230 offset:41984
v_mfma_f32_16x16x32_bf16 v[160:163], v[192:195], v[8:11], v[160:163]
s_mov_b32 s61, 0x11900
s_add_u32 m0, s35, 0x1400
ds_write_b128 v225, v[152:155] offset:2048
v_mfma_f32_16x16x32_bf16 v[168:171], v[192:195], v[40:43], v[168:171]
v_mul_f32 v156, v156, v254
ds_read_b64_tr_b16 v[216:217], v229 offset:43008
v_mfma_f32_16x16x32_bf16 v[176:179], v[192:195], v[72:75], v[176:179]
buffer_load_dwordx4 v235, s[52:55], s61 offen offset:0  lds
v_mfma_f32_16x16x32_bf16 v[184:187], v[192:195], v[104:107], v[184:187]
v_mul_f32 v157, v157, v254
v_mul_f32 v158, v158, v254
v_mfma_f32_16x16x32_bf16 v[164:167], v[196:199], v[8:11], v[164:167]
v_mul_f32 v159, v159, v254
ds_read_b64_tr_b16 v[218:219], v229 offset:44032
v_mfma_f32_16x16x32_bf16 v[172:175], v[196:199], v[40:43], v[172:175]
ds_write_b128 v225, v[156:159] offset:3072
v_mfma_f32_16x16x32_bf16 v[180:183], v[196:199], v[72:75], v[180:183]
ds_read_b64_tr_b16 v[192:193], v230 offset:43008
ds_read_b64_tr_b16 v[194:195], v230 offset:44032
v_mfma_f32_16x16x32_bf16 v[188:191], v[196:199], v[104:107], v[188:191]
v_add_u32 v231, 0x70000, v227
v_mbcnt_lo_u32_b32 v228, -1, 0
v_mfma_f32_16x16x32_bf16 v[160:163], v[200:203], v[12:15], v[160:163]
s_mov_b32 s61, 0x15100
s_add_u32 m0, s35, 0x1800
ds_read_b64_tr_b16 v[196:197], v229 offset:45056
v_mbcnt_hi_u32_b32 v228, -1, v228
v_mfma_f32_16x16x32_bf16 v[168:171], v[200:203], v[44:47], v[168:171]
ds_read_b64_tr_b16 v[198:199], v229 offset:46080
v_and_b32 v228, 15, v228
s_mov_b32 s74, 0x3f80
s_mov_b32 s75, 0x3f800000
v_mfma_f32_16x16x32_bf16 v[176:179], v[200:203], v[76:79], v[176:179]
buffer_load_dwordx4 v235, s[52:55], s61 offen offset:0  lds
v_mfma_f32_16x16x32_bf16 v[184:187], v[200:203], v[108:111], v[184:187]
v_cmp_eq_u32 vcc, 0, v228
v_mov_b32 v238, s74
s_waitcnt lgkmcnt(6)
v_mfma_f32_16x16x32_bf16 v[164:167], v[204:207], v[12:15], v[164:167]
ds_read_b64_tr_b16 v[200:201], v230 offset:45056
v_mov_b32 v239, s75
v_mfma_f32_16x16x32_bf16 v[172:175], v[204:207], v[44:47], v[172:175]
ds_read_b64_tr_b16 v[202:203], v230 offset:46080
v_cndmask_b32 v227, 0, v238, vcc
v_mfma_f32_16x16x32_bf16 v[180:183], v[204:207], v[76:79], v[180:183]
s_waitcnt lgkmcnt(6)
s_barrier
v_mfma_f32_16x16x32_bf16 v[188:191], v[204:207], v[108:111], v[188:191]
s_mov_b32 s61, 0x18900
s_add_u32 m0, s35, 0x1c00
ds_read_b128 v[144:147], v226 offset:0
ds_read_b128 v[148:151], v226 offset:1024
v_mfma_f32_16x16x32_bf16 v[160:163], v[208:211], v[16:19], v[160:163]
ds_read_b128 v[152:155], v226 offset:2048
ds_read_b128 v[156:159], v226 offset:3072
v_mfma_f32_16x16x32_bf16 v[168:171], v[208:211], v[48:51], v[168:171]
buffer_load_dwordx4 v235, s[52:55], s61 offen offset:0  lds
v_mfma_f32_16x16x32_bf16 v[176:179], v[208:211], v[80:83], v[176:179]
LH14:
s_cbranch_execz LHB1
ds_read_b64_tr_b16 v[204:205], v229 offset:47104
ds_read_b64_tr_b16 v[206:207], v229 offset:48128
v_mfma_f32_16x16x32_bf16 v[184:187], v[208:211], v[112:115], v[184:187]
v_cmp_eq_u32 vcc, 1, v228
s_nop 1
v_cndmask_b32 v227, v227, v239, vcc
v_mfma_f32_16x16x32_bf16 v[164:167], v[212:215], v[16:19], v[164:167]
ds_read_b64_tr_b16 v[208:209], v230 offset:47104
v_accvgpr_write_b32 a224, v227
s_mov_b64 s[92:93], 0xffff
v_mfma_f32_16x16x32_bf16 v[172:175], v[212:215], v[48:51], v[172:175]
ds_read_b64_tr_b16 v[210:211], v230 offset:48128
v_cndmask_b32_e64 v238, 0, v227, s[92:93]
v_mfma_f32_16x16x32_bf16 v[180:183], v[212:215], v[80:83], v[180:183]
s_waitcnt lgkmcnt(7)
v_fma_f32 v128, v128, v251, v144
v_fma_f32 v129, v129, v251, v145
v_mfma_f32_16x16x32_bf16 v[188:191], v[212:215], v[112:115], v[188:191]
v_fma_f32 v130, v130, v251, v146
v_fma_f32 v131, v131, v251, v147
v_mfma_f32_16x16x32_bf16 v[160:163], v[216:219], v[20:23], v[160:163]
s_waitcnt lgkmcnt(6)
v_fma_f32 v132, v132, v251, v148
v_accvgpr_write_b32 a228, v238
v_mfma_f32_16x16x32_bf16 v[168:171], v[216:219], v[52:55], v[168:171]
s_waitcnt vmcnt(16)
s_waitcnt lgkmcnt(0)
v_fma_f32 v133, v133, v251, v149
v_mfma_f32_16x16x32_bf16 v[176:179], v[216:219], v[84:87], v[176:179]
s_barrier
v_add_u32 v229, 0x10000, v229
v_mfma_f32_16x16x32_bf16 v[184:187], v[216:219], v[116:119], v[184:187]
s_mov_b32 s61, 0x140
s_add_u32 m0, s35, 0x8000
ds_read_b64_tr_b16 v[212:213], v229 offset:0
ds_read_b64_tr_b16 v[214:215], v229 offset:1024
v_mfma_f32_16x16x32_bf16 v[164:167], v[192:195], v[20:23], v[164:167]
v_fma_f32 v134, v134, v251, v150
v_fma_f32 v135, v135, v251, v151
v_mfma_f32_16x16x32_bf16 v[172:175], v[192:195], v[52:55], v[172:175]
buffer_load_dwordx4 v235, s[52:55], s61 offen offset:0  lds
v_mfma_f32_16x16x32_bf16 v[180:183], v[192:195], v[84:87], v[180:183]
v_cvt_pk_bf16_f32 v128, v128, v129
v_cvt_pk_bf16_f32 v129, v130, v131
v_mfma_f32_16x16x32_bf16 v[188:191], v[192:195], v[116:119], v[188:191]
v_cvt_pk_bf16_f32 v130, v132, v133
v_cvt_pk_bf16_f32 v131, v134, v135
v_mfma_f32_16x16x32_bf16 v[160:163], v[196:199], v[24:27], v[160:163]
buffer_store_dwordx4 v[128:131], v222, s[56:59], s69 offen offset:0 sc1
v_mfma_f32_16x16x32_bf16 v[168:171], v[196:199], v[56:59], v[168:171]
ds_write_b128 v226, v[128:131] offset:0
v_fma_f32 v136, v136, v252, v152
v_mfma_f32_16x16x32_bf16 v[176:179], v[196:199], v[88:91], v[176:179]
s_mov_b32 s61, 0x3940
s_add_u32 m0, s35, 0x8400
v_fma_f32 v137, v137, v252, v153
v_fma_f32 v138, v138, v252, v154
v_mfma_f32_16x16x32_bf16 v[184:187], v[196:199], v[120:123], v[184:187]
v_fma_f32 v139, v139, v252, v155
v_fma_f32 v140, v140, v252, v156
v_mfma_f32_16x16x32_bf16 v[164:167], v[200:203], v[24:27], v[164:167]
buffer_load_dwordx4 v235, s[52:55], s61 offen offset:0  lds
v_mfma_f32_16x16x32_bf16 v[172:175], v[200:203], v[56:59], v[172:175]
v_fma_f32 v141, v141, v252, v157
v_fma_f32 v142, v142, v252, v158
v_mfma_f32_16x16x32_bf16 v[180:183], v[200:203], v[88:91], v[180:183]
v_fma_f32 v143, v143, v252, v159
v_cvt_pk_bf16_f32 v136, v136, v137
v_mfma_f32_16x16x32_bf16 v[188:191], v[200:203], v[120:123], v[188:191]
v_cvt_pk_bf16_f32 v137, v138, v139
v_cvt_pk_bf16_f32 v138, v140, v141
v_mfma_f32_16x16x32_bf16 v[160:163], v[204:207], v[28:31], v[160:163]
v_cvt_pk_bf16_f32 v139, v142, v143
v_add_u32 v230, 0x10000, v230
v_mfma_f32_16x16x32_bf16 v[168:171], v[204:207], v[60:63], v[168:171]
buffer_store_dwordx4 v[136:139], v223, s[56:59], s69 offen offset:1024 sc1
v_mfma_f32_16x16x32_bf16 v[176:179], v[204:207], v[92:95], v[176:179]
ds_write_b128 v226, v[136:139] offset:1024
ds_read_b64_tr_b16 v[216:217], v230 offset:0
s_mov_b32 s61, 0x7140
s_add_u32 m0, s35, 0x8800
v_mfma_f32_16x16x32_bf16 v[184:187], v[204:207], v[124:127], v[184:187]
ds_read_b64_tr_b16 v[218:219], v230 offset:1024
ds_read_b64_tr_b16 v[192:193], v229 offset:2048
v_mfma_f32_16x16x32_bf16 v[164:167], v[208:211], v[28:31], v[164:167]
buffer_load_dwordx4 v235, s[52:55], s61 offen offset:0  lds
v_mfma_f32_16x16x32_bf16 v[172:175], v[208:211], v[60:63], v[172:175]
ds_read_b64_tr_b16 v[194:195], v229 offset:3072
v_cmp_eq_u32 vcc, 2, v228
v_mfma_f32_16x16x32_bf16 v[180:183], v[208:211], v[92:95], v[180:183]
v_mov_b32 v238, s74
v_mov_b32 v239, s75
v_mfma_f32_16x16x32_bf16 v[188:191], v[208:211], v[124:127], v[188:191]
v_cndmask_b32 v227, 0, v238, vcc
v_cmp_eq_u32 vcc, 3, v228
s_nop 1
v_cndmask_b32 v227, v227, v239, vcc
v_accvgpr_write_b32 a225, v227
s_mov_b64 s[92:93], 0xffff
v_cndmask_b32_e64 v238, 0, v227, s[92:93]
v_accvgpr_write_b32 a229, v238
v_cmp_eq_u32 vcc, 4, v228
v_mov_b32 v238, s74
v_mov_b32 v239, s75
v_cndmask_b32 v227, 0, v238, vcc
v_cmp_eq_u32 vcc, 5, v228
s_nop 1
v_cndmask_b32 v227, v227, v239, vcc
v_accvgpr_write_b32 a226, v227
s_mov_b64 s[92:93], 0xffff
v_cndmask_b32_e64 v238, 0, v227, s[92:93]
v_accvgpr_write_b32 a230, v238
v_cmp_eq_u32 vcc, 6, v228
v_mov_b32 v238, s74
v_mov_b32 v239, s75
v_cndmask_b32 v227, 0, v238, vcc
v_cmp_eq_u32 vcc, 7, v228
s_nop 1
v_cndmask_b32 v227, v227, v239, vcc
v_accvgpr_write_b32 a227, v227
s_mov_b64 s[92:93], 0xffff
v_cndmask_b32_e64 v238, 0, v227, s[92:93]
v_accvgpr_write_b32 a231, v238
v_mbcnt_lo_u32_b32 v228, -1, 0
v_mbcnt_hi_u32_b32 v228, -1, v228
v_lshrrev_b32 v228, 4, v228
v_mov_b32 v238, 0x40000000
v_add_u32 v227, 0, v228
v_cmp_eq_u32 vcc, %11, v227
s_nop 1
v_cndmask_b32 v224, v224, v238, vcc
v_add_u32 v227, 4, v228
v_cmp_eq_u32 vcc, %11, v227
s_nop 1
v_cndmask_b32 v231, v231, v238, vcc
LST48:
s_waitcnt lgkmcnt(5)
v_mfma_f32_16x16x32_bf16 v[128:131], v[212:215], v[0:3], 0
ds_read_b64_tr_b16 v[196:197], v230 offset:2048
ds_read_b64_tr_b16 v[198:199], v230 offset:3072
v_mfma_f32_16x16x32_bf16 v[136:139], v[212:215], v[32:35], 0
s_mov_b32 s61, 0xa940
s_add_u32 m0, s35, 0x8c00
ds_read_b64_tr_b16 v[200:201], v229 offset:4096
ds_read_b64_tr_b16 v[202:203], v229 offset:5120
v_mfma_f32_16x16x32_bf16 v[144:147], v[212:215], v[64:67], 0
ds_read_b64_tr_b16 v[204:205], v230 offset:4096
ds_read_b64_tr_b16 v[206:207], v230 offset:5120
s_add_u32 s69, s72, 0x2000
v_mfma_f32_16x16x32_bf16 v[152:155], v[212:215], v[96:99], 0
buffer_load_dwordx4 v235, s[52:55], s61 offen offset:0  lds
s_waitcnt lgkmcnt(8)
v_mfma_f32_16x16x32_bf16 v[132:135], v[216:219], v[0:3], 0
ds_read_b64_tr_b16 v[208:209], v229 offset:6144
ds_read_b64_tr_b16 v[210:211], v229 offset:7168
v_mfma_f32_16x16x32_bf16 v[140:143], v[216:219], v[32:35], 0
ds_read_b64_tr_b16 v[212:213], v230 offset:6144
ds_read_b64_tr_b16 v[214:215], v230 offset:7168
v_mfma_f32_16x16x32_bf16 v[148:151], v[216:219], v[64:67], 0
v_mul_f32 v176, v176, v253
v_mul_f32 v177, v177, v253
v_mfma_f32_16x16x32_bf16 v[156:159], v[216:219], v[96:99], 0
v_mul_f32 v178, v178, v253
v_mul_f32 v179, v179, v253
s_waitcnt lgkmcnt(10)
v_mfma_f32_16x16x32_bf16 v[128:131], v[192:195], v[4:7], v[128:131]
ds_read_b64_tr_b16 v[216:217], v229 offset:8192
v_mfma_f32_16x16x32_bf16 v[136:139], v[192:195], v[36:39], v[136:139]
s_mov_b32 s61, 0xe140
s_add_u32 m0, s35, 0x9000
ds_read_b64_tr_b16 v[218:219], v229 offset:9216
v_mfma_f32_16x16x32_bf16 v[144:147], v[192:195], v[68:71], v[144:147]
ds_write_b128 v225, v[176:179] offset:16384
v_mfma_f32_16x16x32_bf16 v[152:155], v[192:195], v[100:103], v[152:155]
buffer_load_dwordx4 v235, s[52:55], s61 offen offset:0  lds
s_waitcnt lgkmcnt(9)
v_mfma_f32_16x16x32_bf16 v[132:135], v[196:199], v[4:7], v[132:135]
v_mul_f32 v180, v180, v253
v_mul_f32 v181, v181, v253
v_mfma_f32_16x16x32_bf16 v[140:143], v[196:199], v[36:39], v[140:143]
v_mul_f32 v182, v182, v253
v_mul_f32 v183, v183, v253
v_mfma_f32_16x16x32_bf16 v[148:151], v[196:199], v[68:71], v[148:151]
ds_write_b128 v225, v[180:183] offset:17408
v_mfma_f32_16x16x32_bf16 v[156:159], v[196:199], v[100:103], v[156:159]
v_mul_f32 v184, v184, v254
v_mul_f32 v185, v185, v254
v_mfma_f32_16x16x32_bf16 v[128:131], v[200:203], v[8:11], v[128:131]
s_mov_b32 s61, 0x11940
s_add_u32 m0, s35, 0x9400
v_mul_f32 v186, v186, v254
v_mul_f32 v187, v187, v254
v_mfma_f32_16x16x32_bf16 v[136:139], v[200:203], v[40:43], v[136:139]
ds_write_b128 v225, v[184:187] offset:18432
v_mfma_f32_16x16x32_bf16 v[144:147], v[200:203], v[72:75], v[144:147]
buffer_load_dwordx4 v235, s[52:55], s61 offen offset:0  lds
v_mfma_f32_16x16x32_bf16 v[152:155], v[200:203], v[104:107], v[152:155]
v_mul_f32 v188, v188, v254
v_mul_f32 v189, v189, v254
s_waitcnt lgkmcnt(3)
v_mfma_f32_16x16x32_bf16 v[132:135], v[204:207], v[8:11], v[132:135]
v_mul_f32 v190, v190, v254
v_mul_f32 v191, v191, v254
v_mfma_f32_16x16x32_bf16 v[140:143], v[204:207], v[40:43], v[140:143]
ds_write_b128 v225, v[188:191] offset:19456
v_mfma_f32_16x16x32_bf16 v[148:151], v[204:207], v[72:75], v[148:151]
ds_read_b64_tr_b16 v[192:193], v230 offset:8192
ds_read_b64_tr_b16 v[194:195], v230 offset:9216
v_mfma_f32_16x16x32_bf16 v[156:159], v[204:207], v[104:107], v[156:159]
ds_read_b64_tr_b16 v[196:197], v229 offset:10240
ds_read_b64_tr_b16 v[198:199], v229 offset:11264
v_mfma_f32_16x16x32_bf16 v[128:131], v[208:211], v[12:15], v[128:131]
s_mov_b32 s61, 0x15140
s_add_u32 m0, s35, 0x9800
ds_read_b64_tr_b16 v[200:201], v230 offset:10240
ds_read_b64_tr_b16 v[202:203], v230 offset:11264
v_mfma_f32_16x16x32_bf16 v[136:139], v[208:211], v[44:47], v[136:139]
ds_read_b64_tr_b16 v[204:205], v229 offset:12288
ds_read_b64_tr_b16 v[206:207], v229 offset:13312
v_mfma_f32_16x16x32_bf16 v[144:147], v[208:211], v[76:79], v[144:147]
buffer_load_dwordx4 v235, s[52:55], s61 offen offset:0  lds
v_mfma_f32_16x16x32_bf16 v[152:155], v[208:211], v[108:111], v[152:155]
v_mfma_f32_16x16x32_bf16 v[132:135], v[212:215], v[12:15], v[132:135]
ds_read_b64_tr_b16 v[208:209], v230 offset:12288
v_mfma_f32_16x16x32_bf16 v[140:143], v[212:215], v[44:47], v[140:143]
ds_read_b64_tr_b16 v[210:211], v230 offset:13312
v_mfma_f32_16x16x32_bf16 v[148:151], v[212:215], v[76:79], v[148:151]
s_waitcnt lgkmcnt(10)
s_barrier
v_mfma_f32_16x16x32_bf16 v[156:159], v[212:215], v[108:111], v[156:159]
s_mov_b32 s61, 0x18940
s_add_u32 m0, s35, 0x9c00
ds_read_b128 v[176:179], v226 offset:16384
ds_read_b128 v[180:183], v226 offset:17408
v_mfma_f32_16x16x32_bf16 v[128:131], v[216:219], v[16:19], v[128:131]
ds_read_b128 v[184:187], v226 offset:18432
ds_read_b128 v[188:191], v226 offset:19456
v_mfma_f32_16x16x32_bf16 v[136:139], v[216:219], v[48:51], v[136:139]
buffer_load_dwordx4 v235, s[52:55], s61 offen offset:0  lds
v_mfma_f32_16x16x32_bf16 v[144:147], v[216:219], v[80:83], v[144:147]
ds_read_b64_tr_b16 v[212:213], v229 offset:14336
ds_read_b64_tr_b16 v[214:215], v229 offset:15360
v_mfma_f32_16x16x32_bf16 v[152:155], v[216:219], v[112:115], v[152:155]
ds_read_b128 a[112:115], v250 offset:0
s_waitcnt lgkmcnt(9)
v_mfma_f32_16x16x32_bf16 v[132:135], v[192:195], v[16:19], v[132:135]
ds_read_b64_tr_b16 v[216:217], v230 offset:14336
v_mfma_f32_16x16x32_bf16 v[140:143], v[192:195], v[48:51], v[140:143]
ds_read_b64_tr_b16 v[218:219], v230 offset:15360
v_mfma_f32_16x16x32_bf16 v[148:151], v[192:195], v[80:83], v[148:151]
s_waitcnt lgkmcnt(8)
v_fma_f32 v160, v160, v251, v176
v_fma_f32 v161, v161, v251, v177
v_mfma_f32_16x16x32_bf16 v[156:159], v[192:195], v[112:115], v[156:159]
v_fma_f32 v162, v162, v251, v178
v_fma_f32 v163, v163, v251, v179
v_mfma_f32_16x16x32_bf16 v[128:131], v[196:199], v[20:23], v[128:131]
s_waitcnt lgkmcnt(7)
v_fma_f32 v164, v164, v251, v180
v_mfma_f32_16x16x32_bf16 v[136:139], v[196:199], v[52:55], v[136:139]
s_waitcnt vmcnt(18)
s_waitcnt lgkmcnt(0)
v_fma_f32 v165, v165, v251, v181
v_mfma_f32_16x16x32_bf16 v[144:147], v[196:199], v[84:87], v[144:147]
s_barrier
ds_read_b64_tr_b16 v[192:193], v229 offset:32768
v_mfma_f32_16x16x32_bf16 v[152:155], v[196:199], v[116:119], v[152:155]
s_mov_b32 s61, 0x180
s_add_u32 m0, s35, 0x10000
ds_read_b64_tr_b16 v[194:195], v229 offset:33792
v_fma_f32 v166, v166, v251, v182
v_mfma_f32_16x16x32_bf16 v[132:135], v[200:203], v[20:23], v[132:135]
v_fma_f32 v167, v167, v251, v183
v_cvt_pk_bf16_f32 v160, v160, v161
v_mfma_f32_16x16x32_bf16 v[140:143], v[200:203], v[52:55], v[140:143]
buffer_load_dwordx4 v235, s[52:55], s61 offen offset:0  lds
v_mfma_f32_16x16x32_bf16 v[148:151], v[200:203], v[84:87], v[148:151]
v_cvt_pk_bf16_f32 v161, v162, v163
v_cvt_pk_bf16_f32 v162, v164, v165
v_mfma_f32_16x16x32_bf16 v[156:159], v[200:203], v[116:119], v[156:159]
v_cvt_pk_bf16_f32 v163, v166, v167
ds_read_b64_tr_b16 v[196:197], v230 offset:32768
v_mfma_f32_16x16x32_bf16 v[128:131], v[204:207], v[24:27], v[128:131]
buffer_store_dwordx4 v[160:163], v222, s[56:59], s69 offen offset:0 sc1
v_mfma_f32_16x16x32_bf16 v[136:139], v[204:207], v[56:59], v[136:139]
ds_write_b128 v226, v[160:163] offset:16384
v_fma_f32 v168, v168, v252, v184
v_mfma_f32_16x16x32_bf16 v[144:147], v[204:207], v[88:91], v[144:147]
s_mov_b32 s61, 0x3980
s_add_u32 m0, s35, 0x10400
v_fma_f32 v169, v169, v252, v185
v_fma_f32 v170, v170, v252, v186
v_mfma_f32_16x16x32_bf16 v[152:155], v[204:207], v[120:123], v[152:155]
v_fma_f32 v171, v171, v252, v187
v_fma_f32 v172, v172, v252, v188
v_mfma_f32_16x16x32_bf16 v[132:135], v[208:211], v[24:27], v[132:135]
buffer_load_dwordx4 v235, s[52:55], s61 offen offset:0  lds
v_mfma_f32_16x16x32_bf16 v[140:143], v[208:211], v[56:59], v[140:143]
v_fma_f32 v173, v173, v252, v189
v_fma_f32 v174, v174, v252, v190
v_mfma_f32_16x16x32_bf16 v[148:151], v[208:211], v[88:91], v[148:151]
v_fma_f32 v175, v175, v252, v191
v_cvt_pk_bf16_f32 v168, v168, v169
v_mfma_f32_16x16x32_bf16 v[156:159], v[208:211], v[120:123], v[156:159]
v_cvt_pk_bf16_f32 v169, v170, v171
v_cvt_pk_bf16_f32 v170, v172, v173
v_mfma_f32_16x16x32_bf16 v[128:131], v[212:215], v[28:31], v[128:131]
v_cvt_pk_bf16_f32 v171, v174, v175
ds_read_b64_tr_b16 v[198:199], v230 offset:33792
v_mfma_f32_16x16x32_bf16 v[136:139], v[212:215], v[60:63], v[136:139]
buffer_store_dwordx4 v[168:171], v223, s[56:59], s69 offen offset:1024 sc1
v_mfma_f32_16x16x32_bf16 v[144:147], v[212:215], v[92:95], v[144:147]
ds_write_b128 v226, v[168:171] offset:17408
s_mov_b32 s61, 0x7180
s_add_u32 m0, s35, 0x10800
ds_read_b64_tr_b16 v[200:201], v229 offset:34816
v_mfma_f32_16x16x32_bf16 v[152:155], v[212:215], v[124:127], v[152:155]
ds_read_b64_tr_b16 v[202:203], v229 offset:35840
ds_read_b64_tr_b16 v[204:205], v230 offset:34816
v_mfma_f32_16x16x32_bf16 v[132:135], v[216:219], v[28:31], v[132:135]
buffer_load_dwordx4 v235, s[52:55], s61 offen offset:0  lds
v_mfma_f32_16x16x32_bf16 v[140:143], v[216:219], v[60:63], v[140:143]
ds_read_b64_tr_b16 v[206:207], v230 offset:35840
ds_read_b64_tr_b16 v[208:209], v229 offset:36864
v_mfma_f32_16x16x32_bf16 v[148:151], v[216:219], v[92:95], v[148:151]
ds_read_b64_tr_b16 v[210:211], v229 offset:37888
ds_read_b64_tr_b16 v[212:213], v230 offset:36864
v_mfma_f32_16x16x32_bf16 v[156:159], v[216:219], v[124:127], v[156:159]
LST49:
ds_read_b64_tr_b16 v[214:215], v230 offset:37888
s_waitcnt lgkmcnt(10)
v_mfma_f32_16x16x32_bf16 v[160:163], v[192:195], v[0:3], 0
ds_read_b64_tr_b16 v[216:217], v229 offset:38912
v_mfma_f32_16x16x32_bf16 v[168:171], v[192:195], v[32:35], 0
s_mov_b32 s61, 0xa980
s_add_u32 m0, s35, 0x10c00
ds_read_b64_tr_b16 v[218:219], v229 offset:39936
v_mfma_f32_16x16x32_bf16 v[176:179], v[192:195], v[64:67], 0
s_add_u32 s69, s72, 0x4000
v_mul_f32 v144, v144, v253
v_mfma_f32_16x16x32_bf16 v[184:187], v[192:195], v[96:99], 0
buffer_load_dwordx4 v235, s[52:55], s61 offen offset:0  lds
s_waitcnt lgkmcnt(11)
v_mfma_f32_16x16x32_bf16 v[164:167], v[196:199], v[0:3], 0
ds_read_b64_tr_b16 v[192:193], v230 offset:38912
v_mul_f32 v145, v145, v253
v_mfma_f32_16x16x32_bf16 v[172:175], v[196:199], v[32:35], 0
ds_read_b64_tr_b16 v[194:195], v230 offset:39936
v_mul_f32 v146, v146, v253
v_mfma_f32_16x16x32_bf16 v[180:183], v[196:199], v[64:67], 0
v_mul_f32 v147, v147, v253
v_mfma_f32_16x16x32_bf16 v[188:191], v[196:199], v[96:99], 0
ds_write_b128 v225, v[144:147] offset:0
s_waitcnt lgkmcnt(8)
v_mfma_f32_16x16x32_bf16 v[160:163], v[200:203], v[4:7], v[160:163]
ds_read_b64_tr_b16 v[196:197], v229 offset:40960
v_mul_f32 v148, v148, v253
v_mfma_f32_16x16x32_bf16 v[168:171], v[200:203], v[36:39], v[168:171]
s_mov_b32 s61, 0xe180
s_add_u32 m0, s35, 0x11000
ds_read_b64_tr_b16 v[198:199], v229 offset:41984
v_mul_f32 v149, v149, v253
v_mfma_f32_16x16x32_bf16 v[176:179], v[200:203], v[68:71], v[176:179]
v_mul_f32 v150, v150, v253
v_mul_f32 v151, v151, v253
v_mfma_f32_16x16x32_bf16 v[184:187], v[200:203], v[100:103], v[184:187]
buffer_load_dwordx4 v235, s[52:55], s61 offen offset:0  lds
v_mfma_f32_16x16x32_bf16 v[164:167], v[204:207], v[4:7], v[164:167]
ds_write_b128 v225, v[148:151] offset:1024
v_mfma_f32_16x16x32_bf16 v[172:175], v[204:207], v[36:39], v[172:175]
v_mul_f32 v152, v152, v254
v_mul_f32 v153, v153, v254
v_mfma_f32_16x16x32_bf16 v[180:183], v[204:207], v[68:71], v[180:183]
v_mul_f32 v154, v154, v254
ds_read_b64_tr_b16 v[200:201], v230 offset:40960
v_mfma_f32_16x16x32_bf16 v[188:191], v[204:207], v[100:103], v[188:191]
v_mul_f32 v155, v155, v254
ds_read_b64_tr_b16 v[202:203], v230 offset:41984
s_waitcnt lgkmcnt(6)
v_mfma_f32_16x16x32_bf16 v[160:163], v[208:211], v[8:11], v[160:163]
s_mov_b32 s61, 0x11980
s_add_u32 m0, s35, 0x11400
ds_write_b128 v225, v[152:155] offset:2048
v_mfma_f32_16x16x32_bf16 v[168:171], v[208:211], v[40:43], v[168:171]
v_mul_f32 v156, v156, v254
ds_read_b64_tr_b16 v[204:205], v229 offset:43008
v_mfma_f32_16x16x32_bf16 v[176:179], v[208:211], v[72:75], v[176:179]
buffer_load_dwordx4 v235, s[52:55], s61 offen offset:0  lds
v_mfma_f32_16x16x32_bf16 v[184:187], v[208:211], v[104:107], v[184:187]
v_mul_f32 v157, v157, v254
v_mul_f32 v158, v158, v254
v_mfma_f32_16x16x32_bf16 v[164:167], v[212:215], v[8:11], v[164:167]
v_mul_f32 v159, v159, v254
ds_read_b64_tr_b16 v[206:207], v229 offset:44032
v_mfma_f32_16x16x32_bf16 v[172:175], v[212:215], v[40:43], v[172:175]
ds_write_b128 v225, v[156:159] offset:3072
v_mfma_f32_16x16x32_bf16 v[180:183], v[212:215], v[72:75], v[180:183]
ds_read_b64_tr_b16 v[208:209], v230 offset:43008
ds_read_b64_tr_b16 v[210:211], v230 offset:44032
v_mfma_f32_16x16x32_bf16 v[188:191], v[212:215], v[104:107], v[188:191]
v_mfma_f32_16x16x32_bf16 v[160:163], v[216:219], v[12:15], v[160:163]
s_mov_b32 s61, 0x15180
s_add_u32 m0, s35, 0x11800
ds_read_b64_tr_b16 v[212:213], v229 offset:45056
v_mfma_f32_16x16x32_bf16 v[168:171], v[216:219], v[44:47], v[168:171]
ds_read_b64_tr_b16 v[214:215], v229 offset:46080
v_mfma_f32_16x16x32_bf16 v[176:179], v[216:219], v[76:79], v[176:179]
buffer_load_dwordx4 v235, s[52:55], s61 offen offset:0  lds
v_mfma_f32_16x16x32_bf16 v[184:187], v[216:219], v[108:111], v[184:187]
v_mfma_f32_16x16x32_bf16 v[164:167], v[192:195], v[12:15], v[164:167]
ds_read_b64_tr_b16 v[216:217], v230 offset:45056
v_mfma_f32_16x16x32_bf16 v[172:175], v[192:195], v[44:47], v[172:175]
ds_read_b64_tr_b16 v[218:219], v230 offset:46080
v_mfma_f32_16x16x32_bf16 v[180:183], v[192:195], v[76:79], v[180:183]
s_waitcnt lgkmcnt(6)
s_barrier
v_mfma_f32_16x16x32_bf16 v[188:191], v[192:195], v[108:111], v[188:191]
s_mov_b32 s61, 0x18980
s_add_u32 m0, s35, 0x11c00
ds_read_b128 v[144:147], v226 offset:0
ds_read_b128 v[148:151], v226 offset:1024
v_mfma_f32_16x16x32_bf16 v[160:163], v[196:199], v[16:19], v[160:163]
ds_read_b128 v[152:155], v226 offset:2048
ds_read_b128 v[156:159], v226 offset:3072
v_mfma_f32_16x16x32_bf16 v[168:171], v[196:199], v[48:51], v[168:171]
buffer_load_dwordx4 v235, s[52:55], s61 offen offset:0  lds
v_mfma_f32_16x16x32_bf16 v[176:179], v[196:199], v[80:83], v[176:179]
ds_read_b64_tr_b16 v[192:193], v229 offset:47104
ds_read_b64_tr_b16 v[194:195], v229 offset:48128
v_mfma_f32_16x16x32_bf16 v[184:187], v[196:199], v[112:115], v[184:187]
ds_read_b128 a[116:119], v250 offset:16384
v_mfma_f32_16x16x32_bf16 v[164:167], v[200:203], v[16:19], v[164:167]
ds_read_b64_tr_b16 v[196:197], v230 offset:47104
v_mfma_f32_16x16x32_bf16 v[172:175], v[200:203], v[48:51], v[172:175]
ds_read_b64_tr_b16 v[198:199], v230 offset:48128
v_mfma_f32_16x16x32_bf16 v[180:183], v[200:203], v[80:83], v[180:183]
s_waitcnt lgkmcnt(8)
v_fma_f32 v128, v128, v251, v144
v_fma_f32 v129, v129, v251, v145
v_mfma_f32_16x16x32_bf16 v[188:191], v[200:203], v[112:115], v[188:191]
v_fma_f32 v130, v130, v251, v146
v_fma_f32 v131, v131, v251, v147
v_mfma_f32_16x16x32_bf16 v[160:163], v[204:207], v[20:23], v[160:163]
s_waitcnt lgkmcnt(7)
v_fma_f32 v132, v132, v251, v148
v_mfma_f32_16x16x32_bf16 v[168:171], v[204:207], v[52:55], v[168:171]
s_waitcnt vmcnt(20)
s_waitcnt lgkmcnt(0)
v_fma_f32 v133, v133, v251, v149
v_mfma_f32_16x16x32_bf16 v[176:179], v[204:207], v[84:87], v[176:179]
s_barrier
v_add_u32 v229, 0xffff0000, v229
v_mfma_f32_16x16x32_bf16 v[184:187], v[204:207], v[116:119], v[184:187]
s_mov_b32 s61, 0x1c0
s_add_u32 m0, s35, 0x18000
ds_read_b64_tr_b16 v[200:201], v229 offset:0
ds_read_b64_tr_b16 v[202:203], v229 offset:1024
v_mfma_f32_16x16x32_bf16 v[164:167], v[208:211], v[20:23], v[164:167]
v_fma_f32 v134, v134, v251, v150
v_fma_f32 v135, v135, v251, v151
v_mfma_f32_16x16x32_bf16 v[172:175], v[208:211], v[52:55], v[172:175]
buffer_load_dwordx4 v235, s[52:55], s61 offen offset:0  lds
v_mfma_f32_16x16x32_bf16 v[180:183], v[208:211], v[84:87], v[180:183]
v_cvt_pk_bf16_f32 v128, v128, v129
v_cvt_pk_bf16_f32 v129, v130, v131
v_mfma_f32_16x16x32_bf16 v[188:191], v[208:211], v[116:119], v[188:191]
v_cvt_pk_bf16_f32 v130, v132, v133
v_cvt_pk_bf16_f32 v131, v134, v135
v_mfma_f32_16x16x32_bf16 v[160:163], v[212:215], v[24:27], v[160:163]
buffer_store_dwordx4 v[128:131], v222, s[56:59], s69 offen offset:0 sc1
v_mfma_f32_16x16x32_bf16 v[168:171], v[212:215], v[56:59], v[168:171]
ds_write_b128 v226, v[128:131] offset:0
v_fma_f32 v136, v136, v252, v152
v_mfma_f32_16x16x32_bf16 v[176:179], v[212:215], v[88:91], v[176:179]
s_mov_b32 s61, 0x39c0
s_add_u32 m0, s35, 0x18400
v_fma_f32 v137, v137, v252, v153
v_fma_f32 v138, v138, v252, v154
v_mfma_f32_16x16x32_bf16 v[184:187], v[212:215], v[120:123], v[184:187]
v_fma_f32 v139, v139, v252, v155
v_fma_f32 v140, v140, v252, v156
v_mfma_f32_16x16x32_bf16 v[164:167], v[216:219], v[24:27], v[164:167]
buffer_load_dwordx4 v235, s[52:55], s61 offen offset:0  lds
v_mfma_f32_16x16x32_bf16 v[172:175], v[216:219], v[56:59], v[172:175]
v_fma_f32 v141, v141, v252, v157
v_fma_f32 v142, v142, v252, v158
v_mfma_f32_16x16x32_bf16 v[180:183], v[216:219], v[88:91], v[180:183]
v_fma_f32 v143, v143, v252, v159
v_cvt_pk_bf16_f32 v136, v136, v137
v_mfma_f32_16x16x32_bf16 v[188:191], v[216:219], v[120:123], v[188:191]
v_cvt_pk_bf16_f32 v137, v138, v139
v_cvt_pk_bf16_f32 v138, v140, v141
v_mfma_f32_16x16x32_bf16 v[160:163], v[192:195], v[28:31], v[160:163]
v_cvt_pk_bf16_f32 v139, v142, v143
v_add_u32 v230, 0xffff0000, v230
v_mfma_f32_16x16x32_bf16 v[168:171], v[192:195], v[60:63], v[168:171]
buffer_store_dwordx4 v[136:139], v223, s[56:59], s69 offen offset:1024 sc1
v_mfma_f32_16x16x32_bf16 v[176:179], v[192:195], v[92:95], v[176:179]
ds_write_b128 v226, v[136:139] offset:1024
ds_read_b64_tr_b16 v[204:205], v230 offset:0
s_mov_b32 s61, 0x71c0
s_add_u32 m0, s35, 0x18800
v_mfma_f32_16x16x32_bf16 v[184:187], v[192:195], v[124:127], v[184:187]
ds_read_b64_tr_b16 v[206:207], v230 offset:1024
ds_read_b64_tr_b16 v[208:209], v229 offset:2048
v_mfma_f32_16x16x32_bf16 v[164:167], v[196:199], v[28:31], v[164:167]
buffer_load_dwordx4 v235, s[52:55], s61 offen offset:0  lds
v_mfma_f32_16x16x32_bf16 v[172:175], v[196:199], v[60:63], v[172:175]
ds_read_b64_tr_b16 v[210:211], v229 offset:3072
ds_read_b64_tr_b16 v[212:213], v230 offset:2048
v_mfma_f32_16x16x32_bf16 v[180:183], v[196:199], v[92:95], v[180:183]
ds_read_b64_tr_b16 v[214:215], v230 offset:3072
ds_read_b64_tr_b16 v[216:217], v229 offset:4096
v_mfma_f32_16x16x32_bf16 v[188:191], v[196:199], v[124:127], v[188:191]
LST50:
ds_read_b64_tr_b16 v[218:219], v229 offset:5120
ds_read_b64_tr_b16 v[192:193], v230 offset:4096
s_waitcnt lgkmcnt(10)
v_mfma_f32_16x16x32_bf16 v[128:131], v[200:203], v[0:3], 0
ds_read_b64_tr_b16 v[194:195], v230 offset:5120
ds_read_b64_tr_b16 v[196:197], v229 offset:6144
v_mfma_f32_16x16x32_bf16 v[136:139], v[200:203], v[32:35], 0
s_mov_b32 s61, 0xa9c0
s_add_u32 m0, s35, 0x18c00
ds_read_b64_tr_b16 v[198:199], v229 offset:7168
v_mfma_f32_16x16x32_bf16 v[144:147], v[200:203], v[64:67], 0
s_add_u32 s69, s72, 0x6000
v_mul_f32 v176, v176, v253
v_mfma_f32_16x16x32_bf16 v[152:155], v[200:203], v[96:99], 0
buffer_load_dwordx4 v235, s[52:55], s61 offen offset:0  lds
s_waitcnt lgkmcnt(10)
v_mfma_f32_16x16x32_bf16 v[132:135], v[204:207], v[0:3], 0
ds_read_b64_tr_b16 v[200:201], v230 offset:6144
v_mul_f32 v177, v177, v253
v_mfma_f32_16x16x32_bf16 v[140:143], v[204:207], v[32:35], 0
ds_read_b64_tr_b16 v[202:203], v230 offset:7168
v_mul_f32 v178, v178, v253
v_mfma_f32_16x16x32_bf16 v[148:151], v[204:207], v[64:67], 0
v_mul_f32 v179, v179, v253
v_mfma_f32_16x16x32_bf16 v[156:159], v[204:207], v[96:99], 0
ds_write_b128 v225, v[176:179] offset:16384
s_waitcnt lgkmcnt(10)
v_mfma_f32_16x16x32_bf16 v[128:131], v[208:211], v[4:7], v[128:131]
ds_read_b64_tr_b16 v[204:205], v229 offset:8192
v_mul_f32 v180, v180, v253
v_mfma_f32_16x16x32_bf16 v[136:139], v[208:211], v[36:39], v[136:139]
s_mov_b32 s61, 0xe1c0
s_add_u32 m0, s35, 0x19000
ds_read_b64_tr_b16 v[206:207], v229 offset:9216
v_mul_f32 v181, v181, v253
v_mfma_f32_16x16x32_bf16 v[144:147], v[208:211], v[68:71], v[144:147]
v_mul_f32 v182, v182, v253
v_mul_f32 v183, v183, v253
v_mfma_f32_16x16x32_bf16 v[152:155], v[208:211], v[100:103], v[152:155]
buffer_load_dwordx4 v235, s[52:55], s61 offen offset:0  lds
s_waitcnt lgkmcnt(5)
v_mfma_f32_16x16x32_bf16 v[132:135], v[212:215], v[4:7], v[132:135]
ds_write_b128 v225, v[180:183] offset:17408
v_mfma_f32_16x16x32_bf16 v[140:143], v[212:215], v[36:39], v[140:143]
v_mul_f32 v184, v184, v254
v_mul_f32 v185, v185, v254
v_mfma_f32_16x16x32_bf16 v[148:151], v[212:215], v[68:71], v[148:151]
v_mul_f32 v186, v186, v254
ds_read_b64_tr_b16 v[208:209], v230 offset:8192
v_mfma_f32_16x16x32_bf16 v[156:159], v[212:215], v[100:103], v[156:159]
v_mul_f32 v187, v187, v254
ds_read_b64_tr_b16 v[210:211], v230 offset:9216
v_mfma_f32_16x16x32_bf16 v[128:131], v[216:219], v[8:11], v[128:131]
s_mov_b32 s61, 0x119c0
s_add_u32 m0, s35, 0x19400
LH15:
s_cbranch_execz LHB1
ds_write_b128 v225, v[184:187] offset:18432
v_mfma_f32_16x16x32_bf16 v[136:139], v[216:219], v[40:43], v[136:139]
v_mul_f32 v188, v188, v254
ds_read_b64_tr_b16 v[212:213], v229 offset:10240
v_mfma_f32_16x16x32_bf16 v[144:147], v[216:219], v[72:75], v[144:147]
buffer_load_dwordx4 v235, s[52:55], s61 offen offset:0  lds
v_mfma_f32_16x16x32_bf16 v[152:155], v[216:219], v[104:107], v[152:155]
v_mul_f32 v189, v189, v254
v_mul_f32 v190, v190, v254
v_mfma_f32_16x16x32_bf16 v[132:135], v[192:195], v[8:11], v[132:135]
v_mul_f32 v191, v191, v254
ds_read_b64_tr_b16 v[214:215], v229 offset:11264
v_mfma_f32_16x16x32_bf16 v[140:143], v[192:195], v[40:43], v[140:143]
ds_write_b128 v225, v[188:191] offset:19456
v_mfma_f32_16x16x32_bf16 v[148:151], v[192:195], v[72:75], v[148:151]
ds_read_b64_tr_b16 v[216:217], v230 offset:10240
ds_read_b64_tr_b16 v[218:219], v230 offset:11264
v_mfma_f32_16x16x32_bf16 v[156:159], v[192:195], v[104:107], v[156:159]
v_mfma_f32_16x16x32_bf16 v[128:131], v[196:199], v[12:15], v[128:131]
s_mov_b32 s61, 0x151c0
s_add_u32 m0, s35, 0x19800
ds_read_b64_tr_b16 v[192:193], v229 offset:12288
v_mfma_f32_16x16x32_bf16 v[136:139], v[196:199], v[44:47], v[136:139]
ds_read_b64_tr_b16 v[194:195], v229 offset:13312
v_mfma_f32_16x16x32_bf16 v[144:147], v[196:199], v[76:79], v[144:147]
buffer_load_dwordx4 v235, s[52:55], s61 offen offset:0  lds
v_mfma_f32_16x16x32_bf16 v[152:155], v[196:199], v[108:111], v[152:155]
s_waitcnt lgkmcnt(6)
v_mfma_f32_16x16x32_bf16 v[132:135], v[200:203], v[12:15], v[132:135]
ds_read_b64_tr_b16 v[196:197], v230 offset:12288
v_mfma_f32_16x16x32_bf16 v[140:143], v[200:203], v[44:47], v[140:143]
ds_read_b64_tr_b16 v[198:199], v230 offset:13312
v_mfma_f32_16x16x32_bf16 v[148:151], v[200:203], v[76:79], v[148:151]
s_waitcnt lgkmcnt(6)
s_barrier
v_mfma_f32_16x16x32_bf16 v[156:159], v[200:203], v[108:111], v[156:159]
s_mov_b32 s61, 0x189c0
s_add_u32 m0, s35, 0x19c00
ds_read_b128 v[176:179], v226 offset:16384
ds_read_b128 v[180:183], v226 offset:17408
v_mfma_f32_16x16x32_bf16 v[128:131], v[204:207], v[16:19], v[128:131]
ds_read_b128 v[184:187], v226 offset:18432
ds_read_b128 v[188:191], v226 offset:19456
v_mfma_f32_16x16x32_bf16 v[136:139], v[204:207], v[48:51], v[136:139]
buffer_load_dwordx4 v235, s[52:55], s61 offen offset:0  lds
v_mfma_f32_16x16x32_bf16 v[144:147], v[204:207], v[80:83], v[144:147]
ds_read_b64_tr_b16 v[200:201], v229 offset:14336
ds_read_b64_tr_b16 v[202:203], v229 offset:15360
v_mfma_f32_16x16x32_bf16 v[152:155], v[204:207], v[112:115], v[152:155]
ds_read_b128 a[120:123], v250 offset:0
v_mfma_f32_16x16x32_bf16 v[132:135], v[208:211], v[16:19], v[132:135]
ds_read_b64_tr_b16 v[204:205], v230 offset:14336
v_mfma_f32_16x16x32_bf16 v[140:143], v[208:211], v[48:51], v[140:143]
ds_read_b64_tr_b16 v[206:207], v230 offset:15360
v_mfma_f32_16x16x32_bf16 v[148:151], v[208:211], v[80:83], v[148:151]
s_waitcnt lgkmcnt(8)
v_fma_f32 v160, v160, v251, v176
v_fma_f32 v161, v161, v251, v177
v_mfma_f32_16x16x32_bf16 v[156:159], v[208:211], v[112:115], v[156:159]
v_fma_f32 v162, v162, v251, v178
v_fma_f32 v163, v163, v251, v179
v_mfma_f32_16x16x32_bf16 v[128:131], v[212:215], v[20:23], v[128:131]
s_waitcnt lgkmcnt(7)
v_fma_f32 v164, v164, v251, v180
v_mfma_f32_16x16x32_bf16 v[136:139], v[212:215], v[52:55], v[136:139]
s_waitcnt vmcnt(20)
s_waitcnt lgkmcnt(0)
v_fma_f32 v165, v165, v251, v181
v_mfma_f32_16x16x32_bf16 v[144:147], v[212:215], v[84:87], v[144:147]
s_barrier
ds_read_b64_tr_b16 v[208:209], v229 offset:32768
v_mfma_f32_16x16x32_bf16 v[152:155], v[212:215], v[116:119], v[152:155]
s_mov_b32 s61, 0x200
s_add_u32 m0, s35, 0x0
ds_read_b64_tr_b16 v[210:211], v229 offset:33792
v_fma_f32 v166, v166, v251, v182
v_mfma_f32_16x16x32_bf16 v[132:135], v[216:219], v[20:23], v[132:135]
v_fma_f32 v167, v167, v251, v183
v_cvt_pk_bf16_f32 v160, v160, v161
v_mfma_f32_16x16x32_bf16 v[140:143], v[216:219], v[52:55], v[140:143]
buffer_load_dwordx4 v235, s[52:55], s61 offen offset:0  lds
v_mfma_f32_16x16x32_bf16 v[148:151], v[216:219], v[84:87], v[148:151]
v_cvt_pk_bf16_f32 v161, v162, v163
v_cvt_pk_bf16_f32 v162, v164, v165
v_mfma_f32_16x16x32_bf16 v[156:159], v[216:219], v[116:119], v[156:159]
v_cvt_pk_bf16_f32 v163, v166, v167
ds_read_b64_tr_b16 v[212:213], v230 offset:32768
v_mfma_f32_16x16x32_bf16 v[128:131], v[192:195], v[24:27], v[128:131]
buffer_store_dwordx4 v[160:163], v222, s[56:59], s69 offen offset:0 sc1
v_mfma_f32_16x16x32_bf16 v[136:139], v[192:195], v[56:59], v[136:139]
ds_write_b128 v226, v[160:163] offset:16384
v_fma_f32 v168, v168, v252, v184
v_mfma_f32_16x16x32_bf16 v[144:147], v[192:195], v[88:91], v[144:147]
s_mov_b32 s61, 0x3a00
s_add_u32 m0, s35, 0x400
v_fma_f32 v169, v169, v252, v185
v_fma_f32 v170, v170, v252, v186
v_mfma_f32_16x16x32_bf16 v[152:155], v[192:195], v[120:123], v[152:155]
v_fma_f32 v171, v171, v252, v187
v_fma_f32 v172, v172, v252, v188
v_mfma_f32_16x16x32_bf16 v[132:135], v[196:199], v[24:27], v[132:135]
buffer_load_dwordx4 v235, s[52:55], s61 offen offset:0  lds
v_mfma_f32_16x16x32_bf16 v[140:143], v[196:199], v[56:59], v[140:143]
v_fma_f32 v173, v173, v252, v189
v_fma_f32 v174, v174, v252, v190
v_mfma_f32_16x16x32_bf16 v[148:151], v[196:199], v[88:91], v[148:151]
v_fma_f32 v175, v175, v252, v191
v_cvt_pk_bf16_f32 v168, v168, v169
v_mfma_f32_16x16x32_bf16 v[156:159], v[196:199], v[120:123], v[156:159]
v_cvt_pk_bf16_f32 v169, v170, v171
v_cvt_pk_bf16_f32 v170, v172, v173
v_mfma_f32_16x16x32_bf16 v[128:131], v[200:203], v[28:31], v[128:131]
v_cvt_pk_bf16_f32 v171, v174, v175
ds_read_b64_tr_b16 v[214:215], v230 offset:33792
v_mfma_f32_16x16x32_bf16 v[136:139], v[200:203], v[60:63], v[136:139]
buffer_store_dwordx4 v[168:171], v223, s[56:59], s69 offen offset:1024 sc1
v_mfma_f32_16x16x32_bf16 v[144:147], v[200:203], v[92:95], v[144:147]
ds_write_b128 v226, v[168:171] offset:17408
s_mov_b32 s61, 0x7200
s_add_u32 m0, s35, 0x800
ds_read_b64_tr_b16 v[216:217], v229 offset:34816
v_mfma_f32_16x16x32_bf16 v[152:155], v[200:203], v[124:127], v[152:155]
ds_read_b64_tr_b16 v[218:219], v229 offset:35840
ds_read_b64_tr_b16 v[192:193], v230 offset:34816
v_mfma_f32_16x16x32_bf16 v[132:135], v[204:207], v[28:31], v[132:135]
buffer_load_dwordx4 v235, s[52:55], s61 offen offset:0  lds
v_mfma_f32_16x16x32_bf16 v[140:143], v[204:207], v[60:63], v[140:143]
ds_read_b64_tr_b16 v[194:195], v230 offset:35840
ds_read_b64_tr_b16 v[196:197], v229 offset:36864
v_mfma_f32_16x16x32_bf16 v[148:151], v[204:207], v[92:95], v[148:151]
ds_read_b64_tr_b16 v[198:199], v229 offset:37888
ds_read_b64_tr_b16 v[200:201], v230 offset:36864
v_mfma_f32_16x16x32_bf16 v[156:159], v[204:207], v[124:127], v[156:159]
LST51:
ds_read_b64_tr_b16 v[202:203], v230 offset:37888
s_waitcnt lgkmcnt(10)
v_mfma_f32_16x16x32_bf16 v[160:163], v[208:211], v[0:3], 0
ds_read_b64_tr_b16 v[204:205], v229 offset:38912
v_mfma_f32_16x16x32_bf16 v[168:171], v[208:211], v[32:35], 0
s_mov_b32 s61, 0xaa00
s_add_u32 m0, s35, 0xc00
ds_read_b64_tr_b16 v[206:207], v229 offset:39936
v_mfma_f32_16x16x32_bf16 v[176:179], v[208:211], v[64:67], 0
s_add_u32 s69, s72, 0x8000
v_mul_f32 v144, v144, v253
v_mfma_f32_16x16x32_bf16 v[184:187], v[208:211], v[96:99], 0
buffer_load_dwordx4 v235, s[52:55], s61 offen offset:0  lds
s_waitcnt lgkmcnt(11)
v_mfma_f32_16x16x32_bf16 v[164:167], v[212:215], v[0:3], 0
ds_read_b64_tr_b16 v[208:209], v230 offset:38912
v_mul_f32 v145, v145, v253
v_mfma_f32_16x16x32_bf16 v[172:175], v[212:215], v[32:35], 0
ds_read_b64_tr_b16 v[210:211], v230 offset:39936
v_mul_f32 v146, v146, v253
v_mfma_f32_16x16x32_bf16 v[180:183], v[212:215], v[64:67], 0
v_mul_f32 v147, v147, v253
v_mfma_f32_16x16x32_bf16 v[188:191], v[212:215], v[96:99], 0
ds_write_b128 v225, v[144:147] offset:0
s_waitcnt lgkmcnt(8)
v_mfma_f32_16x16x32_bf16 v[160:163], v[216:219], v[4:7], v[160:163]
ds_read_b64_tr_b16 v[212:213], v229 offset:40960
v_mul_f32 v148, v148, v253
v_mfma_f32_16x16x32_bf16 v[168:171], v[216:219], v[36:39], v[168:171]
s_mov_b32 s61, 0xe200
s_add_u32 m0, s35, 0x1000
ds_read_b64_tr_b16 v[214:215], v229 offset:41984
v_mul_f32 v149, v149, v253
v_mfma_f32_16x16x32_bf16 v[176:179], v[216:219], v[68:71], v[176:179]
v_mul_f32 v150, v150, v253
v_mul_f32 v151, v151, v253
v_mfma_f32_16x16x32_bf16 v[184:187], v[216:219], v[100:103], v[184:187]
buffer_load_dwordx4 v235, s[52:55], s61 offen offset:0  lds
v_mfma_f32_16x16x32_bf16 v[164:167], v[192:195], v[4:7], v[164:167]
ds_write_b128 v225, v[148:151] offset:1024
v_mfma_f32_16x16x32_bf16 v[172:175], v[192:195], v[36:39], v[172:175]
v_mul_f32 v152, v152, v254
v_mul_f32 v153, v153, v254
v_mfma_f32_16x16x32_bf16 v[180:183], v[192:195], v[68:71], v[180:183]
v_mul_f32 v154, v154, v254
ds_read_b64_tr_b16 v[216:217], v230 offset:40960
v_mfma_f32_16x16x32_bf16 v[188:191], v[192:195], v[100:103], v[188:191]
v_mul_f32 v155, v155, v254
ds_read_b64_tr_b16 v[218:219], v230 offset:41984
s_waitcnt lgkmcnt(6)
v_mfma_f32_16x16x32_bf16 v[160:163], v[196:199], v[8:11], v[160:163]
s_mov_b32 s61, 0x11a00
s_add_u32 m0, s35, 0x1400
ds_write_b128 v225, v[152:155] offset:2048
v_mfma_f32_16x16x32_bf16 v[168:171], v[196:199], v[40:43], v[168:171]
v_mul_f32 v156, v156, v254
ds_read_b64_tr_b16 v[192:193], v229 offset:43008
v_mfma_f32_16x16x32_bf16 v[176:179], v[196:199], v[72:75], v[176:179]
buffer_load_dwordx4 v235, s[52:55], s61 offen offset:0  lds
v_mfma_f32_16x16x32_bf16 v[184:187], v[196:199], v[104:107], v[184:187]
v_mul_f32 v157, v157, v254
v_mul_f32 v158, v158, v254
v_mfma_f32_16x16x32_bf16 v[164:167], v[200:203], v[8:11], v[164:167]
v_mul_f32 v159, v159, v254
ds_read_b64_tr_b16 v[194:195], v229 offset:44032
v_mfma_f32_16x16x32_bf16 v[172:175], v[200:203], v[40:43], v[172:175]
ds_write_b128 v225, v[156:159] offset:3072
v_mfma_f32_16x16x32_bf16 v[180:183], v[200:203], v[72:75], v[180:183]
ds_read_b64_tr_b16 v[196:197], v230 offset:43008
ds_read_b64_tr_b16 v[198:199], v230 offset:44032
v_mfma_f32_16x16x32_bf16 v[188:191], v[200:203], v[104:107], v[188:191]
v_mfma_f32_16x16x32_bf16 v[160:163], v[204:207], v[12:15], v[160:163]
s_mov_b32 s61, 0x15200
s_add_u32 m0, s35, 0x1800
ds_read_b64_tr_b16 v[200:201], v229 offset:45056
v_mfma_f32_16x16x32_bf16 v[168:171], v[204:207], v[44:47], v[168:171]
ds_read_b64_tr_b16 v[202:203], v229 offset:46080
v_mfma_f32_16x16x32_bf16 v[176:179], v[204:207], v[76:79], v[176:179]
buffer_load_dwordx4 v235, s[52:55], s61 offen offset:0  lds
v_mfma_f32_16x16x32_bf16 v[184:187], v[204:207], v[108:111], v[184:187]
v_mfma_f32_16x16x32_bf16 v[164:167], v[208:211], v[12:15], v[164:167]
ds_read_b64_tr_b16 v[204:205], v230 offset:45056
v_mfma_f32_16x16x32_bf16 v[172:175], v[208:211], v[44:47], v[172:175]
ds_read_b64_tr_b16 v[206:207], v230 offset:46080
v_mfma_f32_16x16x32_bf16 v[180:183], v[208:211], v[76:79], v[180:183]
s_waitcnt lgkmcnt(6)
s_barrier
v_mfma_f32_16x16x32_bf16 v[188:191], v[208:211], v[108:111], v[188:191]
s_mov_b32 s61, 0x18a00
s_add_u32 m0, s35, 0x1c00
ds_read_b128 v[144:147], v226 offset:0
ds_read_b128 v[148:151], v226 offset:1024
v_mfma_f32_16x16x32_bf16 v[160:163], v[212:215], v[16:19], v[160:163]
ds_read_b128 v[152:155], v226 offset:2048
ds_read_b128 v[156:159], v226 offset:3072
v_mfma_f32_16x16x32_bf16 v[168:171], v[212:215], v[48:51], v[168:171]
buffer_load_dwordx4 v235, s[52:55], s61 offen offset:0  lds
v_mfma_f32_16x16x32_bf16 v[176:179], v[212:215], v[80:83], v[176:179]
ds_read_b64_tr_b16 v[208:209], v229 offset:47104
ds_read_b64_tr_b16 v[210:211], v229 offset:48128
v_mfma_f32_16x16x32_bf16 v[184:187], v[212:215], v[112:115], v[184:187]
ds_read_b128 a[124:127], v250 offset:16384
v_mfma_f32_16x16x32_bf16 v[164:167], v[216:219], v[16:19], v[164:167]
ds_read_b64_tr_b16 v[212:213], v230 offset:47104
v_mfma_f32_16x16x32_bf16 v[172:175], v[216:219], v[48:51], v[172:175]
ds_read_b64_tr_b16 v[214:215], v230 offset:48128
v_mfma_f32_16x16x32_bf16 v[180:183], v[216:219], v[80:83], v[180:183]
s_waitcnt lgkmcnt(8)
v_fma_f32 v128, v128, v251, v144
v_fma_f32 v129, v129, v251, v145
v_mfma_f32_16x16x32_bf16 v[188:191], v[216:219], v[112:115], v[188:191]
v_fma_f32 v130, v130, v251, v146
v_fma_f32 v131, v131, v251, v147
v_mfma_f32_16x16x32_bf16 v[160:163], v[192:195], v[20:23], v[160:163]
s_waitcnt lgkmcnt(7)
v_fma_f32 v132, v132, v251, v148
v_mfma_f32_16x16x32_bf16 v[168:171], v[192:195], v[52:55], v[168:171]
s_waitcnt vmcnt(20)
s_waitcnt lgkmcnt(0)
v_fma_f32 v133, v133, v251, v149
v_mfma_f32_16x16x32_bf16 v[176:179], v[192:195], v[84:87], v[176:179]
s_barrier
v_add_u32 v229, 0x10000, v229
v_mfma_f32_16x16x32_bf16 v[184:187], v[192:195], v[116:119], v[184:187]
s_mov_b32 s61, 0x240
s_add_u32 m0, s35, 0x8000
ds_read_b64_tr_b16 v[216:217], v229 offset:0
ds_read_b64_tr_b16 v[218:219], v229 offset:1024
v_mfma_f32_16x16x32_bf16 v[164:167], v[196:199], v[20:23], v[164:167]
v_fma_f32 v134, v134, v251, v150
v_fma_f32 v135, v135, v251, v151
v_mfma_f32_16x16x32_bf16 v[172:175], v[196:199], v[52:55], v[172:175]
buffer_load_dwordx4 v235, s[52:55], s61 offen offset:0  lds
v_mfma_f32_16x16x32_bf16 v[180:183], v[196:199], v[84:87], v[180:183]
v_cvt_pk_bf16_f32 v128, v128, v129
v_cvt_pk_bf16_f32 v129, v130, v131
v_mfma_f32_16x16x32_bf16 v[188:191], v[196:199], v[116:119], v[188:191]
v_cvt_pk_bf16_f32 v130, v132, v133
v_cvt_pk_bf16_f32 v131, v134, v135
v_mfma_f32_16x16x32_bf16 v[160:163], v[200:203], v[24:27], v[160:163]
buffer_store_dwordx4 v[128:131], v222, s[56:59], s69 offen offset:0 sc1
v_mfma_f32_16x16x32_bf16 v[168:171], v[200:203], v[56:59], v[168:171]
ds_write_b128 v226, v[128:131] offset:0
v_fma_f32 v136, v136, v252, v152
v_mfma_f32_16x16x32_bf16 v[176:179], v[200:203], v[88:91], v[176:179]
s_mov_b32 s61, 0x3a40
s_add_u32 m0, s35, 0x8400
v_fma_f32 v137, v137, v252, v153
v_fma_f32 v138, v138, v252, v154
v_mfma_f32_16x16x32_bf16 v[184:187], v[200:203], v[120:123], v[184:187]
v_fma_f32 v139, v139, v252, v155
v_fma_f32 v140, v140, v252, v156
v_mfma_f32_16x16x32_bf16 v[164:167], v[204:207], v[24:27], v[164:167]
buffer_load_dwordx4 v235, s[52:55], s61 offen offset:0  lds
v_mfma_f32_16x16x32_bf16 v[172:175], v[204:207], v[56:59], v[172:175]
v_fma_f32 v141, v141, v252, v157
v_fma_f32 v142, v142, v252, v158
v_mfma_f32_16x16x32_bf16 v[180:183], v[204:207], v[88:91], v[180:183]
v_fma_f32 v143, v143, v252, v159
v_cvt_pk_bf16_f32 v136, v136, v137
v_mfma_f32_16x16x32_bf16 v[188:191], v[204:207], v[120:123], v[188:191]
v_cvt_pk_bf16_f32 v137, v138, v139
v_cvt_pk_bf16_f32 v138, v140, v141
v_mfma_f32_16x16x32_bf16 v[160:163], v[208:211], v[28:31], v[160:163]
v_cvt_pk_bf16_f32 v139, v142, v143
v_add_u32 v230, 0x10000, v230
v_mfma_f32_16x16x32_bf16 v[168:171], v[208:211], v[60:63], v[168:171]
buffer_store_dwordx4 v[136:139], v223, s[56:59], s69 offen offset:1024 sc1
v_mfma_f32_16x16x32_bf16 v[176:179], v[208:211], v[92:95], v[176:179]
ds_write_b128 v226, v[136:139] offset:1024
ds_read_b64_tr_b16 v[192:193], v230 offset:0
s_mov_b32 s61, 0x7240
s_add_u32 m0, s35, 0x8800
v_mfma_f32_16x16x32_bf16 v[184:187], v[208:211], v[124:127], v[184:187]
ds_read_b64_tr_b16 v[194:195], v230 offset:1024
s_waitcnt vmcnt(20)
s_add_u32 s74, s96, 3
v_mfma_f32_16x16x32_bf16 v[164:167], v[212:215], v[28:31], v[164:167]
v_mov_b32 v240, s74
v_mov_b32 v241, s97
v_mfma_f32_16x16x32_bf16 v[172:175], v[212:215], v[60:63], v[172:175]
buffer_store_dwordx2 v[240:241], v233, s[40:43], 0 offen sc1
v_mfma_f32_16x16x32_bf16 v[180:183], v[212:215], v[92:95], v[180:183]
buffer_load_dwordx4 v235, s[52:55], s61 offen offset:0  lds
v_mfma_f32_16x16x32_bf16 v[188:191], v[212:215], v[124:127], v[188:191]
ds_read_b64_tr_b16 v[196:197], v229 offset:2048
ds_read_b64_tr_b16 v[198:199], v229 offset:3072
LST52:
s_waitcnt lgkmcnt(5)
v_mfma_f32_16x16x32_bf16 v[128:131], v[216:219], v[0:3], 0
ds_read_b64_tr_b16 v[200:201], v230 offset:2048
ds_read_b64_tr_b16 v[202:203], v230 offset:3072
v_mfma_f32_16x16x32_bf16 v[136:139], v[216:219], v[32:35], 0
s_mov_b32 s61, 0xaa40
s_add_u32 m0, s35, 0x8c00
ds_read_b64_tr_b16 v[204:205], v229 offset:4096
ds_read_b64_tr_b16 v[206:207], v229 offset:5120
v_mfma_f32_16x16x32_bf16 v[144:147], v[216:219], v[64:67], 0
ds_read_b64_tr_b16 v[208:209], v230 offset:4096
ds_read_b64_tr_b16 v[210:211], v230 offset:5120
s_add_u32 s69, s72, 0xa000
v_mfma_f32_16x16x32_bf16 v[152:155], v[216:219], v[96:99], 0
buffer_load_dwordx4 v235, s[52:55], s61 offen offset:0  lds
s_waitcnt lgkmcnt(8)
v_mfma_f32_16x16x32_bf16 v[132:135], v[192:195], v[0:3], 0
ds_read_b64_tr_b16 v[212:213], v229 offset:6144
ds_read_b64_tr_b16 v[214:215], v229 offset:7168
v_mfma_f32_16x16x32_bf16 v[140:143], v[192:195], v[32:35], 0
ds_read_b64_tr_b16 v[216:217], v230 offset:6144
ds_read_b64_tr_b16 v[218:219], v230 offset:7168
v_mfma_f32_16x16x32_bf16 v[148:151], v[192:195], v[64:67], 0
v_mul_f32 v176, v176, v253
v_mul_f32 v177, v177, v253
v_mfma_f32_16x16x32_bf16 v[156:159], v[192:195], v[96:99], 0
v_mul_f32 v178, v178, v253
v_mul_f32 v179, v179, v253
s_waitcnt lgkmcnt(10)
v_mfma_f32_16x16x32_bf16 v[128:131], v[196:199], v[4:7], v[128:131]
ds_read_b64_tr_b16 v[192:193], v229 offset:8192
v_mfma_f32_16x16x32_bf16 v[136:139], v[196:199], v[36:39], v[136:139]
s_mov_b32 s61, 0xe240
s_add_u32 m0, s35, 0x9000
ds_read_b64_tr_b16 v[194:195], v229 offset:9216
v_mfma_f32_16x16x32_bf16 v[144:147], v[196:199], v[68:71], v[144:147]
ds_write_b128 v225, v[176:179] offset:16384
v_mfma_f32_16x16x32_bf16 v[152:155], v[196:199], v[100:103], v[152:155]
buffer_load_dwordx4 v235, s[52:55], s61 offen offset:0  lds
s_waitcnt lgkmcnt(9)
v_mfma_f32_16x16x32_bf16 v[132:135], v[200:203], v[4:7], v[132:135]
v_mul_f32 v180, v180, v253
v_mul_f32 v181, v181, v253
v_mfma_f32_16x16x32_bf16 v[140:143], v[200:203], v[36:39], v[140:143]
v_mul_f32 v182, v182, v253
v_mul_f32 v183, v183, v253
v_mfma_f32_16x16x32_bf16 v[148:151], v[200:203], v[68:71], v[148:151]
ds_write_b128 v225, v[180:183] offset:17408
v_mfma_f32_16x16x32_bf16 v[156:159], v[200:203], v[100:103], v[156:159]
v_mul_f32 v184, v184, v254
v_mul_f32 v185, v185, v254
v_mfma_f32_16x16x32_bf16 v[128:131], v[204:207], v[8:11], v[128:131]
s_mov_b32 s61, 0x11a40
s_add_u32 m0, s35, 0x9400
v_mul_f32 v186, v186, v254
v_mul_f32 v187, v187, v254
v_mfma_f32_16x16x32_bf16 v[136:139], v[204:207], v[40:43], v[136:139]
ds_write_b128 v225, v[184:187] offset:18432
v_mfma_f32_16x16x32_bf16 v[144:147], v[204:207], v[72:75], v[144:147]
buffer_load_dwordx4 v235, s[52:55], s61 offen offset:0  lds
v_mfma_f32_16x16x32_bf16 v[152:155], v[204:207], v[104:107], v[152:155]
v_mul_f32 v188, v188, v254
v_mul_f32 v189, v189, v254
s_waitcnt lgkmcnt(3)
v_mfma_f32_16x16x32_bf16 v[132:135], v[208:211], v[8:11], v[132:135]
v_mul_f32 v190, v190, v254
v_mul_f32 v191, v191, v254
v_mfma_f32_16x16x32_bf16 v[140:143], v[208:211], v[40:43], v[140:143]
ds_write_b128 v225, v[188:191] offset:19456
v_mfma_f32_16x16x32_bf16 v[148:151], v[208:211], v[72:75], v[148:151]
ds_read_b64_tr_b16 v[196:197], v230 offset:8192
ds_read_b64_tr_b16 v[198:199], v230 offset:9216
v_mfma_f32_16x16x32_bf16 v[156:159], v[208:211], v[104:107], v[156:159]
ds_read_b64_tr_b16 v[200:201], v229 offset:10240
ds_read_b64_tr_b16 v[202:203], v229 offset:11264
v_mfma_f32_16x16x32_bf16 v[128:131], v[212:215], v[12:15], v[128:131]
s_mov_b32 s61, 0x15240
s_add_u32 m0, s35, 0x9800
ds_read_b64_tr_b16 v[204:205], v230 offset:10240
ds_read_b64_tr_b16 v[206:207], v230 offset:11264
v_mfma_f32_16x16x32_bf16 v[136:139], v[212:215], v[44:47], v[136:139]
ds_read_b64_tr_b16 v[208:209], v229 offset:12288
ds_read_b64_tr_b16 v[210:211], v229 offset:13312
v_mfma_f32_16x16x32_bf16 v[144:147], v[212:215], v[76:79], v[144:147]
buffer_load_dwordx4 v235, s[52:55], s61 offen offset:0  lds
v_mfma_f32_16x16x32_bf16 v[152:155], v[212:215], v[108:111], v[152:155]
v_mfma_f32_16x16x32_bf16 v[132:135], v[216:219], v[12:15], v[132:135]
ds_read_b64_tr_b16 v[212:213], v230 offset:12288
v_mfma_f32_16x16x32_bf16 v[140:143], v[216:219], v[44:47], v[140:143]
ds_read_b64_tr_b16 v[214:215], v230 offset:13312
v_mfma_f32_16x16x32_bf16 v[148:151], v[216:219], v[76:79], v[148:151]
s_waitcnt lgkmcnt(10)
s_barrier
v_mfma_f32_16x16x32_bf16 v[156:159], v[216:219], v[108:111], v[156:159]
s_mov_b32 s61, 0x18a40
s_add_u32 m0, s35, 0x9c00
ds_read_b128 v[176:179], v226 offset:16384
ds_read_b128 v[180:183], v226 offset:17408
v_mfma_f32_16x16x32_bf16 v[128:131], v[192:195], v[16:19], v[128:131]
ds_read_b128 v[184:187], v226 offset:18432
ds_read_b128 v[188:191], v226 offset:19456
v_mfma_f32_16x16x32_bf16 v[136:139], v[192:195], v[48:51], v[136:139]
buffer_load_dwordx4 v235, s[52:55], s61 offen offset:0  lds
v_mfma_f32_16x16x32_bf16 v[144:147], v[192:195], v[80:83], v[144:147]
ds_read_b64_tr_b16 v[216:217], v229 offset:14336
ds_read_b64_tr_b16 v[218:219], v229 offset:15360
v_mfma_f32_16x16x32_bf16 v[152:155], v[192:195], v[112:115], v[152:155]
ds_read_b128 a[128:131], v250 offset:0
s_waitcnt lgkmcnt(9)
v_mfma_f32_16x16x32_bf16 v[132:135], v[196:199], v[16:19], v[132:135]
ds_read_b64_tr_b16 v[192:193], v230 offset:14336
v_mfma_f32_16x16x32_bf16 v[140:143], v[196:199], v[48:51], v[140:143]
ds_read_b64_tr_b16 v[194:195], v230 offset:15360
v_mfma_f32_16x16x32_bf16 v[148:151], v[196:199], v[80:83], v[148:151]
s_waitcnt lgkmcnt(8)
v_fma_f32 v160, v160, v251, v176
v_fma_f32 v161, v161, v251, v177
v_mfma_f32_16x16x32_bf16 v[156:159], v[196:199], v[112:115], v[156:159]
v_fma_f32 v162, v162, v251, v178
v_fma_f32 v163, v163, v251, v179
v_mfma_f32_16x16x32_bf16 v[128:131], v[200:203], v[20:23], v[128:131]
s_waitcnt lgkmcnt(7)
v_fma_f32 v164, v164, v251, v180
v_mfma_f32_16x16x32_bf16 v[136:139], v[200:203], v[52:55], v[136:139]
s_waitcnt vmcnt(21)
s_waitcnt lgkmcnt(0)
v_fma_f32 v165, v165, v251, v181
v_mfma_f32_16x16x32_bf16 v[144:147], v[200:203], v[84:87], v[144:147]
s_barrier
ds_read_b64_tr_b16 v[196:197], v229 offset:32768
v_mfma_f32_16x16x32_bf16 v[152:155], v[200:203], v[116:119], v[152:155]
s_mov_b32 s61, 0x280
s_add_u32 m0, s35, 0x10000
ds_read_b64_tr_b16 v[198:199], v229 offset:33792
v_fma_f32 v166, v166, v251, v182
v_mfma_f32_16x16x32_bf16 v[132:135], v[204:207], v[20:23], v[132:135]
buffer_load_dwordx2 v[238:239], v234, s[40:43], 0 offen sc1
v_mfma_f32_16x16x32_bf16 v[140:143], v[204:207], v[52:55], v[140:143]
buffer_load_dwordx4 v235, s[52:55], s61 offen offset:0  lds
v_mfma_f32_16x16x32_bf16 v[148:151], v[204:207], v[84:87], v[148:151]
v_fma_f32 v167, v167, v251, v183
v_cvt_pk_bf16_f32 v160, v160, v161
v_mfma_f32_16x16x32_bf16 v[156:159], v[204:207], v[116:119], v[156:159]
v_cvt_pk_bf16_f32 v161, v162, v163
v_cvt_pk_bf16_f32 v162, v164, v165
v_mfma_f32_16x16x32_bf16 v[128:131], v[208:211], v[24:27], v[128:131]
v_cvt_pk_bf16_f32 v163, v166, v167
ds_read_b64_tr_b16 v[200:201], v230 offset:32768
v_mfma_f32_16x16x32_bf16 v[136:139], v[208:211], v[56:59], v[136:139]
buffer_store_dwordx4 v[160:163], v222, s[56:59], s69 offen offset:0 sc1
v_mfma_f32_16x16x32_bf16 v[144:147], v[208:211], v[88:91], v[144:147]
s_mov_b32 s61, 0x3a80
s_add_u32 m0, s35, 0x10400
ds_write_b128 v226, v[160:163] offset:16384
v_fma_f32 v168, v168, v252, v184
v_mfma_f32_16x16x32_bf16 v[152:155], v[208:211], v[120:123], v[152:155]
v_fma_f32 v169, v169, v252, v185
v_fma_f32 v170, v170, v252, v186
v_mfma_f32_16x16x32_bf16 v[132:135], v[212:215], v[24:27], v[132:135]
buffer_load_dwordx4 v235, s[52:55], s61 offen offset:0  lds
v_mfma_f32_16x16x32_bf16 v[140:143], v[212:215], v[56:59], v[140:143]
v_fma_f32 v171, v171, v252, v187
v_fma_f32 v172, v172, v252, v188
v_mfma_f32_16x16x32_bf16 v[148:151], v[212:215], v[88:91], v[148:151]
v_fma_f32 v173, v173, v252, v189
v_fma_f32 v174, v174, v252, v190
v_mfma_f32_16x16x32_bf16 v[156:159], v[212:215], v[120:123], v[156:159]
v_fma_f32 v175, v175, v252, v191
v_cvt_pk_bf16_f32 v168, v168, v169
v_mfma_f32_16x16x32_bf16 v[128:131], v[216:219], v[28:31], v[128:131]
v_cvt_pk_bf16_f32 v169, v170, v171
v_cvt_pk_bf16_f32 v170, v172, v173
v_mfma_f32_16x16x32_bf16 v[136:139], v[216:219], v[60:63], v[136:139]
v_cvt_pk_bf16_f32 v171, v174, v175
ds_read_b64_tr_b16 v[202:203], v230 offset:33792
v_mfma_f32_16x16x32_bf16 v[144:147], v[216:219], v[92:95], v[144:147]
buffer_store_dwordx4 v[168:171], v223, s[56:59], s69 offen offset:1024 sc1
ds_write_b128 v226, v[168:171] offset:17408
v_mfma_f32_16x16x32_bf16 v[152:155], v[216:219], v[124:127], v[152:155]
s_mov_b32 s61, 0x7280
s_add_u32 m0, s35, 0x10800
ds_read_b64_tr_b16 v[204:205], v229 offset:34816
ds_read_b64_tr_b16 v[206:207], v229 offset:35840
v_mfma_f32_16x16x32_bf16 v[132:135], v[192:195], v[28:31], v[132:135]
buffer_load_dwordx4 v235, s[52:55], s61 offen offset:0  lds
v_mfma_f32_16x16x32_bf16 v[140:143], v[192:195], v[60:63], v[140:143]
ds_read_b64_tr_b16 v[208:209], v230 offset:34816
ds_read_b64_tr_b16 v[210:211], v230 offset:35840
v_mfma_f32_16x16x32_bf16 v[148:151], v[192:195], v[92:95], v[148:151]
ds_read_b64_tr_b16 v[212:213], v229 offset:36864
ds_read_b64_tr_b16 v[214:215], v229 offset:37888
v_mfma_f32_16x16x32_bf16 v[156:159], v[192:195], v[124:127], v[156:159]
LST53:
ds_read_b64_tr_b16 v[216:217], v230 offset:36864
ds_read_b64_tr_b16 v[218:219], v230 offset:37888
s_waitcnt lgkmcnt(10)
v_mfma_f32_16x16x32_bf16 v[160:163], v[196:199], v[0:3], 0
ds_read_b64_tr_b16 v[192:193], v229 offset:38912
v_mfma_f32_16x16x32_bf16 v[168:171], v[196:199], v[32:35], 0
s_mov_b32 s61, 0xaa80
s_add_u32 m0, s35, 0x10c00
ds_read_b64_tr_b16 v[194:195], v229 offset:39936
v_mfma_f32_16x16x32_bf16 v[176:179], v[196:199], v[64:67], 0
s_add_u32 s69, s72, 0xc000
v_mul_f32 v144, v144, v253
v_mfma_f32_16x16x32_bf16 v[184:187], v[196:199], v[96:99], 0
buffer_load_dwordx4 v235, s[52:55], s61 offen offset:0  lds
s_waitcnt lgkmcnt(11)
v_mfma_f32_16x16x32_bf16 v[164:167], v[200:203], v[0:3], 0
ds_read_b64_tr_b16 v[196:197], v230 offset:38912
v_mul_f32 v145, v145, v253
v_mfma_f32_16x16x32_bf16 v[172:175], v[200:203], v[32:35], 0
ds_read_b64_tr_b16 v[198:199], v230 offset:39936
v_mul_f32 v146, v146, v253
v_mfma_f32_16x16x32_bf16 v[180:183], v[200:203], v[64:67], 0
v_mul_f32 v147, v147, v253
v_mfma_f32_16x16x32_bf16 v[188:191], v[200:203], v[96:99], 0
ds_write_b128 v225, v[144:147] offset:0
s_waitcnt lgkmcnt(9)
v_mfma_f32_16x16x32_bf16 v[160:163], v[204:207], v[4:7], v[160:163]
ds_read_b64_tr_b16 v[200:201], v229 offset:40960
v_mul_f32 v148, v148, v253
v_mfma_f32_16x16x32_bf16 v[168:171], v[204:207], v[36:39], v[168:171]
s_mov_b32 s61, 0xe280
s_add_u32 m0, s35, 0x11000
ds_read_b64_tr_b16 v[202:203], v229 offset:41984
v_mul_f32 v149, v149, v253
v_mfma_f32_16x16x32_bf16 v[176:179], v[204:207], v[68:71], v[176:179]
v_mul_f32 v150, v150, v253
v_mul_f32 v151, v151, v253
v_mfma_f32_16x16x32_bf16 v[184:187], v[204:207], v[100:103], v[184:187]
buffer_load_dwordx4 v235, s[52:55], s61 offen offset:0  lds
v_mfma_f32_16x16x32_bf16 v[164:167], v[208:211], v[4:7], v[164:167]
ds_write_b128 v225, v[148:151] offset:1024
v_mfma_f32_16x16x32_bf16 v[172:175], v[208:211], v[36:39], v[172:175]
v_mul_f32 v152, v152, v254
v_mul_f32 v153, v153, v254
v_mfma_f32_16x16x32_bf16 v[180:183], v[208:211], v[68:71], v[180:183]
v_mul_f32 v154, v154, v254
ds_read_b64_tr_b16 v[204:205], v230 offset:40960
v_mfma_f32_16x16x32_bf16 v[188:191], v[208:211], v[100:103], v[188:191]
v_mul_f32 v155, v155, v254
ds_read_b64_tr_b16 v[206:207], v230 offset:41984
s_waitcnt lgkmcnt(6)
v_mfma_f32_16x16x32_bf16 v[160:163], v[212:215], v[8:11], v[160:163]
s_mov_b32 s61, 0x11a80
s_add_u32 m0, s35, 0x11400
ds_write_b128 v225, v[152:155] offset:2048
v_mfma_f32_16x16x32_bf16 v[168:171], v[212:215], v[40:43], v[168:171]
v_mul_f32 v156, v156, v254
ds_read_b64_tr_b16 v[208:209], v229 offset:43008
v_mfma_f32_16x16x32_bf16 v[176:179], v[212:215], v[72:75], v[176:179]
buffer_load_dwordx4 v235, s[52:55], s61 offen offset:0  lds
v_mfma_f32_16x16x32_bf16 v[184:187], v[212:215], v[104:107], v[184:187]
v_mul_f32 v157, v157, v254
v_mul_f32 v158, v158, v254
v_mfma_f32_16x16x32_bf16 v[164:167], v[216:219], v[8:11], v[164:167]
v_mul_f32 v159, v159, v254
ds_read_b64_tr_b16 v[210:211], v229 offset:44032
v_mfma_f32_16x16x32_bf16 v[172:175], v[216:219], v[40:43], v[172:175]
ds_write_b128 v225, v[156:159] offset:3072
v_mfma_f32_16x16x32_bf16 v[180:183], v[216:219], v[72:75], v[180:183]
ds_read_b64_tr_b16 v[212:213], v230 offset:43008
ds_read_b64_tr_b16 v[214:215], v230 offset:44032
v_mfma_f32_16x16x32_bf16 v[188:191], v[216:219], v[104:107], v[188:191]
v_mfma_f32_16x16x32_bf16 v[160:163], v[192:195], v[12:15], v[160:163]
s_mov_b32 s61, 0x15280
s_add_u32 m0, s35, 0x11800
ds_read_b64_tr_b16 v[216:217], v229 offset:45056
v_mfma_f32_16x16x32_bf16 v[168:171], v[192:195], v[44:47], v[168:171]
ds_read_b64_tr_b16 v[218:219], v229 offset:46080
v_mfma_f32_16x16x32_bf16 v[176:179], v[192:195], v[76:79], v[176:179]
buffer_load_dwordx4 v235, s[52:55], s61 offen offset:0  lds
v_mfma_f32_16x16x32_bf16 v[184:187], v[192:195], v[108:111], v[184:187]
v_mfma_f32_16x16x32_bf16 v[164:167], v[196:199], v[12:15], v[164:167]
ds_read_b64_tr_b16 v[192:193], v230 offset:45056
v_mfma_f32_16x16x32_bf16 v[172:175], v[196:199], v[44:47], v[172:175]
ds_read_b64_tr_b16 v[194:195], v230 offset:46080
v_mfma_f32_16x16x32_bf16 v[180:183], v[196:199], v[76:79], v[180:183]
s_waitcnt lgkmcnt(6)
s_barrier
v_mfma_f32_16x16x32_bf16 v[188:191], v[196:199], v[108:111], v[188:191]
s_mov_b32 s61, 0x18a80
s_add_u32 m0, s35, 0x11c00
ds_read_b128 v[144:147], v226 offset:0
ds_read_b128 v[148:151], v226 offset:1024
v_mfma_f32_16x16x32_bf16 v[160:163], v[200:203], v[16:19], v[160:163]
ds_read_b128 v[152:155], v226 offset:2048
ds_read_b128 v[156:159], v226 offset:3072
v_mfma_f32_16x16x32_bf16 v[168:171], v[200:203], v[48:51], v[168:171]
buffer_load_dwordx4 v235, s[52:55], s61 offen offset:0  lds
v_mfma_f32_16x16x32_bf16 v[176:179], v[200:203], v[80:83], v[176:179]
ds_read_b64_tr_b16 v[196:197], v229 offset:47104
ds_read_b64_tr_b16 v[198:199], v229 offset:48128
v_mfma_f32_16x16x32_bf16 v[184:187], v[200:203], v[112:115], v[184:187]
ds_read_b128 a[132:135], v250 offset:16384
v_mfma_f32_16x16x32_bf16 v[164:167], v[204:207], v[16:19], v[164:167]
ds_read_b64_tr_b16 v[200:201], v230 offset:47104
v_mfma_f32_16x16x32_bf16 v[172:175], v[204:207], v[48:51], v[172:175]
ds_read_b64_tr_b16 v[202:203], v230 offset:48128
v_mfma_f32_16x16x32_bf16 v[180:183], v[204:207], v[80:83], v[180:183]
s_waitcnt lgkmcnt(8)
v_fma_f32 v128, v128, v251, v144
v_fma_f32 v129, v129, v251, v145
v_mfma_f32_16x16x32_bf16 v[188:191], v[204:207], v[112:115], v[188:191]
v_fma_f32 v130, v130, v251, v146
v_fma_f32 v131, v131, v251, v147
v_mfma_f32_16x16x32_bf16 v[160:163], v[208:211], v[20:23], v[160:163]
s_waitcnt lgkmcnt(7)
v_fma_f32 v132, v132, v251, v148
v_mfma_f32_16x16x32_bf16 v[168:171], v[208:211], v[52:55], v[168:171]
s_waitcnt vmcnt(22)
s_waitcnt lgkmcnt(0)
v_fma_f32 v133, v133, v251, v149
v_mfma_f32_16x16x32_bf16 v[176:179], v[208:211], v[84:87], v[176:179]
s_barrier
v_add_u32 v229, 0xffff0000, v229
v_mfma_f32_16x16x32_bf16 v[184:187], v[208:211], v[116:119], v[184:187]
s_mov_b32 s61, 0x2c0
s_add_u32 m0, s35, 0x18000
ds_read_b64_tr_b16 v[204:205], v229 offset:0
ds_read_b64_tr_b16 v[206:207], v229 offset:1024
v_mfma_f32_16x16x32_bf16 v[164:167], v[212:215], v[20:23], v[164:167]
v_fma_f32 v134, v134, v251, v150
v_fma_f32 v135, v135, v251, v151
v_mfma_f32_16x16x32_bf16 v[172:175], v[212:215], v[52:55], v[172:175]
buffer_load_dwordx4 v235, s[52:55], s61 offen offset:0  lds
v_mfma_f32_16x16x32_bf16 v[180:183], v[212:215], v[84:87], v[180:183]
v_cvt_pk_bf16_f32 v128, v128, v129
v_cvt_pk_bf16_f32 v129, v130, v131
v_mfma_f32_16x16x32_bf16 v[188:191], v[212:215], v[116:119], v[188:191]
v_cvt_pk_bf16_f32 v130, v132, v133
v_cvt_pk_bf16_f32 v131, v134, v135
v_mfma_f32_16x16x32_bf16 v[160:163], v[216:219], v[24:27], v[160:163]
buffer_store_dwordx4 v[128:131], v222, s[56:59], s69 offen offset:0 sc1
v_mfma_f32_16x16x32_bf16 v[168:171], v[216:219], v[56:59], v[168:171]
ds_write_b128 v226, v[128:131] offset:0
v_fma_f32 v136, v136, v252, v152
v_mfma_f32_16x16x32_bf16 v[176:179], v[216:219], v[88:91], v[176:179]
s_mov_b32 s61, 0x3ac0
s_add_u32 m0, s35, 0x18400
s_waitcnt vmcnt(12)
v_fma_f32 v137, v137, v252, v153
v_mfma_f32_16x16x32_bf16 v[184:187], v[216:219], v[120:123], v[184:187]
s_add_u32 s75, s96, 3
v_subrev_u32 v238, s75, v238
v_cmp_ge_u32 vcc, 11, v238
v_cmp_eq_u32 s[64:65], s97, v239
s_nop 1
s_and_b64 vcc, vcc, s[64:65]
s_cmp_eq_u64 vcc, -1
s_cbranch_scc0 LP0
LPB0:
v_mfma_f32_16x16x32_bf16 v[164:167], v[192:195], v[24:27], v[164:167]
buffer_load_dwordx4 v235, s[52:55], s61 offen offset:0  lds
v_mfma_f32_16x16x32_bf16 v[172:175], v[192:195], v[56:59], v[172:175]
v_fma_f32 v138, v138, v252, v154
v_fma_f32 v139, v139, v252, v155
v_mfma_f32_16x16x32_bf16 v[180:183], v[192:195], v[88:91], v[180:183]
v_fma_f32 v140, v140, v252, v156
v_fma_f32 v141, v141, v252, v157
v_mfma_f32_16x16x32_bf16 v[188:191], v[192:195], v[120:123], v[188:191]
v_fma_f32 v142, v142, v252, v158
v_fma_f32 v143, v143, v252, v159
v_mfma_f32_16x16x32_bf16 v[160:163], v[196:199], v[28:31], v[160:163]
v_cvt_pk_bf16_f32 v136, v136, v137
v_cvt_pk_bf16_f32 v137, v138, v139
v_mfma_f32_16x16x32_bf16 v[168:171], v[196:199], v[60:63], v[168:171]
v_cvt_pk_bf16_f32 v138, v140, v141
v_cvt_pk_bf16_f32 v139, v142, v143
v_mfma_f32_16x16x32_bf16 v[176:179], v[196:199], v[92:95], v[176:179]
buffer_store_dwordx4 v[136:139], v223, s[56:59], s69 offen offset:1024 sc1
ds_write_b128 v226, v[136:139] offset:1024
v_mfma_f32_16x16x32_bf16 v[184:187], v[196:199], v[124:127], v[184:187]
v_add_u32 v230, 0xffff0000, v230
ds_read_b64_tr_b16 v[208:209], v230 offset:0
ds_read_b64_tr_b16 v[210:211], v230 offset:1024
s_mov_b32 s61, 0x72c0
s_add_u32 m0, s35, 0x18800
v_mfma_f32_16x16x32_bf16 v[164:167], v[200:203], v[28:31], v[164:167]
buffer_load_dwordx4 v235, s[52:55], s61 offen offset:0  lds
v_mfma_f32_16x16x32_bf16 v[172:175], v[200:203], v[60:63], v[172:175]
ds_read_b64_tr_b16 v[212:213], v229 offset:2048
ds_read_b64_tr_b16 v[214:215], v229 offset:3072
v_mfma_f32_16x16x32_bf16 v[180:183], v[200:203], v[92:95], v[180:183]
ds_read_b64_tr_b16 v[216:217], v230 offset:2048
ds_read_b64_tr_b16 v[218:219], v230 offset:3072
v_mfma_f32_16x16x32_bf16 v[188:191], v[200:203], v[124:127], v[188:191]
LST54:
ds_read_b64_tr_b16 v[192:193], v229 offset:4096
ds_read_b64_tr_b16 v[194:195], v229 offset:5120
s_waitcnt lgkmcnt(9)
v_mfma_f32_16x16x32_bf16 v[128:131], v[204:207], v[0:3], 0
s_add_u32 s34, s73, 0x0
ds_read_b64_tr_b16 v[196:197], v230 offset:4096
ds_read_b64_tr_b16 v[198:199], v230 offset:5120
v_mfma_f32_16x16x32_bf16 v[136:139], v[204:207], v[32:35], 0
s_mov_b32 s61, 0xaac0
s_add_u32 m0, s35, 0x18c00
ds_read_b64_tr_b16 v[200:201], v229 offset:6144
ds_read_b64_tr_b16 v[202:203], v229 offset:7168
v_mfma_f32_16x16x32_bf16 v[144:147], v[204:207], v[64:67], 0
buffer_load_dwordx4 a[0:3], v224, s[56:59], s34 offen sc1
s_add_u32 s69, s72, 0xe000
v_mfma_f32_16x16x32_bf16 v[152:155], v[204:207], v[96:99], 0
buffer_load_dwordx4 v235, s[52:55], s61 offen offset:0  lds
s_waitcnt lgkmcnt(10)
v_mfma_f32_16x16x32_bf16 v[132:135], v[208:211], v[0:3], 0
buffer_load_dwordx4 a[4:7], v231, s[56:59], s34 offen sc1
v_mfma_f32_16x16x32_bf16 v[140:143], v[208:211], v[32:35], 0
ds_read_b64_tr_b16 v[204:205], v230 offset:6144
ds_read_b64_tr_b16 v[206:207], v230 offset:7168
v_mfma_f32_16x16x32_bf16 v[148:151], v[208:211], v[64:67], 0
s_add_u32 s34, s73, 0x2000
v_mul_f32 v176, v176, v253
v_mul_f32 v177, v177, v253
v_mfma_f32_16x16x32_bf16 v[156:159], v[208:211], v[96:99], 0
v_mul_f32 v178, v178, v253
v_mul_f32 v179, v179, v253
s_waitcnt lgkmcnt(10)
v_mfma_f32_16x16x32_bf16 v[128:131], v[212:215], v[4:7], v[128:131]
buffer_load_dwordx4 a[8:11], v224, s[56:59], s34 offen sc1
v_mfma_f32_16x16x32_bf16 v[136:139], v[212:215], v[36:39], v[136:139]
s_mov_b32 s61, 0xe2c0
s_add_u32 m0, s35, 0x19000
ds_read_b64_tr_b16 v[208:209], v229 offset:8192
ds_read_b64_tr_b16 v[210:211], v229 offset:9216
v_mfma_f32_16x16x32_bf16 v[144:147], v[212:215], v[68:71], v[144:147]
buffer_load_dwordx4 a[12:15], v231, s[56:59], s34 offen sc1
v_mfma_f32_16x16x32_bf16 v[152:155], v[212:215], v[100:103], v[152:155]
buffer_load_dwordx4 v235, s[52:55], s61 offen offset:0  lds
s_waitcnt lgkmcnt(4)
v_mfma_f32_16x16x32_bf16 v[132:135], v[216:219], v[4:7], v[132:135]
s_add_u32 s34, s73, 0x4000
ds_write_b128 v225, v[176:179] offset:16384
v_mfma_f32_16x16x32_bf16 v[140:143], v[216:219], v[36:39], v[140:143]
v_mul_f32 v180, v180, v253
v_mul_f32 v181, v181, v253
v_mfma_f32_16x16x32_bf16 v[148:151], v[216:219], v[68:71], v[148:151]
buffer_load_dwordx4 a[16:19], v224, s[56:59], s34 offen sc1
v_mfma_f32_16x16x32_bf16 v[156:159], v[216:219], v[100:103], v[156:159]
v_mul_f32 v182, v182, v253
v_mul_f32 v183, v183, v253
v_mfma_f32_16x16x32_bf16 v[128:131], v[192:195], v[8:11], v[128:131]
s_mov_b32 s61, 0x11ac0
s_add_u32 m0, s35, 0x19400
buffer_load_dwordx4 a[20:23], v231, s[56:59], s34 offen sc1
v_mfma_f32_16x16x32_bf16 v[136:139], v[192:195], v[40:43], v[136:139]
ds_write_b128 v225, v[180:183] offset:17408
v_mfma_f32_16x16x32_bf16 v[144:147], v[192:195], v[72:75], v[144:147]
buffer_load_dwordx4 v235, s[52:55], s61 offen offset:0  lds
v_mfma_f32_16x16x32_bf16 v[152:155], v[192:195], v[104:107], v[152:155]
v_mul_f32 v184, v184, v254
v_mul_f32 v185, v185, v254
v_mfma_f32_16x16x32_bf16 v[132:135], v[196:199], v[8:11], v[132:135]
v_mul_f32 v186, v186, v254
v_mul_f32 v187, v187, v254
v_mfma_f32_16x16x32_bf16 v[140:143], v[196:199], v[40:43], v[140:143]
ds_write_b128 v225, v[184:187] offset:18432
v_mfma_f32_16x16x32_bf16 v[148:151], v[196:199], v[72:75], v[148:151]
v_mul_f32 v188, v188, v254
v_mul_f32 v189, v189, v254
v_mfma_f32_16x16x32_bf16 v[156:159], v[196:199], v[104:107], v[156:159]
v_mul_f32 v190, v190, v254
v_mul_f32 v191, v191, v254
v_mfma_f32_16x16x32_bf16 v[128:131], v[200:203], v[12:15], v[128:131]
ds_write_b128 v225, v[188:191] offset:19456
s_mov_b32 s61, 0x152c0
s_add_u32 m0, s35, 0x19800
v_mfma_f32_16x16x32_bf16 v[136:139], v[200:203], v[44:47], v[136:139]
ds_read_b64_tr_b16 v[212:213], v230 offset:8192
ds_read_b64_tr_b16 v[214:215], v230 offset:9216
v_mfma_f32_16x16x32_bf16 v[144:147], v[200:203], v[76:79], v[144:147]
buffer_load_dwordx4 v235, s[52:55], s61 offen offset:0  lds
v_mfma_f32_16x16x32_bf16 v[152:155], v[200:203], v[108:111], v[152:155]
ds_read_b64_tr_b16 v[216:217], v229 offset:10240
ds_read_b64_tr_b16 v[218:219], v229 offset:11264
s_waitcnt lgkmcnt(6)
v_mfma_f32_16x16x32_bf16 v[132:135], v[204:207], v[12:15], v[132:135]
ds_read_b64_tr_b16 v[192:193], v230 offset:10240
ds_read_b64_tr_b16 v[194:195], v230 offset:11264
v_mfma_f32_16x16x32_bf16 v[140:143], v[204:207], v[44:47], v[140:143]
ds_read_b64_tr_b16 v[196:197], v229 offset:12288
ds_read_b64_tr_b16 v[198:199], v229 offset:13312
v_mfma_f32_16x16x32_bf16 v[148:151], v[204:207], v[76:79], v[148:151]
s_waitcnt lgkmcnt(8)
s_barrier
v_mfma_f32_16x16x32_bf16 v[156:159], v[204:207], v[108:111], v[156:159]
s_mov_b32 s61, 0x18ac0
s_add_u32 m0, s35, 0x19c00
ds_read_b128 v[176:179], v226 offset:16384
ds_read_b128 v[180:183], v226 offset:17408
v_mfma_f32_16x16x32_bf16 v[128:131], v[208:211], v[16:19], v[128:131]
ds_read_b128 v[184:187], v226 offset:18432
ds_read_b128 v[188:191], v226 offset:19456
v_mfma_f32_16x16x32_bf16 v[136:139], v[208:211], v[48:51], v[136:139]
buffer_load_dwordx4 v235, s[52:55], s61 offen offset:0  lds
v_mfma_f32_16x16x32_bf16 v[144:147], v[208:211], v[80:83], v[144:147]
ds_read_b64_tr_b16 v[200:201], v230 offset:12288
ds_read_b64_tr_b16 v[202:203], v230 offset:13312
v_mfma_f32_16x16x32_bf16 v[152:155], v[208:211], v[112:115], v[152:155]
ds_read_b64_tr_b16 v[204:205], v229 offset:14336
ds_read_b64_tr_b16 v[206:207], v229 offset:15360
s_waitcnt lgkmcnt(14)
v_mfma_f32_16x16x32_bf16 v[132:135], v[212:215], v[16:19], v[132:135]
ds_read_b64_tr_b16 v[208:209], v230 offset:14336
ds_read_b128 a[136:139], v250 offset:0
v_mfma_f32_16x16x32_bf16 v[140:143], v[212:215], v[48:51], v[140:143]
ds_read_b64_tr_b16 v[210:211], v230 offset:15360
v_mfma_f32_16x16x32_bf16 v[148:151], v[212:215], v[80:83], v[148:151]
s_waitcnt lgkmcnt(10)
v_fma_f32 v160, v160, v251, v176
v_fma_f32 v161, v161, v251, v177
v_mfma_f32_16x16x32_bf16 v[156:159], v[212:215], v[112:115], v[156:159]
v_fma_f32 v162, v162, v251, v178
v_fma_f32 v163, v163, v251, v179
v_mfma_f32_16x16x32_bf16 v[128:131], v[216:219], v[20:23], v[128:131]
s_waitcnt lgkmcnt(9)
v_fma_f32 v164, v164, v251, v180
v_mfma_f32_16x16x32_bf16 v[136:139], v[216:219], v[52:55], v[136:139]
s_waitcnt lgkmcnt(0)
v_fma_f32 v165, v165, v251, v181
v_mfma_f32_16x16x32_bf16 v[144:147], v[216:219], v[84:87], v[144:147]
s_barrier
ds_read_b64_tr_b16 v[212:213], v229 offset:32768
v_mfma_f32_16x16x32_bf16 v[152:155], v[216:219], v[116:119], v[152:155]
s_mov_b32 s61, 0x300
s_add_u32 m0, s35, 0x0
ds_read_b64_tr_b16 v[214:215], v229 offset:33792
v_fma_f32 v166, v166, v251, v182
v_mfma_f32_16x16x32_bf16 v[132:135], v[192:195], v[20:23], v[132:135]
v_fma_f32 v167, v167, v251, v183
v_cvt_pk_bf16_f32 v160, v160, v161
v_mfma_f32_16x16x32_bf16 v[140:143], v[192:195], v[52:55], v[140:143]
buffer_load_dwordx4 v235, s[52:55], s61 offen offset:0  lds
v_mfma_f32_16x16x32_bf16 v[148:151], v[192:195], v[84:87], v[148:151]
v_cvt_pk_bf16_f32 v161, v162, v163
v_cvt_pk_bf16_f32 v162, v164, v165
v_mfma_f32_16x16x32_bf16 v[156:159], v[192:195], v[116:119], v[156:159]
v_cvt_pk_bf16_f32 v163, v166, v167
ds_read_b64_tr_b16 v[216:217], v230 offset:32768
v_mfma_f32_16x16x32_bf16 v[128:131], v[196:199], v[24:27], v[128:131]
buffer_store_dwordx4 v[160:163], v222, s[56:59], s69 offen offset:0 sc1
v_mfma_f32_16x16x32_bf16 v[136:139], v[196:199], v[56:59], v[136:139]
ds_write_b128 v226, v[160:163] offset:16384
v_fma_f32 v168, v168, v252, v184
v_mfma_f32_16x16x32_bf16 v[144:147], v[196:199], v[88:91], v[144:147]
s_mov_b32 s61, 0x3b00
s_add_u32 m0, s35, 0x400
v_fma_f32 v169, v169, v252, v185
v_fma_f32 v170, v170, v252, v186
v_mfma_f32_16x16x32_bf16 v[152:155], v[196:199], v[120:123], v[152:155]
v_fma_f32 v171, v171, v252, v187
v_fma_f32 v172, v172, v252, v188
v_mfma_f32_16x16x32_bf16 v[132:135], v[200:203], v[24:27], v[132:135]
buffer_load_dwordx4 v235, s[52:55], s61 offen offset:0  lds
v_mfma_f32_16x16x32_bf16 v[140:143], v[200:203], v[56:59], v[140:143]
v_fma_f32 v173, v173, v252, v189
v_fma_f32 v174, v174, v252, v190
v_mfma_f32_16x16x32_bf16 v[148:151], v[200:203], v[88:91], v[148:151]
v_fma_f32 v175, v175, v252, v191
v_cvt_pk_bf16_f32 v168, v168, v169
v_mfma_f32_16x16x32_bf16 v[156:159], v[200:203], v[120:123], v[156:159]
v_cvt_pk_bf16_f32 v169, v170, v171
v_cvt_pk_bf16_f32 v170, v172, v173
v_mfma_f32_16x16x32_bf16 v[128:131], v[204:207], v[28:31], v[128:131]
v_cvt_pk_bf16_f32 v171, v174, v175
ds_read_b64_tr_b16 v[218:219], v230 offset:33792
v_mfma_f32_16x16x32_bf16 v[136:139], v[204:207], v[60:63], v[136:139]
buffer_store_dwordx4 v[168:171], v223, s[56:59], s69 offen offset:1024 sc1
v_mfma_f32_16x16x32_bf16 v[144:147], v[204:207], v[92:95], v[144:147]
ds_write_b128 v226, v[168:171] offset:17408
s_mov_b32 s61, 0x7300
s_add_u32 m0, s35, 0x800
s_waitcnt vmcnt(26)
s_add_u32 s74, s96, 6
v_mfma_f32_16x16x32_bf16 v[152:155], v[204:207], v[124:127], v[152:155]
v_mov_b32 v240, s74
v_mov_b32 v241, s97
v_mfma_f32_16x16x32_bf16 v[132:135], v[208:211], v[28:31], v[132:135]
buffer_store_dwordx2 v[240:241], v233, s[40:43], 0 offen sc1
v_mfma_f32_16x16x32_bf16 v[140:143], v[208:211], v[60:63], v[140:143]
buffer_load_dwordx4 v235, s[52:55], s61 offen offset:0  lds
v_mfma_f32_16x16x32_bf16 v[148:151], v[208:211], v[92:95], v[148:151]
ds_read_b64_tr_b16 v[192:193], v229 offset:34816
ds_read_b64_tr_b16 v[194:195], v229 offset:35840
v_mfma_f32_16x16x32_bf16 v[156:159], v[208:211], v[124:127], v[156:159]
LST55:
ds_read_b64_tr_b16 v[196:197], v230 offset:34816
ds_read_b64_tr_b16 v[198:199], v230 offset:35840
s_waitcnt lgkmcnt(6)
v_mfma_f32_16x16x32_bf16 v[160:163], v[212:215], v[0:3], 0
ds_read_b64_tr_b16 v[200:201], v229 offset:36864
ds_read_b64_tr_b16 v[202:203], v229 offset:37888
v_mfma_f32_16x16x32_bf16 v[168:171], v[212:215], v[32:35], 0
s_mov_b32 s61, 0xab00
s_add_u32 m0, s35, 0xc00
ds_read_b64_tr_b16 v[204:205], v230 offset:36864
ds_read_b64_tr_b16 v[206:207], v230 offset:37888
v_mfma_f32_16x16x32_bf16 v[176:179], v[212:215], v[64:67], 0
ds_read_b64_tr_b16 v[208:209], v229 offset:38912
ds_read_b64_tr_b16 v[210:211], v229 offset:39936
s_add_u32 s69, s72, 0x10000
v_mfma_f32_16x16x32_bf16 v[184:187], v[212:215], v[96:99], 0
buffer_load_dwordx4 v235, s[52:55], s61 offen offset:0  lds
s_waitcnt lgkmcnt(11)
v_mfma_f32_16x16x32_bf16 v[164:167], v[216:219], v[0:3], 0
ds_read_b64_tr_b16 v[212:213], v230 offset:38912
v_mul_f32 v144, v144, v253
v_mfma_f32_16x16x32_bf16 v[172:175], v[216:219], v[32:35], 0
ds_read_b64_tr_b16 v[214:215], v230 offset:39936
v_mul_f32 v145, v145, v253
v_mfma_f32_16x16x32_bf16 v[180:183], v[216:219], v[64:67], 0
v_mul_f32 v146, v146, v253
v_mul_f32 v147, v147, v253
v_mfma_f32_16x16x32_bf16 v[188:191], v[216:219], v[96:99], 0
ds_write_b128 v225, v[144:147] offset:0
s_waitcnt lgkmcnt(11)
v_mfma_f32_16x16x32_bf16 v[160:163], v[192:195], v[4:7], v[160:163]
ds_read_b64_tr_b16 v[216:217], v229 offset:40960
v_mul_f32 v148, v148, v253
v_mfma_f32_16x16x32_bf16 v[168:171], v[192:195], v[36:39], v[168:171]
s_mov_b32 s61, 0xe300
s_add_u32 m0, s35, 0x1000
ds_read_b64_tr_b16 v[218:219], v229 offset:41984
v_mul_f32 v149, v149, v253
v_mfma_f32_16x16x32_bf16 v[176:179], v[192:195], v[68:71], v[176:179]
v_mul_f32 v150, v150, v253
v_mul_f32 v151, v151, v253
v_mfma_f32_16x16x32_bf16 v[184:187], v[192:195], v[100:103], v[184:187]
buffer_load_dwordx4 v235, s[52:55], s61 offen offset:0  lds
s_waitcnt lgkmcnt(7)
v_mfma_f32_16x16x32_bf16 v[164:167], v[196:199], v[4:7], v[164:167]
ds_write_b128 v225, v[148:151] offset:1024
v_mfma_f32_16x16x32_bf16 v[172:175], v[196:199], v[36:39], v[172:175]
v_mul_f32 v152, v152, v254
v_mul_f32 v153, v153, v254
v_mfma_f32_16x16x32_bf16 v[180:183], v[196:199], v[68:71], v[180:183]
v_mul_f32 v154, v154, v254
ds_read_b64_tr_b16 v[192:193], v230 offset:40960
v_mfma_f32_16x16x32_bf16 v[188:191], v[196:199], v[100:103], v[188:191]
v_mul_f32 v155, v155, v254
ds_read_b64_tr_b16 v[194:195], v230 offset:41984
v_mfma_f32_16x16x32_bf16 v[160:163], v[200:203], v[8:11], v[160:163]
s_mov_b32 s61, 0x11b00
s_add_u32 m0, s35, 0x1400
ds_write_b128 v225, v[152:155] offset:2048
v_mfma_f32_16x16x32_bf16 v[168:171], v[200:203], v[40:43], v[168:171]
v_mul_f32 v156, v156, v254
ds_read_b64_tr_b16 v[196:197], v229 offset:43008
v_mfma_f32_16x16x32_bf16 v[176:179], v[200:203], v[72:75], v[176:179]
buffer_load_dwordx4 v235, s[52:55], s61 offen offset:0  lds
v_mfma_f32_16x16x32_bf16 v[184:187], v[200:203], v[104:107], v[184:187]
v_mul_f32 v157, v157, v254
v_mul_f32 v158, v158, v254
v_mfma_f32_16x16x32_bf16 v[164:167], v[204:207], v[8:11], v[164:167]
v_mul_f32 v159, v159, v254
ds_read_b64_tr_b16 v[198:199], v229 offset:44032
v_mfma_f32_16x16x32_bf16 v[172:175], v[204:207], v[40:43], v[172:175]
ds_write_b128 v225, v[156:159] offset:3072
v_mfma_f32_16x16x32_bf16 v[180:183], v[204:207], v[72:75], v[180:183]
ds_read_b64_tr_b16 v[200:201], v230 offset:43008
ds_read_b64_tr_b16 v[202:203], v230 offset:44032
v_mfma_f32_16x16x32_bf16 v[188:191], v[204:207], v[104:107], v[188:191]
s_waitcnt lgkmcnt(8)
v_mfma_f32_16x16x32_bf16 v[160:163], v[208:211], v[12:15], v[160:163]
s_mov_b32 s61, 0x15300
s_add_u32 m0, s35, 0x1800
ds_read_b64_tr_b16 v[204:205], v229 offset:45056
v_mfma_f32_16x16x32_bf16 v[168:171], v[208:211], v[44:47], v[168:171]
ds_read_b64_tr_b16 v[206:207], v229 offset:46080
v_mfma_f32_16x16x32_bf16 v[176:179], v[208:211], v[76:79], v[176:179]
buffer_load_dwordx4 v235, s[52:55], s61 offen offset:0  lds
v_mfma_f32_16x16x32_bf16 v[184:187], v[208:211], v[108:111], v[184:187]
v_mfma_f32_16x16x32_bf16 v[164:167], v[212:215], v[12:15], v[164:167]
ds_read_b64_tr_b16 v[208:209], v230 offset:45056
v_mfma_f32_16x16x32_bf16 v[172:175], v[212:215], v[44:47], v[172:175]
ds_read_b64_tr_b16 v[210:211], v230 offset:46080
v_mfma_f32_16x16x32_bf16 v[180:183], v[212:215], v[76:79], v[180:183]
s_waitcnt lgkmcnt(6)
s_barrier
v_mfma_f32_16x16x32_bf16 v[188:191], v[212:215], v[108:111], v[188:191]
s_mov_b32 s61, 0x18b00
s_add_u32 m0, s35, 0x1c00
ds_read_b128 v[144:147], v226 offset:0
ds_read_b128 v[148:151], v226 offset:1024
v_mfma_f32_16x16x32_bf16 v[160:163], v[216:219], v[16:19], v[160:163]
ds_read_b128 v[152:155], v226 offset:2048
ds_read_b128 v[156:159], v226 offset:3072
v_mfma_f32_16x16x32_bf16 v[168:171], v[216:219], v[48:51], v[168:171]
buffer_load_dwordx4 v235, s[52:55], s61 offen offset:0  lds
v_mfma_f32_16x16x32_bf16 v[176:179], v[216:219], v[80:83], v[176:179]
ds_read_b64_tr_b16 v[212:213], v229 offset:47104
ds_read_b64_tr_b16 v[214:215], v229 offset:48128
v_mfma_f32_16x16x32_bf16 v[184:187], v[216:219], v[112:115], v[184:187]
ds_read_b128 a[140:143], v250 offset:16384
v_mfma_f32_16x16x32_bf16 v[164:167], v[192:195], v[16:19], v[164:167]
ds_read_b64_tr_b16 v[216:217], v230 offset:47104
v_mfma_f32_16x16x32_bf16 v[172:175], v[192:195], v[48:51], v[172:175]
ds_read_b64_tr_b16 v[218:219], v230 offset:48128
v_mfma_f32_16x16x32_bf16 v[180:183], v[192:195], v[80:83], v[180:183]
s_waitcnt lgkmcnt(8)
v_fma_f32 v128, v128, v251, v144
v_fma_f32 v129, v129, v251, v145
v_mfma_f32_16x16x32_bf16 v[188:191], v[192:195], v[112:115], v[188:191]
v_fma_f32 v130, v130, v251, v146
v_fma_f32 v131, v131, v251, v147
v_mfma_f32_16x16x32_bf16 v[160:163], v[196:199], v[20:23], v[160:163]
s_waitcnt lgkmcnt(7)
v_fma_f32 v132, v132, v251, v148
v_mfma_f32_16x16x32_bf16 v[168:171], v[196:199], v[52:55], v[168:171]
s_waitcnt vmcnt(27)
s_waitcnt lgkmcnt(0)
v_fma_f32 v133, v133, v251, v149
v_mfma_f32_16x16x32_bf16 v[176:179], v[196:199], v[84:87], v[176:179]
s_barrier
v_add_u32 v229, 0x10000, v229
v_mfma_f32_16x16x32_bf16 v[184:187], v[196:199], v[116:119], v[184:187]
s_mov_b32 s61, 0x340
s_add_u32 m0, s35, 0x8000
ds_read_b64_tr_b16 v[192:193], v229 offset:0
ds_read_b64_tr_b16 v[194:195], v229 offset:1024
v_mfma_f32_16x16x32_bf16 v[164:167], v[200:203], v[20:23], v[164:167]
buffer_load_dwordx2 v[238:239], v234, s[40:43], 0 offen sc1
v_mfma_f32_16x16x32_bf16 v[172:175], v[200:203], v[52:55], v[172:175]
buffer_load_dwordx4 v235, s[52:55], s61 offen offset:0  lds
v_mfma_f32_16x16x32_bf16 v[180:183], v[200:203], v[84:87], v[180:183]
v_fma_f32 v134, v134, v251, v150
v_fma_f32 v135, v135, v251, v151
v_mfma_f32_16x16x32_bf16 v[188:191], v[200:203], v[116:119], v[188:191]
v_cvt_pk_bf16_f32 v128, v128, v129
v_cvt_pk_bf16_f32 v129, v130, v131
v_mfma_f32_16x16x32_bf16 v[160:163], v[204:207], v[24:27], v[160:163]
v_cvt_pk_bf16_f32 v130, v132, v133
v_cvt_pk_bf16_f32 v131, v134, v135
v_mfma_f32_16x16x32_bf16 v[168:171], v[204:207], v[56:59], v[168:171]
buffer_store_dwordx4 v[128:131], v222, s[56:59], s69 offen offset:0 sc1
v_mfma_f32_16x16x32_bf16 v[176:179], v[204:207], v[88:91], v[176:179]
s_mov_b32 s61, 0x3b40
s_add_u32 m0, s35, 0x8400
ds_write_b128 v226, v[128:131] offset:0
v_fma_f32 v136, v136, v252, v152
v_mfma_f32_16x16x32_bf16 v[184:187], v[204:207], v[120:123], v[184:187]
v_fma_f32 v137, v137, v252, v153
v_fma_f32 v138, v138, v252, v154
v_mfma_f32_16x16x32_bf16 v[164:167], v[208:211], v[24:27], v[164:167]
buffer_load_dwordx4 v235, s[52:55], s61 offen offset:0  lds
v_mfma_f32_16x16x32_bf16 v[172:175], v[208:211], v[56:59], v[172:175]
v_fma_f32 v139, v139, v252, v155
v_fma_f32 v140, v140, v252, v156
v_mfma_f32_16x16x32_bf16 v[180:183], v[208:211], v[88:91], v[180:183]
v_fma_f32 v141, v141, v252, v157
v_fma_f32 v142, v142, v252, v158
v_mfma_f32_16x16x32_bf16 v[188:191], v[208:211], v[120:123], v[188:191]
v_fma_f32 v143, v143, v252, v159
v_cvt_pk_bf16_f32 v136, v136, v137
v_mfma_f32_16x16x32_bf16 v[160:163], v[212:215], v[28:31], v[160:163]
v_cvt_pk_bf16_f32 v137, v138, v139
v_cvt_pk_bf16_f32 v138, v140, v141
v_mfma_f32_16x16x32_bf16 v[168:171], v[212:215], v[60:63], v[168:171]
v_cvt_pk_bf16_f32 v139, v142, v143
v_add_u32 v230, 0x10000, v230
v_mfma_f32_16x16x32_bf16 v[176:179], v[212:215], v[92:95], v[176:179]
buffer_store_dwordx4 v[136:139], v223, s[56:59], s69 offen offset:1024 sc1
ds_write_b128 v226, v[136:139] offset:1024
v_mfma_f32_16x16x32_bf16 v[184:187], v[212:215], v[124:127], v[184:187]
ds_read_b64_tr_b16 v[196:197], v230 offset:0
ds_read_b64_tr_b16 v[198:199], v230 offset:1024
s_mov_b32 s61, 0x7340
s_add_u32 m0, s35, 0x8800
v_mfma_f32_16x16x32_bf16 v[164:167], v[216:219], v[28:31], v[164:167]
buffer_load_dwordx4 v235, s[52:55], s61 offen offset:0  lds
v_mfma_f32_16x16x32_bf16 v[172:175], v[216:219], v[60:63], v[172:175]
ds_read_b64_tr_b16 v[200:201], v229 offset:2048
ds_read_b64_tr_b16 v[202:203], v229 offset:3072
v_mfma_f32_16x16x32_bf16 v[180:183], v[216:219], v[92:95], v[180:183]
ds_read_b64_tr_b16 v[204:205], v230 offset:2048
ds_read_b64_tr_b16 v[206:207], v230 offset:3072
v_mfma_f32_16x16x32_bf16 v[188:191], v[216:219], v[124:127], v[188:191]
LST56:
ds_read_b64_tr_b16 v[208:209], v229 offset:4096
ds_read_b64_tr_b16 v[210:211], v229 offset:5120
s_waitcnt lgkmcnt(9)
v_mfma_f32_16x16x32_bf16 v[128:131], v[192:195], v[0:3], 0
ds_read_b64_tr_b16 v[212:213], v230 offset:4096
ds_read_b64_tr_b16 v[214:215], v230 offset:5120
v_mfma_f32_16x16x32_bf16 v[136:139], v[192:195], v[32:35], 0
s_mov_b32 s61, 0xab40
s_add_u32 m0, s35, 0x8c00
ds_read_b64_tr_b16 v[216:217], v229 offset:6144
ds_read_b64_tr_b16 v[218:219], v229 offset:7168
v_mfma_f32_16x16x32_bf16 v[144:147], v[192:195], v[64:67], 0
s_add_u32 s69, s72, 0x12000
v_mul_f32 v176, v176, v253
v_mfma_f32_16x16x32_bf16 v[152:155], v[192:195], v[96:99], 0
buffer_load_dwordx4 v235, s[52:55], s61 offen offset:0  lds
s_waitcnt lgkmcnt(10)
v_mfma_f32_16x16x32_bf16 v[132:135], v[196:199], v[0:3], 0
ds_read_b64_tr_b16 v[192:193], v230 offset:6144
v_mul_f32 v177, v177, v253
v_mfma_f32_16x16x32_bf16 v[140:143], v[196:199], v[32:35], 0
ds_read_b64_tr_b16 v[194:195], v230 offset:7168
v_mul_f32 v178, v178, v253
v_mfma_f32_16x16x32_bf16 v[148:151], v[196:199], v[64:67], 0
v_mul_f32 v179, v179, v253
v_mfma_f32_16x16x32_bf16 v[156:159], v[196:199], v[96:99], 0
ds_write_b128 v225, v[176:179] offset:16384
s_waitcnt lgkmcnt(11)
v_mfma_f32_16x16x32_bf16 v[128:131], v[200:203], v[4:7], v[128:131]
ds_read_b64_tr_b16 v[196:197], v229 offset:8192
v_mul_f32 v180, v180, v253
v_mfma_f32_16x16x32_bf16 v[136:139], v[200:203], v[36:39], v[136:139]
s_mov_b32 s61, 0xe340
s_add_u32 m0, s35, 0x9000
ds_read_b64_tr_b16 v[198:199], v229 offset:9216
v_mul_f32 v181, v181, v253
v_mfma_f32_16x16x32_bf16 v[144:147], v[200:203], v[68:71], v[144:147]
v_mul_f32 v182, v182, v253
v_mul_f32 v183, v183, v253
v_mfma_f32_16x16x32_bf16 v[152:155], v[200:203], v[100:103], v[152:155]
buffer_load_dwordx4 v235, s[52:55], s61 offen offset:0  lds
s_waitcnt lgkmcnt(5)
v_mfma_f32_16x16x32_bf16 v[132:135], v[204:207], v[4:7], v[132:135]
ds_write_b128 v225, v[180:183] offset:17408
v_mfma_f32_16x16x32_bf16 v[140:143], v[204:207], v[36:39], v[140:143]
v_mul_f32 v184, v184, v254
v_mul_f32 v185, v185, v254
v_mfma_f32_16x16x32_bf16 v[148:151], v[204:207], v[68:71], v[148:151]
v_mul_f32 v186, v186, v254
ds_read_b64_tr_b16 v[200:201], v230 offset:8192
v_mfma_f32_16x16x32_bf16 v[156:159], v[204:207], v[100:103], v[156:159]
v_mul_f32 v187, v187, v254
ds_read_b64_tr_b16 v[202:203], v230 offset:9216
v_mfma_f32_16x16x32_bf16 v[128:131], v[208:211], v[8:11], v[128:131]
s_mov_b32 s61, 0x11b40
s_add_u32 m0, s35, 0x9400
ds_write_b128 v225, v[184:187] offset:18432
v_mfma_f32_16x16x32_bf16 v[136:139], v[208:211], v[40:43], v[136:139]
v_mul_f32 v188, v188, v254
ds_read_b64_tr_b16 v[204:205], v229 offset:10240
v_mfma_f32_16x16x32_bf16 v[144:147], v[208:211], v[72:75], v[144:147]
buffer_load_dwordx4 v235, s[52:55], s61 offen offset:0  lds
v_mfma_f32_16x16x32_bf16 v[152:155], v[208:211], v[104:107], v[152:155]
v_mul_f32 v189, v189, v254
v_mul_f32 v190, v190, v254
v_mfma_f32_16x16x32_bf16 v[132:135], v[212:215], v[8:11], v[132:135]
v_mul_f32 v191, v191, v254
ds_read_b64_tr_b16 v[206:207], v229 offset:11264
v_mfma_f32_16x16x32_bf16 v[140:143], v[212:215], v[40:43], v[140:143]
ds_write_b128 v225, v[188:191] offset:19456
v_mfma_f32_16x16x32_bf16 v[148:151], v[212:215], v[72:75], v[148:151]
ds_read_b64_tr_b16 v[208:209], v230 offset:10240
ds_read_b64_tr_b16 v[210:211], v230 offset:11264
v_mfma_f32_16x16x32_bf16 v[156:159], v[212:215], v[104:107], v[156:159]
v_mfma_f32_16x16x32_bf16 v[128:131], v[216:219], v[12:15], v[128:131]
s_mov_b32 s61, 0x15340
s_add_u32 m0, s35, 0x9800
ds_read_b64_tr_b16 v[212:213], v229 offset:12288
v_mfma_f32_16x16x32_bf16 v[136:139], v[216:219], v[44:47], v[136:139]
ds_read_b64_tr_b16 v[214:215], v229 offset:13312
v_mfma_f32_16x16x32_bf16 v[144:147], v[216:219], v[76:79], v[144:147]
buffer_load_dwordx4 v235, s[52:55], s61 offen offset:0  lds
v_mfma_f32_16x16x32_bf16 v[152:155], v[216:219], v[108:111], v[152:155]
s_waitcnt lgkmcnt(6)
v_mfma_f32_16x16x32_bf16 v[132:135], v[192:195], v[12:15], v[132:135]
ds_read_b64_tr_b16 v[216:217], v230 offset:12288
v_mfma_f32_16x16x32_bf16 v[140:143], v[192:195], v[44:47], v[140:143]
ds_read_b64_tr_b16 v[218:219], v230 offset:13312
v_mfma_f32_16x16x32_bf16 v[148:151], v[192:195], v[76:79], v[148:151]
s_waitcnt lgkmcnt(6)
s_barrier
v_mfma_f32_16x16x32_bf16 v[156:159], v[192:195], v[108:111], v[156:159]
s_mov_b32 s61, 0x18b40
s_add_u32 m0, s35, 0x9c00
ds_read_b128 v[176:179], v226 offset:16384
ds_read_b128 v[180:183], v226 offset:17408
v_mfma_f32_16x16x32_bf16 v[128:131], v[196:199], v[16:19], v[128:131]
ds_read_b128 v[184:187], v226 offset:18432
ds_read_b128 v[188:191], v226 offset:19456
v_mfma_f32_16x16x32_bf16 v[136:139], v[196:199], v[48:51], v[136:139]
buffer_load_dwordx4 v235, s[52:55], s61 offen offset:0  lds
v_mfma_f32_16x16x32_bf16 v[144:147], v[196:199], v[80:83], v[144:147]
ds_read_b64_tr_b16 v[192:193], v229 offset:14336
ds_read_b64_tr_b16 v[194:195], v229 offset:15360
v_mfma_f32_16x16x32_bf16 v[152:155], v[196:199], v[112:115], v[152:155]
ds_read_b128 a[144:147], v250 offset:0
v_mfma_f32_16x16x32_bf16 v[132:135], v[200:203], v[16:19], v[132:135]
ds_read_b64_tr_b16 v[196:197], v230 offset:14336
v_mfma_f32_16x16x32_bf16 v[140:143], v[200:203], v[48:51], v[140:143]
ds_read_b64_tr_b16 v[198:199], v230 offset:15360
v_mfma_f32_16x16x32_bf16 v[148:151], v[200:203], v[80:83], v[148:151]
s_waitcnt lgkmcnt(8)
v_fma_f32 v160, v160, v251, v176
v_fma_f32 v161, v161, v251, v177
v_mfma_f32_16x16x32_bf16 v[156:159], v[200:203], v[112:115], v[156:159]
v_fma_f32 v162, v162, v251, v178
v_fma_f32 v163, v163, v251, v179
v_mfma_f32_16x16x32_bf16 v[128:131], v[204:207], v[20:23], v[128:131]
s_waitcnt lgkmcnt(7)
v_fma_f32 v164, v164, v251, v180
v_mfma_f32_16x16x32_bf16 v[136:139], v[204:207], v[52:55], v[136:139]
s_waitcnt vmcnt(22)
s_waitcnt lgkmcnt(0)
v_fma_f32 v165, v165, v251, v181
v_mfma_f32_16x16x32_bf16 v[144:147], v[204:207], v[84:87], v[144:147]
s_barrier
ds_read_b64_tr_b16 v[200:201], v229 offset:32768
v_mfma_f32_16x16x32_bf16 v[152:155], v[204:207], v[116:119], v[152:155]
ds_read_b64_tr_b16 v[202:203], v229 offset:33792
v_fma_f32 v166, v166, v251, v182
v_mfma_f32_16x16x32_bf16 v[132:135], v[208:211], v[20:23], v[132:135]
v_fma_f32 v167, v167, v251, v183
v_cvt_pk_bf16_f32 v160, v160, v161
v_mfma_f32_16x16x32_bf16 v[140:143], v[208:211], v[52:55], v[140:143]
v_cvt_pk_bf16_f32 v161, v162, v163
v_cvt_pk_bf16_f32 v162, v164, v165
v_mfma_f32_16x16x32_bf16 v[148:151], v[208:211], v[84:87], v[148:151]
v_cvt_pk_bf16_f32 v163, v166, v167
ds_read_b64_tr_b16 v[204:205], v230 offset:32768
v_mfma_f32_16x16x32_bf16 v[156:159], v[208:211], v[116:119], v[156:159]
buffer_store_dwordx4 v[160:163], v222, s[56:59], s69 offen offset:0 sc1
v_mfma_f32_16x16x32_bf16 v[128:131], v[212:215], v[24:27], v[128:131]
ds_write_b128 v226, v[160:163] offset:16384
v_fma_f32 v168, v168, v252, v184
v_mfma_f32_16x16x32_bf16 v[136:139], v[212:215], v[56:59], v[136:139]
v_fma_f32 v169, v169, v252, v185
v_fma_f32 v170, v170, v252, v186
v_mfma_f32_16x16x32_bf16 v[144:147], v[212:215], v[88:91], v[144:147]
s_waitcnt vmcnt(11)
v_fma_f32 v171, v171, v252, v187
v_mfma_f32_16x16x32_bf16 v[152:155], v[212:215], v[120:123], v[152:155]
s_add_u32 s75, s96, 6
v_subrev_u32 v238, s75, v238
v_cmp_ge_u32 vcc, 8, v238
v_cmp_eq_u32 s[64:65], s97, v239
s_nop 1
s_and_b64 vcc, vcc, s[64:65]
s_cmp_eq_u64 vcc, -1
s_cbranch_scc0 LP1
LPB1:
v_mfma_f32_16x16x32_bf16 v[132:135], v[216:219], v[24:27], v[132:135]
v_fma_f32 v172, v172, v252, v188
v_fma_f32 v173, v173, v252, v189
v_mfma_f32_16x16x32_bf16 v[140:143], v[216:219], v[56:59], v[140:143]
v_fma_f32 v174, v174, v252, v190
v_fma_f32 v175, v175, v252, v191
v_mfma_f32_16x16x32_bf16 v[148:151], v[216:219], v[88:91], v[148:151]
v_cvt_pk_bf16_f32 v168, v168, v169
v_cvt_pk_bf16_f32 v169, v170, v171
v_mfma_f32_16x16x32_bf16 v[156:159], v[216:219], v[120:123], v[156:159]
v_cvt_pk_bf16_f32 v170, v172, v173
v_cvt_pk_bf16_f32 v171, v174, v175
v_mfma_f32_16x16x32_bf16 v[128:131], v[192:195], v[28:31], v[128:131]
buffer_store_dwordx4 v[168:171], v223, s[56:59], s69 offen offset:1024 sc1
v_mfma_f32_16x16x32_bf16 v[136:139], v[192:195], v[60:63], v[136:139]
ds_write_b128 v226, v[168:171] offset:17408
ds_read_b64_tr_b16 v[206:207], v230 offset:33792
v_mfma_f32_16x16x32_bf16 v[144:147], v[192:195], v[92:95], v[144:147]
ds_read_b64_tr_b16 v[208:209], v229 offset:34816
ds_read_b64_tr_b16 v[210:211], v229 offset:35840
v_mfma_f32_16x16x32_bf16 v[152:155], v[192:195], v[124:127], v[152:155]
ds_read_b64_tr_b16 v[212:213], v230 offset:34816
ds_read_b64_tr_b16 v[214:215], v230 offset:35840
v_mfma_f32_16x16x32_bf16 v[132:135], v[196:199], v[28:31], v[132:135]
ds_read_b64_tr_b16 v[216:217], v229 offset:36864
ds_read_b64_tr_b16 v[218:219], v229 offset:37888
v_mfma_f32_16x16x32_bf16 v[140:143], v[196:199], v[60:63], v[140:143]
ds_read_b64_tr_b16 v[192:193], v230 offset:36864
ds_read_b64_tr_b16 v[194:195], v230 offset:37888
v_mfma_f32_16x16x32_bf16 v[148:151], v[196:199], v[92:95], v[148:151]
v_mfma_f32_16x16x32_bf16 v[156:159], v[196:199], v[124:127], v[156:159]
LST57:
s_waitcnt lgkmcnt(10)
v_mfma_f32_16x16x32_bf16 v[160:163], v[200:203], v[0:3], 0
s_add_u32 s34, s73, 0x6000
ds_read_b64_tr_b16 v[196:197], v229 offset:38912
v_mfma_f32_16x16x32_bf16 v[168:171], v[200:203], v[32:35], 0
ds_read_b64_tr_b16 v[198:199], v229 offset:39936
v_mfma_f32_16x16x32_bf16 v[176:179], v[200:203], v[64:67], 0
buffer_load_dwordx4 a[24:27], v224, s[56:59], s34 offen sc1
s_add_u32 s69, s72, 0x14000
v_mfma_f32_16x16x32_bf16 v[184:187], v[200:203], v[96:99], 0
v_mul_f32 v144, v144, v253
v_mul_f32 v145, v145, v253
s_waitcnt lgkmcnt(10)
v_mfma_f32_16x16x32_bf16 v[164:167], v[204:207], v[0:3], 0
buffer_load_dwordx4 a[28:31], v231, s[56:59], s34 offen sc1
v_mfma_f32_16x16x32_bf16 v[172:175], v[204:207], v[32:35], 0
ds_read_b64_tr_b16 v[200:201], v230 offset:38912
ds_read_b64_tr_b16 v[202:203], v230 offset:39936
v_mfma_f32_16x16x32_bf16 v[180:183], v[204:207], v[64:67], 0
s_add_u32 s34, s73, 0x8000
v_mul_f32 v146, v146, v253
v_mul_f32 v147, v147, v253
v_mfma_f32_16x16x32_bf16 v[188:191], v[204:207], v[96:99], 0
ds_write_b128 v225, v[144:147] offset:0
s_waitcnt lgkmcnt(5)
v_mfma_f32_16x16x32_bf16 v[160:163], v[208:211], v[4:7], v[160:163]
buffer_load_dwordx4 a[32:35], v224, s[56:59], s34 offen sc1
v_mfma_f32_16x16x32_bf16 v[168:171], v[208:211], v[36:39], v[168:171]
ds_read_b64_tr_b16 v[204:205], v229 offset:40960
ds_read_b64_tr_b16 v[206:207], v229 offset:41984
v_mfma_f32_16x16x32_bf16 v[176:179], v[208:211], v[68:71], v[176:179]
buffer_load_dwordx4 a[36:39], v231, s[56:59], s34 offen sc1
v_mfma_f32_16x16x32_bf16 v[184:187], v[208:211], v[100:103], v[184:187]
v_mul_f32 v148, v148, v253
v_mul_f32 v149, v149, v253
v_mfma_f32_16x16x32_bf16 v[164:167], v[212:215], v[4:7], v[164:167]
s_add_u32 s34, s73, 0xa000
v_mul_f32 v150, v150, v253
v_mul_f32 v151, v151, v253
v_mfma_f32_16x16x32_bf16 v[172:175], v[212:215], v[36:39], v[172:175]
ds_write_b128 v225, v[148:151] offset:1024
v_mfma_f32_16x16x32_bf16 v[180:183], v[212:215], v[68:71], v[180:183]
buffer_load_dwordx4 a[40:43], v224, s[56:59], s34 offen sc1
v_mfma_f32_16x16x32_bf16 v[188:191], v[212:215], v[100:103], v[188:191]
v_mul_f32 v152, v152, v254
v_mul_f32 v153, v153, v254
v_mfma_f32_16x16x32_bf16 v[160:163], v[216:219], v[8:11], v[160:163]
buffer_load_dwordx4 a[44:47], v231, s[56:59], s34 offen sc1
v_mfma_f32_16x16x32_bf16 v[168:171], v[216:219], v[40:43], v[168:171]
v_mul_f32 v154, v154, v254
v_mul_f32 v155, v155, v254
v_mfma_f32_16x16x32_bf16 v[176:179], v[216:219], v[72:75], v[176:179]
ds_write_b128 v225, v[152:155] offset:2048
v_mfma_f32_16x16x32_bf16 v[184:187], v[216:219], v[104:107], v[184:187]
v_mul_f32 v156, v156, v254
v_mul_f32 v157, v157, v254
v_mfma_f32_16x16x32_bf16 v[164:167], v[192:195], v[8:11], v[164:167]
v_mul_f32 v158, v158, v254
v_mul_f32 v159, v159, v254
v_mfma_f32_16x16x32_bf16 v[172:175], v[192:195], v[40:43], v[172:175]
ds_write_b128 v225, v[156:159] offset:3072
v_mfma_f32_16x16x32_bf16 v[180:183], v[192:195], v[72:75], v[180:183]
ds_read_b64_tr_b16 v[208:209], v230 offset:40960
ds_read_b64_tr_b16 v[210:211], v230 offset:41984
v_mfma_f32_16x16x32_bf16 v[188:191], v[192:195], v[104:107], v[188:191]
ds_read_b64_tr_b16 v[212:213], v229 offset:43008
ds_read_b64_tr_b16 v[214:215], v229 offset:44032
s_waitcnt lgkmcnt(6)
v_mfma_f32_16x16x32_bf16 v[160:163], v[196:199], v[12:15], v[160:163]
ds_read_b64_tr_b16 v[216:217], v230 offset:43008
ds_read_b64_tr_b16 v[218:219], v230 offset:44032
v_mfma_f32_16x16x32_bf16 v[168:171], v[196:199], v[44:47], v[168:171]
ds_read_b64_tr_b16 v[192:193], v229 offset:45056
ds_read_b64_tr_b16 v[194:195], v229 offset:46080
v_mfma_f32_16x16x32_bf16 v[176:179], v[196:199], v[76:79], v[176:179]
v_mfma_f32_16x16x32_bf16 v[184:187], v[196:199], v[108:111], v[184:187]
v_mfma_f32_16x16x32_bf16 v[164:167], v[200:203], v[12:15], v[164:167]
ds_read_b64_tr_b16 v[196:197], v230 offset:45056
v_mfma_f32_16x16x32_bf16 v[172:175], v[200:203], v[44:47], v[172:175]
ds_read_b64_tr_b16 v[198:199], v230 offset:46080
v_mfma_f32_16x16x32_bf16 v[180:183], v[200:203], v[76:79], v[180:183]
s_waitcnt lgkmcnt(10)
s_barrier
v_mfma_f32_16x16x32_bf16 v[188:191], v[200:203], v[108:111], v[188:191]
ds_read_b128 v[144:147], v226 offset:0
ds_read_b128 v[148:151], v226 offset:1024
v_mfma_f32_16x16x32_bf16 v[160:163], v[204:207], v[16:19], v[160:163]
ds_read_b128 v[152:155], v226 offset:2048
ds_read_b128 v[156:159], v226 offset:3072
v_mfma_f32_16x16x32_bf16 v[168:171], v[204:207], v[48:51], v[168:171]
ds_read_b64_tr_b16 v[200:201], v229 offset:47104
ds_read_b64_tr_b16 v[202:203], v229 offset:48128
v_mfma_f32_16x16x32_bf16 v[176:179], v[204:207], v[80:83], v[176:179]
ds_read_b128 a[148:151], v250 offset:16384
v_mfma_f32_16x16x32_bf16 v[184:187], v[204:207], v[112:115], v[184:187]
s_waitcnt lgkmcnt(9)
v_mfma_f32_16x16x32_bf16 v[164:167], v[208:211], v[16:19], v[164:167]
ds_read_b64_tr_b16 v[204:205], v230 offset:47104
v_mfma_f32_16x16x32_bf16 v[172:175], v[208:211], v[48:51], v[172:175]
ds_read_b64_tr_b16 v[206:207], v230 offset:48128
v_mfma_f32_16x16x32_bf16 v[180:183], v[208:211], v[80:83], v[180:183]
s_waitcnt lgkmcnt(8)
v_fma_f32 v128, v128, v251, v144
v_fma_f32 v129, v129, v251, v145
v_mfma_f32_16x16x32_bf16 v[188:191], v[208:211], v[112:115], v[188:191]
v_fma_f32 v130, v130, v251, v146
v_fma_f32 v131, v131, v251, v147
v_mfma_f32_16x16x32_bf16 v[160:163], v[212:215], v[20:23], v[160:163]
s_waitcnt lgkmcnt(7)
v_fma_f32 v132, v132, v251, v148
v_mfma_f32_16x16x32_bf16 v[168:171], v[212:215], v[52:55], v[168:171]
s_waitcnt lgkmcnt(0)
v_fma_f32 v133, v133, v251, v149
v_mfma_f32_16x16x32_bf16 v[176:179], v[212:215], v[84:87], v[176:179]
s_barrier
v_add_u32 v229, 0xffff0000, v229
v_mfma_f32_16x16x32_bf16 v[184:187], v[212:215], v[116:119], v[184:187]
ds_read_b64_tr_b16 v[208:209], v229 offset:0
ds_read_b64_tr_b16 v[210:211], v229 offset:1024
v_mfma_f32_16x16x32_bf16 v[164:167], v[216:219], v[20:23], v[164:167]
v_fma_f32 v134, v134, v251, v150
v_fma_f32 v135, v135, v251, v151
v_mfma_f32_16x16x32_bf16 v[172:175], v[216:219], v[52:55], v[172:175]
v_cvt_pk_bf16_f32 v128, v128, v129
v_cvt_pk_bf16_f32 v129, v130, v131
v_mfma_f32_16x16x32_bf16 v[180:183], v[216:219], v[84:87], v[180:183]
v_cvt_pk_bf16_f32 v130, v132, v133
v_cvt_pk_bf16_f32 v131, v134, v135
v_mfma_f32_16x16x32_bf16 v[188:191], v[216:219], v[116:119], v[188:191]
buffer_store_dwordx4 v[128:131], v222, s[56:59], s69 offen offset:0 sc1
v_mfma_f32_16x16x32_bf16 v[160:163], v[192:195], v[24:27], v[160:163]
ds_write_b128 v226, v[128:131] offset:0
v_fma_f32 v136, v136, v252, v152
v_mfma_f32_16x16x32_bf16 v[168:171], v[192:195], v[56:59], v[168:171]
v_fma_f32 v137, v137, v252, v153
v_fma_f32 v138, v138, v252, v154
v_mfma_f32_16x16x32_bf16 v[176:179], v[192:195], v[88:91], v[176:179]
v_fma_f32 v139, v139, v252, v155
v_fma_f32 v140, v140, v252, v156
v_mfma_f32_16x16x32_bf16 v[184:187], v[192:195], v[120:123], v[184:187]
v_fma_f32 v141, v141, v252, v157
v_fma_f32 v142, v142, v252, v158
v_mfma_f32_16x16x32_bf16 v[164:167], v[196:199], v[24:27], v[164:167]
v_fma_f32 v143, v143, v252, v159
v_cvt_pk_bf16_f32 v136, v136, v137
v_mfma_f32_16x16x32_bf16 v[172:175], v[196:199], v[56:59], v[172:175]
v_cvt_pk_bf16_f32 v137, v138, v139
v_cvt_pk_bf16_f32 v138, v140, v141
v_mfma_f32_16x16x32_bf16 v[180:183], v[196:199], v[88:91], v[180:183]
v_cvt_pk_bf16_f32 v139, v142, v143
v_add_u32 v230, 0xffff0000, v230
v_mfma_f32_16x16x32_bf16 v[188:191], v[196:199], v[120:123], v[188:191]
buffer_store_dwordx4 v[136:139], v223, s[56:59], s69 offen offset:1024 sc1
v_mfma_f32_16x16x32_bf16 v[160:163], v[200:203], v[28:31], v[160:163]
ds_write_b128 v226, v[136:139] offset:1024
ds_read_b64_tr_b16 v[212:213], v230 offset:0
v_mfma_f32_16x16x32_bf16 v[168:171], v[200:203], v[60:63], v[168:171]
ds_read_b64_tr_b16 v[214:215], v230 offset:1024
ds_read_b64_tr_b16 v[216:217], v229 offset:2048
v_mfma_f32_16x16x32_bf16 v[176:179], v[200:203], v[92:95], v[176:179]
s_waitcnt vmcnt(16)
s_add_u32 s74, s96, 9
v_mov_b32 v240, s74
v_mfma_f32_16x16x32_bf16 v[184:187], v[200:203], v[124:127], v[184:187]
v_mov_b32 v241, s97
ds_read_b64_tr_b16 v[218:219], v229 offset:3072
v_mfma_f32_16x16x32_bf16 v[164:167], v[204:207], v[28:31], v[164:167]
buffer_store_dwordx2 v[240:241], v233, s[40:43], 0 offen sc1
v_mfma_f32_16x16x32_bf16 v[172:175], v[204:207], v[60:63], v[172:175]
ds_read_b64_tr_b16 v[192:193], v230 offset:2048
ds_read_b64_tr_b16 v[194:195], v230 offset:3072
v_mfma_f32_16x16x32_bf16 v[180:183], v[204:207], v[92:95], v[180:183]
ds_read_b64_tr_b16 v[196:197], v229 offset:4096
ds_read_b64_tr_b16 v[198:199], v229 offset:5120
v_mfma_f32_16x16x32_bf16 v[188:191], v[204:207], v[124:127], v[188:191]
LST58:
ds_read_b64_tr_b16 v[200:201], v230 offset:4096
ds_read_b64_tr_b16 v[202:203], v230 offset:5120
s_waitcnt lgkmcnt(11)
v_mfma_f32_16x16x32_bf16 v[128:131], v[208:211], v[0:3], 0
ds_read_b64_tr_b16 v[204:205], v229 offset:6144
v_mfma_f32_16x16x32_bf16 v[136:139], v[208:211], v[32:35], 0
ds_read_b64_tr_b16 v[206:207], v229 offset:7168
v_mfma_f32_16x16x32_bf16 v[144:147], v[208:211], v[64:67], 0
s_add_u32 s69, s72, 0x16000
v_mul_f32 v176, v176, v253
v_mfma_f32_16x16x32_bf16 v[152:155], v[208:211], v[96:99], 0
v_mul_f32 v177, v177, v253
s_waitcnt lgkmcnt(9)
v_mfma_f32_16x16x32_bf16 v[132:135], v[212:215], v[0:3], 0
ds_read_b64_tr_b16 v[208:209], v230 offset:6144
v_mul_f32 v178, v178, v253
v_mfma_f32_16x16x32_bf16 v[140:143], v[212:215], v[32:35], 0
ds_read_b64_tr_b16 v[210:211], v230 offset:7168
v_mul_f32 v179, v179, v253
v_mfma_f32_16x16x32_bf16 v[148:151], v[212:215], v[64:67], 0
ds_write_b128 v225, v[176:179] offset:16384
v_mfma_f32_16x16x32_bf16 v[156:159], v[212:215], v[96:99], 0
v_mul_f32 v180, v180, v253
s_waitcnt lgkmcnt(9)
v_mfma_f32_16x16x32_bf16 v[128:131], v[216:219], v[4:7], v[128:131]
ds_read_b64_tr_b16 v[212:213], v229 offset:8192
v_mul_f32 v181, v181, v253
v_mfma_f32_16x16x32_bf16 v[136:139], v[216:219], v[36:39], v[136:139]
ds_read_b64_tr_b16 v[214:215], v229 offset:9216
v_mul_f32 v182, v182, v253
v_mfma_f32_16x16x32_bf16 v[144:147], v[216:219], v[68:71], v[144:147]
v_mul_f32 v183, v183, v253
v_mfma_f32_16x16x32_bf16 v[152:155], v[216:219], v[100:103], v[152:155]
ds_write_b128 v225, v[180:183] offset:17408
v_mfma_f32_16x16x32_bf16 v[132:135], v[192:195], v[4:7], v[132:135]
v_mul_f32 v184, v184, v254
ds_read_b64_tr_b16 v[216:217], v230 offset:8192
v_mfma_f32_16x16x32_bf16 v[140:143], v[192:195], v[36:39], v[140:143]
v_mul_f32 v185, v185, v254
ds_read_b64_tr_b16 v[218:219], v230 offset:9216
v_mfma_f32_16x16x32_bf16 v[148:151], v[192:195], v[68:71], v[148:151]
v_mul_f32 v186, v186, v254
v_mfma_f32_16x16x32_bf16 v[156:159], v[192:195], v[100:103], v[156:159]
v_mul_f32 v187, v187, v254
s_waitcnt lgkmcnt(6)
v_mfma_f32_16x16x32_bf16 v[128:131], v[196:199], v[8:11], v[128:131]
ds_write_b128 v225, v[184:187] offset:18432
v_mfma_f32_16x16x32_bf16 v[136:139], v[196:199], v[40:43], v[136:139]
v_mul_f32 v188, v188, v254
ds_read_b64_tr_b16 v[192:193], v229 offset:10240
v_mfma_f32_16x16x32_bf16 v[144:147], v[196:199], v[72:75], v[144:147]
v_mul_f32 v189, v189, v254
ds_read_b64_tr_b16 v[194:195], v229 offset:11264
v_mfma_f32_16x16x32_bf16 v[152:155], v[196:199], v[104:107], v[152:155]
v_mul_f32 v190, v190, v254
v_mfma_f32_16x16x32_bf16 v[132:135], v[200:203], v[8:11], v[132:135]
v_mul_f32 v191, v191, v254
ds_read_b64_tr_b16 v[196:197], v230 offset:10240
v_mfma_f32_16x16x32_bf16 v[140:143], v[200:203], v[40:43], v[140:143]
ds_write_b128 v225, v[188:191] offset:19456
v_mfma_f32_16x16x32_bf16 v[148:151], v[200:203], v[72:75], v[148:151]
ds_read_b64_tr_b16 v[198:199], v230 offset:11264
v_mfma_f32_16x16x32_bf16 v[156:159], v[200:203], v[104:107], v[156:159]
v_mfma_f32_16x16x32_bf16 v[128:131], v[204:207], v[12:15], v[128:131]
ds_read_b64_tr_b16 v[200:201], v229 offset:12288
v_mfma_f32_16x16x32_bf16 v[136:139], v[204:207], v[44:47], v[136:139]
ds_read_b64_tr_b16 v[202:203], v229 offset:13312
v_mfma_f32_16x16x32_bf16 v[144:147], v[204:207], v[76:79], v[144:147]
v_mfma_f32_16x16x32_bf16 v[152:155], v[204:207], v[108:111], v[152:155]
v_mfma_f32_16x16x32_bf16 v[132:135], v[208:211], v[12:15], v[132:135]
ds_read_b64_tr_b16 v[204:205], v230 offset:12288
v_mfma_f32_16x16x32_bf16 v[140:143], v[208:211], v[44:47], v[140:143]
ds_read_b64_tr_b16 v[206:207], v230 offset:13312
v_mfma_f32_16x16x32_bf16 v[148:151], v[208:211], v[76:79], v[148:151]
s_waitcnt lgkmcnt(5)
s_barrier
v_mfma_f32_16x16x32_bf16 v[156:159], v[208:211], v[108:111], v[156:159]
ds_read_b128 v[176:179], v226 offset:16384
ds_read_b128 v[180:183], v226 offset:17408
v_mfma_f32_16x16x32_bf16 v[128:131], v[212:215], v[16:19], v[128:131]
ds_read_b128 v[184:187], v226 offset:18432
ds_read_b128 v[188:191], v226 offset:19456
v_mfma_f32_16x16x32_bf16 v[136:139], v[212:215], v[48:51], v[136:139]
ds_read_b64_tr_b16 v[208:209], v229 offset:14336
ds_read_b64_tr_b16 v[210:211], v229 offset:15360
v_mfma_f32_16x16x32_bf16 v[144:147], v[212:215], v[80:83], v[144:147]
ds_read_b128 a[152:155], v250 offset:0
v_mfma_f32_16x16x32_bf16 v[152:155], v[212:215], v[112:115], v[152:155]
v_mfma_f32_16x16x32_bf16 v[132:135], v[216:219], v[16:19], v[132:135]
ds_read_b64_tr_b16 v[212:213], v230 offset:14336
v_mfma_f32_16x16x32_bf16 v[140:143], v[216:219], v[48:51], v[140:143]
ds_read_b64_tr_b16 v[214:215], v230 offset:15360
v_mfma_f32_16x16x32_bf16 v[148:151], v[216:219], v[80:83], v[148:151]
s_waitcnt lgkmcnt(8)
v_fma_f32 v160, v160, v251, v176
v_fma_f32 v161, v161, v251, v177
v_mfma_f32_16x16x32_bf16 v[156:159], v[216:219], v[112:115], v[156:159]
v_fma_f32 v162, v162, v251, v178
v_fma_f32 v163, v163, v251, v179
v_mfma_f32_16x16x32_bf16 v[128:131], v[192:195], v[20:23], v[128:131]
s_waitcnt lgkmcnt(7)
v_fma_f32 v164, v164, v251, v180
v_mfma_f32_16x16x32_bf16 v[136:139], v[192:195], v[52:55], v[136:139]
s_waitcnt vmcnt(11)
s_waitcnt lgkmcnt(0)
v_fma_f32 v165, v165, v251, v181
v_mfma_f32_16x16x32_bf16 v[144:147], v[192:195], v[84:87], v[144:147]
s_barrier
ds_read_b64_tr_b16 v[216:217], v229 offset:32768
v_mfma_f32_16x16x32_bf16 v[152:155], v[192:195], v[116:119], v[152:155]
ds_read_b64_tr_b16 v[218:219], v229 offset:33792
v_fma_f32 v166, v166, v251, v182
v_mfma_f32_16x16x32_bf16 v[132:135], v[196:199], v[20:23], v[132:135]
buffer_load_dwordx2 v[238:239], v234, s[40:43], 0 offen sc1
v_mfma_f32_16x16x32_bf16 v[140:143], v[196:199], v[52:55], v[140:143]
v_fma_f32 v167, v167, v251, v183
v_cvt_pk_bf16_f32 v160, v160, v161
v_mfma_f32_16x16x32_bf16 v[148:151], v[196:199], v[84:87], v[148:151]
v_cvt_pk_bf16_f32 v161, v162, v163
v_cvt_pk_bf16_f32 v162, v164, v165
v_mfma_f32_16x16x32_bf16 v[156:159], v[196:199], v[116:119], v[156:159]
v_cvt_pk_bf16_f32 v163, v166, v167
ds_read_b64_tr_b16 v[192:193], v230 offset:32768
v_mfma_f32_16x16x32_bf16 v[128:131], v[200:203], v[24:27], v[128:131]
buffer_store_dwordx4 v[160:163], v222, s[56:59], s69 offen offset:0 sc1
v_mfma_f32_16x16x32_bf16 v[136:139], v[200:203], v[56:59], v[136:139]
ds_write_b128 v226, v[160:163] offset:16384
v_fma_f32 v168, v168, v252, v184
v_mfma_f32_16x16x32_bf16 v[144:147], v[200:203], v[88:91], v[144:147]
v_fma_f32 v169, v169, v252, v185
v_fma_f32 v170, v170, v252, v186
v_mfma_f32_16x16x32_bf16 v[152:155], v[200:203], v[120:123], v[152:155]
v_fma_f32 v171, v171, v252, v187
v_fma_f32 v172, v172, v252, v188
v_mfma_f32_16x16x32_bf16 v[132:135], v[204:207], v[24:27], v[132:135]
v_fma_f32 v173, v173, v252, v189
v_fma_f32 v174, v174, v252, v190
v_mfma_f32_16x16x32_bf16 v[140:143], v[204:207], v[56:59], v[140:143]
v_fma_f32 v175, v175, v252, v191
v_cvt_pk_bf16_f32 v168, v168, v169
v_mfma_f32_16x16x32_bf16 v[148:151], v[204:207], v[88:91], v[148:151]
v_cvt_pk_bf16_f32 v169, v170, v171
v_cvt_pk_bf16_f32 v170, v172, v173
v_mfma_f32_16x16x32_bf16 v[156:159], v[204:207], v[120:123], v[156:159]
v_cvt_pk_bf16_f32 v171, v174, v175
ds_read_b64_tr_b16 v[194:195], v230 offset:33792
v_mfma_f32_16x16x32_bf16 v[128:131], v[208:211], v[28:31], v[128:131]
buffer_store_dwordx4 v[168:171], v223, s[56:59], s69 offen offset:1024 sc1
v_mfma_f32_16x16x32_bf16 v[136:139], v[208:211], v[60:63], v[136:139]
ds_write_b128 v226, v[168:171] offset:17408
ds_read_b64_tr_b16 v[196:197], v229 offset:34816
v_mfma_f32_16x16x32_bf16 v[144:147], v[208:211], v[92:95], v[144:147]
ds_read_b64_tr_b16 v[198:199], v229 offset:35840
ds_read_b64_tr_b16 v[200:201], v230 offset:34816
v_mfma_f32_16x16x32_bf16 v[152:155], v[208:211], v[124:127], v[152:155]
ds_read_b64_tr_b16 v[202:203], v230 offset:35840
ds_read_b64_tr_b16 v[204:205], v229 offset:36864
v_mfma_f32_16x16x32_bf16 v[132:135], v[212:215], v[28:31], v[132:135]
ds_read_b64_tr_b16 v[206:207], v229 offset:37888
ds_read_b64_tr_b16 v[208:209], v230 offset:36864
v_mfma_f32_16x16x32_bf16 v[140:143], v[212:215], v[60:63], v[140:143]
ds_read_b64_tr_b16 v[210:211], v230 offset:37888
v_mfma_f32_16x16x32_bf16 v[148:151], v[212:215], v[92:95], v[148:151]
v_mfma_f32_16x16x32_bf16 v[156:159], v[212:215], v[124:127], v[156:159]
LST59:
s_waitcnt lgkmcnt(10)
v_mfma_f32_16x16x32_bf16 v[160:163], v[216:219], v[0:3], 0
ds_read_b64_tr_b16 v[212:213], v229 offset:38912
v_mfma_f32_16x16x32_bf16 v[168:171], v[216:219], v[32:35], 0
ds_read_b64_tr_b16 v[214:215], v229 offset:39936
v_mfma_f32_16x16x32_bf16 v[176:179], v[216:219], v[64:67], 0
s_add_u32 s69, s72, 0x18000
v_mul_f32 v144, v144, v253
v_mfma_f32_16x16x32_bf16 v[184:187], v[216:219], v[96:99], 0
v_mul_f32 v145, v145, v253
s_waitcnt lgkmcnt(9)
v_mfma_f32_16x16x32_bf16 v[164:167], v[192:195], v[0:3], 0
ds_read_b64_tr_b16 v[216:217], v230 offset:38912
v_mul_f32 v146, v146, v253
v_mfma_f32_16x16x32_bf16 v[172:175], v[192:195], v[32:35], 0
ds_read_b64_tr_b16 v[218:219], v230 offset:39936
v_mul_f32 v147, v147, v253
v_mfma_f32_16x16x32_bf16 v[180:183], v[192:195], v[64:67], 0
ds_write_b128 v225, v[144:147] offset:0
v_mfma_f32_16x16x32_bf16 v[188:191], v[192:195], v[96:99], 0
v_mul_f32 v148, v148, v253
s_waitcnt lgkmcnt(5)
v_mfma_f32_16x16x32_bf16 v[160:163], v[196:199], v[4:7], v[160:163]
ds_read_b64_tr_b16 v[192:193], v229 offset:40960
v_mul_f32 v149, v149, v253
v_mfma_f32_16x16x32_bf16 v[168:171], v[196:199], v[36:39], v[168:171]
ds_read_b64_tr_b16 v[194:195], v229 offset:41984
v_mul_f32 v150, v150, v253
v_mfma_f32_16x16x32_bf16 v[176:179], v[196:199], v[68:71], v[176:179]
v_mul_f32 v151, v151, v253
v_mfma_f32_16x16x32_bf16 v[184:187], v[196:199], v[100:103], v[184:187]
ds_write_b128 v225, v[148:151] offset:1024
v_mfma_f32_16x16x32_bf16 v[164:167], v[200:203], v[4:7], v[164:167]
v_mul_f32 v152, v152, v254
ds_read_b64_tr_b16 v[196:197], v230 offset:40960
v_mfma_f32_16x16x32_bf16 v[172:175], v[200:203], v[36:39], v[172:175]
v_mul_f32 v153, v153, v254
ds_read_b64_tr_b16 v[198:199], v230 offset:41984
v_mfma_f32_16x16x32_bf16 v[180:183], v[200:203], v[68:71], v[180:183]
v_mul_f32 v154, v154, v254
v_mfma_f32_16x16x32_bf16 v[188:191], v[200:203], v[100:103], v[188:191]
v_mul_f32 v155, v155, v254
v_mfma_f32_16x16x32_bf16 v[160:163], v[204:207], v[8:11], v[160:163]
ds_write_b128 v225, v[152:155] offset:2048
v_mfma_f32_16x16x32_bf16 v[168:171], v[204:207], v[40:43], v[168:171]
v_mul_f32 v156, v156, v254
ds_read_b64_tr_b16 v[200:201], v229 offset:43008
v_mfma_f32_16x16x32_bf16 v[176:179], v[204:207], v[72:75], v[176:179]
v_mul_f32 v157, v157, v254
ds_read_b64_tr_b16 v[202:203], v229 offset:44032
v_mfma_f32_16x16x32_bf16 v[184:187], v[204:207], v[104:107], v[184:187]
v_mul_f32 v158, v158, v254
v_mfma_f32_16x16x32_bf16 v[164:167], v[208:211], v[8:11], v[164:167]
v_mul_f32 v159, v159, v254
ds_read_b64_tr_b16 v[204:205], v230 offset:43008
v_mfma_f32_16x16x32_bf16 v[172:175], v[208:211], v[40:43], v[172:175]
ds_write_b128 v225, v[156:159] offset:3072
v_mfma_f32_16x16x32_bf16 v[180:183], v[208:211], v[72:75], v[180:183]
ds_read_b64_tr_b16 v[206:207], v230 offset:44032
v_mfma_f32_16x16x32_bf16 v[188:191], v[208:211], v[104:107], v[188:191]
s_waitcnt lgkmcnt(6)
v_mfma_f32_16x16x32_bf16 v[160:163], v[212:215], v[12:15], v[160:163]
ds_read_b64_tr_b16 v[208:209], v229 offset:45056
v_mfma_f32_16x16x32_bf16 v[168:171], v[212:215], v[44:47], v[168:171]
ds_read_b64_tr_b16 v[210:211], v229 offset:46080
v_mfma_f32_16x16x32_bf16 v[176:179], v[212:215], v[76:79], v[176:179]
v_mfma_f32_16x16x32_bf16 v[184:187], v[212:215], v[108:111], v[184:187]
v_mfma_f32_16x16x32_bf16 v[164:167], v[216:219], v[12:15], v[164:167]
ds_read_b64_tr_b16 v[212:213], v230 offset:45056
v_mfma_f32_16x16x32_bf16 v[172:175], v[216:219], v[44:47], v[172:175]
ds_read_b64_tr_b16 v[214:215], v230 offset:46080
v_mfma_f32_16x16x32_bf16 v[180:183], v[216:219], v[76:79], v[180:183]
s_waitcnt lgkmcnt(5)
s_barrier
v_mfma_f32_16x16x32_bf16 v[188:191], v[216:219], v[108:111], v[188:191]
ds_read_b128 v[144:147], v226 offset:0
ds_read_b128 v[148:151], v226 offset:1024
v_mfma_f32_16x16x32_bf16 v[160:163], v[192:195], v[16:19], v[160:163]
ds_read_b128 v[152:155], v226 offset:2048
ds_read_b128 v[156:159], v226 offset:3072
v_mfma_f32_16x16x32_bf16 v[168:171], v[192:195], v[48:51], v[168:171]
ds_read_b64_tr_b16 v[216:217], v229 offset:47104
ds_read_b64_tr_b16 v[218:219], v229 offset:48128
v_mfma_f32_16x16x32_bf16 v[176:179], v[192:195], v[80:83], v[176:179]
ds_read_b128 a[156:159], v250 offset:16384
v_mfma_f32_16x16x32_bf16 v[184:187], v[192:195], v[112:115], v[184:187]
v_mfma_f32_16x16x32_bf16 v[164:167], v[196:199], v[16:19], v[164:167]
ds_read_b64_tr_b16 v[192:193], v230 offset:47104
v_mfma_f32_16x16x32_bf16 v[172:175], v[196:199], v[48:51], v[172:175]
ds_read_b64_tr_b16 v[194:195], v230 offset:48128
v_mfma_f32_16x16x32_bf16 v[180:183], v[196:199], v[80:83], v[180:183]
s_waitcnt lgkmcnt(8)
v_fma_f32 v128, v128, v251, v144
v_fma_f32 v129, v129, v251, v145
v_mfma_f32_16x16x32_bf16 v[188:191], v[196:199], v[112:115], v[188:191]
v_fma_f32 v130, v130, v251, v146
v_fma_f32 v131, v131, v251, v147
v_mfma_f32_16x16x32_bf16 v[160:163], v[200:203], v[20:23], v[160:163]
s_waitcnt lgkmcnt(7)
v_fma_f32 v132, v132, v251, v148
v_mfma_f32_16x16x32_bf16 v[168:171], v[200:203], v[52:55], v[168:171]
v_fma_f32 v133, v133, v251, v149
v_fma_f32 v134, v134, v251, v150
v_mfma_f32_16x16x32_bf16 v[176:179], v[200:203], v[84:87], v[176:179]
v_fma_f32 v135, v135, v251, v151
v_mfma_f32_16x16x32_bf16 v[184:187], v[200:203], v[116:119], v[184:187]
v_cvt_pk_bf16_f32 v128, v128, v129
v_cvt_pk_bf16_f32 v129, v130, v131
v_mfma_f32_16x16x32_bf16 v[164:167], v[204:207], v[20:23], v[164:167]
v_cvt_pk_bf16_f32 v130, v132, v133
v_mfma_f32_16x16x32_bf16 v[172:175], v[204:207], v[52:55], v[172:175]
v_cvt_pk_bf16_f32 v131, v134, v135
v_mfma_f32_16x16x32_bf16 v[180:183], v[204:207], v[84:87], v[180:183]
buffer_store_dwordx4 v[128:131], v222, s[56:59], s69 offen offset:0 sc1
v_mfma_f32_16x16x32_bf16 v[188:191], v[204:207], v[116:119], v[188:191]
ds_write_b128 v226, v[128:131] offset:0
s_waitcnt lgkmcnt(1)
v_fma_f32 v136, v136, v252, v152
v_mfma_f32_16x16x32_bf16 v[160:163], v[208:211], v[24:27], v[160:163]
v_fma_f32 v137, v137, v252, v153
v_fma_f32 v138, v138, v252, v154
v_mfma_f32_16x16x32_bf16 v[168:171], v[208:211], v[56:59], v[168:171]
v_fma_f32 v139, v139, v252, v155
v_fma_f32 v140, v140, v252, v156
v_mfma_f32_16x16x32_bf16 v[176:179], v[208:211], v[88:91], v[176:179]
s_waitcnt vmcnt(3)
v_fma_f32 v141, v141, v252, v157
v_mfma_f32_16x16x32_bf16 v[184:187], v[208:211], v[120:123], v[184:187]
s_add_u32 s75, s96, 9
v_subrev_u32 v238, s75, v238
v_cmp_ge_u32 vcc, 5, v238
v_cmp_eq_u32 s[64:65], s97, v239
s_nop 1
s_and_b64 vcc, vcc, s[64:65]
s_cmp_eq_u64 vcc, -1
s_cbranch_scc0 LP2
LPB2:
v_mfma_f32_16x16x32_bf16 v[164:167], v[212:215], v[24:27], v[164:167]
v_fma_f32 v142, v142, v252, v158
v_fma_f32 v143, v143, v252, v159
v_mfma_f32_16x16x32_bf16 v[172:175], v[212:215], v[56:59], v[172:175]
v_cvt_pk_bf16_f32 v136, v136, v137
v_cvt_pk_bf16_f32 v137, v138, v139
v_mfma_f32_16x16x32_bf16 v[180:183], v[212:215], v[88:91], v[180:183]
v_cvt_pk_bf16_f32 v138, v140, v141
v_cvt_pk_bf16_f32 v139, v142, v143
v_mfma_f32_16x16x32_bf16 v[188:191], v[212:215], v[120:123], v[188:191]
buffer_store_dwordx4 v[136:139], v223, s[56:59], s69 offen offset:1024 sc1
v_mfma_f32_16x16x32_bf16 v[160:163], v[216:219], v[28:31], v[160:163]
ds_write_b128 v226, v[136:139] offset:1024
v_mfma_f32_16x16x32_bf16 v[168:171], v[216:219], v[60:63], v[168:171]
v_mfma_f32_16x16x32_bf16 v[176:179], v[216:219], v[92:95], v[176:179]
v_mfma_f32_16x16x32_bf16 v[184:187], v[216:219], v[124:127], v[184:187]
v_mfma_f32_16x16x32_bf16 v[164:167], v[192:195], v[28:31], v[164:167]
v_mfma_f32_16x16x32_bf16 v[172:175], v[192:195], v[60:63], v[172:175]
v_mfma_f32_16x16x32_bf16 v[180:183], v[192:195], v[92:95], v[180:183]
v_mfma_f32_16x16x32_bf16 v[188:191], v[192:195], v[124:127], v[188:191]
s_add_u32 s34, s73, 0xc000
buffer_load_dwordx4 a[48:51], v224, s[56:59], s34 offen sc1
buffer_load_dwordx4 a[52:55], v231, s[56:59], s34 offen sc1
s_add_u32 s34, s73, 0xe000
buffer_load_dwordx4 a[56:59], v224, s[56:59], s34 offen sc1
buffer_load_dwordx4 a[60:63], v231, s[56:59], s34 offen sc1
s_add_u32 s34, s73, 0x10000
buffer_load_dwordx4 a[64:67], v224, s[56:59], s34 offen sc1
buffer_load_dwordx4 a[68:71], v231, s[56:59], s34 offen sc1
s_add_u32 s69, s72, 0x1a000
v_mul_f32 v176, v176, v253
v_mul_f32 v177, v177, v253
v_mul_f32 v178, v178, v253
v_mul_f32 v179, v179, v253
ds_write_b128 v225, v[176:179] offset:16384
v_mul_f32 v180, v180, v253
v_mul_f32 v181, v181, v253
v_mul_f32 v182, v182, v253
v_mul_f32 v183, v183, v253
ds_write_b128 v225, v[180:183] offset:17408
v_mul_f32 v184, v184, v254
v_mul_f32 v185, v185, v254
v_mul_f32 v186, v186, v254
v_mul_f32 v187, v187, v254
ds_write_b128 v225, v[184:187] offset:18432
v_mul_f32 v188, v188, v254
v_mul_f32 v189, v189, v254
v_mul_f32 v190, v190, v254
v_mul_f32 v191, v191, v254
ds_write_b128 v225, v[188:191] offset:19456
s_waitcnt lgkmcnt(0)
s_barrier
ds_read_b128 v[176:179], v226 offset:16384
ds_read_b128 v[180:183], v226 offset:17408
ds_read_b128 v[184:187], v226 offset:18432
ds_read_b128 v[188:191], v226 offset:19456
ds_read_b128 a[160:163], v250 offset:0
s_waitcnt lgkmcnt(4)
v_fma_f32 v160, v160, v251, v176
v_fma_f32 v161, v161, v251, v177
v_fma_f32 v162, v162, v251, v178
v_fma_f32 v163, v163, v251, v179
s_waitcnt lgkmcnt(3)
v_fma_f32 v164, v164, v251, v180
v_fma_f32 v165, v165, v251, v181
v_fma_f32 v166, v166, v251, v182
v_fma_f32 v167, v167, v251, v183
v_cvt_pk_bf16_f32 v160, v160, v161
v_cvt_pk_bf16_f32 v161, v162, v163
v_cvt_pk_bf16_f32 v162, v164, v165
v_cvt_pk_bf16_f32 v163, v166, v167
buffer_store_dwordx4 v[160:163], v222, s[56:59], s69 offen offset:0 sc1
ds_write_b128 v226, v[160:163] offset:16384
s_waitcnt lgkmcnt(3)
v_fma_f32 v168, v168, v252, v184
v_fma_f32 v169, v169, v252, v185
v_fma_f32 v170, v170, v252, v186
v_fma_f32 v171, v171, v252, v187
s_waitcnt lgkmcnt(2)
v_fma_f32 v172, v172, v252, v188
v_fma_f32 v173, v173, v252, v189
v_fma_f32 v174, v174, v252, v190
v_fma_f32 v175, v175, v252, v191
v_cvt_pk_bf16_f32 v168, v168, v169
v_cvt_pk_bf16_f32 v169, v170, v171
v_cvt_pk_bf16_f32 v170, v172, v173
v_cvt_pk_bf16_f32 v171, v174, v175
buffer_store_dwordx4 v[168:171], v223, s[56:59], s69 offen offset:1024 sc1
ds_write_b128 v226, v[168:171] offset:17408
s_waitcnt vmcnt(0)
s_add_u32 s74, s96, 14
v_mov_b32 v240, s74
v_mov_b32 v241, s97
buffer_store_dwordx2 v[240:241], v233, s[40:43], 0 offen sc1
s_waitcnt lgkmcnt(0)
s_barrier
ds_read_b128 a[164:167], v250 offset:16384
s_mov_b32 s63, 0x400000
s_add_u32 s75, s96, 14
buffer_load_dwordx2 v[128:129], v234, s[40:43], 0 offen sc1
s_sleep 8
buffer_load_dwordx2 v[130:131], v234, s[40:43], 0 offen sc1
s_sleep 8
buffer_load_dwordx2 v[132:133], v234, s[40:43], 0 offen sc1
LSP:
s_waitcnt vmcnt(2)
v_cmp_eq_u32 vcc, s75, v128
v_cmp_eq_u32 s[64:65], s97, v129
s_nop 1
s_and_b64 vcc, vcc, s[64:65]
s_cmp_eq_u64 vcc, -1
s_cbranch_scc1 LSX
buffer_load_dwordx2 v[128:129], v234, s[40:43], 0 offen sc1
s_waitcnt vmcnt(2)
v_cmp_eq_u32 vcc, s75, v130
v_cmp_eq_u32 s[64:65], s97, v131
s_nop 1
s_and_b64 vcc, vcc, s[64:65]
s_cmp_eq_u64 vcc, -1
s_cbranch_scc1 LSX
buffer_load_dwordx2 v[130:131], v234, s[40:43], 0 offen sc1
s_waitcnt vmcnt(2)
v_cmp_eq_u32 vcc, s75, v132
v_cmp_eq_u32 s[64:65], s97, v133
s_nop 1
s_and_b64 vcc, vcc, s[64:65]
s_cmp_eq_u64 vcc, -1
s_cbranch_scc1 LSX
buffer_load_dwordx2 v[132:133], v234, s[40:43], 0 offen sc1
s_sub_u32 s63, s63, 1
s_cmp_lg_u32 s63, 0
s_cbranch_scc1 LSP
LSX:
s_add_u32 s34, s73, 0x12000
buffer_load_dwordx4 a[72:75], v224, s[56:59], s34 offen sc1
buffer_load_dwordx4 a[76:79], v231, s[56:59], s34 offen sc1
s_add_u32 s34, s73, 0x14000
buffer_load_dwordx4 a[80:83], v224, s[56:59], s34 offen sc1
buffer_load_dwordx4 a[84:87], v231, s[56:59], s34 offen sc1
s_add_u32 s34, s73, 0x16000
buffer_load_dwordx4 a[88:91], v224, s[56:59], s34 offen sc1
buffer_load_dwordx4 a[92:95], v231, s[56:59], s34 offen sc1
s_add_u32 s34, s73, 0x18000
buffer_load_dwordx4 a[96:99], v224, s[56:59], s34 offen sc1
buffer_load_dwordx4 a[100:103], v231, s[56:59], s34 offen sc1
s_add_u32 s34, s73, 0x1a000
buffer_load_dwordx4 a[104:107], v224, s[56:59], s34 offen sc1
buffer_load_dwordx4 a[108:111], v231, s[56:59], s34 offen sc1
v_mfma_f32_16x16x32_bf16 v[242:245], a[224:227], a[0:3], 0
v_mfma_f32_16x16x32_bf16 v[242:245], a[224:227], a[4:7], v[242:245]
v_mfma_f32_16x16x32_bf16 v[242:245], a[228:231], a[112:115], v[242:245]
v_mfma_f32_16x16x32_bf16 v[246:249], a[224:227], a[8:11], 0
v_mfma_f32_16x16x32_bf16 v[246:249], a[224:227], a[12:15], v[246:249]
v_mfma_f32_16x16x32_bf16 v[246:249], a[228:231], a[116:119], v[246:249]
s_nop 4
v_cvt_pk_bf16_f32 v242, v242, v243
v_cvt_pk_bf16_f32 v243, v244, v245
buffer_store_dwordx2 v[242:243], v232, s[36:39], 0 offen offset:0 
s_nop 1
v_mfma_f32_16x16x32_bf16 v[242:245], a[224:227], a[16:19], 0
v_mfma_f32_16x16x32_bf16 v[242:245], a[224:227], a[20:23], v[242:245]
v_mfma_f32_16x16x32_bf16 v[242:245], a[228:231], a[120:123], v[242:245]
v_cvt_pk_bf16_f32 v246, v246, v247
v_cvt_pk_bf16_f32 v247, v248, v249
buffer_store_dwordx2 v[246:247], v232, s[36:39], 0 offen offset:64 
s_nop 1
v_mfma_f32_16x16x32_bf16 v[246:249], a[224:227], a[24:27], 0
v_mfma_f32_16x16x32_bf16 v[246:249], a[224:227], a[28:31], v[246:249]
v_mfma_f32_16x16x32_bf16 v[246:249], a[228:231], a[124:127], v[246:249]
v_cvt_pk_bf16_f32 v242, v242, v243
v_cvt_pk_bf16_f32 v243, v244, v245
buffer_store_dwordx2 v[242:243], v232, s[36:39], 0 offen offset:128 
s_nop 1
v_mfma_f32_16x16x32_bf16 v[242:245], a[224:227], a[32:35], 0
v_mfma_f32_16x16x32_bf16 v[242:245], a[224:227], a[36:39], v[242:245]
v_mfma_f32_16x16x32_bf16 v[242:245], a[228:231], a[128:131], v[242:245]
v_cvt_pk_bf16_f32 v246, v246, v247
v_cvt_pk_bf16_f32 v247, v248, v249
buffer_store_dwordx2 v[246:247], v232, s[36:39], 0 offen offset:192 
s_nop 1
v_mfma_f32_16x16x32_bf16 v[246:249], a[224:227], a[40:43], 0
v_mfma_f32_16x16x32_bf16 v[246:249], a[224:227], a[44:47], v[246:249]
v_mfma_f32_16x16x32_bf16 v[246:249], a[228:231], a[132:135], v[246:249]
v_cvt_pk_bf16_f32 v242, v242, v243
v_cvt_pk_bf16_f32 v243, v244, v245
buffer_store_dwordx2 v[242:243], v232, s[36:39], 0 offen offset:256 
s_nop 1
v_mfma_f32_16x16x32_bf16 v[242:245], a[224:227], a[48:51], 0
v_mfma_f32_16x16x32_bf16 v[242:245], a[224:227], a[52:55], v[242:245]
v_mfma_f32_16x16x32_bf16 v[242:245], a[228:231], a[136:139], v[242:245]
v_cvt_pk_bf16_f32 v246, v246, v247
v_cvt_pk_bf16_f32 v247, v248, v249
buffer_store_dwordx2 v[246:247], v232, s[36:39], 0 offen offset:320 
s_nop 1
v_mfma_f32_16x16x32_bf16 v[246:249], a[224:227], a[56:59], 0
v_mfma_f32_16x16x32_bf16 v[246:249], a[224:227], a[60:63], v[246:249]
v_mfma_f32_16x16x32_bf16 v[246:249], a[228:231], a[140:143], v[246:249]
v_cvt_pk_bf16_f32 v242, v242, v243
v_cvt_pk_bf16_f32 v243, v244, v245
buffer_store_dwordx2 v[242:243], v232, s[36:39], 0 offen offset:384 
s_nop 1
v_mfma_f32_16x16x32_bf16 v[242:245], a[224:227], a[64:67], 0
v_mfma_f32_16x16x32_bf16 v[242:245], a[224:227], a[68:71], v[242:245]
v_mfma_f32_16x16x32_bf16 v[242:245], a[228:231], a[144:147], v[242:245]
v_cvt_pk_bf16_f32 v246, v246, v247
v_cvt_pk_bf16_f32 v247, v248, v249
buffer_store_dwordx2 v[246:247], v232, s[36:39], 0 offen offset:448 
s_waitcnt vmcnt(16)
s_nop 0
v_mfma_f32_16x16x32_bf16 v[246:249], a[224:227], a[72:75], 0
v_mfma_f32_16x16x32_bf16 v[246:249], a[224:227], a[76:79], v[246:249]
v_mfma_f32_16x16x32_bf16 v[246:249], a[228:231], a[148:151], v[246:249]
v_cvt_pk_bf16_f32 v242, v242, v243
v_cvt_pk_bf16_f32 v243, v244, v245
buffer_store_dwordx2 v[242:243], v232, s[36:39], 0 offen offset:512 
s_waitcnt vmcnt(15)
s_nop 0
v_mfma_f32_16x16x32_bf16 v[242:245], a[224:227], a[80:83], 0
v_mfma_f32_16x16x32_bf16 v[242:245], a[224:227], a[84:87], v[242:245]
v_mfma_f32_16x16x32_bf16 v[242:245], a[228:231], a[152:155], v[242:245]
v_cvt_pk_bf16_f32 v246, v246, v247
v_cvt_pk_bf16_f32 v247, v248, v249
buffer_store_dwordx2 v[246:247], v232, s[36:39], 0 offen offset:576 
s_waitcnt vmcnt(14)
s_nop 0
v_mfma_f32_16x16x32_bf16 v[246:249], a[224:227], a[88:91], 0
v_mfma_f32_16x16x32_bf16 v[246:249], a[224:227], a[92:95], v[246:249]
v_mfma_f32_16x16x32_bf16 v[246:249], a[228:231], a[156:159], v[246:249]
v_cvt_pk_bf16_f32 v242, v242, v243
v_cvt_pk_bf16_f32 v243, v244, v245
buffer_store_dwordx2 v[242:243], v232, s[36:39], 0 offen offset:640 
s_waitcnt vmcnt(13)
s_nop 0
v_mfma_f32_16x16x32_bf16 v[242:245], a[224:227], a[96:99], 0
v_mfma_f32_16x16x32_bf16 v[242:245], a[224:227], a[100:103], v[242:245]
v_mfma_f32_16x16x32_bf16 v[242:245], a[228:231], a[160:163], v[242:245]
v_cvt_pk_bf16_f32 v246, v246, v247
v_cvt_pk_bf16_f32 v247, v248, v249
buffer_store_dwordx2 v[246:247], v232, s[36:39], 0 offen offset:704 
s_waitcnt vmcnt(12)
s_nop 0
v_mfma_f32_16x16x32_bf16 v[246:249], a[224:227], a[104:107], 0
v_mfma_f32_16x16x32_bf16 v[246:249], a[224:227], a[108:111], v[246:249]
s_waitcnt lgkmcnt(0)
v_mfma_f32_16x16x32_bf16 v[246:249], a[228:231], a[164:167], v[246:249]
v_cvt_pk_bf16_f32 v242, v242, v243
v_cvt_pk_bf16_f32 v243, v244, v245
buffer_store_dwordx2 v[242:243], v232, s[36:39], 0 offen offset:768 
s_nop 4
v_cvt_pk_bf16_f32 v246, v246, v247
v_cvt_pk_bf16_f32 v247, v248, v249
buffer_store_dwordx2 v[246:247], v232, s[36:39], 0 offen offset:832 
s_cmp_lg_u32 %11, 0
s_cbranch_scc1 LNE
s_cmp_lg_u32 %6, 0
s_cbranch_scc1 LNE
s_add_u32 s74, s96, 16
s_addc_u32 s75, s97, 0
v_mov_b32 v0, s74
v_mov_b32 v1, s75
v_mov_b32 v2, 0
global_store_dwordx2 v2, v[0:1], %10 offset:512 sc1
LNE:
s_branch LEND
LR1:
s_swappc_b64 s[64:65], s[70:71]
s_branch LRB1
LS1:
s_swappc_b64 s[64:65], s[66:67]
s_branch LSB1
LR2:
s_swappc_b64 s[64:65], s[70:71]
s_branch LRB2
LS2:
s_swappc_b64 s[64:65], s[66:67]
s_branch LSB2
LR3:
s_swappc_b64 s[64:65], s[70:71]
s_branch LRB3
LS3:
s_swappc_b64 s[64:65], s[66:67]
s_branch LSB3
LR4:
s_swappc_b64 s[64:65], s[70:71]
s_branch LRB4
LS4:
s_swappc_b64 s[64:65], s[66:67]
s_branch LSB4
LR5:
s_swappc_b64 s[64:65], s[70:71]
s_branch LRB5
LS5:
s_swappc_b64 s[64:65], s[66:67]
s_branch LSB5
LR6:
s_swappc_b64 s[64:65], s[70:71]
s_branch LRB6
LS6:
s_swappc_b64 s[64:65], s[66:67]
s_branch LSB6
LR7:
s_swappc_b64 s[64:65], s[70:71]
s_branch LRB7
LS7:
s_swappc_b64 s[64:65], s[66:67]
s_branch LSB7
LR8:
s_swappc_b64 s[64:65], s[70:71]
s_branch LRB8
LS8:
s_swappc_b64 s[64:65], s[66:67]
s_branch LSB8
LR9:
s_swappc_b64 s[64:65], s[70:71]
s_branch LRB9
LS9:
s_swappc_b64 s[64:65], s[66:67]
s_branch LSB9
LR10:
s_swappc_b64 s[64:65], s[70:71]
s_branch LRB10
LS10:
s_swappc_b64 s[64:65], s[66:67]
s_branch LSB10
LR11:
s_swappc_b64 s[64:65], s[70:71]
s_branch LRB11
LS11:
s_swappc_b64 s[64:65], s[66:67]
s_branch LSB11
LR12:
s_swappc_b64 s[64:65], s[70:71]
s_branch LRB12
LS12:
s_swappc_b64 s[64:65], s[66:67]
s_branch LSB12
LR13:
s_swappc_b64 s[64:65], s[70:71]
s_branch LRB13
LS13:
s_swappc_b64 s[64:65], s[66:67]
s_branch LSB13
LR14:
s_swappc_b64 s[64:65], s[70:71]
s_branch LRB14
LS14:
s_swappc_b64 s[64:65], s[66:67]
s_branch LSB14
LR15:
s_swappc_b64 s[64:65], s[70:71]
s_branch LRB15
LS15:
s_swappc_b64 s[64:65], s[66:67]
s_branch LSB15
LP0:
s_mov_b32 s63, 0x400000
LQ0:
s_sleep 1
buffer_load_dwordx2 v[238:239], v234, s[40:43], 0 offen sc1
s_waitcnt vmcnt(0)
s_add_u32 s75, s96, 3
v_subrev_u32 v238, s75, v238
v_cmp_ge_u32 vcc, 11, v238
v_cmp_eq_u32 s[64:65], s97, v239
s_nop 1
s_and_b64 vcc, vcc, s[64:65]
s_cmp_eq_u64 vcc, -1
s_cbranch_scc1 LPB0
s_sub_u32 s63, s63, 1
s_cmp_lg_u32 s63, 0
s_cbranch_scc1 LQ0
s_branch LPB0
LP1:
s_mov_b32 s63, 0x400000
LQ1:
s_sleep 1
buffer_load_dwordx2 v[238:239], v234, s[40:43], 0 offen sc1
s_waitcnt vmcnt(0)
s_add_u32 s75, s96, 6
v_subrev_u32 v238, s75, v238
v_cmp_ge_u32 vcc, 8, v238
v_cmp_eq_u32 s[64:65], s97, v239
s_nop 1
s_and_b64 vcc, vcc, s[64:65]
s_cmp_eq_u64 vcc, -1
s_cbranch_scc1 LPB1
s_sub_u32 s63, s63, 1
s_cmp_lg_u32 s63, 0
s_cbranch_scc1 LQ1
s_branch LPB1
LP2:
s_mov_b32 s63, 0x400000
LQ2:
s_sleep 1
buffer_load_dwordx2 v[238:239], v234, s[40:43], 0 offen sc1
s_waitcnt vmcnt(0)
s_add_u32 s75, s96, 9
v_subrev_u32 v238, s75, v238
v_cmp_ge_u32 vcc, 5, v238
v_cmp_eq_u32 s[64:65], s97, v239
s_nop 1
s_and_b64 vcc, vcc, s[64:65]
s_cmp_eq_u64 vcc, -1
s_cbranch_scc1 LPB2
s_sub_u32 s63, s63, 1
s_cmp_lg_u32 s63, 0
s_cbranch_scc1 LQ2
s_branch LPB2
LUPD:
s_nop 1
v_max_f32 v255, v237, v247
v_sub_f32 v248, v237, v255
v_mul_f32 v248, s62, v248
v_exp_f32 v251, v248
v_exp_f32 v252, v248
v_mov_b32 v237, v255
v_add_f32 v238, s68, v255
v_mul_f32 v239, s62, v255
v_mul_f32 v240, s62, v255
s_nop 2
v_permlane16_swap_b32 v251, v252
s_nop 1
v_mov_b32 v253, v251
v_mov_b32 v254, v252
s_nop 2
v_permlane32_swap_b32 v251, v253
v_permlane32_swap_b32 v252, v254
s_nop 2
s_nop 2
v_permlane16_swap_b32 v239, v240
s_nop 1
v_mov_b32 v241, v239
v_mov_b32 v242, v240
s_nop 2
v_permlane32_swap_b32 v239, v241
v_permlane32_swap_b32 v240, v242
s_nop 2
v_mul_f32 v243, v243, v251
v_mul_f32 v244, v244, v252
v_mul_f32 v245, v245, v253
v_mul_f32 v246, v246, v254
s_mov_b32 s63, 1
s_nop 2
s_setpc_b64 s[64:65]
LRES:
s_nop 7
s_nop 7
v_accvgpr_read_b32 v247, a0
v_accvgpr_read_b32 v248, a1
v_accvgpr_read_b32 v249, a2
v_accvgpr_read_b32 v250, a3
v_mul_f32 v247, v247, v251
v_mul_f32 v248, v248, v251
v_mul_f32 v249, v249, v251
v_mul_f32 v250, v250, v251
v_accvgpr_write_b32 a0, v247
v_accvgpr_write_b32 a1, v248
v_accvgpr_write_b32 a2, v249
v_accvgpr_write_b32 a3, v250
v_accvgpr_read_b32 v247, a4
v_accvgpr_read_b32 v248, a5
v_accvgpr_read_b32 v249, a6
v_accvgpr_read_b32 v250, a7
v_mul_f32 v247, v247, v252
v_mul_f32 v248, v248, v252
v_mul_f32 v249, v249, v252
v_mul_f32 v250, v250, v252
v_accvgpr_write_b32 a4, v247
v_accvgpr_write_b32 a5, v248
v_accvgpr_write_b32 a6, v249
v_accvgpr_write_b32 a7, v250
v_accvgpr_read_b32 v247, a8
v_accvgpr_read_b32 v248, a9
v_accvgpr_read_b32 v249, a10
v_accvgpr_read_b32 v250, a11
v_mul_f32 v247, v247, v253
v_mul_f32 v248, v248, v253
v_mul_f32 v249, v249, v253
v_mul_f32 v250, v250, v253
v_accvgpr_write_b32 a8, v247
v_accvgpr_write_b32 a9, v248
v_accvgpr_write_b32 a10, v249
v_accvgpr_write_b32 a11, v250
v_accvgpr_read_b32 v247, a12
v_accvgpr_read_b32 v248, a13
v_accvgpr_read_b32 v249, a14
v_accvgpr_read_b32 v250, a15
v_mul_f32 v247, v247, v254
v_mul_f32 v248, v248, v254
v_mul_f32 v249, v249, v254
v_mul_f32 v250, v250, v254
v_accvgpr_write_b32 a12, v247
v_accvgpr_write_b32 a13, v248
v_accvgpr_write_b32 a14, v249
v_accvgpr_write_b32 a15, v250
v_accvgpr_read_b32 v247, a16
v_accvgpr_read_b32 v248, a17
v_accvgpr_read_b32 v249, a18
v_accvgpr_read_b32 v250, a19
v_mul_f32 v247, v247, v251
v_mul_f32 v248, v248, v251
v_mul_f32 v249, v249, v251
v_mul_f32 v250, v250, v251
v_accvgpr_write_b32 a16, v247
v_accvgpr_write_b32 a17, v248
v_accvgpr_write_b32 a18, v249
v_accvgpr_write_b32 a19, v250
v_accvgpr_read_b32 v247, a20
v_accvgpr_read_b32 v248, a21
v_accvgpr_read_b32 v249, a22
v_accvgpr_read_b32 v250, a23
v_mul_f32 v247, v247, v252
v_mul_f32 v248, v248, v252
v_mul_f32 v249, v249, v252
v_mul_f32 v250, v250, v252
v_accvgpr_write_b32 a20, v247
v_accvgpr_write_b32 a21, v248
v_accvgpr_write_b32 a22, v249
v_accvgpr_write_b32 a23, v250
v_accvgpr_read_b32 v247, a24
v_accvgpr_read_b32 v248, a25
v_accvgpr_read_b32 v249, a26
v_accvgpr_read_b32 v250, a27
v_mul_f32 v247, v247, v253
v_mul_f32 v248, v248, v253
v_mul_f32 v249, v249, v253
v_mul_f32 v250, v250, v253
v_accvgpr_write_b32 a24, v247
v_accvgpr_write_b32 a25, v248
v_accvgpr_write_b32 a26, v249
v_accvgpr_write_b32 a27, v250
v_accvgpr_read_b32 v247, a28
v_accvgpr_read_b32 v248, a29
v_accvgpr_read_b32 v249, a30
v_accvgpr_read_b32 v250, a31
v_mul_f32 v247, v247, v254
v_mul_f32 v248, v248, v254
v_mul_f32 v249, v249, v254
v_mul_f32 v250, v250, v254
v_accvgpr_write_b32 a28, v247
v_accvgpr_write_b32 a29, v248
v_accvgpr_write_b32 a30, v249
v_accvgpr_write_b32 a31, v250
v_accvgpr_read_b32 v247, a32
v_accvgpr_read_b32 v248, a33
v_accvgpr_read_b32 v249, a34
v_accvgpr_read_b32 v250, a35
v_mul_f32 v247, v247, v251
v_mul_f32 v248, v248, v251
v_mul_f32 v249, v249, v251
v_mul_f32 v250, v250, v251
v_accvgpr_write_b32 a32, v247
v_accvgpr_write_b32 a33, v248
v_accvgpr_write_b32 a34, v249
v_accvgpr_write_b32 a35, v250
v_accvgpr_read_b32 v247, a36
v_accvgpr_read_b32 v248, a37
v_accvgpr_read_b32 v249, a38
v_accvgpr_read_b32 v250, a39
v_mul_f32 v247, v247, v252
v_mul_f32 v248, v248, v252
v_mul_f32 v249, v249, v252
v_mul_f32 v250, v250, v252
v_accvgpr_write_b32 a36, v247
v_accvgpr_write_b32 a37, v248
v_accvgpr_write_b32 a38, v249
v_accvgpr_write_b32 a39, v250
v_accvgpr_read_b32 v247, a40
v_accvgpr_read_b32 v248, a41
v_accvgpr_read_b32 v249, a42
v_accvgpr_read_b32 v250, a43
v_mul_f32 v247, v247, v253
v_mul_f32 v248, v248, v253
v_mul_f32 v249, v249, v253
v_mul_f32 v250, v250, v253
v_accvgpr_write_b32 a40, v247
v_accvgpr_write_b32 a41, v248
v_accvgpr_write_b32 a42, v249
v_accvgpr_write_b32 a43, v250
v_accvgpr_read_b32 v247, a44
v_accvgpr_read_b32 v248, a45
v_accvgpr_read_b32 v249, a46
v_accvgpr_read_b32 v250, a47
v_mul_f32 v247, v247, v254
v_mul_f32 v248, v248, v254
v_mul_f32 v249, v249, v254
v_mul_f32 v250, v250, v254
v_accvgpr_write_b32 a44, v247
v_accvgpr_write_b32 a45, v248
v_accvgpr_write_b32 a46, v249
v_accvgpr_write_b32 a47, v250
v_accvgpr_read_b32 v247, a48
v_accvgpr_read_b32 v248, a49
v_accvgpr_read_b32 v249, a50
v_accvgpr_read_b32 v250, a51
v_mul_f32 v247, v247, v251
v_mul_f32 v248, v248, v251
v_mul_f32 v249, v249, v251
v_mul_f32 v250, v250, v251
v_accvgpr_write_b32 a48, v247
v_accvgpr_write_b32 a49, v248
v_accvgpr_write_b32 a50, v249
v_accvgpr_write_b32 a51, v250
v_accvgpr_read_b32 v247, a52
v_accvgpr_read_b32 v248, a53
v_accvgpr_read_b32 v249, a54
v_accvgpr_read_b32 v250, a55
v_mul_f32 v247, v247, v252
v_mul_f32 v248, v248, v252
v_mul_f32 v249, v249, v252
v_mul_f32 v250, v250, v252
v_accvgpr_write_b32 a52, v247
v_accvgpr_write_b32 a53, v248
v_accvgpr_write_b32 a54, v249
v_accvgpr_write_b32 a55, v250
v_accvgpr_read_b32 v247, a56
v_accvgpr_read_b32 v248, a57
v_accvgpr_read_b32 v249, a58
v_accvgpr_read_b32 v250, a59
v_mul_f32 v247, v247, v253
v_mul_f32 v248, v248, v253
v_mul_f32 v249, v249, v253
v_mul_f32 v250, v250, v253
v_accvgpr_write_b32 a56, v247
v_accvgpr_write_b32 a57, v248
v_accvgpr_write_b32 a58, v249
v_accvgpr_write_b32 a59, v250
v_accvgpr_read_b32 v247, a60
v_accvgpr_read_b32 v248, a61
v_accvgpr_read_b32 v249, a62
v_accvgpr_read_b32 v250, a63
v_mul_f32 v247, v247, v254
v_mul_f32 v248, v248, v254
v_mul_f32 v249, v249, v254
v_mul_f32 v250, v250, v254
v_accvgpr_write_b32 a60, v247
v_accvgpr_write_b32 a61, v248
v_accvgpr_write_b32 a62, v249
v_accvgpr_write_b32 a63, v250
v_accvgpr_read_b32 v247, a64
v_accvgpr_read_b32 v248, a65
v_accvgpr_read_b32 v249, a66
v_accvgpr_read_b32 v250, a67
v_mul_f32 v247, v247, v251
v_mul_f32 v248, v248, v251
v_mul_f32 v249, v249, v251
v_mul_f32 v250, v250, v251
v_accvgpr_write_b32 a64, v247
v_accvgpr_write_b32 a65, v248
v_accvgpr_write_b32 a66, v249
v_accvgpr_write_b32 a67, v250
v_accvgpr_read_b32 v247, a68
v_accvgpr_read_b32 v248, a69
v_accvgpr_read_b32 v249, a70
v_accvgpr_read_b32 v250, a71
v_mul_f32 v247, v247, v252
v_mul_f32 v248, v248, v252
v_mul_f32 v249, v249, v252
v_mul_f32 v250, v250, v252
v_accvgpr_write_b32 a68, v247
v_accvgpr_write_b32 a69, v248
v_accvgpr_write_b32 a70, v249
v_accvgpr_write_b32 a71, v250
v_accvgpr_read_b32 v247, a72
v_accvgpr_read_b32 v248, a73
v_accvgpr_read_b32 v249, a74
v_accvgpr_read_b32 v250, a75
v_mul_f32 v247, v247, v253
v_mul_f32 v248, v248, v253
v_mul_f32 v249, v249, v253
v_mul_f32 v250, v250, v253
v_accvgpr_write_b32 a72, v247
v_accvgpr_write_b32 a73, v248
v_accvgpr_write_b32 a74, v249
v_accvgpr_write_b32 a75, v250
v_accvgpr_read_b32 v247, a76
v_accvgpr_read_b32 v248, a77
v_accvgpr_read_b32 v249, a78
v_accvgpr_read_b32 v250, a79
v_mul_f32 v247, v247, v254
v_mul_f32 v248, v248, v254
v_mul_f32 v249, v249, v254
v_mul_f32 v250, v250, v254
v_accvgpr_write_b32 a76, v247
v_accvgpr_write_b32 a77, v248
v_accvgpr_write_b32 a78, v249
v_accvgpr_write_b32 a79, v250
v_accvgpr_read_b32 v247, a80
v_accvgpr_read_b32 v248, a81
v_accvgpr_read_b32 v249, a82
v_accvgpr_read_b32 v250, a83
v_mul_f32 v247, v247, v251
v_mul_f32 v248, v248, v251
v_mul_f32 v249, v249, v251
v_mul_f32 v250, v250, v251
v_accvgpr_write_b32 a80, v247
v_accvgpr_write_b32 a81, v248
v_accvgpr_write_b32 a82, v249
v_accvgpr_write_b32 a83, v250
v_accvgpr_read_b32 v247, a84
v_accvgpr_read_b32 v248, a85
v_accvgpr_read_b32 v249, a86
v_accvgpr_read_b32 v250, a87
v_mul_f32 v247, v247, v252
v_mul_f32 v248, v248, v252
v_mul_f32 v249, v249, v252
v_mul_f32 v250, v250, v252
v_accvgpr_write_b32 a84, v247
v_accvgpr_write_b32 a85, v248
v_accvgpr_write_b32 a86, v249
v_accvgpr_write_b32 a87, v250
v_accvgpr_read_b32 v247, a88
v_accvgpr_read_b32 v248, a89
v_accvgpr_read_b32 v249, a90
v_accvgpr_read_b32 v250, a91
v_mul_f32 v247, v247, v253
v_mul_f32 v248, v248, v253
v_mul_f32 v249, v249, v253
v_mul_f32 v250, v250, v253
v_accvgpr_write_b32 a88, v247
v_accvgpr_write_b32 a89, v248
v_accvgpr_write_b32 a90, v249
v_accvgpr_write_b32 a91, v250
v_accvgpr_read_b32 v247, a92
v_accvgpr_read_b32 v248, a93
v_accvgpr_read_b32 v249, a94
v_accvgpr_read_b32 v250, a95
v_mul_f32 v247, v247, v254
v_mul_f32 v248, v248, v254
v_mul_f32 v249, v249, v254
v_mul_f32 v250, v250, v254
v_accvgpr_write_b32 a92, v247
v_accvgpr_write_b32 a93, v248
v_accvgpr_write_b32 a94, v249
v_accvgpr_write_b32 a95, v250
v_accvgpr_read_b32 v247, a96
v_accvgpr_read_b32 v248, a97
v_accvgpr_read_b32 v249, a98
v_accvgpr_read_b32 v250, a99
v_mul_f32 v247, v247, v251
v_mul_f32 v248, v248, v251
v_mul_f32 v249, v249, v251
v_mul_f32 v250, v250, v251
v_accvgpr_write_b32 a96, v247
v_accvgpr_write_b32 a97, v248
v_accvgpr_write_b32 a98, v249
v_accvgpr_write_b32 a99, v250
v_accvgpr_read_b32 v247, a100
v_accvgpr_read_b32 v248, a101
v_accvgpr_read_b32 v249, a102
v_accvgpr_read_b32 v250, a103
v_mul_f32 v247, v247, v252
v_mul_f32 v248, v248, v252
v_mul_f32 v249, v249, v252
v_mul_f32 v250, v250, v252
v_accvgpr_write_b32 a100, v247
v_accvgpr_write_b32 a101, v248
v_accvgpr_write_b32 a102, v249
v_accvgpr_write_b32 a103, v250
v_accvgpr_read_b32 v247, a104
v_accvgpr_read_b32 v248, a105
v_accvgpr_read_b32 v249, a106
v_accvgpr_read_b32 v250, a107
v_mul_f32 v247, v247, v253
v_mul_f32 v248, v248, v253
v_mul_f32 v249, v249, v253
v_mul_f32 v250, v250, v253
v_accvgpr_write_b32 a104, v247
v_accvgpr_write_b32 a105, v248
v_accvgpr_write_b32 a106, v249
v_accvgpr_write_b32 a107, v250
v_accvgpr_read_b32 v247, a108
v_accvgpr_read_b32 v248, a109
v_accvgpr_read_b32 v249, a110
v_accvgpr_read_b32 v250, a111
v_mul_f32 v247, v247, v254
v_mul_f32 v248, v248, v254
v_mul_f32 v249, v249, v254
v_mul_f32 v250, v250, v254
v_accvgpr_write_b32 a108, v247
v_accvgpr_write_b32 a109, v248
v_accvgpr_write_b32 a110, v249
v_accvgpr_write_b32 a111, v250
v_accvgpr_read_b32 v247, a112
v_accvgpr_read_b32 v248, a113
v_accvgpr_read_b32 v249, a114
v_accvgpr_read_b32 v250, a115
v_mul_f32 v247, v247, v251
v_mul_f32 v248, v248, v251
v_mul_f32 v249, v249, v251
v_mul_f32 v250, v250, v251
v_accvgpr_write_b32 a112, v247
v_accvgpr_write_b32 a113, v248
v_accvgpr_write_b32 a114, v249
v_accvgpr_write_b32 a115, v250
v_accvgpr_read_b32 v247, a116
v_accvgpr_read_b32 v248, a117
v_accvgpr_read_b32 v249, a118
v_accvgpr_read_b32 v250, a119
v_mul_f32 v247, v247, v252
v_mul_f32 v248, v248, v252
v_mul_f32 v249, v249, v252
v_mul_f32 v250, v250, v252
v_accvgpr_write_b32 a116, v247
v_accvgpr_write_b32 a117, v248
v_accvgpr_write_b32 a118, v249
v_accvgpr_write_b32 a119, v250
v_accvgpr_read_b32 v247, a120
v_accvgpr_read_b32 v248, a121
v_accvgpr_read_b32 v249, a122
v_accvgpr_read_b32 v250, a123
v_mul_f32 v247, v247, v253
v_mul_f32 v248, v248, v253
v_mul_f32 v249, v249, v253
v_mul_f32 v250, v250, v253
v_accvgpr_write_b32 a120, v247
v_accvgpr_write_b32 a121, v248
v_accvgpr_write_b32 a122, v249
v_accvgpr_write_b32 a123, v250
v_accvgpr_read_b32 v247, a124
v_accvgpr_read_b32 v248, a125
v_accvgpr_read_b32 v249, a126
v_accvgpr_read_b32 v250, a127
v_mul_f32 v247, v247, v254
v_mul_f32 v248, v248, v254
v_mul_f32 v249, v249, v254
v_mul_f32 v250, v250, v254
v_accvgpr_write_b32 a124, v247
v_accvgpr_write_b32 a125, v248
v_accvgpr_write_b32 a126, v249
v_accvgpr_write_b32 a127, v250
v_accvgpr_read_b32 v247, a128
v_accvgpr_read_b32 v248, a129
v_accvgpr_read_b32 v249, a130
v_accvgpr_read_b32 v250, a131
v_mul_f32 v247, v247, v251
v_mul_f32 v248, v248, v251
v_mul_f32 v249, v249, v251
v_mul_f32 v250, v250, v251
v_accvgpr_write_b32 a128, v247
v_accvgpr_write_b32 a129, v248
v_accvgpr_write_b32 a130, v249
v_accvgpr_write_b32 a131, v250
v_accvgpr_read_b32 v247, a132
v_accvgpr_read_b32 v248, a133
v_accvgpr_read_b32 v249, a134
v_accvgpr_read_b32 v250, a135
v_mul_f32 v247, v247, v252
v_mul_f32 v248, v248, v252
v_mul_f32 v249, v249, v252
v_mul_f32 v250, v250, v252
v_accvgpr_write_b32 a132, v247
v_accvgpr_write_b32 a133, v248
v_accvgpr_write_b32 a134, v249
v_accvgpr_write_b32 a135, v250
v_accvgpr_read_b32 v247, a136
v_accvgpr_read_b32 v248, a137
v_accvgpr_read_b32 v249, a138
v_accvgpr_read_b32 v250, a139
v_mul_f32 v247, v247, v253
v_mul_f32 v248, v248, v253
v_mul_f32 v249, v249, v253
v_mul_f32 v250, v250, v253
v_accvgpr_write_b32 a136, v247
v_accvgpr_write_b32 a137, v248
v_accvgpr_write_b32 a138, v249
v_accvgpr_write_b32 a139, v250
v_accvgpr_read_b32 v247, a140
v_accvgpr_read_b32 v248, a141
v_accvgpr_read_b32 v249, a142
v_accvgpr_read_b32 v250, a143
v_mul_f32 v247, v247, v254
v_mul_f32 v248, v248, v254
v_mul_f32 v249, v249, v254
v_mul_f32 v250, v250, v254
v_accvgpr_write_b32 a140, v247
v_accvgpr_write_b32 a141, v248
v_accvgpr_write_b32 a142, v249
v_accvgpr_write_b32 a143, v250
v_accvgpr_read_b32 v247, a144
v_accvgpr_read_b32 v248, a145
v_accvgpr_read_b32 v249, a146
v_accvgpr_read_b32 v250, a147
v_mul_f32 v247, v247, v251
v_mul_f32 v248, v248, v251
v_mul_f32 v249, v249, v251
v_mul_f32 v250, v250, v251
v_accvgpr_write_b32 a144, v247
v_accvgpr_write_b32 a145, v248
v_accvgpr_write_b32 a146, v249
v_accvgpr_write_b32 a147, v250
v_accvgpr_read_b32 v247, a148
v_accvgpr_read_b32 v248, a149
v_accvgpr_read_b32 v249, a150
v_accvgpr_read_b32 v250, a151
v_mul_f32 v247, v247, v252
v_mul_f32 v248, v248, v252
v_mul_f32 v249, v249, v252
v_mul_f32 v250, v250, v252
v_accvgpr_write_b32 a148, v247
v_accvgpr_write_b32 a149, v248
v_accvgpr_write_b32 a150, v249
v_accvgpr_write_b32 a151, v250
v_accvgpr_read_b32 v247, a152
v_accvgpr_read_b32 v248, a153
v_accvgpr_read_b32 v249, a154
v_accvgpr_read_b32 v250, a155
v_mul_f32 v247, v247, v253
v_mul_f32 v248, v248, v253
v_mul_f32 v249, v249, v253
v_mul_f32 v250, v250, v253
v_accvgpr_write_b32 a152, v247
v_accvgpr_write_b32 a153, v248
v_accvgpr_write_b32 a154, v249
v_accvgpr_write_b32 a155, v250
v_accvgpr_read_b32 v247, a156
v_accvgpr_read_b32 v248, a157
v_accvgpr_read_b32 v249, a158
v_accvgpr_read_b32 v250, a159
v_mul_f32 v247, v247, v254
v_mul_f32 v248, v248, v254
v_mul_f32 v249, v249, v254
v_mul_f32 v250, v250, v254
v_accvgpr_write_b32 a156, v247
v_accvgpr_write_b32 a157, v248
v_accvgpr_write_b32 a158, v249
v_accvgpr_write_b32 a159, v250
v_accvgpr_read_b32 v247, a160
v_accvgpr_read_b32 v248, a161
v_accvgpr_read_b32 v249, a162
v_accvgpr_read_b32 v250, a163
v_mul_f32 v247, v247, v251
v_mul_f32 v248, v248, v251
v_mul_f32 v249, v249, v251
v_mul_f32 v250, v250, v251
v_accvgpr_write_b32 a160, v247
v_accvgpr_write_b32 a161, v248
v_accvgpr_write_b32 a162, v249
v_accvgpr_write_b32 a163, v250
v_accvgpr_read_b32 v247, a164
v_accvgpr_read_b32 v248, a165
v_accvgpr_read_b32 v249, a166
v_accvgpr_read_b32 v250, a167
v_mul_f32 v247, v247, v252
v_mul_f32 v248, v248, v252
v_mul_f32 v249, v249, v252
v_mul_f32 v250, v250, v252
v_accvgpr_write_b32 a164, v247
v_accvgpr_write_b32 a165, v248
v_accvgpr_write_b32 a166, v249
v_accvgpr_write_b32 a167, v250
v_accvgpr_read_b32 v247, a168
v_accvgpr_read_b32 v248, a169
v_accvgpr_read_b32 v249, a170
v_accvgpr_read_b32 v250, a171
v_mul_f32 v247, v247, v253
v_mul_f32 v248, v248, v253
v_mul_f32 v249, v249, v253
v_mul_f32 v250, v250, v253
v_accvgpr_write_b32 a168, v247
v_accvgpr_write_b32 a169, v248
v_accvgpr_write_b32 a170, v249
v_accvgpr_write_b32 a171, v250
v_accvgpr_read_b32 v247, a172
v_accvgpr_read_b32 v248, a173
v_accvgpr_read_b32 v249, a174
v_accvgpr_read_b32 v250, a175
v_mul_f32 v247, v247, v254
v_mul_f32 v248, v248, v254
v_mul_f32 v249, v249, v254
v_mul_f32 v250, v250, v254
v_accvgpr_write_b32 a172, v247
v_accvgpr_write_b32 a173, v248
v_accvgpr_write_b32 a174, v249
v_accvgpr_write_b32 a175, v250
v_accvgpr_read_b32 v247, a176
v_accvgpr_read_b32 v248, a177
v_accvgpr_read_b32 v249, a178
v_accvgpr_read_b32 v250, a179
v_mul_f32 v247, v247, v251
v_mul_f32 v248, v248, v251
v_mul_f32 v249, v249, v251
v_mul_f32 v250, v250, v251
v_accvgpr_write_b32 a176, v247
v_accvgpr_write_b32 a177, v248
v_accvgpr_write_b32 a178, v249
v_accvgpr_write_b32 a179, v250
v_accvgpr_read_b32 v247, a180
v_accvgpr_read_b32 v248, a181
v_accvgpr_read_b32 v249, a182
v_accvgpr_read_b32 v250, a183
v_mul_f32 v247, v247, v252
v_mul_f32 v248, v248, v252
v_mul_f32 v249, v249, v252
v_mul_f32 v250, v250, v252
v_accvgpr_write_b32 a180, v247
v_accvgpr_write_b32 a181, v248
v_accvgpr_write_b32 a182, v249
v_accvgpr_write_b32 a183, v250
v_accvgpr_read_b32 v247, a184
v_accvgpr_read_b32 v248, a185
v_accvgpr_read_b32 v249, a186
v_accvgpr_read_b32 v250, a187
v_mul_f32 v247, v247, v253
v_mul_f32 v248, v248, v253
v_mul_f32 v249, v249, v253
v_mul_f32 v250, v250, v253
v_accvgpr_write_b32 a184, v247
v_accvgpr_write_b32 a185, v248
v_accvgpr_write_b32 a186, v249
v_accvgpr_write_b32 a187, v250
v_accvgpr_read_b32 v247, a188
v_accvgpr_read_b32 v248, a189
v_accvgpr_read_b32 v249, a190
v_accvgpr_read_b32 v250, a191
v_mul_f32 v247, v247, v254
v_mul_f32 v248, v248, v254
v_mul_f32 v249, v249, v254
v_mul_f32 v250, v250, v254
v_accvgpr_write_b32 a188, v247
v_accvgpr_write_b32 a189, v248
v_accvgpr_write_b32 a190, v249
v_accvgpr_write_b32 a191, v250
v_accvgpr_read_b32 v247, a192
v_accvgpr_read_b32 v248, a193
v_accvgpr_read_b32 v249, a194
v_accvgpr_read_b32 v250, a195
v_mul_f32 v247, v247, v251
v_mul_f32 v248, v248, v251
v_mul_f32 v249, v249, v251
v_mul_f32 v250, v250, v251
v_accvgpr_write_b32 a192, v247
v_accvgpr_write_b32 a193, v248
v_accvgpr_write_b32 a194, v249
v_accvgpr_write_b32 a195, v250
v_accvgpr_read_b32 v247, a196
v_accvgpr_read_b32 v248, a197
v_accvgpr_read_b32 v249, a198
v_accvgpr_read_b32 v250, a199
v_mul_f32 v247, v247, v252
v_mul_f32 v248, v248, v252
v_mul_f32 v249, v249, v252
v_mul_f32 v250, v250, v252
v_accvgpr_write_b32 a196, v247
v_accvgpr_write_b32 a197, v248
v_accvgpr_write_b32 a198, v249
v_accvgpr_write_b32 a199, v250
v_accvgpr_read_b32 v247, a200
v_accvgpr_read_b32 v248, a201
v_accvgpr_read_b32 v249, a202
v_accvgpr_read_b32 v250, a203
v_mul_f32 v247, v247, v253
v_mul_f32 v248, v248, v253
v_mul_f32 v249, v249, v253
v_mul_f32 v250, v250, v253
v_accvgpr_write_b32 a200, v247
v_accvgpr_write_b32 a201, v248
v_accvgpr_write_b32 a202, v249
v_accvgpr_write_b32 a203, v250
v_accvgpr_read_b32 v247, a204
v_accvgpr_read_b32 v248, a205
v_accvgpr_read_b32 v249, a206
v_accvgpr_read_b32 v250, a207
v_mul_f32 v247, v247, v254
v_mul_f32 v248, v248, v254
v_mul_f32 v249, v249, v254
v_mul_f32 v250, v250, v254
v_accvgpr_write_b32 a204, v247
v_accvgpr_write_b32 a205, v248
v_accvgpr_write_b32 a206, v249
v_accvgpr_write_b32 a207, v250
v_accvgpr_read_b32 v247, a208
v_accvgpr_read_b32 v248, a209
v_accvgpr_read_b32 v249, a210
v_accvgpr_read_b32 v250, a211
v_mul_f32 v247, v247, v251
v_mul_f32 v248, v248, v251
v_mul_f32 v249, v249, v251
v_mul_f32 v250, v250, v251
v_accvgpr_write_b32 a208, v247
v_accvgpr_write_b32 a209, v248
v_accvgpr_write_b32 a210, v249
v_accvgpr_write_b32 a211, v250
v_accvgpr_read_b32 v247, a212
v_accvgpr_read_b32 v248, a213
v_accvgpr_read_b32 v249, a214
v_accvgpr_read_b32 v250, a215
v_mul_f32 v247, v247, v252
v_mul_f32 v248, v248, v252
v_mul_f32 v249, v249, v252
v_mul_f32 v250, v250, v252
v_accvgpr_write_b32 a212, v247
v_accvgpr_write_b32 a213, v248
v_accvgpr_write_b32 a214, v249
v_accvgpr_write_b32 a215, v250
v_accvgpr_read_b32 v247, a216
v_accvgpr_read_b32 v248, a217
v_accvgpr_read_b32 v249, a218
v_accvgpr_read_b32 v250, a219
v_mul_f32 v247, v247, v253
v_mul_f32 v248, v248, v253
v_mul_f32 v249, v249, v253
v_mul_f32 v250, v250, v253
v_accvgpr_write_b32 a216, v247
v_accvgpr_write_b32 a217, v248
v_accvgpr_write_b32 a218, v249
v_accvgpr_write_b32 a219, v250
v_accvgpr_read_b32 v247, a220
v_accvgpr_read_b32 v248, a221
v_accvgpr_read_b32 v249, a222
v_accvgpr_read_b32 v250, a223
v_mul_f32 v247, v247, v254
v_mul_f32 v248, v248, v254
v_mul_f32 v249, v249, v254
v_mul_f32 v250, v250, v254
v_accvgpr_write_b32 a220, v247
v_accvgpr_write_b32 a221, v248
v_accvgpr_write_b32 a222, v249
v_accvgpr_write_b32 a223, v250
v_accvgpr_read_b32 v247, a224
v_accvgpr_read_b32 v248, a225
v_accvgpr_read_b32 v249, a226
v_accvgpr_read_b32 v250, a227
v_mul_f32 v247, v247, v251
v_mul_f32 v248, v248, v251
v_mul_f32 v249, v249, v251
v_mul_f32 v250, v250, v251
v_accvgpr_write_b32 a224, v247
v_accvgpr_write_b32 a225, v248
v_accvgpr_write_b32 a226, v249
v_accvgpr_write_b32 a227, v250
v_accvgpr_read_b32 v247, a228
v_accvgpr_read_b32 v248, a229
v_accvgpr_read_b32 v249, a230
v_accvgpr_read_b32 v250, a231
v_mul_f32 v247, v247, v252
v_mul_f32 v248, v248, v252
v_mul_f32 v249, v249, v252
v_mul_f32 v250, v250, v252
v_accvgpr_write_b32 a228, v247
v_accvgpr_write_b32 a229, v248
v_accvgpr_write_b32 a230, v249
v_accvgpr_write_b32 a231, v250
v_accvgpr_read_b32 v247, a232
v_accvgpr_read_b32 v248, a233
v_accvgpr_read_b32 v249, a234
v_accvgpr_read_b32 v250, a235
v_mul_f32 v247, v247, v253
v_mul_f32 v248, v248, v253
v_mul_f32 v249, v249, v253
v_mul_f32 v250, v250, v253
v_accvgpr_write_b32 a232, v247
v_accvgpr_write_b32 a233, v248
v_accvgpr_write_b32 a234, v249
v_accvgpr_write_b32 a235, v250
v_accvgpr_read_b32 v247, a236
v_accvgpr_read_b32 v248, a237
v_accvgpr_read_b32 v249, a238
v_accvgpr_read_b32 v250, a239
v_mul_f32 v247, v247, v254
v_mul_f32 v248, v248, v254
v_mul_f32 v249, v249, v254
v_mul_f32 v250, v250, v254
v_accvgpr_write_b32 a236, v247
v_accvgpr_write_b32 a237, v248
v_accvgpr_write_b32 a238, v249
v_accvgpr_write_b32 a239, v250
v_accvgpr_read_b32 v247, a240
v_accvgpr_read_b32 v248, a241
v_accvgpr_read_b32 v249, a242
v_accvgpr_read_b32 v250, a243
v_mul_f32 v247, v247, v251
v_mul_f32 v248, v248, v251
v_mul_f32 v249, v249, v251
v_mul_f32 v250, v250, v251
v_accvgpr_write_b32 a240, v247
v_accvgpr_write_b32 a241, v248
v_accvgpr_write_b32 a242, v249
v_accvgpr_write_b32 a243, v250
v_accvgpr_read_b32 v247, a244
v_accvgpr_read_b32 v248, a245
v_accvgpr_read_b32 v249, a246
v_accvgpr_read_b32 v250, a247
v_mul_f32 v247, v247, v252
v_mul_f32 v248, v248, v252
v_mul_f32 v249, v249, v252
v_mul_f32 v250, v250, v252
v_accvgpr_write_b32 a244, v247
v_accvgpr_write_b32 a245, v248
v_accvgpr_write_b32 a246, v249
v_accvgpr_write_b32 a247, v250
v_accvgpr_read_b32 v247, a248
v_accvgpr_read_b32 v248, a249
v_accvgpr_read_b32 v249, a250
v_accvgpr_read_b32 v250, a251
v_mul_f32 v247, v247, v253
v_mul_f32 v248, v248, v253
v_mul_f32 v249, v249, v253
v_mul_f32 v250, v250, v253
v_accvgpr_write_b32 a248, v247
v_accvgpr_write_b32 a249, v248
v_accvgpr_write_b32 a250, v249
v_accvgpr_write_b32 a251, v250
v_accvgpr_read_b32 v247, a252
v_accvgpr_read_b32 v248, a253
v_accvgpr_read_b32 v249, a254
v_accvgpr_read_b32 v250, a255
v_mul_f32 v247, v247, v254
v_mul_f32 v248, v248, v254
v_mul_f32 v249, v249, v254
v_mul_f32 v250, v250, v254
v_accvgpr_write_b32 a252, v247
v_accvgpr_write_b32 a253, v248
v_accvgpr_write_b32 a254, v249
v_accvgpr_write_b32 a255, v250
s_mov_b32 s63, 0
s_nop 3
s_setpc_b64 s[64:65]
LEND:
)ASM"
               :
               : "s"(x), "s"(wq), "s"(k), "s"(v), "s"(wo), "s"(part), "s"(wave), "s"(ldsBase), "s"(dbg), "s"(out), "s"(flags), "s"(hp), "s"(b * 2 + qh)
               : "v0","v1","v2","v3","v4","v5","v6","v7","v8","v9","v10","v11","v12","v13","v14","v15","v16","v17","v18","v19","v20","v21","v22","v23","v24","v25","v26","v27","v28","v29","v30","v31","v32","v33","v34","v35","v36","v37","v38","v39","v40","v41","v42","v43","v44","v45","v46","v47","v48","v49","v50","v51","v52","v53","v54","v55","v56","v57","v58","v59","v60","v61","v62","v63","v64","v65","v66","v67","v68","v69","v70","v71","v72","v73","v74","v75","v76","v77","v78","v79","v80","v81","v82","v83","v84","v85","v86","v87","v88","v89","v90","v91","v92","v93","v94","v95","v96","v97","v98","v99","v100","v101","v102","v103","v104","v105","v106","v107","v108","v109","v110","v111","v112","v113","v114","v115","v116","v117","v118","v119","v120","v121","v122","v123","v124","v125","v126","v127","v128","v129","v130","v131","v132","v133","v134","v135","v136","v137","v138","v139","v140","v141","v142","v143","v144","v145","v146","v147","v148","v149","v150","v151","v152","v153","v154","v155","v156","v157","v158","v159","v160","v161","v162","v163","v164","v165","v166","v167","v168","v169","v170","v171","v172","v173","v174","v175","v176","v177","v178","v179","v180","v181","v182","v183","v184","v185","v186","v187","v188","v189","v190","v191","v192","v193","v194","v195","v196","v197","v198","v199","v200","v201","v202","v203","v204","v205","v206","v207","v208","v209","v210","v211","v212","v213","v214","v215","v216","v217","v218","v219","v220","v221","v222","v223","v224","v225","v226","v227","v228","v229","v230","v231","v232","v233","v234","v235","v236","v237","v238","v239","v240","v241","v242","v243","v244","v245","v246","v247","v248","v249","v250","v251","v252","v253","v254","v255","a0","a1","a2","a3","a4","a5","a6","a7","a8","a9","a10","a11","a12","a13","a14","a15","a16","a17","a18","a19","a20","a21","a22","a23","a24","a25","a26","a27","a28","a29","a30","a31","a32","a33","a34","a35","a36","a37","a38","a39","a40","a41","a42","a43","a44","a45","a46","a47","a48","a49","a50","a51","a52","a53","a54","a55","a56","a57","a58","a59","a60","a61","a62","a63","a64","a65","a66","a67","a68","a69","a70","a71","a72","a73","a74","a75","a76","a77","a78","a79","a80","a81","a82","a83","a84","a85","a86","a87","a88","a89","a90","a91","a92","a93","a94","a95","a96","a97","a98","a99","a100","a101","a102","a103","a104","a105","a106","a107","a108","a109","a110","a111","a112","a113","a114","a115","a116","a117","a118","a119","a120","a121","a122","a123","a124","a125","a126","a127","a128","a129","a130","a131","a132","a133","a134","a135","a136","a137","a138","a139","a140","a141","a142","a143","a144","a145","a146","a147","a148","a149","a150","a151","a152","a153","a154","a155","a156","a157","a158","a159","a160","a161","a162","a163","a164","a165","a166","a167","a168","a169","a170","a171","a172","a173","a174","a175","a176","a177","a178","a179","a180","a181","a182","a183","a184","a185","a186","a187","a188","a189","a190","a191","a192","a193","a194","a195","a196","a197","a198","a199","a200","a201","a202","a203","a204","a205","a206","a207","a208","a209","a210","a211","a212","a213","a214","a215","a216","a217","a218","a219","a220","a221","a222","a223","a224","a225","a226","a227","a228","a229","a230","a231","a232","a233","a234","a235","a236","a237","a238","a239","a240","a241","a242","a243","a244","a245","a246","a247","a248","a249","a250","a251","a252","a253","a254","a255","s34","s35","s36","s37","s38","s39","s40","s41","s42","s43","s44","s45","s46","s47","s48","s49","s50","s51","s52","s53","s54","s55","s56","s57","s58","s59","s60","s61","s62","s63","s64","s65","s66","s67","s68","s69","s70","s71","s72","s73","s74","s75","s76","s77","s78","s79","s80","s81","s82","s83","s84","s85","s86","s87","s88","s89","s90","s91","s92","s93","s94","s95","s96","s97","s98","s99","s100","s101", "vcc", "scc", "memory");
}

extern "C" void kernel_launch(void* const* d_in, const int* in_sizes, int n_in, void* d_out, int out_size, void* d_ws, size_t ws_size,
                              hipStream_t stream) {
  mha_fused_kernel<<<256, 256, 0, stream>>>((const char*)d_in[0], (const char*)d_in[1], (const char*)d_in[2], (const char*)d_in[3],
                                            (const char*)d_in[4], (const char*)d_in[5], (const char*)d_in[6], (const char*)d_in[7],
                                            (const char*)d_in[8], (const char*)d_in[9], (char*)d_ws + WS_OFF, (char*)d_out);
}
```
